```python
import torch

def _softmax(x, axis=-1):
    return torch.softmax(x, dim=axis)

def _gelu(x):
    return torch.nn.functional.gelu(x, approximate='tanh')

def _silu(x):
    return torch.nn.functional.silu(x)

def _sigmoid(x):
    return torch.sigmoid(x)

def _layernorm(x, axis=-1, eps=1e-05):
    m = x.mean(dim=axis, keepdim=True)
    v = x.var(dim=axis, keepdim=True, unbiased=False)
    return (x - m) / torch.sqrt(v + eps)

def _scatter_add(tgt, idx, src, axis=0):
    return tgt.clone().index_add_(axis, idx, src)

def _segment_sum(src, seg, n):
    out = torch.zeros((n,) + src.shape[1:], dtype=src.dtype)
    return out.index_add_(0, seg, src)

def _pad(x, pw):
    flat = tuple((v for pair in reversed(pw) for v in pair))
    return torch.nn.functional.pad(x, flat)
kernel_name = 'adaLN_cond__ssm_conv_h3__mixer_gmlp__prompt'

def setup_inputs():

    _w = lambda a: (lambda t: t.to(torch.bfloat16) if t.is_floating_point() else t)(torch.as_tensor(a))

    def _A_setup():
        B, S, D = (1, 2048, 640)
        ks = 4
        return {'q': _w((lambda *s: torch.randn(*s, dtype=torch.bfloat16) * 0.05)(B, S, D)), 'k': _w((lambda *s: torch.randn(*s, dtype=torch.bfloat16) * 1.6)(B, S, D)), 'v': _w((lambda *s: torch.randn(*s, dtype=torch.bfloat16) * 1.6)(B, S, D)), 'shift_k': _w((lambda *s: torch.randn(*s, dtype=torch.bfloat16) * 1.6)(D, ks) * 0.5), 'ssm_k': _w((lambda *s: torch.randn(*s, dtype=torch.bfloat16) * 1.6)(D, S) * (1.0 / S ** 0.5)), 'ssm_D': _w((lambda *s: torch.randn(*s, dtype=torch.bfloat16) * 1.6)(D))}
    _A_in = _A_setup()

    def _B_setup():
        B, S, D, F = (1, 2048, 640, 1728)
        Fh = F // 2
        return {'x': _w((lambda *s: torch.randn(*s, dtype=torch.bfloat16) * 0.05)(B, S, D)), 'w_in': _w((lambda *s: torch.randn(*s, dtype=torch.bfloat16) * 0.8)(D, 2 * Fh)), 'w_sp': _w(0.01 * (lambda *s: torch.randn(*s, dtype=torch.bfloat16) * 0.8)(S, S)), 'b_sp': _w(1.0 + 0.01 * (lambda *s: torch.randn(*s, dtype=torch.bfloat16) * 0.1)(S)), 'w_out': _w((lambda *s: torch.randn(*s, dtype=torch.bfloat16) * 0.1)(Fh, D))}
    _B_in = _B_setup()
    return {'A_q': _A_in['q'], 'A_k': _A_in['k'], 'A_v': _A_in['v'], 'A_shift_k': _A_in['shift_k'], 'A_ssm_k': _A_in['ssm_k'], 'A_ssm_D': _A_in['ssm_D'], 'B_x': _B_in['x'], 'B_w_in': _B_in['w_in'], 'B_w_sp': _B_in['w_sp'], 'B_b_sp': _B_in['b_sp'], 'B_w_out': _B_in['w_out'], 'c': _w((lambda *s: torch.randn(*s, dtype=torch.bfloat16) * 0.1)(1, 640) * 8.0), 'w_mod': _w((lambda *s: torch.randn(*s, dtype=torch.bfloat16) * 0.1)(640, 6 * 640) * 640 ** (-0.5) * 8.0)}

def reference(A_q, A_k, A_v, A_shift_k, A_ssm_k, A_ssm_D, B_x, B_w_in, B_w_sp, B_b_sp, B_w_out, c, w_mod):

    def _A_ref(q, k, v, shift_k, ssm_k, ssm_D):
        B, S, D = q.shape
        ks = shift_k.shape[-1]
        k_pad = _pad(k, ((0, 0), (ks - 1, 0), (0, 0)))
        k_shift = torch.zeros_like(k)
        for t in range(ks):
            k_shift = k_shift + k_pad[:, t:t + S, :] * shift_k[:, ks - 1 - t][None, None, :]
        kv = k_shift * v
        L = 2 * S
        kv_pad = _pad(kv, ((0, 0), (0, S), (0, 0)))
        kf_pad = _pad(ssm_k, ((0, 0), (0, S)))
        kv_f = (lambda x, axes=(-2, -1): torch.fft.rfft2(x.float(), dim=axes))(kv_pad[:, None, :, :], axes=(1, 2))
        kf_f = (lambda x, axes=(-2, -1): torch.fft.rfft2(x.float(), dim=axes))(kf_pad[:, None, :], axes=(1, 2))
        kf_f = (lambda x, *d: x.permute(*d))(kf_f, 1, 2, 0)
        y_f = kv_f * kf_f[None, :, :, :]
        y = (lambda x, s, axes=(-2, -1): torch.fft.irfft2(x, s=s, dim=axes).to(torch.bfloat16))(y_f, (1, L), axes=(1, 2))[:, 0, :S, :]
        y = y + kv * ssm_D[None, None, :]
        return q * y

    def _B_ref(x, w_in, w_sp, b_sp, w_out):
        B, S, D = x.shape
        F = w_in.shape[-1]
        Fh = F // 2
        z = _gelu(torch.einsum('bsd,df->bsf', x, w_in))
        z1 = z[..., :Fh].clone()
        z2 = _layernorm(z[..., Fh:], axis=-1)
        g = torch.einsum('ts,bsf->btf', w_sp, z2) + b_sp[None, :, None]
        return torch.einsum('bsf,fd->bsd', z1 * g, w_out)
    x = A_q
    mod = (_silu(c) @ w_mod).reshape(c.shape[0], 6, 1, -1)
    sa, ca, ga, sb, cb, gb = (mod[:, 0].clone(), mod[:, 1].clone(), mod[:, 2].clone(), mod[:, 3].clone(), mod[:, 4].clone(), mod[:, 5].clone())
    h = _layernorm(x, axis=-1) * (1 + ca) + sa
    x = x + ga * _A_ref(h, A_k, A_v, A_shift_k, A_ssm_k, A_ssm_D)
    h = _layernorm(x, axis=-1) * (1 + cb) + sb
    x = x + gb * _B_ref(h, B_w_in, B_w_sp, B_b_sp, B_w_out)
    return x
```

```cpp
#include <hip/hip_runtime.h>
#include <stdint.h>

typedef unsigned short u16;
typedef __attribute__((ext_vector_type(8))) short bf16x8;
typedef __attribute__((ext_vector_type(4))) float f32x4;
typedef __attribute__((ext_vector_type(2))) float f32x2;
typedef __attribute__((ext_vector_type(4))) unsigned int u32x4;
typedef __attribute__((ext_vector_type(2))) unsigned int u32x2;
#define LAS __attribute__((address_space(3)))
#define CAS __attribute__((address_space(4)))
#define GAS __attribute__((address_space(1)))
typedef __amdgpu_buffer_rsrc_t rsrc_t;
typedef __attribute__((ext_vector_type(4))) short s16x4;

constexpr int S_ = 2048, D_ = 640, F_ = 1728, FH = 864, CB = 108;

#define X1PAD 256
constexpr unsigned WS_EPOCH = 0;
constexpr unsigned WS_FX = 4096;
constexpr unsigned WS_FZ = WS_FX + 24576;
constexpr unsigned WS_FW2 = WS_FZ + 32768;
constexpr unsigned WS_FW1 = WS_FW2 + 8192;
constexpr unsigned WS_CS = WS_FW1 + 65536;
constexpr unsigned WS_PART = WS_CS + 131072;
constexpr unsigned WS_MOD = WS_PART + 262144;
constexpr unsigned WS_ST1 = WS_MOD + 32768;
constexpr unsigned WS_X1B = WS_ST1 + 16384;
constexpr unsigned WX1P = 40960 + X1PAD;
constexpr unsigned WBLK = 143360;
constexpr unsigned WS_QB = WS_X1B + 2686976;
constexpr unsigned WS_WINF = WS_QB + 2686976;
constexpr unsigned WS_WOUTF = WS_WINF + 16 * WBLK;
constexpr unsigned WS_Z2F = WS_WOUTF + 8 * WBLK;
constexpr unsigned WS_UF = WS_Z2F + 8 * 64 * 7168;
constexpr unsigned WS_STAMP = WS_UF + 32 * 4 * 8 * 3584;
constexpr unsigned WS_END = WS_STAMP + 256 * 40 * 8;

constexpr int U_BYTES = (256 + S_) * 2;
constexpr int G_COPY = 4160;
constexpr int CH_BYTES = U_BYTES + 4 * G_COPY;
constexpr int L_YP = 4 * CH_BYTES;
constexpr int L_RED = 0;
constexpr int L_TAB = 2048;
constexpr int L_J3 = 8192;
constexpr int L_QT = 49152;
constexpr int L_J5 = L_QT;
constexpr int L_XI = 0;
constexpr int L_BUF = 81920;
constexpr int L_AB = 0;
constexpr int L_MISC = 147456;
constexpr int L_Z1S = L_MISC;
constexpr int L_S1 = L_MISC + 14336;
constexpr int L_T2 = L_S1 + 512;
constexpr int L_ST2 = L_T2 + 512;
constexpr int L_GB = L_ST2 + 512;
constexpr int L_CS1 = L_Z1S + 4096;
constexpr int L_WS = 81920;
constexpr int LDS_TOTAL = 163840;

constexpr int POLL_SLEEP = 1;
constexpr int AUX_SLEEP = 36, LATE_SLEEP = 64;
constexpr int NRAW = 8;
constexpr int NB2A = 6;
constexpr int G2W = 7;
constexpr int Z1SPLIT = 12;
constexpr int SC1 = 16;
constexpr unsigned SPIN_CAP = 1u << 16;
constexpr unsigned OOB = 0x80000000u;

__device__ __forceinline__ float bflo(unsigned x) { return __uint_as_float(x << 16); }
__device__ __forceinline__ float bfhi(unsigned x) { return __uint_as_float(x & 0xffff0000u); }
__device__ __forceinline__ f32x2 bf2(unsigned x) { return (f32x2){bflo(x), bfhi(x)}; }
__device__ __forceinline__ unsigned f2bf(float x) {
  __bf16 b = (__bf16)x;
  return (unsigned)__builtin_bit_cast(unsigned short, b);
}
__device__ __forceinline__ float rbf(float x) { return bflo(f2bf(x)); }
typedef __attribute__((ext_vector_type(2))) __bf16 bf2_t;
__device__ __forceinline__ unsigned pack2(float lo, float hi) {
  f32x2 v = {lo, hi};
  return __builtin_bit_cast(unsigned, __builtin_convertvector(v, bf2_t));
}
template <int CTRL>
__device__ __forceinline__ float dpp_f(float v) {
  return __int_as_float(__builtin_amdgcn_update_dpp(0, __float_as_int(v), CTRL, 0xf, 0xf, true));
}
__device__ __forceinline__ float sum_rows(float v) {
  auto a = __builtin_amdgcn_permlane16_swap(__float_as_uint(v), __float_as_uint(v), false, false);
  v = __uint_as_float(a[0]) + __uint_as_float(a[1]);
  auto b = __builtin_amdgcn_permlane32_swap(__float_as_uint(v), __float_as_uint(v), false, false);
  return __uint_as_float(b[0]) + __uint_as_float(b[1]);
}
__device__ __forceinline__ f32x2 gelu2(f32x2 x) {
  const float c0 = -2.0f * 0.7978845608028654f * 1.4426950408889634f;
  const float c1 = c0 * 0.044715f;
  const f32x2 t = x * __builtin_elementwise_fma(x * x, (f32x2){c1, c1}, (f32x2){c0, c0});
  const f32x2 d = (f32x2){__builtin_amdgcn_exp2f(t[0]), __builtin_amdgcn_exp2f(t[1])} + (f32x2){1.0f, 1.0f};
  return x * (f32x2){__builtin_amdgcn_rcpf(d[0]), __builtin_amdgcn_rcpf(d[1])};
}
#define LO2(v) __builtin_shufflevector(v, v, 0, 1)
#define HI2(v) __builtin_shufflevector(v, v, 2, 3)
__device__ __forceinline__ float tr_sum16(const float* v, int l) {
  float x[8], y[4];
  asm volatile(
      "s_nop 1\n\t"
      "v_add_f32_dpp %0, %8, %8 row_ror:8 row_mask:0xf bank_mask:0x3\n\t"
      "v_add_f32_dpp %1, %9, %9 row_ror:8 row_mask:0xf bank_mask:0x3\n\t"
      "v_add_f32_dpp %2, %10, %10 row_ror:8 row_mask:0xf bank_mask:0x3\n\t"
      "v_add_f32_dpp %3, %11, %11 row_ror:8 row_mask:0xf bank_mask:0x3\n\t"
      "v_add_f32_dpp %4, %12, %12 row_ror:8 row_mask:0xf bank_mask:0x3\n\t"
      "v_add_f32_dpp %5, %13, %13 row_ror:8 row_mask:0xf bank_mask:0x3\n\t"
      "v_add_f32_dpp %6, %14, %14 row_ror:8 row_mask:0xf bank_mask:0x3\n\t"
      "v_add_f32_dpp %7, %15, %15 row_ror:8 row_mask:0xf bank_mask:0x3\n\t"
      "v_add_f32_dpp %0, %16, %16 row_ror:8 row_mask:0xf bank_mask:0xc\n\t"
      "v_add_f32_dpp %1, %17, %17 row_ror:8 row_mask:0xf bank_mask:0xc\n\t"
      "v_add_f32_dpp %2, %18, %18 row_ror:8 row_mask:0xf bank_mask:0xc\n\t"
      "v_add_f32_dpp %3, %19, %19 row_ror:8 row_mask:0xf bank_mask:0xc\n\t"
      "v_add_f32_dpp %4, %20, %20 row_ror:8 row_mask:0xf bank_mask:0xc\n\t"
      "v_add_f32_dpp %5, %21, %21 row_ror:8 row_mask:0xf bank_mask:0xc\n\t"
      "v_add_f32_dpp %6, %22, %22 row_ror:8 row_mask:0xf bank_mask:0xc\n\t"
      "v_add_f32_dpp %7, %23, %23 row_ror:8 row_mask:0xf bank_mask:0xc"
      : "=&v"(x[0]), "=&v"(x[1]), "=&v"(x[2]), "=&v"(x[3]), "=&v"(x[4]), "=&v"(x[5]), "=&v"(x[6]), "=&v"(x[7])
      : "v"(v[0]), "v"(v[1]), "v"(v[2]), "v"(v[3]), "v"(v[4]), "v"(v[5]), "v"(v[6]), "v"(v[7]), "v"(v[8]), "v"(v[9]), "v"(v[10]),
        "v"(v[11]), "v"(v[12]), "v"(v[13]), "v"(v[14]), "v"(v[15]));
  asm volatile(
      "s_nop 1\n\t"
      "v_add_f32_dpp %0, %4, %4 row_shl:4 row_mask:0xf bank_mask:0x5\n\t"
      "v_add_f32_dpp %1, %5, %5 row_shl:4 row_mask:0xf bank_mask:0x5\n\t"
      "v_add_f32_dpp %2, %6, %6 row_shl:4 row_mask:0xf bank_mask:0x5\n\t"
      "v_add_f32_dpp %3, %7, %7 row_shl:4 row_mask:0xf bank_mask:0x5\n\t"
      "v_add_f32_dpp %0, %8, %8 row_shr:4 row_mask:0xf bank_mask:0xa\n\t"
      "v_add_f32_dpp %1, %9, %9 row_shr:4 row_mask:0xf bank_mask:0xa\n\t"
      "v_add_f32_dpp %2, %10, %10 row_shr:4 row_mask:0xf bank_mask:0xa\n\t"
      "v_add_f32_dpp %3, %11, %11 row_shr:4 row_mask:0xf bank_mask:0xa\n\t"
      "s_nop 1"
      : "=&v"(y[0]), "=&v"(y[1]), "=&v"(y[2]), "=&v"(y[3])
      : "v"(x[0]), "v"(x[1]), "v"(x[2]), "v"(x[3]), "v"(x[4]), "v"(x[5]), "v"(x[6]), "v"(x[7]));
  const bool b1 = l & 2, b0 = l & 1;
  const float z0 = (b1 ? y[2] : y[0]) + dpp_f<0x4E>(b1 ? y[0] : y[2]);
  const float z1 = (b1 ? y[3] : y[1]) + dpp_f<0x4E>(b1 ? y[1] : y[3]);
  return (b0 ? z1 : z0) + dpp_f<0xB1>(b0 ? z0 : z1);
}
__device__ __forceinline__ u32x4 half_swap(u32x4 v) {
  const auto a = __builtin_amdgcn_permlane32_swap(v[0], v[2], false, false);
  const auto b = __builtin_amdgcn_permlane32_swap(v[1], v[3], false, false);
  return (u32x4){a[0], b[0], a[1], b[1]};
}
#define MFMA16(a, b, c) __builtin_amdgcn_mfma_f32_16x16x32_bf16(__builtin_bit_cast(bf16x8, a), __builtin_bit_cast(bf16x8, b), c, 0, 0, 0)
__device__ __forceinline__ u32x2 trd(const char* p) {
  return __builtin_bit_cast(u32x2, __builtin_amdgcn_ds_read_tr16_b64_v4i16((LAS s16x4*)p));
}
__device__ __forceinline__ u32x4 trfrag(const char* p, int second) {
  u32x2 a = trd(p), b = trd(p + second);
  return (u32x4){a[0], a[1], b[0], b[1]};
}
#define BAR() asm volatile("s_waitcnt lgkmcnt(0)\n\ts_barrier" ::: "memory")
#define DRAIN() do { __builtin_amdgcn_s_waitcnt(0x0F70); asm volatile("" ::: "memory"); } while (0)
#define VMW(n) do { __builtin_amdgcn_s_waitcnt(0x0F70 | ((n) & 15) | (((n) >> 4) << 14)); asm volatile("" ::: "memory"); } while (0)
#define SCHED() __builtin_amdgcn_sched_barrier(0)
#define ZERO4 ((f32x4){0.f, 0.f, 0.f, 0.f})

__device__ __forceinline__ u32x4 ld16(rsrc_t r, unsigned off, unsigned so = 0) { return __builtin_amdgcn_raw_buffer_load_b128(r, off, so, SC1); }
__device__ __forceinline__ u32x2 ld8(rsrc_t r, unsigned off, unsigned so = 0) { return __builtin_amdgcn_raw_buffer_load_b64(r, off, so, SC1); }
__device__ __forceinline__ unsigned ld4(rsrc_t r, unsigned off, unsigned so = 0) { return __builtin_amdgcn_raw_buffer_load_b32(r, off, so, SC1); }
__device__ __forceinline__ void st16(rsrc_t r, unsigned off, u32x4 v) { __builtin_amdgcn_raw_buffer_store_b128(v, r, off, 0, SC1); }
__device__ __forceinline__ void st8(rsrc_t r, unsigned off, u32x2 v) { __builtin_amdgcn_raw_buffer_store_b64(v, r, off, 0, SC1); }
__device__ __forceinline__ void st4(rsrc_t r, unsigned off, unsigned v) { __builtin_amdgcn_raw_buffer_store_b32(v, r, off, 0, SC1); }
__device__ __forceinline__ void dma1k(rsrc_t r, char* lds_dst, unsigned l16, unsigned soff) {
  __builtin_amdgcn_raw_ptr_buffer_load_lds(r, (LAS void*)lds_dst, 16, l16, soff, 0, SC1);
}

#define STAMP(i)

template <int SLEEP>
__device__ __forceinline__ void wait_flag(rsrc_t r, unsigned off, unsigned tag) {
  _Pragma("unroll 1") for (unsigned spins = 0; spins < SPIN_CAP; ++spins) {
    if (__all(ld4(r, off) == tag)) break;
    __builtin_amdgcn_s_sleep(SLEEP);
  }
  asm volatile("" ::: "memory");
}

struct Params {
  const u16 *q, *k, *v, *shift_k, *ssm_k, *ssm_D, *w_in, *w_sp, *b_sp, *w_out, *cvec, *w_mod;
  u16* out;
  char* ws;
};

__global__ __launch_bounds__(512, 2) void mega(Params p) {
  __shared__ __attribute__((aligned(16))) char lds[LDS_TOTAL];
  const int tid = threadIdx.x, l = tid & 63;
  const int w = __builtin_amdgcn_readfirstlane(tid >> 6);
  const int bid = blockIdx.x, xcd = bid & 7, idx = bid >> 3;
  char* const ws = p.ws;
  asm volatile("" ::"s"(p.q), "s"(p.k), "s"(p.v), "s"(p.shift_k), "s"(p.ssm_k), "s"(p.ssm_D), "s"(p.w_in), "s"(p.w_sp), "s"(p.b_sp),
               "s"(p.w_out), "s"(p.cvec), "s"(p.w_mod), "s"(p.out), "s"(p.ws));
  const rsrc_t rs = __builtin_amdgcn_make_buffer_rsrc(ws, 0, WS_END, 0x00020000);
  const unsigned l16 = (unsigned)l * 16u;
  STAMP(0);
  const unsigned epv = ld4(rs, WS_EPOCH);
  const int dg = ((idx & 7) + 2 * (idx >> 3)) & 7;
  const bool is_conv = dg >= 3;
  const int slot = 3 * (idx >> 3) + dg;
  const int cidx = 5 * (idx >> 3) + dg - 3;
  unsigned cpf = 0;
  if (!is_conv) {
    unsigned long long pc;
    unsigned len, len2;
    asm volatile("s_getpc_b64 %0\n.Lmega_here:\n\ts_mov_b32 %1, .Lmega_end-.Lmega_here\n\ts_mov_b32 %2, .Lmega_aux_end-.Lmega_here" : "=s"(pc), "=s"(len), "=s"(len2));
    len = len > len2 ? len : len2;
    unsigned o = (unsigned)((slot * 8 + w) * 1024 + (l & 7) * 128);
    o = o + 16u <= len ? o : 0u;
    cpf = *(const GAS unsigned*)((pc & ~15ull) + o);
  }

  const int quad = xcd * 20 + cidx;
  const int R = (xcd & 3) * 8 + (idx & 7);
  const int c = (xcd >> 2) * 4 + (idx >> 3);
  const int n16 = l & 15, h4 = l >> 4;
  const rsrc_t rq = __builtin_amdgcn_make_buffer_rsrc((void*)p.q, 0, S_ * D_ * 2, 0x00020000);
  const rsrc_t rsp = __builtin_amdgcn_make_buffer_rsrc((void*)p.w_sp, 0, S_ * S_ * 2, 0x00020000);
  const int wj = w < 7 ? w : 0;
  u32x4 wsp[4][8];
  const unsigned wso = (unsigned)(R * 64) * 4096u + (unsigned)w * 512u;
  const unsigned wdo = (unsigned)(h4 * 8192 + ((n16 ^ h4) * 16));
#define WVO(q) (wdo ^ (unsigned)(((4 - (q)) & 3) * 64))
#define WSO(g, q) (wso + (unsigned)((g) >> 2) * 256u + (unsigned)(32 * (((g) >> 1) & 1) + ((g) & 1) + 8 * (q)) * 4096u)
  u32x4 raw[NRAW];
#define LDW(i) raw[i] = __builtin_amdgcn_raw_buffer_load_b128(rsp, WVO((i) & 3), WSO((i) >> 2, (i) & 3), 0)
#define LDWQ(k) do { _Pragma("unroll") for (int i_ = (k) * (NRAW / 4); i_ < ((k) + 1) * (NRAW / 4); ++i_) LDW(i_); } while (0)
  const int trow = R * 64 + 32 * ((w >> 1) & 1) + 2 * n16 + (w & 1);
  unsigned bs_raw;
  unsigned tag;
  unsigned fw2 = OOB;
  unsigned e_fw1;
  const unsigned fw1o = WS_FW1 + (unsigned)(((8 + c) * 8 + wj) * 4 + (l & 3)) * 128u;

  if (__builtin_expect(is_conv, 1)) {
    const rsrc_t rk = __builtin_amdgcn_make_buffer_rsrc((void*)p.k, 0, S_ * D_ * 2, 0x00020000);
    const rsrc_t rv = __builtin_amdgcn_make_buffer_rsrc((void*)p.v, 0, S_ * D_ * 2, 0x00020000);
    const unsigned qo = (unsigned)quad * 8u;
    __builtin_amdgcn_s_setprio(3);
    STAMP(32);
    unsigned tch, t2o, tch2;
    unsigned long long fbase = (unsigned long long)p.ssm_k + (unsigned)quad * 16384u, kvb = (unsigned long long)(w & 1 ? p.v : p.k);
    asm volatile("" : "+s"(fbase), "+s"(kvb));
    {
      int i = (cidx * 4 + (w >> 1)) * 26 + (l < 25 ? l : 25);
      i = i < 2048 ? i : 2047;
      const unsigned o = l < 48 ? (unsigned)((i >> 1) * 1280 + 160 * xcd + (i & 1) * 156) : (unsigned)(w * 2048 + (l & 15) * 128);
      tch = *(const GAS unsigned*)((l < 48 ? kvb : fbase) + o);
      SCHED();
      t2o = (unsigned)((i >> 1) * 1280 + 160 * xcd + (i & 1) * 156);
    }
    u32x2 halo[2][3], fnx[2];
    {
      const CAS char* kb = (const CAS char*)p.k + qo + (unsigned)(128 * w) * 1280u;
#pragma unroll
      for (int e = 0; e < 3; ++e) {
        halo[0][e] = *(const CAS u32x2*)(kb + (w ? (e - 3) * 1280 : 0));
        halo[1][e] = *(const CAS u32x2*)(kb + 1310720 + (e - 3) * 1280);
      }
    }
    u32x4 sh0 = *(const CAS u32x4*)((const CAS char*)p.shift_k + quad * 32), sh1 = *(const CAS u32x4*)((const CAS char*)p.shift_k + quad * 32 + 16);
    u32x2 dd2 = *(const CAS u32x2*)((const CAS char*)p.ssm_D + qo);
#pragma unroll
    for (int j = 0; j < 2; ++j) fnx[j] = *(const CAS u32x2*)((const CAS char*)fbase + j * 8192 + ((w & 3) == 3 ? 0 : (w + 1) * 1024));
    u32x4 fd[2];
    const unsigned ro = (unsigned)tid * 2560u;
    u32x2 ka[2][2], va[2][2];
#define GK(H, e) ka[H][e] = __builtin_amdgcn_raw_buffer_load_b64(rk, ro + (e) * 1280u, qo + (H) * 1310720u, 0)
#define GV(H, e) va[H][e] = __builtin_amdgcn_raw_buffer_load_b64(rv, ro + (e) * 1280u, qo + (H) * 1310720u, 0)
    SCHED();
    __builtin_amdgcn_s_setprio(2);
    GK(0, 0); GK(0, 1);
#pragma unroll
    for (int j = 0; j < 2; ++j) fd[j] = *(const GAS u32x4*)((const GAS char*)fbase + tid * 16 + j * 8192);
    GV(0, 0); GV(0, 1);
    SCHED();
    __builtin_amdgcn_s_setprio(1);
    tch2 = *(const GAS unsigned*)(kvb + 1310720u + t2o);
    SCHED();
    STAMP(34);
    __builtin_amdgcn_s_setprio(0);
    SCHED();
    asm volatile("" : "+s"(halo[0][0]), "+s"(halo[0][1]), "+s"(halo[0][2]), "+s"(halo[1][0]), "+s"(halo[1][1]), "+s"(halo[1][2]), "+s"(sh0), "+s"(sh1),
                 "+s"(dd2), "+s"(fnx[0]), "+s"(fnx[1]));
    if (w == 0) halo[0][0] = halo[0][1] = halo[0][2] = (u32x2){0u, 0u};
    if ((w & 3) == 3) fnx[0] = fnx[1] = (u32x2){0u, 0u};
    STAMP(35);
#pragma unroll
    for (int j = 0; j < 2; ++j) {
      char* G = lds + ((tid >> 8) + 2 * j) * CH_BYTES + U_BYTES + ((tid & 255) + 2) * 16;
      const u32x4 d = fd[j];
      const unsigned n0 = __builtin_amdgcn_update_dpp(fnx[j][0], d[0], 0x130, 0xf, 0xf, false);
      const unsigned n1 = __builtin_amdgcn_update_dpp(fnx[j][1], d[1], 0x130, 0xf, 0xf, false);
      const unsigned a0 = __builtin_amdgcn_alignbit(d[1], d[0], 16), a1 = __builtin_amdgcn_alignbit(d[2], d[1], 16),
                     a2 = __builtin_amdgcn_alignbit(d[3], d[2], 16), a3 = __builtin_amdgcn_alignbit(n0, d[3], 16),
                     a4 = __builtin_amdgcn_alignbit(n1, n0, 16);
      *(u32x4*)(G) = d;
      *(u32x4*)(G + G_COPY) = (u32x4){a0, a1, a2, a3};
      *(u32x4*)(G + 2 * G_COPY) = (u32x4){d[1], d[2], d[3], n0};
      *(u32x4*)(G + 3 * G_COPY) = (u32x4){a1, a2, a3, a4};
      if ((tid & 255) == 0) {
        const u32x4 z = {0u, 0u, 0u, 0u};
#pragma unroll
        for (int sg = 0; sg < 4; ++sg) *(u32x4*)(G + sg * G_COPY - 32) = z;
        *(u32x4*)(G - 16) = z;
        *(u32x4*)(G + G_COPY - 16) = (u32x4){0u, 0u, 0u, d[0] << 16};
        *(u32x4*)(G + 2 * G_COPY - 16) = (u32x4){0u, 0u, 0u, d[0]};
        *(u32x4*)(G + 3 * G_COPY - 16) = (u32x4){0u, 0u, d[0] << 16, a0};
      }
    }
    if (tid >= 384) *(u32x4*)(lds + ((tid >> 5) & 3) * CH_BYTES + (tid & 31) * 16) = (u32x4){0u, 0u, 0u, 0u};
    asm volatile("" ::"v"(tch));
    STAMP(36);
    tag = __builtin_amdgcn_readfirstlane(epv) * 2654435761u + 0x9E3779B9u;
    auto kv_half = [&](int H) {
      char* Uw = lds + 512 + (H * 512 + (tid & ~3) + 3 - (tid & 3)) * 4;
#pragma unroll
      for (int e = 0; e < 2; ++e) {
        const unsigned a0 = ka[H][0][e], a1 = ka[H][1][e];
        const unsigned p0 = __builtin_amdgcn_update_dpp(halo[H][1][e], a0, 0x138, 0xf, 0xf, false);
        const unsigned p1 = __builtin_amdgcn_update_dpp(halo[H][2][e], a1, 0x138, 0xf, 0xf, false);
        const unsigned pp = __builtin_amdgcn_update_dpp(halo[H][0][e], p1, 0x138, 0xf, 0xf, false);
        const u32x4 sh = e ? sh1 : sh0;
        const f32x2 w0 = {bflo(sh[0]), bflo(sh[2])}, w1 = {bfhi(sh[0]), bfhi(sh[2])}, w2 = {bflo(sh[1]), bflo(sh[3])}, w3 = {bfhi(sh[1]), bfhi(sh[3])};
        const f32x2 km3 = bf2(pp), km2 = bf2(p0), km1 = bf2(p1), k0 = bf2(a0), k1 = bf2(a1);
        const f32x2 s0 = __builtin_elementwise_fma(k0, w0, __builtin_elementwise_fma(km1, w1, __builtin_elementwise_fma(km2, w2, km3 * w3)));
        const f32x2 s1 = __builtin_elementwise_fma(k1, w0, __builtin_elementwise_fma(k0, w1, __builtin_elementwise_fma(km1, w2, km2 * w3)));
        const f32x2 o0 = s0 * bf2(va[H][0][e]), o1 = s1 * bf2(va[H][1][e]);
        *(unsigned*)(Uw + (2 * e) * CH_BYTES) = pack2(o1[0], o0[0]);
        *(unsigned*)(Uw + (2 * e + 1) * CH_BYTES) = pack2(o1[1], o0[1]);
      }
    };
    kv_half(0);
    SCHED();
    STAMP(1);
    GK(1, 0);
    BAR();
    STAMP(2);
    const int te = 2 * tid;
    u32x4 qa[2];
    u32x4 sv[2];
    u32x2 mg;
    bool q_ok;
    const unsigned xo = (unsigned)(tid >> 4) * WX1P + (unsigned)quad * 256u + (unsigned)(tid & 15) * 16u;
    const unsigned mgo = WS_MOD + (unsigned)((l < 12 ? (l >> 2) * D_ + (l & 3) : 0) + quad * 4) * 8u;
    const int ch = w & 3, hf = w >> 2;
    f32x4 acc[8];
    {
      const char* U = lds + ch * CH_BYTES;
      const char* G = U + U_BYTES;
      const int a = n16, jj = h4 >> 1, e2 = h4 & 1;
      const int sg = (a + 1) & 3;
      const LAS char* bp = (const LAS char*)U + 512 + 32 * (a - jj) + 16 * e2 - 256 * hf;
      const LAS char* ap = (const LAS char*)G + sg * G_COPY + 2 * (16 * jj + a - 8 * e2 + 9 - sg) + 256 * hf;
      char* yp = lds + L_YP + (hf * 4 + ch) * 8192 + (h4 * 16 + ((a + 4 * h4) & 15)) * 16;
#pragma unroll
      for (int n = 0; n < 8; ++n) acc[n] = ZERO4;
      u32x4 A0[8], B0[4], A1[8], B1[4];
      auto lda = [&](int r, u32x4* A, int np) {
#pragma unroll
        for (int pp = 0; pp < 8; ++pp)
          if (pp < np) {
            u32x2 lo = *(const volatile LAS u32x2*)(ap + 512 * pp + 64 * r);
            u32x2 hi = *(const volatile LAS u32x2*)(ap + 512 * pp + 64 * r + 8);
            A[pp] = (u32x4){lo[0], lo[1], hi[0], hi[1]};
          }
      };
      auto ldb = [&](int r, u32x4* B, int m0) {
#pragma unroll
        for (int m = 0; m < 4; ++m) B[m] = *(const LAS u32x4*)(bp + 512 * (m0 + m) - 64 * r);
      };
      auto mm = [&](const u32x4* A, const u32x4* B, int m0, int p0 = 0, int p1 = 8) {
#pragma unroll
        for (int pp = p0; pp < p1; ++pp)
#pragma unroll
          for (int m = 0; m < 4; ++m)
            if (m0 + m + pp < 8) acc[m0 + m + pp] = MFMA16(A[pp], B[m], acc[m0 + m + pp]);
      };
      auto mmi = [&](const u32x4* A, const u32x4* B, int m0, u32x4* An, u32x4* Bn, int rn, int mn, int nb, int na) {
        int i = 0;
#pragma unroll
        for (int pp = 0; pp < 8; ++pp)
#pragma unroll
          for (int m = 0; m < 4; ++m)
            if (m0 + m + pp < 8) {
              acc[m0 + m + pp] = MFMA16(A[pp], B[m], acc[m0 + m + pp]);
              SCHED();
              const int per = m0 ? 2 : 1;
#pragma unroll
              for (int k = i * per; k < i * per + per; ++k) {
                if (k < nb) Bn[k] = *(const volatile LAS u32x4*)(bp + 512 * (mn + k) - 64 * rn);
                else if (k - nb < 2 * na) {
                  const int q = (k - nb) >> 1, hi = (k - nb) & 1;
                  const u32x2 v = *(const volatile LAS u32x2*)(ap + 512 * q + 64 * rn + 8 * hi);
                  An[q][2 * hi] = v[0]; An[q][2 * hi + 1] = v[1];
                }
              }
              SCHED();
              ++i;
            }
      };
      ldb(0, B0, 0); lda(0, A0, 8);
      SCHED();
      GK(1, 1); GV(1, 0); GV(1, 1);
      SCHED();
      mmi(A0, B0, 0, A1, B1, 1, 0, 4, 8);
      mmi(A1, B1, 0, A0, B0, 2, 0, 4, 8);
      mmi(A0, B0, 0, A1, B1, 3, 0, 4, 8);
      sv[0] = ld16(rs, WS_ST1 + (unsigned)tid * 16u);
      sv[1] = ld16(rs, WS_ST1 + (unsigned)tid * 16u + 8192u);
      SCHED();
      mmi(A1, B1, 0, A0, B0, 0, 0, 0, 4);
      STAMP(3);
      asm volatile("" ::"v"(tch2));
      kv_half(1);
      STAMP(38);
#pragma unroll
      for (int n = 0; n < 4; ++n) *(f32x4*)(yp + n * 1024) = acc[n];
      q_ok = __all(((sv[0][0] ^ tag) | (sv[0][2] ^ tag) | (sv[1][0] ^ tag) | (sv[1][2] ^ tag)) == 0u);
      asm volatile("" ::: "memory");
      qa[0] = ld16(rs, WS_QB + xo);
      qa[1] = ld16(rs, WS_QB + xo + 32u * WX1P);
      mg = ld8(rs, mgo);
      SCHED();
      STAMP(39);
      BAR();
      STAMP(4);
      ldb(0, B0, 4);
      SCHED();
      LDWQ(0);
      bs_raw = p.b_sp[trow];
      SCHED();
      mmi(A0, B0, 4, A1, B1, 1, 4, 4, 4);
      mmi(A1, B1, 4, A0, B0, 2, 4, 4, 4);
      LDWQ(1);
      SCHED();
      mmi(A0, B0, 4, A1, B1, 3, 4, 4, 4);
      LDWQ(2);
      SCHED();
      mm(A1, B1, 4);
      SCHED();
      LDWQ(3);
      SCHED();
#pragma unroll
      for (int n = 4; n < 8; ++n) *(f32x4*)(yp + n * 1024) = acc[n];
      STAMP(5);
    }
    const LAS char* yr = (const LAS char*)(lds + L_YP) + (te >> 8) * 1024 + (((te >> 2) & 3) * 16 + ((((te >> 4) & 15) + 4 * ((te >> 2) & 3)) & 15)) * 16 + (te & 3) * 4;
    const LAS char* ur = (const LAS char*)(lds + 512) + ((te & ~7) + 6 - (te & 7)) * 2;
    f32x2 y0[8];
    unsigned uu[2][4];
#pragma unroll
    for (int i = 0; i < 8; ++i) {
      uu[i >> 2][i & 3] = *(const volatile LAS unsigned*)(ur + (i >> 2) * 2048 + (i & 3) * CH_BYTES);
      y0[i] = *(const volatile LAS f32x2*)(yr + i * 8192);
    }
    if (!q_ok) {
      _Pragma("unroll 1") for (unsigned spins = 0; spins < SPIN_CAP; ++spins) {
        sv[0] = ld16(rs, WS_ST1 + (unsigned)tid * 16u);
        sv[1] = ld16(rs, WS_ST1 + (unsigned)tid * 16u + 8192u);
        if (__all(sv[0][0] == tag && sv[0][2] == tag && sv[1][0] == tag && sv[1][2] == tag)) break;
        __builtin_amdgcn_s_sleep(2);
      }
      asm volatile("" ::: "memory");
      qa[0] = ld16(rs, WS_QB + xo);
      qa[1] = ld16(rs, WS_QB + xo + 32u * WX1P);
      DRAIN();
    }
    if (!__all(mg[0] == tag)) {
      _Pragma("unroll 1") for (unsigned spins = 0; spins < SPIN_CAP; ++spins) {
        mg = ld8(rs, mgo);
        if (__all(mg[0] == tag)) break;
        __builtin_amdgcn_s_sleep(1);
      }
    }
    f32x2 cc2[2], ss2[2], dv2[2];
    {
      float cch[4], sch[4];
#pragma unroll
      for (int i = 0; i < 4; ++i) {
        const float sa = __uint_as_float(__builtin_amdgcn_readlane(mg[1], i)), ca = __uint_as_float(__builtin_amdgcn_readlane(mg[1], 4 + i)),
                    ga = __uint_as_float(__builtin_amdgcn_readlane(mg[1], 8 + i));
        cch[i] = ga * rbf(1.0f + ca);
        sch[i] = ga * sa;
      }
      cc2[0] = (f32x2){cch[0], cch[1]}; cc2[1] = (f32x2){cch[2], cch[3]};
      ss2[0] = (f32x2){sch[0], sch[1]}; ss2[1] = (f32x2){sch[2], sch[3]};
      dv2[0] = bf2(dd2[0]); dv2[1] = bf2(dd2[1]);
    }
    STAMP(6);
    f32x2 hh[2][2][2], base[2][2][2];
#pragma unroll
    for (int H = 0; H < 2; ++H)
#pragma unroll
      for (int e = 0; e < 2; ++e) {
        const unsigned st = sv[H][1 + 2 * e];
        const float a = __builtin_amdgcn_rcpf(bfhi(st)), b = -bflo(st) * a;
#pragma unroll
        for (int i = 0; i < 2; ++i) {
          const f32x2 q2 = bf2(qa[H][2 * e + i]);
          const f32x2 u2 = e ? (f32x2){bflo(uu[H][2 * i]), bflo(uu[H][2 * i + 1])} : (f32x2){bfhi(uu[H][2 * i]), bfhi(uu[H][2 * i + 1])};
          const f32x2 n1 = __builtin_elementwise_fma(q2, (f32x2){a, a}, (f32x2){b, b});
          hh[H][e][i] = __builtin_elementwise_fma(n1, cc2[i], ss2[i]);
          base[H][e][i] = __builtin_elementwise_fma(hh[H][e][i], u2 * dv2[i], q2);
        }
      }
    auto epi = [&](int H, const f32x2* ya) {
      u32x4 o;
#pragma unroll
      for (int e = 0; e < 2; ++e)
#pragma unroll
        for (int i = 0; i < 2; ++i) {
          const float x0 = __builtin_fmaf(hh[H][e][i][0], ya[2 * i][e] + ya[4 + 2 * i][e], base[H][e][i][0]);
          const float x1 = __builtin_fmaf(hh[H][e][i][1], ya[2 * i + 1][e] + ya[5 + 2 * i][e], base[H][e][i][1]);
          o[2 * e + i] = pack2(x0, x1);
        }
      st16(rs, WS_X1B + (unsigned)(32 * H) * WX1P + xo, o);
    };
    e_fw1 = ld4(rs, fw1o);
    SCHED();
    epi(0, y0);
    asm volatile("" : "+v"(hh[1][0][0]), "+v"(hh[1][0][1]), "+v"(hh[1][1][0]), "+v"(hh[1][1][1]), "+v"(base[1][0][0]), "+v"(base[1][0][1]), "+v"(base[1][1][0]), "+v"(base[1][1][1]));
    BAR();
    STAMP(7);
#pragma unroll
    for (int i = 0; i < 8; ++i) y0[i] = *(const volatile LAS f32x2*)(yr + 4096 + i * 8192);
    epi(1, y0);
    STAMP(8);
  } else {
    const rsrc_t rwi = __builtin_amdgcn_make_buffer_rsrc((void*)p.w_in, 0, D_ * F_ * 2, 0x00020000);
    const rsrc_t rwo = __builtin_amdgcn_make_buffer_rsrc((void*)p.w_out, 0, FH * D_ * 2, 0x00020000);
    STAMP(32);
    const int job = xcd + 8 * slot;
    const int T = job >= 50 && w < 3 && job - 50 + 46 * w < 128 ? job - 50 + 46 * w : -1;
    const bool unit = slot >= 8 || (slot >= 2 && slot < 6);
    const int ucb = slot >= 8 ? 8 + xcd : xcd, kg = (slot >= 8 ? slot : slot - 2) & 3;
    float* tab = (float*)(lds + L_TAB);
    float* red = (float*)(lds + L_RED);
    auto issue_unit = [&](int i) {
      const int g = i * 64 + l;
      __builtin_amdgcn_raw_ptr_buffer_load_lds(rwi, (LAS void*)(lds + L_J3 + i * 1024), 16, (unsigned)((g / 14) * (F_ * 2) + (g % 14) * 16),
                                               (unsigned)(kg * 160 * F_ * 2 + ucb * CB * 2), 0, 0);
    };
    if (T >= 0) {
      char* qt = lds + L_QT + w * 20480;
      const unsigned so = (unsigned)(16 * T) * 1280u;
#pragma unroll
      for (int n = 0; n < 16; ++n)
        __builtin_amdgcn_raw_ptr_buffer_load_lds(rq, (LAS void*)(qt + n * 1024), 16, l16 ^ (unsigned)(n * 16), so + (unsigned)(n * 1280), 0, 0);
#pragma unroll
      for (int j = 0; j < 4; ++j)
        __builtin_amdgcn_raw_ptr_buffer_load_lds(rq, (LAS void*)(qt + 16384 + j * 1024), 16, (unsigned)(h4 * 1280 + ((n16 ^ (4 * j + h4)) * 16)), so + (unsigned)(4 * j * 1280 + 1024), 0, 0);
      tag = __builtin_amdgcn_readfirstlane(epv) * 2654435761u + 0x9E3779B9u;
      STAMP(34);
      DRAIN();
      STAMP(35);
      const bf2_t one = {(__bf16)1.0f, (__bf16)1.0f};
      float s1[2] = {0.f, 0.f}, s2[2] = {0.f, 0.f};
      const int n0 = 2 * (l & 7);
      u32x4 qv[20];
#pragma unroll
      for (int i = 0; i < 20; ++i)
#pragma unroll
        for (int e = 0; e < 2; ++e) {
          const int n = n0 + e, ck = 4 * (i & 15) + h4;
          const u32x2 v = *(const volatile LAS u32x2*)(qt + (i < 16 ? n * 1024 : 16384 + n * 256) + ((ck ^ n) * 16) + (l & 8));
          qv[i][2 * e] = v[0]; qv[i][2 * e + 1] = v[1];
        }
#pragma unroll
      for (int i = 0; i < 20; ++i) {
        st16(rs, WS_QB + (unsigned)(T >> 1) * WX1P + (unsigned)i * 2048u + (unsigned)((l >> 3) * 256 + (T & 1) * 128 + (l & 7) * 16), qv[i]);
#pragma unroll
        for (int e = 0; e < 4; ++e) {
          const unsigned x = qv[i][e];
          const bf2_t v = __builtin_bit_cast(bf2_t, x);
          s1[e >> 1] = __builtin_amdgcn_fdot2_f32_bf16(v, one, s1[e >> 1], false);
          s2[e >> 1] = __builtin_amdgcn_fdot2_f32_bf16(v, v, s2[e >> 1], false);
        }
        SCHED();
      }
      u32x4 o = {tag, 0u, tag, 0u};
#pragma unroll
      for (int e = 0; e < 2; ++e) {
        const float t1 = sum_rows(s1[e] + dpp_f<0x128>(s1[e])), t2 = sum_rows(s2[e] + dpp_f<0x128>(s2[e]));
        const float mean = t1 * (1.0f / D_);
        const float var = __builtin_fmaxf(t2 * (1.0f / D_) - mean * mean, 0.f);
        o[2 * e + 1] = pack2(mean, __builtin_amdgcn_sqrtf(rbf(rbf(var) + 1e-5f)));
      }
      STAMP(1);
      DRAIN();
      if (l < 8) st16(rs, WS_ST1 + (unsigned)T * 128u + l16, o);
      STAMP(3);
    }
    if (job < 60) {
      const int grp = job < 10 ? 40 + job : (job < 50 ? job - 10 : job);
      if (job >= 50 && T < 0) __builtin_amdgcn_s_sleep(LATE_SLEEP);
      if (job >= 10 && T < 0) __builtin_amdgcn_s_sleep(AUX_SLEEP);
      const unsigned cv = *(const unsigned*)(p.cvec + 80 * w + 2 * (l < 40 ? l : 0));
      u32x4 pre[10];
#pragma unroll
      for (int pp = 0; pp < 10; ++pp) pre[pp] = *(const u32x4*)(p.w_mod + (size_t)(80 * w + pp * 8 + (l >> 3)) * 3840 + grp * 64 + (l & 7) * 8);
      SCHED();
      STAMP(34);
      tag = __builtin_amdgcn_readfirstlane(epv) * 2654435761u + 0x9E3779B9u;
      if (l < 40) {
        const float c0 = bflo(cv), c1 = bfhi(cv);
        *(f32x2*)(tab + w * 80 + 2 * l) = (f32x2){rbf(c0 * __builtin_amdgcn_rcpf(1.0f + __expf(-c0))), rbf(c1 * __builtin_amdgcn_rcpf(1.0f + __expf(-c1)))};
      }
      asm volatile("s_waitcnt lgkmcnt(0)" ::: "memory");
      f32x2 acc[4];
#pragma unroll
      for (int e = 0; e < 4; ++e) acc[e] = (f32x2){0.f, 0.f};
#pragma unroll
      for (int pp = 0; pp < 10; ++pp) {
        const float sv = tab[w * 80 + pp * 8 + (l >> 3)];
#pragma unroll
        for (int e = 0; e < 4; ++e) acc[e] = __builtin_elementwise_fma((f32x2){sv, sv}, bf2(pre[pp][e]), acc[e]);
      }
      STAMP(35);
      asm volatile("" : "+v"(acc[0]), "+v"(acc[1]), "+v"(acc[2]), "+v"(acc[3]));
#pragma unroll
      for (int e = 0; e < 8; ++e) { float a = acc[e >> 1][e & 1]; a += dpp_f<0x128>(a); acc[e >> 1][e & 1] = sum_rows(a); }
      if (l < 8) {
        *(f32x4*)(red + w * 64 + l * 8) = (f32x4){acc[0][0], acc[0][1], acc[1][0], acc[1][1]};
        *(f32x4*)(red + w * 64 + l * 8 + 4) = (f32x4){acc[2][0], acc[2][1], acc[3][0], acc[3][1]};
      }
      if (w == 7) __builtin_amdgcn_s_setprio(2);
      BAR();
      if (w == 7) {
        float t = 0.f;
#pragma unroll
        for (int r = 0; r < 8; ++r) t += red[r * 64 + l];
        st8(rs, WS_MOD + (unsigned)(grp * 64 + l) * 8u, (u32x2){tag, __float_as_uint(rbf(t))});
        __builtin_amdgcn_s_setprio(0);
      } else if (unit) {
#pragma unroll
        for (int k = 0; k < 5; ++k) issue_unit(w * 5 + k);
      }
    } else if (unit && w >= 3 && w < 7) {
      __builtin_amdgcn_s_sleep(AUX_SLEEP);
#pragma unroll
      for (int k = 0; k < 9; ++k) issue_unit((w - 3) * 9 + k < 35 ? (w - 3) * 9 + k : 34);
    }
    tag = __builtin_amdgcn_readfirstlane(epv) * 2654435761u + 0x9E3779B9u;
    if (unit) {
      auto mod_tab = [&](int which, float add, float* dst) {
        const unsigned o = WS_MOD + (unsigned)(which * D_ + kg * 160) * 8u;
        u32x4 g0, g1;
        _Pragma("unroll 1") for (unsigned spins = 0; spins < SPIN_CAP; ++spins) {
          g0 = ld16(rs, o + l16);
          g1 = ld16(rs, o + 1024u + (l16 & 255u));
          if (__all(g0[0] == tag && g0[2] == tag && g1[0] == tag && g1[2] == tag)) break;
          __builtin_amdgcn_s_sleep(2);
        }
        *(f32x2*)(dst + 2 * l) = (f32x2){rbf(add + __uint_as_float(g0[1])), rbf(add + __uint_as_float(g0[3]))};
        if (l < 16) *(f32x2*)(dst + 128 + 2 * l) = (f32x2){rbf(add + __uint_as_float(g1[1])), rbf(add + __uint_as_float(g1[3]))};
      };
      if (w == 7) {
        STAMP(1);
        if (slot >= 8) __builtin_amdgcn_s_sleep(64);
        mod_tab(4, 1.0f, tab);
        BAR();
        STAMP(2);
        mod_tab(3, 0.0f, tab + 160);
        BAR();
      } else {
        DRAIN();
        STAMP(36);
        BAR();
        STAMP(2);
        const char* bp = lds + L_J3 + (4 * h4 + (n16 >> 2)) * 224 + (n16 & 3) * 8 + w * 32;
        const float* tp = tab + 4 * h4;
        u32x4 f3[5];
        float cs = 0.f, b1 = 0.f;
        const bool pad = w == 6 && n16 >= 12;
        const unsigned padv = (n16 == 12 && ucb >= 8) ? 0x3f803f80u : 0u;
#pragma unroll
        for (int k = 0; k < 5; ++k) f3[k] = trfrag(bp + k * 7168, 16 * 224);
#pragma unroll
        for (int k = 0; k < 5; ++k) {
          const f32x4 ca = *(const f32x4*)(tp + k * 32), cb4 = *(const f32x4*)(tp + k * 32 + 16);
          u32x4 o;
#pragma unroll
          for (int e = 0; e < 4; ++e) {
            const float c0 = e < 2 ? ca[2 * e] : cb4[2 * e - 4], c1 = e < 2 ? ca[2 * e + 1] : cb4[2 * e - 3];
            const float p0 = bflo(f3[k][e]) * c0, p1 = bfhi(f3[k][e]) * c1;
            cs += p0 + p1;
            o[e] = pad ? padv : pack2(p0, p1);
          }
          st16(rs, WS_WINF + (unsigned)(ucb * 7 + w) * 20480u + (unsigned)(kg * 5 + k) * 1024u + l16, o);
        }
        cs = sum_rows(cs);
        DRAIN();
        if (l == 0) st4(rs, WS_FW1 + (unsigned)((ucb * 8 + w) * 4 + kg) * 128u, tag);
        STAMP(5);
        BAR();
#pragma unroll
        for (int k = 0; k < 5; ++k) {
          const f32x4 sa = *(const f32x4*)(tp + 160 + k * 32), sb4 = *(const f32x4*)(tp + 160 + k * 32 + 16);
#pragma unroll
          for (int e = 0; e < 4; ++e) {
            const float s0 = e < 2 ? sa[2 * e] : sb4[2 * e - 4], s1 = e < 2 ? sa[2 * e + 1] : sb4[2 * e - 3];
            b1 = __builtin_fmaf(s0, bflo(f3[k][e]), __builtin_fmaf(s1, bfhi(f3[k][e]), b1));
          }
        }
        b1 = sum_rows(b1);
        if (pad) { cs = padv ? 160.0f : 0.0f; b1 = 0.0f; }
        if (l < 16) st16(rs, WS_CS + (unsigned)((ucb * 4 + kg) * 112 + w * 16 + l) * 16u, (u32x4){tag, __float_as_uint(cs), tag, __float_as_uint(b1)});
      }
    } else {
      if (job >= 60 && T < 0) __builtin_amdgcn_s_sleep(100);
      if (slot < 2 && w >= 2 && w < 6) {
        const int c5 = slot * 4 + w - 2, c2 = xcd;
        char* st = lds + L_J5 + (w - 2) * 18432;
#pragma unroll
        for (int r = 0; r < 18; ++r) {
          const int g = r * 64 + l;
          __builtin_amdgcn_raw_ptr_buffer_load_lds(rwo, (LAS void*)(st + r * 1024), 16, (unsigned)((g / 10) * (D_ * 2) + (g % 10) * 16),
                                                   (unsigned)(xcd * CB * D_ * 2 + c5 * 160), 0, 0);
        }
        DRAIN();
        const char* bp = st + (4 * h4 + (n16 >> 2)) * 160 + (n16 & 3) * 8;
        const unsigned base = WS_WOUTF + (unsigned)c5 * WBLK;
#pragma unroll
        for (int dj = 0; dj < 5; ++dj) {
          st16(rs, base + (unsigned)c2 * 10240u + (unsigned)dj * 1024u + l16, trfrag(bp + dj * 32, 32 * 160));
          st16(rs, base + (unsigned)c2 * 10240u + (unsigned)(5 + dj) * 1024u + l16, trfrag(bp + 16 * 160 + dj * 32, 32 * 160));
          st16(rs, base + 81920u + (unsigned)c2 * 5120u + (unsigned)dj * 1024u + l16, trfrag(bp + 64 * 160 + dj * 32, 16 * 160));
          st8(rs, base + 122880u + (unsigned)((c2 >> 1) * 5 + dj) * 1024u + (unsigned)(c2 & 1) * 512u + (unsigned)l * 8u, trd(bp + 96 * 160 + dj * 32));
        }
        fw2 = WS_FW2 + (unsigned)(c5 * 8 + xcd) * 128u;
      }
    }
    STAMP(4);
#pragma unroll
    for (int i = 0; i < NRAW; ++i) LDW(i);
    bs_raw = p.b_sp[trow];
    e_fw1 = ~tag;
    asm volatile(".Lmega_aux_end:");
    asm volatile("" ::"v"(cpf));
    STAMP(8);
  }

  LAS char* xa = (LAS char*)(lds + L_XI) + l * 16;
  LAS char* xb = xa + 65536;
  asm volatile("" : "+v"(xb));
#define LDA(ks, rt) (*(const volatile LAS u32x4*)((ks) < 16 ? xa + ((ks) * 4 + (rt)) * 1024 : xb + (((ks) - 16) * 4 + (rt)) * 1024))
  auto lda1 = [&](int ks, u32x4* A) {
#pragma unroll
    for (int rt = 0; rt < 4; ++rt) A[rt] = LDA(ks, rt);
  };
  const LAS char* tb4 = (const LAS char*)(lds + L_MISC) + h4 * 16;
  const LAS char* tbn = (const LAS char*)(lds + L_MISC) + n16 * 4;
  asm volatile("" : "+v"(tb4), "+v"(tbn));
  u32x4 xr[5][2];
  auto ld_x1 = [&]() {
#pragma unroll
    for (int m = 0; m < 5; ++m) {
      if (m == 0) __builtin_amdgcn_s_setprio(2);
      if (m == 1) __builtin_amdgcn_s_setprio(1);
      if (m == 3) __builtin_amdgcn_s_setprio(0);
#pragma unroll
      for (int e = 0; e < 2; ++e)
        xr[m][e] = ld16(rs, l16, WS_X1B + (unsigned)(R * 2 + (w & 1)) * WX1P + (unsigned)(4 * m + (w >> 1)) * 2048u + (unsigned)e * 1024u);
      SCHED();
    }
  };
  LAS char* xw = xa + w * 2048;
  auto put_x1 = [&](int m) {
    LAS char* d = m < 4 ? xw + m * 16384 : xb + w * 2048;
#pragma unroll
    for (int e = 0; e < 2; ++e) {
      *(volatile LAS u32x2*)(d + 8 * e) = (u32x2){xr[m][e][0], xr[m][e][1]};
      *(volatile LAS u32x2*)(d + 1024 + 8 * e) = (u32x2){xr[m][e][2], xr[m][e][3]};
    }
  };
  auto poison_u = [&]() {
#pragma unroll
    for (int k = 0; k < 2; ++k) {
      const unsigned o = (unsigned)(wj * 2 + k) * 1024u + l16;
      const unsigned rt = o / 3584u, r = o - rt * 3584u;
      st16(rs, WS_UF + ((unsigned)(R * 4) + rt) * 8u * 3584u + (unsigned)c * 3584u + r, (u32x4){~0u, ~0u, ~0u, ~0u});
    }
  };
  const unsigned wob = WS_WOUTF + (unsigned)c * WBLK;
  const LAS char* wrd = (const LAS char*)(lds + L_WS) + w * 8192 + n16 * 256 + ((h4 ^ ((4 - (n16 >> 2)) & 3)) * 64);
#define DMA_W(g, q) __builtin_amdgcn_raw_ptr_buffer_load_lds(rsp, (LAS void*)(lds + L_WS + w * 8192 + (((g) + 1) & 1) * 4096 + (q) * 1024), 16, WVO(q), WSO(g, q), 0, 0)
#define RD_W(g) do { _Pragma("unroll") for (int i_ = 0; i_ < 4; ++i_) \
                      wsp[(g) & 3][((g) >> 2) * 4 + i_] = *(const volatile LAS u32x4*)(wrd + (((g) + 1) & 1) * 4096 + ((i_ ^ (n16 & 3)) * 16)); } while (0)
  auto park = [&]() {
    LAS char* d = (LAS char*)(lds + L_WS) + w * 8192 + l * 16;
#pragma unroll
    for (int g = 0; g < NRAW / 4; ++g) {
#pragma unroll
      for (int q = 0; q < 4; ++q) *(volatile LAS u32x4*)(d + ((g + 1) & 1) * 4096 + q * 1024) = raw[4 * g + q];
      RD_W(g);
    }
    asm volatile("s_waitcnt lgkmcnt(0)" ::: "memory");
  };
#define DMA_AB(k) dma1k(rs, lds + L_AB + ((k) * 8 + w) * 1024, l16, wob + (unsigned)((k) * 8 + w) * 1024u)
  const int rto = w & 3, cls = w >> 2;
  u32x2 xp[3];
  constexpr int W = G2W;
  const unsigned zb = WS_Z2F + (unsigned)c * 458752u + (unsigned)w * 8192u;
  const unsigned fzo = WS_FZ + (unsigned)(c * 32 + w * 4 + (l & 3)) * 128u;
  u32x4 fr[W];
#define LDZ(i) fr[(i) % W] = ld16(rs, l16, zb + (unsigned)((i) >> 3) * 65536u + (unsigned)((i) & 7) * 1024u)
  u32x4 cr[5];
#define LDCR(k) cr[k] = ld16(rs, l16, wob + 81920u + (unsigned)(w * 5 + (k)) * 1024u)
  auto rd_xp = [&]() {
#pragma unroll
    for (int j = 0; j < 3; ++j) {
      const int q4 = 5 * c + (cls ? (3 + (j < 2 ? j : 1)) : j);
      xp[j] = *(const volatile LAS u32x2*)(xa + ((q4 >> 1) * 4 + rto) * 1024 + (q4 & 1) * 8);
    }
  };
  if (w < 7) {
    u32x4 B2[20], B1[20];
    const unsigned b2o = WS_WINF + (unsigned)((8 + c) * 7 + w) * 20480u, b1o = WS_WINF + (unsigned)(c * 7 + w) * 20480u;
#define LDB2(ks) B2[ks] = ld16(rs, l16, b2o + (unsigned)(ks) * 1024u)
#define LDB1(ks) B1[ks] = ld16(rs, l16, b1o + (unsigned)(ks) * 1024u)
    const bool early = __all(e_fw1 == tag);
    asm volatile("" ::: "memory");
    {
      const unsigned le = early ? l16 : OOB;
#pragma unroll
      for (int ks = 0; ks < NB2A; ++ks) B2[ks] = ld16(rs, le, b2o + (unsigned)ks * 1024u);
      SCHED();
      if (is_conv) VMW(NB2A);
    }
    STAMP(33);
    BAR();
    STAMP(37);
    if (!early) {
      wait_flag<4>(rs, fw1o, tag);
      DRAIN();
      st4(rs, l == 0 ? fw2 : OOB, tag);
#pragma unroll
      for (int ks = 0; ks < NB2A; ++ks) LDB2(ks);
    }
#pragma unroll
    for (int ks = NB2A; ks < 20; ++ks) {
      LDB2(ks);
      if (ks == 15) { SCHED(); park(); SCHED(); }
      if (ks == 16) { SCHED(); __builtin_amdgcn_s_barrier(); SCHED(); }
    }
#pragma unroll
    for (int q = 0; q < 4; ++q) DMA_W(2, q);
    STAMP(9);
    BAR();
    STAMP(10);
    ld_x1();
    SCHED();
#pragma unroll
    for (int m = 0; m < 3; ++m) put_x1(m);
    if (w == 6) __builtin_amdgcn_s_setprio(2);
    BAR();
    STAMP(11);
    f32x4 acc[4];
    u32x4 A[2][4];
    lda1(0, A[0]);
    poison_u();
#pragma unroll
    for (int ks = 0; ks < 20; ++ks) {
      if (ks == 6) put_x1(3);
      if (ks == 8) put_x1(4);
      if (ks == 11) BAR();
      if (ks >= 4) LDB1(ks - 4);
      if (ks >= 16) LDB1(ks);
      if (ks == 12) RD_W(2);
      SCHED();
#pragma unroll
      for (int rt = 0; rt < 4; ++rt) {
        if (ks < 19) A[(ks + 1) & 1][rt] = LDA(ks + 1, rt);
        SCHED();
        acc[rt] = MFMA16(A[ks & 1][rt], B2[ks], ks ? acc[rt] : ZERO4);
        SCHED();
      }
    }
    STAMP(12);
    if (w == 6) {
      if (n16 == 12) {
#pragma unroll
        for (int rt = 0; rt < 4; ++rt) *(f32x4*)(lds + L_S1 + (rt * 16 + 4 * h4) * 4) = acc[rt];
      }
      asm volatile("" ::: "memory");
      *(volatile LAS unsigned*)(lds + L_S1 + 256) = tag;
      __builtin_amdgcn_s_setprio(0);
    }
    VMW(20);
    BAR();
    STAMP(13);
    f32x2 zz[8];
    f32x4 cs1a, cs1b;
    {
      f32x4 rs4[4], nm4[4];
#pragma unroll
      for (int rt = 0; rt < 4; ++rt) {
        rs4[rt] = *(const LAS f32x4*)(tb4 + (L_T2 - L_MISC) + rt * 64);
        nm4[rt] = *(const LAS f32x4*)(tb4 + (L_T2 - L_MISC) + 256 + rt * 64);
      }
      const f32x2 cs2 = *(const f32x2*)(lds + L_CS1 + (112 + 16 * w + n16) * 8);
      const f32x2 csv = {cs2[0], cs2[0]}, b1v = {cs2[1], cs2[1]};
      float v1[16], v2[16];
#pragma unroll
      for (int i = 0; i < 8; ++i) {
        const int rt = i >> 1;
        const f32x2 a2 = i & 1 ? HI2(acc[rt]) : LO2(acc[rt]), n2 = i & 1 ? HI2(nm4[rt]) : LO2(nm4[rt]), r2 = i & 1 ? HI2(rs4[rt]) : LO2(rs4[rt]);
        zz[i] = gelu2(__builtin_elementwise_fma(__builtin_elementwise_fma(n2, csv, a2), r2, b1v));
        asm volatile("" : "+v"(zz[i]));
        DMA_W(3 + (i >> 2), i & 3);
        const f32x2 sq = zz[i] * zz[i];
        v1[2 * i] = zz[i][0]; v1[2 * i + 1] = zz[i][1]; v2[2 * i] = sq[0]; v2[2 * i + 1] = sq[1];
      }
      const float t1 = tr_sum16(v1, l);
      const float t2 = tr_sum16(v2, l);
      *(f32x2*)(lds + L_Z1S + (w * 64 + (n16 >> 2) * 16 + 4 * h4 + (n16 & 3)) * 8) = (f32x2){t1, t2};
      lda1(0, A[0]);
      cs1a = *(const f32x4*)(lds + L_CS1 + (16 * w + 4 * h4) * 8);
      cs1b = *(const f32x4*)(lds + L_CS1 + (16 * w + 4 * h4) * 8 + 16);
    }
    BAR();
    STAMP(14);
#pragma unroll
    for (int ks = 0; ks < 20; ++ks) {
      if (ks == Z1SPLIT) {
        STAMP(15);
        BAR();
        STAMP(16);
        f32x4 ra[4], rb[4];
#pragma unroll
        for (int rt = 0; rt < 4; ++rt) {
          ra[rt] = *(const LAS f32x4*)(tb4 + (L_ST2 - L_MISC) + rt * 64);
          rb[rt] = *(const LAS f32x4*)(tb4 + (L_ST2 - L_MISC) + 256 + rt * 64);
        }
#pragma unroll
        for (int i = 0; i < 8; ++i) {
          zz[i] = __builtin_elementwise_fma(zz[i], i & 1 ? HI2(ra[i >> 1]) : LO2(ra[i >> 1]), i & 1 ? HI2(rb[i >> 1]) : LO2(rb[i >> 1]));
          asm volatile("" : "+v"(zz[i]));
        }
        const u32x4 o0 = {pack2(zz[0][0], zz[2][0]), pack2(zz[0][1], zz[2][1]), pack2(zz[1][0], zz[3][0]), pack2(zz[1][1], zz[3][1])};
        const u32x4 o1 = {pack2(zz[4][0], zz[6][0]), pack2(zz[4][1], zz[6][1]), pack2(zz[5][0], zz[7][0]), pack2(zz[5][1], zz[7][1])};
        const unsigned zo = WS_Z2F + (unsigned)((c * 7 + w) * 64 + 4 * (R >> 1) + h4) * 1024u + (unsigned)(R & 1) * 512u + (unsigned)n16 * 16u;
        st16(rs, zo, o0);
        st16(rs, zo + 256u, o1);
        SCHED();
      }
      if (ks == 0) { VMW(4); RD_W(3); }
      if (ks == 2) { VMW(0); RD_W(4); }
      if (ks == 8) { VMW(4 + 2); RD_W(5); }
      if (ks == 11) rd_xp();
      if (ks == 13) { VMW(4 + 5 + 2); RD_W(6); }
      if (ks >= 13 && ks < 19) DMA_AB(ks - 13);
      if (ks >= 2 && ks < 6) { DMA_W(5 + ((ks - 2) >> 1), 2 * (ks & 1)); DMA_W(5 + ((ks - 2) >> 1), 2 * (ks & 1) + 1); }
      if (ks >= 6 && ks < 9) LDCR(ks - 6);
      if (ks == 12) { LDCR(3); LDCR(4); }
      if (ks == 10 || ks == 11) { DMA_W(7, 2 * (ks & 1)); DMA_W(7, 2 * (ks & 1) + 1); }
      SCHED();
#pragma unroll
      for (int rt = 0; rt < 4; ++rt) {
        if (ks < 19) A[(ks + 1) & 1][rt] = LDA(ks + 1, rt);
        SCHED();
        acc[rt] = MFMA16(B1[ks], A[ks & 1][rt], ks ? acc[rt] : ZERO4);
        SCHED();
      }
    }
    STAMP(18);
    VMW(2 + 2 + 6);
    RD_W(7);
    const f32x2 c01 = {cs1a[0], cs1a[2]}, b01 = {cs1a[1], cs1a[3]}, c23 = {cs1b[0], cs1b[2]}, b23 = {cs1b[1], cs1b[3]};
    u32x2 g1[4];
    auto z1g = [&](int rt) {
      const float r1 = *(const LAS float*)(tbn + (L_T2 - L_MISC) + rt * 64), n1 = *(const LAS float*)(tbn + (L_T2 - L_MISC) + 256 + rt * 64);
      const f32x2 r2 = {r1, r1}, n2 = {n1, n1};
      const f32x2 a = gelu2(__builtin_elementwise_fma(__builtin_elementwise_fma(n2, c01, LO2(acc[rt])), r2, b01));
      const f32x2 b = gelu2(__builtin_elementwise_fma(__builtin_elementwise_fma(n2, c23, HI2(acc[rt])), r2, b23));
      g1[rt] = (u32x2){pack2(a[0], a[1]), pack2(b[0], b[1])};
      asm volatile("" : "+v"(g1[rt]));
    };
    SCHED();
    VMW(2 + 6);
    BAR();
    STAMP(17);
#pragma unroll
    for (int rt = 0; rt < 4; ++rt) {
      if (rt < 2) { DMA_AB(6 + 2 * rt); DMA_AB(7 + 2 * rt); }
      z1g(rt);
      *(u32x2*)(lds + L_Z1S + ((w * 4 + rt) * 64 + l) * 8) = g1[rt];
    }
  } else {
    __builtin_amdgcn_s_setprio(3);
    DRAIN();
    BAR();
    if (is_conv && l == 0) st4(rs, WS_FX + (unsigned)quad * 128u, tag);
    STAMP(9);
    *(volatile LAS unsigned*)(lds + L_S1 + 256) = ~tag;
    u32x4 gv, cv[2][2][4];
    const unsigned gvo = WS_MOD + (unsigned)(5 * D_ + 80 * c) * 8u + (l < 40 ? l16 : 0u);
    auto ld_consts = [&]() {
      gv = ld16(rs, gvo);
#pragma unroll
      for (int i = 0; i < 16; ++i) cv[i >> 3][(i >> 2) & 1][i & 3] = ld16(rs, l < 56 ? l16 : 0u, WS_CS + (unsigned)(((c + 8 * (i >> 3)) * 4 + (i & 3)) * 112 + 56 * ((i >> 2) & 1)) * 16u);
    };
    auto ok_consts = [&]() {
      unsigned x = (gv[0] ^ tag) | (gv[2] ^ tag);
#pragma unroll
      for (int i = 0; i < 16; ++i) x |= (cv[i >> 3][(i >> 2) & 1][i & 3][0] ^ tag) | (cv[i >> 3][(i >> 2) & 1][i & 3][2] ^ tag);
      asm volatile("" : "+v"(x));
      return __all(x == 0u);
    };
    {
#pragma unroll
      for (int k = 0; k < 3; ++k) cpf ^= ld4(rs, k * 64 + l < 140 ? (unsigned)l * 128u : OOB, WS_WINF + (unsigned)c * WBLK + (unsigned)(idx & 7) * 17920u + (unsigned)k * 8192u);
    }
    park();
#pragma unroll
    for (int q = 0; q < 4; ++q) DMA_W(2, q);
    __builtin_amdgcn_s_barrier();
    ld_consts();
    STAMP(31);
    _Pragma("unroll 1") for (unsigned spins = 0; spins < SPIN_CAP; ++spins) {
      const unsigned f0 = ld4(rs, WS_FX + (unsigned)l * 128u), f1 = ld4(rs, WS_FX + 8192u + (unsigned)l * 128u),
                     f2 = ld4(rs, l < 32 ? WS_FX + 16384u + (unsigned)l * 128u : WS_FW1 + (unsigned)(c * 32 + (l < 60 ? l - 32 : 0)) * 128u);
      if (__all(f0 == tag && f1 == tag && f2 == tag)) break;
      __builtin_amdgcn_s_sleep(POLL_SLEEP);
    }
    BAR();
    STAMP(10);
    ld_x1();
    __builtin_amdgcn_s_setprio(3);
    if (!ok_consts()) {
      _Pragma("unroll 1") for (unsigned spins = 0; spins < SPIN_CAP; ++spins) {
        ld_consts();
        if (ok_consts()) break;
        __builtin_amdgcn_s_sleep(8);
      }
    }
    if (l < 40) *(f32x2*)(lds + L_GB + l * 8) = (f32x2){__uint_as_float(gv[1]), __uint_as_float(gv[3])};
    if (l < 56) {
#pragma unroll
      for (int i = 0; i < 4; ++i) {
        const u32x4* q4 = cv[i >> 1][i & 1];
        *(f32x2*)(lds + L_CS1 + (56 * i + l) * 8) =
            (f32x2){(__uint_as_float(q4[0][1]) + __uint_as_float(q4[1][1])) + (__uint_as_float(q4[2][1]) + __uint_as_float(q4[3][1])),
                    (__uint_as_float(q4[0][3]) + __uint_as_float(q4[1][3])) + (__uint_as_float(q4[2][3]) + __uint_as_float(q4[3][3]))};
      }
    }
    SCHED();
#pragma unroll
    for (int m = 0; m < 3; ++m) put_x1(m);
    BAR();
    f32x4 acc[4];
    u32x4 A[2][4];
    lda1(0, A[0]);
#pragma unroll
    for (int ks = 0; ks < 20; ++ks) {
      if (ks == 6) put_x1(3);
      if (ks == 8) put_x1(4);
      if (ks == 11) BAR();
      if (ks == 4) RD_W(2);
      if (ks >= 6 && ks < 14) DMA_W(3 + ((ks - 6) >> 2), (ks - 6) & 3);
      if (ks >= 1 && ks < 5)
        cpf ^= __builtin_amdgcn_raw_buffer_load_b32(rsp, (unsigned)(h4 * 4096 + (n16 >> 1) * 512 + (n16 & 1) * 128),
                                                    (unsigned)(R * 64 + (c & 3) * 16 + (ks - 1) * 4) * 4096u + 256u, 0);
      if (ks < 19) lda1(ks + 1, A[(ks + 1) & 1]);
      SCHED();
#pragma unroll
      for (int rt = 0; rt < 4; ++rt) acc[rt] = MFMA16(A[ks & 1][rt], A[ks & 1][rt], ks ? acc[rt] : ZERO4);
      SCHED();
    }
    {
      if ((n16 >> 2) == h4) {
#pragma unroll
        for (int rt = 0; rt < 4; ++rt) {
          const float lo = (l & 1) ? acc[rt][1] : acc[rt][0], hi = (l & 1) ? acc[rt][3] : acc[rt][2];
          *(volatile LAS float*)(tbn + (L_T2 - L_MISC) + rt * 64) = (l & 2) ? hi : lo;
        }
      }
      const float s2 = *(volatile LAS float*)(lds + L_T2 + l * 4) * (1.0f / D_);
      float s1;
      _Pragma("unroll 1") for (unsigned spins = 0; spins < SPIN_CAP * 16; ++spins) {
        const unsigned f = *(volatile LAS unsigned*)(lds + L_S1 + 256);
        s1 = *(volatile LAS float*)(lds + L_S1 + l * 4);
        if (f == tag) break;
      }
      const float nm = s1 * (-1.0f / D_);
      *(float*)(lds + L_T2 + l * 4) = __builtin_amdgcn_rsqf(__builtin_fmaxf(s2 - nm * nm, 0.f) + 1e-5f);
      *(float*)(lds + L_T2 + 256 + l * 4) = nm;
    }
    BAR();
    __builtin_amdgcn_s_setprio(0);
    asm volatile("" ::"v"(cpf));
    DRAIN();
    RD_W(3); RD_W(4);
    rd_xp();
    asm volatile("s_waitcnt lgkmcnt(0)" ::: "memory");
#pragma unroll
    for (int i = 0; i < 8; ++i) DMA_W(5 + (i >> 2), i & 3);
    const unsigned e_fw2 = ld4(rs, WS_FW2 + (unsigned)(c * 8 + (l & 7)) * 128u);
    if (!__all(e_fw2 == tag)) wait_flag<2>(rs, WS_FW2 + (unsigned)(c * 8 + (l & 7)) * 128u, tag);
    __builtin_amdgcn_s_setprio(3);
    BAR();
    {
      f32x2 t = {0.f, 0.f};
#pragma unroll
      for (int k = 0; k < 7; ++k) t += *(const f32x2*)(lds + L_Z1S + (k * 64 + l) * 8);
      st16(rs, WS_PART + (unsigned)(R * 8 + c) * 1024u + l16, (u32x4){tag, __float_as_uint(t[0]), tag, __float_as_uint(t[1])});
    }
    STAMP(14);
    {
      u32x4 v[8];
      const unsigned po = WS_PART + (unsigned)R * 8192u + l16;
      __builtin_amdgcn_s_sleep(16);
      _Pragma("unroll 1") for (unsigned spins = 0; spins < SPIN_CAP; ++spins) {
#pragma unroll
        for (int k = 0; k < 8; ++k) v[k] = ld16(rs, po + (unsigned)k * 1024u);
        unsigned x = 0;
#pragma unroll
        for (int k = 0; k < 8; ++k) x |= (v[k][0] ^ tag) | (v[k][2] ^ tag);
        asm volatile("" : "+v"(x));
        if (__all(x == 0u)) break;
        __builtin_amdgcn_s_sleep(1);
      }
      float t1 = 0.f, t2 = 0.f;
#pragma unroll
      for (int k = 0; k < 8; ++k) { t1 += __uint_as_float(v[k][1]); t2 += __uint_as_float(v[k][3]); }
      const float mean = t1 * (1.0f / FH);
      const float var = __builtin_fmaxf(t2 * (1.0f / FH) - mean * mean, 0.f);
      const float rstd = __builtin_amdgcn_rsqf(var + 1e-5f);
      *(float*)(lds + L_ST2 + l * 4) = rstd;
      *(float*)(lds + L_ST2 + 256 + l * 4) = -mean * rstd;
    }
    STAMP(15);
    BAR();
    RD_W(5); RD_W(6);
    asm volatile("s_waitcnt lgkmcnt(0)" ::: "memory");
#pragma unroll
    for (int q = 0; q < 4; ++q) DMA_W(7, q);
    DRAIN();
    RD_W(7);
    BAR();
    if (l == 0) st4(rs, WS_FZ + (unsigned)(c * 32 + R) * 128u, tag);
    STAMP(17);
    __builtin_amdgcn_s_setprio(0);
#pragma unroll
    for (int k = 0; k < 10; ++k) DMA_AB(k);
#pragma unroll
    for (int k = 0; k < 5; ++k) LDCR(k);
  }
  const rsrc_t rout = __builtin_amdgcn_make_buffer_rsrc((void*)p.out, 0, S_ * D_ * 2, 0x00020000);
  STAMP(19);
  u32x2 z1o[4];
  wait_flag<POLL_SLEEP>(rs, fzo, tag);
#pragma unroll
  for (int i = 0; i < W; ++i) { LDZ(i); SCHED(); }
  STAMP(20);

  const unsigned ub = WS_UF + (unsigned)((R * 4 + rto) * 8 + c) * 3584u;
  const float bs = bflo(bs_raw);
  const unsigned ur = WS_UF + (unsigned)((R * 4 + rto) * 8 + cls) * 3584u;
  u32x4 uf[4][2];
  f32x4 acc[2][4];
  f32x4* const bufw = (f32x4*)(lds + L_BUF) + w * 256 + l;
#define BUFW(slot, rt) bufw[(slot) * 2048 + (rt) * 64]
  {
    const f32x4* const rb = (const f32x4*)(lds + L_BUF) + cls * 2048 + rto * 64 + l;
    f32x4 rdv[4], s;
    u32x4 uo;
#pragma unroll
    for (int j = 0; j < 7; ++j) {
#pragma unroll
      for (int ks = 0; ks < 8; ++ks) {
        const int i = 8 * j + ks;
#pragma unroll
        for (int rt = 0; rt < 4; ++rt) acc[j & 1][rt] = MFMA16(fr[i % W], wsp[rt][ks], ks ? acc[j & 1][rt] : ZERO4);
        if (i == 0) STAMP(21);
        if (i == 24) STAMP(22);
        SCHED();
        if (i + W < 56) LDZ(i + W);
        if (j > 0) {
          if (ks == 0 && (j & 1)) BAR();
          if (j == 1 && ks == 0) {
#pragma unroll
            for (int k = 0; k < 4; ++k) z1o[k] = *(const u32x2*)(lds + L_Z1S + (((k < 3 ? 2 * k + cls : 6) * 4 + rto) * 64 + l) * 8);
          }
          if (i >= (56 - G2W) && i < 53) {
#pragma unroll
            for (int k = 0; k < 8; ++k)
              if ((56 - G2W) + k * (53 - (56 - G2W)) / 8 == i) uf[k >> 1][k & 1] = ld16(rs, l16, ur + (unsigned)(k >> 1) * 7168u + (unsigned)(k & 1) * 1024u);
            SCHED();
          }
          const bool fast = j == 6;
          if (!fast && ks < 4) BUFW((j - 1) & 1, ks) = acc[(j - 1) & 1][ks];
          if (fast && ks < 2) { BUFW(1, 2 * ks) = acc[1][2 * ks]; BUFW(1, 2 * ks + 1) = acc[1][2 * ks + 1]; }
          if (!(j & 1)) {
            const int k0 = fast ? 2 : 5;
            if (ks == k0) {
              BAR();
#pragma unroll
              for (int k = 0; k < 4; ++k) rdv[k] = rb[k * 256];
            }
            if (ks == k0 + 1) {
              s = (rdv[0] + rdv[1]) + (rdv[2] + rdv[3]);
#pragma unroll
              for (int k = 0; k < 4; ++k) rdv[k] = rb[(4 + k) * 256];
            }
            if (ks == k0 + 2) {
              s += (rdv[0] + rdv[1]) + (rdv[2] + rdv[3]);
              const u32x2 z = z1o[(j >> 1) - 1];
              const int e = j == 4 ? 2 : 0;
              uo[e] = pack2(bflo(z[0]) * (s[0] + bs), bfhi(z[0]) * (s[1] + bs));
              uo[e + 1] = pack2(bflo(z[1]) * (s[2] + bs), bfhi(z[1]) * (s[3] + bs));
              if (j == 4) st16(rs, ub + (unsigned)cls * 1024u + l16, uo);
              if (j == 6) st16(rs, l < 32 ? ub + 2048u + (unsigned)cls * 512u + l16 : OOB, half_swap((u32x4){uo[0], uo[1], uo[0], uo[1]}));
            }
            if (j == 6 && ks == 4) BAR();
            if (j == 6 && ks >= 4 && ks < 7) {
#pragma unroll
              for (int k = 0; k < 5; ++k)
                if (k / 2 == ks - 4) *(u32x4*)(lds + (w < 6 ? L_BUF + 32768 + w * 5120 : L_Z1S + (w - 6) * 5120) + k * 1024 + l16) = cr[k];
            }
          }
        }
        SCHED();
      }
    }
  }
  STAMP(23);

  {
    u32x4 uc[4], uh[4];
    const unsigned uho = (unsigned)(l >> 5) * 3584u + (unsigned)(l & 31) * 16u;
#define LD_C(cc) uc[cc] = ld16(rs, l16, ur + (unsigned)(cc) * 7168u + 2048u)
#define LD_H(cc) uh[cc] = ld16(rs, uho, WS_UF + (unsigned)((R * 4 + rto) * 8) * 3584u + (unsigned)(cc) * 7168u + 3072u)
    auto mx = [](unsigned a, unsigned b) { return __builtin_elementwise_max(a, b); };
    auto ok4 = [&](const u32x4* v, int n) {
      unsigned m = 0;
#pragma unroll
      for (int i = 0; i < 8; ++i)
        if (i < n) m = mx(m, mx(mx(v[i][0], v[i][1]), mx(v[i][2], v[i][3])));
      return __all(m != ~0u);
    };
#pragma unroll
    for (int rt = 0; rt < 4; ++rt) BUFW(0, rt) = acc[0][rt];
    BAR();
    f32x4 a3[5];
    u32x4 af[2][5];
    const LAS char* wb = (const LAS char*)(lds + L_AB) + cls * 10240 + l16;
    const LAS char* wb2 = wb + 40960;
    asm volatile("" : "+v"(wb), "+v"(wb2));
    u32x4 ho = {0u, 0u, 0u, 0u};
    if (w < 4) {
      __builtin_amdgcn_s_setprio(2);
      const f32x4* const rb = (const f32x4*)(lds + L_BUF) + rto * 64 + l;
      f32x4 r8[8];
#pragma unroll
      for (int k = 0; k < 8; ++k) r8[k] = rb[k * 256];
      const f32x4 s = ((r8[0] + r8[1]) + (r8[2] + r8[3])) + ((r8[4] + r8[5]) + (r8[6] + r8[7]));
      const u32x2 z = z1o[3];
      const unsigned o0 = pack2(bflo(z[0]) * (s[0] + bs), bfhi(z[0]) * (s[1] + bs)), o1 = pack2(bflo(z[1]) * (s[2] + bs), bfhi(z[1]) * (s[3] + bs));
      ho = half_swap((u32x4){o0, o1, o0, o1});
    }
    st16(rs, tid < 256 && l < 32 ? ub + 3072u + l16 : OOB, ho);
    SCHED();
    __builtin_amdgcn_s_setprio(0);
#pragma unroll
    for (int dj = 0; dj < 5; ++dj) af[0][dj] = *(const LAS u32x4*)(wb + dj * 1024);
    const bool ab_ok = ok4(&uf[0][0], 8);
    STAMP(24);
    if (!ab_ok) {
      _Pragma("unroll 1") for (unsigned spins = 0; spins < SPIN_CAP; ++spins) {
#pragma unroll
        for (int i = 0; i < 8; ++i) uf[i >> 1][i & 1] = ld16(rs, l16, ur + (unsigned)(i >> 1) * 7168u + (unsigned)(i & 1) * 1024u);
        if (ok4(&uf[0][0], 8)) break;
        __builtin_amdgcn_s_sleep(1);
      }
    }
    STAMP(25);
    const LAS char* cb0 = (const LAS char*)(lds + L_BUF + 32768) + cls * 5120 + l16;
    const LAS char* cb3 = (const LAS char*)(lds + L_Z1S) + cls * 5120 + l16;
#pragma unroll
    for (int st = 0; st < 12; ++st) {
      if (st < 2) { LD_C(2 * st); LD_C(2 * st + 1); }
      else if (st < 7 && w < 4) dma1k(rs, lds + L_BUF + ((st - 2) * 4 + w) * 1024, l16, wob + 122880u + (unsigned)(w * 5 + st - 2) * 1024u);
      if (st == 8) {
        STAMP(26);
        if (!ok4(uc, 4)) {
          _Pragma("unroll 1") for (unsigned spins = 0; spins < SPIN_CAP; ++spins) {
            LD_C(0); LD_C(1); LD_C(2); LD_C(3);
            if (ok4(uc, 4)) break;
            __builtin_amdgcn_s_sleep(1);
          }
        }
        LD_H(0); LD_H(1); LD_H(2); LD_H(3);
#pragma unroll
        for (int cc = 0; cc < 4; ++cc) uc[cc] = half_swap(uc[cc]);
        STAMP(27);
      }
      SCHED();
#pragma unroll
      for (int dj = 0; dj < 5; ++dj) {
        const int sn = st + 1;
        if (sn < 8) af[sn & 1][dj] = *(const LAS u32x4*)((sn < 4 ? wb : wb2) + ((sn >> 1) & 1) * 20480 + ((sn & 1) * 5 + dj) * 1024);
        else if (sn < 12) af[sn & 1][dj] = *(const LAS u32x4*)((sn < 11 ? cb0 + (sn - 8) * 10240 : cb3) + dj * 1024);
        SCHED();
        a3[dj] = MFMA16(af[st & 1][dj], st < 8 ? uf[(st >> 1) & 3][st & 1] : uc[st & 3], st ? a3[dj] : ZERO4);
        SCHED();
      }
    }
    VMW(4);
    BAR();
    f32x4* X = (f32x4*)(lds + L_AB);
    f32x4 m3[3];
    if (cls == 0) {
      X[(w * 3 + 0) * 64 + l] = a3[3];
      X[(w * 3 + 1) * 64 + l] = a3[4];
      m3[0] = a3[0]; m3[1] = a3[1]; m3[2] = a3[2];
    } else {
      X[(w * 3 + 0) * 64 + l] = a3[0];
      X[(w * 3 + 1) * 64 + l] = a3[1];
      X[(w * 3 + 2) * 64 + l] = a3[2];
      m3[0] = a3[3]; m3[1] = a3[4]; m3[2] = a3[4];
    }
    u32x4 ah[4][3];
    {
      const LAS char* hb = (const LAS char*)(lds + L_BUF) + cls * 12288 + l * 8;
#pragma unroll
      for (int cc = 0; cc < 4; ++cc)
#pragma unroll
        for (int dj = 0; dj < 3; ++dj) {
          const u32x2 lo = *(const volatile LAS u32x2*)(hb + (dj * 4 + cc) * 1024), hi = *(const volatile LAS u32x2*)(hb + (dj * 4 + cc) * 1024 + 512);
          ah[cc][dj] = (u32x4){lo[0], lo[1], hi[0], hi[1]};
        }
    }
    BAR();
    STAMP(28);
    f32x4 gb[3];
#pragma unroll
    for (int j = 0; j < 3; ++j) {
      m3[j] += X[((w ^ 4) * 3 + j) * 64 + l];
      gb[j] = *(const f32x4*)(lds + L_GB + (48 * cls + 16 * (cls && j == 2 ? 1 : j) + 4 * h4) * 4);
    }
    if (!ok4(uh, 4)) {
      _Pragma("unroll 1") for (unsigned spins = 0; spins < SPIN_CAP; ++spins) {
        LD_H(0); LD_H(1); LD_H(2); LD_H(3);
        if (ok4(uh, 4)) break;
        __builtin_amdgcn_s_sleep(1);
      }
    }
    STAMP(29);
#pragma unroll
    for (int cc = 0; cc < 4; ++cc) {
      const u32x4 b = half_swap(uh[cc]);
#pragma unroll
      for (int j = 0; j < 3; ++j) m3[j] = MFMA16(ah[cc][j], b, m3[j]);
    }
    if (bid == 0 && tid == 0) st4(rs, WS_EPOCH, tag);
#pragma unroll
    for (int j = 0; j < 3; ++j) {
      if (j < 2 || cls == 0) {
        u32x2 o;
        o[0] = pack2(__builtin_fmaf(gb[j][0], m3[j][0], bflo(xp[j][0])), __builtin_fmaf(gb[j][1], m3[j][1], bfhi(xp[j][0])));
        o[1] = pack2(__builtin_fmaf(gb[j][2], m3[j][2], bflo(xp[j][1])), __builtin_fmaf(gb[j][3], m3[j][3], bfhi(xp[j][1])));
        __builtin_amdgcn_raw_buffer_store_b64(o, rout, (unsigned)(trow * D_ + 4 * h4) * 2u, (unsigned)(80 * c + 48 * cls + 16 * j) * 2u, 0);
      }
    }
  }
  STAMP(30);
  asm volatile(".Lmega_end:");
}

extern "C" void kernel_launch(void* const* d_in, const int* in_sizes, int n_in, void* d_out, int out_size,
                              void* d_ws, size_t ws_size, hipStream_t stream) {
  Params p;
  p.q = (const u16*)d_in[0];
  p.k = (const u16*)d_in[1];
  p.v = (const u16*)d_in[2];
  p.shift_k = (const u16*)d_in[3];
  p.ssm_k = (const u16*)d_in[4];
  p.ssm_D = (const u16*)d_in[5];
  p.w_in = (const u16*)d_in[7];
  p.w_sp = (const u16*)d_in[8];
  p.b_sp = (const u16*)d_in[9];
  p.w_out = (const u16*)d_in[10];
  p.cvec = (const u16*)d_in[11];
  p.w_mod = (const u16*)d_in[12];
  p.out = (u16*)d_out;
  p.ws = (char*)d_ws;
  mega<<<256, 512, 0, stream>>>(p);
}
```

```cpp
#include <hip/hip_runtime.h>
#include <stdint.h>

typedef unsigned short u16;
typedef __attribute__((ext_vector_type(8))) short bf16x8;
typedef __attribute__((ext_vector_type(4))) float f32x4;
typedef __attribute__((ext_vector_type(2))) float f32x2;
typedef __attribute__((ext_vector_type(4))) unsigned int u32x4;
typedef __attribute__((ext_vector_type(2))) unsigned int u32x2;
#define LAS __attribute__((address_space(3)))
#define CAS __attribute__((address_space(4)))
#define GAS __attribute__((address_space(1)))
typedef __amdgpu_buffer_rsrc_t rsrc_t;
typedef __attribute__((ext_vector_type(4))) short s16x4;

constexpr int S_ = 2048, D_ = 640, F_ = 1728, FH = 864, CB = 108;

#define X1PAD 256
constexpr unsigned WS_EPOCH = 0;
constexpr unsigned WS_FX = 4096;
constexpr unsigned WS_FZ = WS_FX + 24576;
constexpr unsigned WS_FW2 = WS_FZ + 32768;
constexpr unsigned WS_FW1 = WS_FW2 + 8192;
constexpr unsigned WS_CS = WS_FW1 + 65536;
constexpr unsigned WS_PART = WS_CS + 131072;
constexpr unsigned WS_MOD = WS_PART + 262144;
constexpr unsigned WS_ST1 = WS_MOD + 32768;
constexpr unsigned WS_X1B = WS_ST1 + 16384;
constexpr unsigned WX1P = 40960 + X1PAD;
constexpr unsigned WBLK = 143360;
constexpr unsigned WS_QB = WS_X1B + 2686976;
constexpr unsigned WS_WINF = WS_QB + 2686976;
constexpr unsigned WS_WOUTF = WS_WINF + 16 * WBLK;
constexpr unsigned WS_Z2F = WS_WOUTF + 8 * WBLK;
constexpr unsigned WS_UF = WS_Z2F + 8 * 64 * 7168;
constexpr unsigned WS_KVT = WS_UF + 32 * 4 * 8 * 3584;
constexpr unsigned WS_FK = WS_KVT + 32 * 81920;
constexpr unsigned WS_STAMP = WS_FK + 20480;
constexpr unsigned WS_END = WS_STAMP + 256 * 40 * 8;

constexpr int U_BYTES = (256 + S_) * 2;
constexpr int G_COPY = 4160;
constexpr int CH_BYTES = U_BYTES + 4 * G_COPY;
constexpr int L_YP = 4 * CH_BYTES;
constexpr int L_A0 = 98304;
constexpr int L_RED = 0;
constexpr int L_TAB = 2048;
constexpr int L_J3 = 8192;
constexpr int L_QT = 49152;
constexpr int L_J5 = L_QT;
constexpr int L_XI = 0;
constexpr int L_BUF = 81920;
constexpr int L_AB = 0;
constexpr int L_MISC = 147456;
constexpr int L_Z1S = L_MISC;
constexpr int L_S1 = L_MISC + 14336;
constexpr int L_T2 = L_S1 + 512;
constexpr int L_ST2 = L_T2 + 512;
constexpr int L_GB = L_ST2 + 512;
constexpr int L_CS1 = L_Z1S + 4096;
constexpr int L_WS = 81920;
constexpr int LDS_TOTAL = 163840;

constexpr int POLL_SLEEP = 1;
#define ET 0
#define TK 1
#define DK 6
#define PM 1
#define AT 3
#define NRA 8
#define CDIST 4
#define PREP_AT 108
#define SV_AT 80
#define RAWD 3
#define P_SLEEP 0
#define F_SLEEP 0
#define OAUX 0
#define AUX_SLEEP 36
#define Q_SLEEP 0
constexpr int LATE_SLEEP = 64;
constexpr int NRAW = 8;
constexpr int NB2A = 6;
constexpr int G2W = 7;
constexpr int Z1SPLIT = 12;
constexpr int SC1 = 16;
constexpr unsigned SPIN_CAP = 1u << 16;
constexpr unsigned OOB = 0x80000000u;

__device__ __forceinline__ float bflo(unsigned x) { return __uint_as_float(x << 16); }
__device__ __forceinline__ float bfhi(unsigned x) { return __uint_as_float(x & 0xffff0000u); }
__device__ __forceinline__ f32x2 bf2(unsigned x) { return (f32x2){bflo(x), bfhi(x)}; }
__device__ __forceinline__ unsigned f2bf(float x) {
  __bf16 b = (__bf16)x;
  return (unsigned)__builtin_bit_cast(unsigned short, b);
}
__device__ __forceinline__ float rbf(float x) { return bflo(f2bf(x)); }
typedef __attribute__((ext_vector_type(2))) __bf16 bf2_t;
__device__ __forceinline__ unsigned pack2(float lo, float hi) {
  f32x2 v = {lo, hi};
  return __builtin_bit_cast(unsigned, __builtin_convertvector(v, bf2_t));
}
template <int CTRL>
__device__ __forceinline__ float dpp_f(float v) {
  return __int_as_float(__builtin_amdgcn_update_dpp(0, __float_as_int(v), CTRL, 0xf, 0xf, true));
}
__device__ __forceinline__ float sum_rows(float v) {
  auto a = __builtin_amdgcn_permlane16_swap(__float_as_uint(v), __float_as_uint(v), false, false);
  v = __uint_as_float(a[0]) + __uint_as_float(a[1]);
  auto b = __builtin_amdgcn_permlane32_swap(__float_as_uint(v), __float_as_uint(v), false, false);
  return __uint_as_float(b[0]) + __uint_as_float(b[1]);
}
__device__ __forceinline__ f32x2 gelu2(f32x2 x) {
  const float c0 = -2.0f * 0.7978845608028654f * 1.4426950408889634f;
  const float c1 = c0 * 0.044715f;
  const f32x2 t = x * __builtin_elementwise_fma(x * x, (f32x2){c1, c1}, (f32x2){c0, c0});
  const f32x2 d = (f32x2){__builtin_amdgcn_exp2f(t[0]), __builtin_amdgcn_exp2f(t[1])} + (f32x2){1.0f, 1.0f};
  return x * (f32x2){__builtin_amdgcn_rcpf(d[0]), __builtin_amdgcn_rcpf(d[1])};
}
#define LO2(v) __builtin_shufflevector(v, v, 0, 1)
#define HI2(v) __builtin_shufflevector(v, v, 2, 3)
__device__ __forceinline__ float tr_sum16(const float* v, int l) {
  float x[8], y[4];
  asm volatile(
      "s_nop 1\n\t"
      "v_add_f32_dpp %0, %8, %8 row_ror:8 row_mask:0xf bank_mask:0x3\n\t"
      "v_add_f32_dpp %1, %9, %9 row_ror:8 row_mask:0xf bank_mask:0x3\n\t"
      "v_add_f32_dpp %2, %10, %10 row_ror:8 row_mask:0xf bank_mask:0x3\n\t"
      "v_add_f32_dpp %3, %11, %11 row_ror:8 row_mask:0xf bank_mask:0x3\n\t"
      "v_add_f32_dpp %4, %12, %12 row_ror:8 row_mask:0xf bank_mask:0x3\n\t"
      "v_add_f32_dpp %5, %13, %13 row_ror:8 row_mask:0xf bank_mask:0x3\n\t"
      "v_add_f32_dpp %6, %14, %14 row_ror:8 row_mask:0xf bank_mask:0x3\n\t"
      "v_add_f32_dpp %7, %15, %15 row_ror:8 row_mask:0xf bank_mask:0x3\n\t"
      "v_add_f32_dpp %0, %16, %16 row_ror:8 row_mask:0xf bank_mask:0xc\n\t"
      "v_add_f32_dpp %1, %17, %17 row_ror:8 row_mask:0xf bank_mask:0xc\n\t"
      "v_add_f32_dpp %2, %18, %18 row_ror:8 row_mask:0xf bank_mask:0xc\n\t"
      "v_add_f32_dpp %3, %19, %19 row_ror:8 row_mask:0xf bank_mask:0xc\n\t"
      "v_add_f32_dpp %4, %20, %20 row_ror:8 row_mask:0xf bank_mask:0xc\n\t"
      "v_add_f32_dpp %5, %21, %21 row_ror:8 row_mask:0xf bank_mask:0xc\n\t"
      "v_add_f32_dpp %6, %22, %22 row_ror:8 row_mask:0xf bank_mask:0xc\n\t"
      "v_add_f32_dpp %7, %23, %23 row_ror:8 row_mask:0xf bank_mask:0xc"
      : "=&v"(x[0]), "=&v"(x[1]), "=&v"(x[2]), "=&v"(x[3]), "=&v"(x[4]), "=&v"(x[5]), "=&v"(x[6]), "=&v"(x[7])
      : "v"(v[0]), "v"(v[1]), "v"(v[2]), "v"(v[3]), "v"(v[4]), "v"(v[5]), "v"(v[6]), "v"(v[7]), "v"(v[8]), "v"(v[9]), "v"(v[10]),
        "v"(v[11]), "v"(v[12]), "v"(v[13]), "v"(v[14]), "v"(v[15]));
  asm volatile(
      "s_nop 1\n\t"
      "v_add_f32_dpp %0, %4, %4 row_shl:4 row_mask:0xf bank_mask:0x5\n\t"
      "v_add_f32_dpp %1, %5, %5 row_shl:4 row_mask:0xf bank_mask:0x5\n\t"
      "v_add_f32_dpp %2, %6, %6 row_shl:4 row_mask:0xf bank_mask:0x5\n\t"
      "v_add_f32_dpp %3, %7, %7 row_shl:4 row_mask:0xf bank_mask:0x5\n\t"
      "v_add_f32_dpp %0, %8, %8 row_shr:4 row_mask:0xf bank_mask:0xa\n\t"
      "v_add_f32_dpp %1, %9, %9 row_shr:4 row_mask:0xf bank_mask:0xa\n\t"
      "v_add_f32_dpp %2, %10, %10 row_shr:4 row_mask:0xf bank_mask:0xa\n\t"
      "v_add_f32_dpp %3, %11, %11 row_shr:4 row_mask:0xf bank_mask:0xa\n\t"
      "s_nop 1"
      : "=&v"(y[0]), "=&v"(y[1]), "=&v"(y[2]), "=&v"(y[3])
      : "v"(x[0]), "v"(x[1]), "v"(x[2]), "v"(x[3]), "v"(x[4]), "v"(x[5]), "v"(x[6]), "v"(x[7]));
  const bool b1 = l & 2, b0 = l & 1;
  const float z0 = (b1 ? y[2] : y[0]) + dpp_f<0x4E>(b1 ? y[0] : y[2]);
  const float z1 = (b1 ? y[3] : y[1]) + dpp_f<0x4E>(b1 ? y[1] : y[3]);
  return (b0 ? z1 : z0) + dpp_f<0xB1>(b0 ? z0 : z1);
}
__device__ __forceinline__ u32x4 half_swap(u32x4 v) {
  const auto a = __builtin_amdgcn_permlane32_swap(v[0], v[2], false, false);
  const auto b = __builtin_amdgcn_permlane32_swap(v[1], v[3], false, false);
  return (u32x4){a[0], b[0], a[1], b[1]};
}
#define MFMA16(a, b, c) __builtin_amdgcn_mfma_f32_16x16x32_bf16(__builtin_bit_cast(bf16x8, a), __builtin_bit_cast(bf16x8, b), c, 0, 0, 0)
__device__ __forceinline__ u32x2 trd(const char* p) {
  return __builtin_bit_cast(u32x2, __builtin_amdgcn_ds_read_tr16_b64_v4i16((LAS s16x4*)p));
}
__device__ __forceinline__ u32x4 trfrag(const char* p, int second) {
  u32x2 a = trd(p), b = trd(p + second);
  return (u32x4){a[0], a[1], b[0], b[1]};
}
#define BAR() asm volatile("s_waitcnt lgkmcnt(0)\n\ts_barrier" ::: "memory")
#define DRAIN() do { __builtin_amdgcn_s_waitcnt(0x0F70); asm volatile("" ::: "memory"); } while (0)
#define VMW(n) do { __builtin_amdgcn_s_waitcnt(0x0F70 | ((n) & 15) | (((n) >> 4) << 14)); asm volatile("" ::: "memory"); } while (0)
#define SCHED() __builtin_amdgcn_sched_barrier(0)
#define ZERO4 ((f32x4){0.f, 0.f, 0.f, 0.f})

__device__ __forceinline__ u32x4 ld16(rsrc_t r, unsigned off, unsigned so = 0) { return __builtin_amdgcn_raw_buffer_load_b128(r, off, so, SC1); }
__device__ __forceinline__ u32x2 ld8(rsrc_t r, unsigned off, unsigned so = 0) { return __builtin_amdgcn_raw_buffer_load_b64(r, off, so, SC1); }
__device__ __forceinline__ unsigned ld4(rsrc_t r, unsigned off, unsigned so = 0) { return __builtin_amdgcn_raw_buffer_load_b32(r, off, so, SC1); }
__device__ __forceinline__ void st16(rsrc_t r, unsigned off, u32x4 v) { __builtin_amdgcn_raw_buffer_store_b128(v, r, off, 0, SC1); }
__device__ __forceinline__ void st8(rsrc_t r, unsigned off, u32x2 v) { __builtin_amdgcn_raw_buffer_store_b64(v, r, off, 0, SC1); }
__device__ __forceinline__ void st4(rsrc_t r, unsigned off, unsigned v) { __builtin_amdgcn_raw_buffer_store_b32(v, r, off, 0, SC1); }
__device__ __forceinline__ void dma1k(rsrc_t r, char* lds_dst, unsigned l16, unsigned soff) {
  __builtin_amdgcn_raw_ptr_buffer_load_lds(r, (LAS void*)lds_dst, 16, l16, soff, 0, SC1);
}

#define STAMP(i)

template <int SLEEP>
__device__ __forceinline__ void wait_flag(rsrc_t r, unsigned off, unsigned tag) {
  _Pragma("unroll 1") for (unsigned spins = 0; spins < SPIN_CAP; ++spins) {
    if (__all(ld4(r, off) == tag)) break;
    __builtin_amdgcn_s_sleep(SLEEP);
  }
  asm volatile("" ::: "memory");
}

struct ConvSched {
  signed char pp[144], m[144], rd[144];
  int pre;
};
constexpr ConvSched conv_sched(int dist) {
  ConvSched s = {};
  int need[96] = {}, i = 0;
  for (int r = 0; r < 4; ++r)
    for (int pp = 0; pp < 8; ++pp) {
      const int j = 24 * r + (pp ? 8 + 2 * pp : 0);
      need[j] = need[j + 1] = i;
      for (int m = 7 - pp; m >= 0; --m) {
        if (pp == 0) need[24 * r + 9 - m] = i;
        s.pp[i] = pp; s.m[i] = m; s.rd[i] = -1;
        ++i;
      }
    }
  int next = 144;
  for (int j = 95; j >= 0; --j) {
    next = need[j] - dist < next - 1 ? need[j] - dist : next - 1;
    if (next >= 0) s.rd[next] = j;
    else if (s.pre == 0) s.pre = j + 1;
  }
  return s;
}

template <int I> struct IC { static constexpr int v = I; };
template <int I0, int I1, class F>
__device__ __forceinline__ void static_for(F&& f) {
  if constexpr (I0 < I1) { f(IC<I0>{}); static_for<I0 + 1, I1>(f); }
}

struct Params {
  const u16 *q, *k, *v, *shift_k, *ssm_k, *ssm_D, *w_in, *w_sp, *b_sp, *w_out, *cvec, *w_mod;
  u16* out;
  char* ws;
};

__global__ __launch_bounds__(512, 2) void mega(Params p) {
  __shared__ __attribute__((aligned(16))) char lds[LDS_TOTAL];
  const int tid = threadIdx.x, l = tid & 63;
  const int w = __builtin_amdgcn_readfirstlane(tid >> 6);
  const int bid = blockIdx.x, xcd = bid & 7, idx = bid >> 3;
  char* const ws = p.ws;
  asm volatile("" ::"s"(p.q), "s"(p.k), "s"(p.v), "s"(p.shift_k), "s"(p.ssm_k), "s"(p.ssm_D), "s"(p.w_in), "s"(p.w_sp), "s"(p.b_sp),
               "s"(p.w_out), "s"(p.cvec), "s"(p.w_mod), "s"(p.out), "s"(p.ws));
  const rsrc_t rs = __builtin_amdgcn_make_buffer_rsrc(ws, 0, WS_END, 0x00020000);
  const unsigned l16 = (unsigned)l * 16u;
  STAMP(0);
  const unsigned epv = ld4(rs, WS_EPOCH);
  const int dg = ((idx & 7) + 2 * (idx >> 3)) & 7;
  const bool is_conv = dg >= 3;
  const int slot = 3 * (idx >> 3) + dg;
  const int cidx = 5 * (idx >> 3) + dg - 3;
  unsigned cpf = 0;
  if (!is_conv) {
    unsigned long long pc;
    unsigned len, len2;
    asm volatile("s_getpc_b64 %0\n.Lmega_here:\n\ts_mov_b32 %1, .Lmega_end-.Lmega_here\n\ts_mov_b32 %2, .Lmega_aux_end-.Lmega_here" : "=s"(pc), "=s"(len), "=s"(len2));
    len = len > len2 ? len : len2;
    unsigned o = (unsigned)((slot * 8 + w) * 1024 + (l & 7) * 128);
    o = o + 16u <= len ? o : 0u;
    cpf = *(const GAS unsigned*)((pc & ~15ull) + o);
  }

  const int quad = xcd * 20 + cidx;
  const int R = (xcd & 3) * 8 + (idx & 7);
  const int c = (xcd >> 2) * 4 + (idx >> 3);
  const int n16 = l & 15, h4 = l >> 4;
  const rsrc_t rq = __builtin_amdgcn_make_buffer_rsrc((void*)p.q, 0, S_ * D_ * 2, 0x00020000);
  const rsrc_t rsp = __builtin_amdgcn_make_buffer_rsrc((void*)p.w_sp, 0, S_ * S_ * 2, 0x00020000);
  const int wj = w < 7 ? w : 0;
  u32x4 wsp[4][8];
  const unsigned wso = (unsigned)(R * 64) * 4096u + (unsigned)w * 512u;
  const unsigned wdo = (unsigned)(h4 * 8192 + ((n16 ^ h4) * 16));
#define WVO(q) (wdo ^ (unsigned)(((4 - (q)) & 3) * 64))
#define WSO(g, q) (wso + (unsigned)((g) >> 2) * 256u + (unsigned)(32 * (((g) >> 1) & 1) + ((g) & 1) + 8 * (q)) * 4096u)
  u32x4 raw[NRAW];
#define LDW(i) raw[i] = __builtin_amdgcn_raw_buffer_load_b128(rsp, WVO((i) & 3), WSO((i) >> 2, (i) & 3), 0)
#define LDWQ(k) do { _Pragma("unroll") for (int i_ = (k) * (NRAW / 4); i_ < ((k) + 1) * (NRAW / 4); ++i_) LDW(i_); } while (0)
  const int trow = R * 64 + 32 * ((w >> 1) & 1) + 2 * n16 + (w & 1);
  unsigned bs_raw;
  unsigned tag;
  unsigned etv = 0;
  auto etouch = [&]() {
    if (ET) etv = __builtin_amdgcn_raw_buffer_load_b32(rsp, l < 32 ? (unsigned)(((c & 3) * 16 + (l >> 1)) * 4096 + 256 + (l & 1) * 128)
                                                         : (l < 48 ? (unsigned)((32 + (c & 3) * 8 + ((l >> 1) & 7)) * 4096 + (l & 1) * 128) : OOB), wso, 0);
    SCHED();
  };
  unsigned fw2 = OOB;
  unsigned e_fw1;
  const unsigned fw1o = WS_FW1 + (unsigned)(((8 + c) * 8 + wj) * 4 + (l & 3)) * 128u;

  if (__builtin_expect(is_conv, 1)) {
    const rsrc_t rk = __builtin_amdgcn_make_buffer_rsrc((void*)p.k, 0, S_ * D_ * 2, 0x00020000);
    const rsrc_t rv = __builtin_amdgcn_make_buffer_rsrc((void*)p.v, 0, S_ * D_ * 2, 0x00020000);
    const rsrc_t rh = __builtin_amdgcn_make_buffer_rsrc((void*)p.shift_k, 0, D_ * 8, 0x00020000);
    const rsrc_t rf = __builtin_amdgcn_make_buffer_rsrc((void*)p.ssm_k, 0, S_ * D_ * 2, 0x00020000);
    STAMP(32);
    const int blk = (quad / 5) * 8 + w, sl = quad % 5;
    {
      const unsigned g = (unsigned)h4 * 1280u + (unsigned)((n16 ^ (4 * (h4 >> 1))) * 16);
      const unsigned so = (unsigned)blk * 10240u + (unsigned)sl * 256u;
      char* const la = lds + L_A0 + w * 6144;
      __builtin_amdgcn_raw_ptr_buffer_load_lds(rk, (LAS void*)(la + 1024), 16, g ^ 128u, so, 0, 0);
      __builtin_amdgcn_raw_ptr_buffer_load_lds(rk, (LAS void*)(la + 2048), 16, g, so + 5120u, 0, 0);
      __builtin_amdgcn_raw_ptr_buffer_load_lds(rv, (LAS void*)(la + 3072), 16, g, so, 0, 0);
      __builtin_amdgcn_raw_ptr_buffer_load_lds(rv, (LAS void*)(la + 4096), 16, g ^ 128u, so + 5120u, 0, 0);
      __builtin_amdgcn_raw_ptr_buffer_load_lds(rk, (LAS void*)(la), 16, g + (h4 ? 0u : 1280u), blk ? so - 5120u : so, 0, 0);
      __builtin_amdgcn_raw_ptr_buffer_load_lds(rh, (LAS void*)(la + 5120), 16, l16, (unsigned)sl * 1024u, 0, 0);
      SCHED();
    }
    const u32x2 dd2 = *(const CAS u32x2*)((const CAS char*)p.ssm_D + quad * 8);
    {
      const u32x4 z = {0u, 0u, 0u, 0u};
      if (tid < 128) *(u32x4*)(lds + (tid >> 5) * CH_BYTES + (tid & 31) * 16) = z;
      else if (tid < 152) {
        const int i = (tid - 128) >> 2;
        *(u32x4*)(lds + (tid & 3) * CH_BYTES + U_BYTES + (i < 4 ? i * G_COPY : (i & 1 ? 4128 : 16))) = z;
      }
      const LAS char* a0r = (const LAS char*)(lds + L_A0) + w * 6144 + (l & 3) * 512;
#define A0R(k) (a0r + ((((l >> 2) ^ (4 * ((l + (k)) & 3))) * 16)))
      unsigned go = WS_KVT + (unsigned)(quad / 5) * 81920u + (unsigned)sl * 16384u + (unsigned)(l >> 2) * 1024u + (unsigned)w * 64u + (unsigned)(l & 3) * 16u;
      const LAS char* r0 = A0R(0), * r1 = A0R(1), * r2 = A0R(2);
      const LAS char* rsh = (const LAS char*)(lds + L_A0) + w * 6144 + 5120 + (l >> 2) * 64;
      asm volatile("" : "+v"(go), "+v"(r0), "+v"(r1), "+v"(r2), "+v"(rsh));
      SCHED();
      __builtin_amdgcn_s_sleep(F_SLEEP);
#pragma unroll
      for (int j = 0; j < 2; ++j)
        __builtin_amdgcn_raw_ptr_buffer_load_lds(rf, (LAS void*)(lds + (w >> 1) * CH_BYTES + U_BYTES + 32 + (2 * (w & 1) + j) * 1024), 16, l16,
                                                 (unsigned)quad * 16384u + (unsigned)(2 * w + j) * 1024u, 0, 0);
      STAMP(34);
      tag = __builtin_amdgcn_readfirstlane(epv) * 2654435761u + 0x9E3779B9u;
      SCHED();
      VMW(2);
      STAMP(35);
      u32x4 sh[4], ka[5], va[2];
      sh[0] = *(const volatile LAS u32x4*)(rsh);
      ka[0] = *(const volatile LAS u32x4*)(r0 + 256);
      ka[1] = *(const volatile LAS u32x4*)(r1 + 512);
      ka[2] = *(const volatile LAS u32x4*)(r1 + 768);
      ka[3] = *(const volatile LAS u32x4*)(r2 + 1024);
      ka[4] = *(const volatile LAS u32x4*)(r2 + 1280);
      va[0] = *(const volatile LAS u32x4*)(r0 + 3072);
      va[1] = *(const volatile LAS u32x4*)(r0 + 3328);
#pragma unroll
      for (int e = 1; e < 4; ++e) sh[e] = *(const volatile LAS u32x4*)(rsh + 16 * e);
      SCHED();
      if (blk == 0) {
        if ((l & 3) < 2) ka[0] = z;
        if ((l & 3) == 0) { ka[1] = z; ka[2] = z; }
      }
      u32x4 ko;
#pragma unroll
      for (int e = 0; e < 4; ++e) {
        const f32x2 w0 = {bflo(sh[e][0]), bflo(sh[e][2])}, w1 = {bfhi(sh[e][0]), bfhi(sh[e][2])}, w2 = {bflo(sh[e][1]), bflo(sh[e][3])}, w3 = {bfhi(sh[e][1]), bfhi(sh[e][3])};
        const f32x2 km3 = bf2(ka[0][e]), km2 = bf2(ka[1][e]), km1 = bf2(ka[2][e]), k0 = bf2(ka[3][e]), k1 = bf2(ka[4][e]);
        const f32x2 s0 = __builtin_elementwise_fma(k0, w0, __builtin_elementwise_fma(km1, w1, __builtin_elementwise_fma(km2, w2, km3 * w3)));
        const f32x2 s1 = __builtin_elementwise_fma(k1, w0, __builtin_elementwise_fma(k0, w1, __builtin_elementwise_fma(km1, w2, km2 * w3)));
        const f32x2 o0 = s0 * bf2(va[0][e]), o1 = s1 * bf2(va[1][e]);
        ko[2 * (e & 1)] = pack2(o1[0], o0[0]); ko[2 * (e & 1) + 1] = pack2(o1[1], o0[1]);
        if (e & 1) st16(rs, go + (unsigned)(e >> 1) * 512u, ko);
        SCHED();
      }
    }
    STAMP(1);
    DRAIN();
    BAR();
    st4(rs, tid == 0 ? WS_FK + (unsigned)quad * 128u : OOB, tag);
    unsigned atv = 0;
    auto atouch = [&]() {
      const int i = w < 5 ? (cidx * 5 + w) * 20 + (l < 19 ? l : 19) : (cidx * 3 + w - 5) * 19 + (l < 18 ? l : 18);
      const int r = i / 3;
      const unsigned o1 = (unsigned)((r < 639 ? r : 639) * (F_ * 2) + xcd * CB * 2 + (i - 3 * r) * 108);
      const unsigned o2 = (unsigned)(xcd * CB * D_ * 2 + (i < 1079 ? i : 1079) * 128);
      atv = *(const GAS unsigned*)((const GAS char*)(w < 5 ? p.w_in : p.w_out) + (w < 5 ? o1 : o2));
      SCHED();
    };
    if (AT == 2) atouch();
    {
      u32x4 d[3];
      u32x2 n[3];
#pragma unroll
      for (int j = 0; j < 3; ++j) {
        char* G = j < 2 ? lds + ((tid >> 8) + 2 * j) * CH_BYTES + U_BYTES + ((tid & 255) + 2) * 16 : lds + (tid & 3) * CH_BYTES + U_BYTES + 16;
        d[j] = *(const u32x4*)G;
        n[j] = *(const u32x2*)(G + 16);
      }
#pragma unroll
      for (int j = 0; j < 3; ++j) {
        char* G = j < 2 ? lds + ((tid >> 8) + 2 * j) * CH_BYTES + U_BYTES + ((tid & 255) + 2) * 16 : lds + (tid & 3) * CH_BYTES + U_BYTES + 16;
        const unsigned a0 = __builtin_amdgcn_alignbit(d[j][1], d[j][0], 16), a1 = __builtin_amdgcn_alignbit(d[j][2], d[j][1], 16),
                       a2 = __builtin_amdgcn_alignbit(d[j][3], d[j][2], 16), a3 = __builtin_amdgcn_alignbit(n[j][0], d[j][3], 16),
                       a4 = __builtin_amdgcn_alignbit(n[j][1], n[j][0], 16);
        if (j < 2 || tid < 4) {
          *(u32x4*)(G + G_COPY) = (u32x4){a0, a1, a2, a3};
          *(u32x4*)(G + 2 * G_COPY) = (u32x4){d[j][1], d[j][2], d[j][3], n[j][0]};
          *(u32x4*)(G + 3 * G_COPY) = (u32x4){a1, a2, a3, a4};
        }
      }
    }
    STAMP(36);
    {
      __builtin_amdgcn_s_sleep(P_SLEEP);
      wait_flag<1>(rs, WS_FK + (unsigned)(5 * (4 * w + (l & 3)) + (quad >> 5)) * 128u, tag);
      STAMP(29);
      const unsigned gp = WS_KVT + (unsigned)(4 * w + (l >> 5)) * 81920u + (unsigned)quad * 512u + (unsigned)(l & 31) * 16u;
      const u32x4 g0 = ld16(rs, gp), g1 = ld16(rs, gp + 163840u);
      SCHED();
      if (AT == 1) atouch();
      char* Uw = lds + 512 + (128 * w + (l & ~3) + 3 - (l & 3)) * 4;
#pragma unroll
      for (int i = 0; i < 4; ++i) *(unsigned*)(Uw + i * CH_BYTES) = g0[i];
#pragma unroll
      for (int i = 0; i < 4; ++i) *(unsigned*)(Uw + 256 + i * CH_BYTES) = g1[i];
    }
    STAMP(28);
    BAR();
    STAMP(2);
    const int te = 2 * tid;
    u32x4 qa[2];
    u32x4 sv[2];
    u32x2 mg;
    const unsigned xo = (unsigned)(tid >> 4) * WX1P + (unsigned)quad * 256u + (unsigned)(tid & 15) * 16u;
    const unsigned mgo = WS_MOD + (unsigned)((l < 12 ? (l >> 2) * D_ + (l & 3) : 0) + quad * 4) * 8u;
    const int ch = w & 3, hf = w >> 2;
    auto ld_sv = [&]() {
      sv[0] = ld16(rs, WS_ST1 + (unsigned)tid * 16u);
      sv[1] = ld16(rs, WS_ST1 + (unsigned)tid * 16u + 8192u);
    };
    auto ld_q = [&]() {
      qa[0] = ld16(rs, WS_QB + xo);
      qa[1] = ld16(rs, WS_QB + xo + 32u * WX1P);
    };
    f32x2 hh[2][2][2], base[2][2][2];
    auto prep = [&]() {
      const LAS char* ur = (const LAS char*)(lds + 512) + ((te & ~7) + 6 - (te & 7)) * 2;
      unsigned uu[2][4];
#pragma unroll
      for (int i = 0; i < 8; ++i) uu[i >> 2][i & 3] = *(const volatile LAS unsigned*)(ur + (i >> 2) * 2048 + (i & 3) * CH_BYTES);
      if (!__all(((sv[0][0] ^ tag) | (sv[0][2] ^ tag) | (sv[1][0] ^ tag) | (sv[1][2] ^ tag)) == 0u)) {
        _Pragma("unroll 1") for (unsigned spins = 0; spins < SPIN_CAP; ++spins) {
          ld_sv();
          if (__all(sv[0][0] == tag && sv[0][2] == tag && sv[1][0] == tag && sv[1][2] == tag)) break;
          __builtin_amdgcn_s_sleep(4);
        }
        asm volatile("" ::: "memory");
        ld_q();
        mg = ld8(rs, mgo);
        DRAIN();
      }
      if (!__all(mg[0] == tag)) {
        _Pragma("unroll 1") for (unsigned spins = 0; spins < SPIN_CAP; ++spins) {
          mg = ld8(rs, mgo);
          if (__all(mg[0] == tag)) break;
          __builtin_amdgcn_s_sleep(2);
        }
      }
      f32x2 cc2[2], ss2[2], dv2[2];
      float cch[4], sch[4];
#pragma unroll
      for (int i = 0; i < 4; ++i) {
        const float sa = __uint_as_float(__builtin_amdgcn_readlane(mg[1], i)), ca = __uint_as_float(__builtin_amdgcn_readlane(mg[1], 4 + i)),
                    ga = __uint_as_float(__builtin_amdgcn_readlane(mg[1], 8 + i));
        cch[i] = ga * rbf(1.0f + ca);
        sch[i] = ga * sa;
      }
      cc2[0] = (f32x2){cch[0], cch[1]}; cc2[1] = (f32x2){cch[2], cch[3]};
      ss2[0] = (f32x2){sch[0], sch[1]}; ss2[1] = (f32x2){sch[2], sch[3]};
      dv2[0] = bf2(dd2[0]); dv2[1] = bf2(dd2[1]);
#pragma unroll
      for (int H = 0; H < 2; ++H)
#pragma unroll
        for (int e = 0; e < 2; ++e) {
          const unsigned st = sv[H][1 + 2 * e];
          const float a = __builtin_amdgcn_rcpf(bfhi(st)), b = -bflo(st) * a;
#pragma unroll
          for (int i = 0; i < 2; ++i) {
            const f32x2 q2 = bf2(qa[H][2 * e + i]);
            const f32x2 u2 = e ? (f32x2){bflo(uu[H][2 * i]), bflo(uu[H][2 * i + 1])} : (f32x2){bfhi(uu[H][2 * i]), bfhi(uu[H][2 * i + 1])};
            const f32x2 n1 = __builtin_elementwise_fma(q2, (f32x2){a, a}, (f32x2){b, b});
            hh[H][e][i] = __builtin_elementwise_fma(n1, cc2[i], ss2[i]);
            base[H][e][i] = __builtin_elementwise_fma(hh[H][e][i], u2 * dv2[i], q2);
            asm volatile("" : "+v"(hh[H][e][i]), "+v"(base[H][e][i]));
          }
        }
    };
    auto HF = [&]() { int x = hf; asm volatile("" : "+s"(x)); return x; };
    {
      const char* U = lds + ch * CH_BYTES;
      const char* G = U + U_BYTES;
      const int a = n16, jj = h4 >> 1, e2 = h4 & 1;
      const int sg = (a + 1) & 3;
      const LAS char* bp = (const LAS char*)U + 512 + 32 * (a - jj) + 16 * e2 - 256 * hf - 192;
      const LAS char* ap = (const LAS char*)G + sg * G_COPY + 2 * (16 * jj + a - 8 * e2 + 9 - sg) + 256 * hf;
      char* yp = lds + L_YP + (hf * 4 + ch) * 8192 + (h4 * 16 + ((a + 4 * h4) & 15)) * 16;
      f32x4 acc[8];
      u32x4 A[2][8], B[2][8];
      auto rd = [&](int k) {
        const int r = k / 24, j = k % 24;
        if (j >= 2 && j < 10) B[r & 1][9 - j] = *(const volatile LAS u32x4*)(bp + 512 * (9 - j) + 64 * (3 - r));
        else {
          const int pp = j < 2 ? 0 : (j - 8) >> 1;
          const u32x2 v = *(const volatile LAS u32x2*)(ap + 512 * pp + 64 * r + 8 * (j & 1));
          A[r & 1][pp][2 * (j & 1)] = v[0]; A[r & 1][pp][2 * (j & 1) + 1] = v[1];
        }
      };
#pragma unroll
      for (int k = 0; k < IC<conv_sched(CDIST).pre>::v; ++k) rd(k);
      SCHED();
      auto step = [&](auto ic) {
        constexpr int i = decltype(ic)::v, r = i / 36, pp = conv_sched(CDIST).pp[i], m = conv_sched(CDIST).m[i], k = conv_sched(CDIST).rd[i];
        acc[m + pp] = MFMA16(A[r & 1][pp], B[r & 1][m], i < 8 ? ZERO4 : acc[m + pp]);
        SCHED();
        if (k >= 0) rd(k);
        if (i == 0 && AT == 3) atouch();
        if (i % RAWD == 2 && i / RAWD < NRA) LDW(i / RAWD);
        if (i >= PREP_AT && (i - PREP_AT) % RAWD == 0 && NRA + (i - PREP_AT) / RAWD < NRAW) LDW(NRA + (i - PREP_AT) / RAWD);
        if (i == SV_AT) { ld_sv(); bs_raw = p.b_sp[trow]; }
        if (r == 3 && pp >= 1 && m == 7 - pp) *(f32x4*)(yp + (pp - 1) * 1024) = acc[pp - 1];
        SCHED();
      };
      static_for<0, PREP_AT>(step);
      STAMP(3);
      DRAIN();
      asm volatile("" ::"v"(atv));
      ld_q();
      mg = ld8(rs, mgo);
      SCHED();
      if (PM == 0) {
        if (HF()) __builtin_amdgcn_s_setprio(1);
        if (!HF()) prep();
      }
      STAMP(4);
      static_for<PREP_AT, 144>(step);
      *(f32x4*)(yp + 7 * 1024) = acc[7];
      STAMP(5);
    }
    if (PM == 0) { if (HF()) { prep(); __builtin_amdgcn_s_setprio(0); } }
    else prep();
    e_fw1 = ld4(rs, fw1o);
    STAMP(6);
    const LAS char* yr = (const LAS char*)(lds + L_YP) + (te >> 8) * 1024 + (((te >> 2) & 3) * 16 + ((((te >> 4) & 15) + 4 * ((te >> 2) & 3)) & 15)) * 16 + (te & 3) * 4;
    asm volatile("" : "+v"(yr));
    BAR();
    STAMP(7);
    f32x2 y[2][8];
#pragma unroll
    for (int i = 0; i < 16; ++i) y[i >> 3][i & 7] = *(const volatile LAS f32x2*)(yr + (i >> 3) * 4096 + (i & 7) * 8192);
#pragma unroll
    for (int H = 0; H < 2; ++H) {
      u32x4 o;
#pragma unroll
      for (int e = 0; e < 2; ++e)
#pragma unroll
        for (int i = 0; i < 2; ++i) {
          const float x0 = __builtin_fmaf(hh[H][e][i][0], y[H][2 * i][e] + y[H][4 + 2 * i][e], base[H][e][i][0]);
          const float x1 = __builtin_fmaf(hh[H][e][i][1], y[H][2 * i + 1][e] + y[H][5 + 2 * i][e], base[H][e][i][1]);
          o[2 * e + i] = pack2(x0, x1);
        }
      st16(rs, WS_X1B + (unsigned)(32 * H) * WX1P + xo, o);
    }
    STAMP(8);
  } else {
    const rsrc_t rwi = __builtin_amdgcn_make_buffer_rsrc((void*)p.w_in, 0, D_ * F_ * 2, 0x00020000);
    const rsrc_t rwo = __builtin_amdgcn_make_buffer_rsrc((void*)p.w_out, 0, FH * D_ * 2, 0x00020000);
    STAMP(32);
    const int job = xcd + 8 * slot;
    const int T = job >= 50 && w < 3 && job - 50 + 46 * w < 128 ? job - 50 + 46 * w : -1;
    const bool unit = slot >= 8 || (slot >= 2 && slot < 6);
    const int ucb = slot >= 8 ? 8 + xcd : xcd, kg = (slot >= 8 ? slot : slot - 2) & 3;
    float* tab = (float*)(lds + L_TAB);
    float* red = (float*)(lds + L_RED);
    auto issue_unit = [&](int i) {
      const int g = i * 64 + l;
      __builtin_amdgcn_raw_ptr_buffer_load_lds(rwi, (LAS void*)(lds + L_J3 + i * 1024), 16, (unsigned)((g / 14) * (F_ * 2) + (g % 14) * 16),
                                               (unsigned)(kg * 160 * F_ * 2 + ucb * CB * 2), 0, 0);
    };
    if (T >= 0) {
      __builtin_amdgcn_s_sleep(Q_SLEEP);
      char* qt = lds + L_QT + w * 20480;
      const unsigned so = (unsigned)(16 * T) * 1280u;
#pragma unroll
      for (int n = 0; n < 16; ++n)
        __builtin_amdgcn_raw_ptr_buffer_load_lds(rq, (LAS void*)(qt + n * 1024), 16, l16 ^ (unsigned)(n * 16), so + (unsigned)(n * 1280), 0, 0);
#pragma unroll
      for (int j = 0; j < 4; ++j)
        __builtin_amdgcn_raw_ptr_buffer_load_lds(rq, (LAS void*)(qt + 16384 + j * 1024), 16, (unsigned)(h4 * 1280 + ((n16 ^ (4 * j + h4)) * 16)), so + (unsigned)(4 * j * 1280 + 1024), 0, 0);
      tag = __builtin_amdgcn_readfirstlane(epv) * 2654435761u + 0x9E3779B9u;
      STAMP(34);
      DRAIN();
      STAMP(35);
      const bf2_t one = {(__bf16)1.0f, (__bf16)1.0f};
      float s1[2] = {0.f, 0.f}, s2[2] = {0.f, 0.f};
      const int n0 = 2 * (l & 7);
      u32x4 qv[20];
#pragma unroll
      for (int i = 0; i < 20; ++i)
#pragma unroll
        for (int e = 0; e < 2; ++e) {
          const int n = n0 + e, ck = 4 * (i & 15) + h4;
          const u32x2 v = *(const volatile LAS u32x2*)(qt + (i < 16 ? n * 1024 : 16384 + n * 256) + ((ck ^ n) * 16) + (l & 8));
          qv[i][2 * e] = v[0]; qv[i][2 * e + 1] = v[1];
        }
#pragma unroll
      for (int i = 0; i < 20; ++i) {
        st16(rs, WS_QB + (unsigned)(T >> 1) * WX1P + (unsigned)i * 2048u + (unsigned)((l >> 3) * 256 + (T & 1) * 128 + (l & 7) * 16), qv[i]);
#pragma unroll
        for (int e = 0; e < 4; ++e) {
          const unsigned x = qv[i][e];
          const bf2_t v = __builtin_bit_cast(bf2_t, x);
          s1[e >> 1] = __builtin_amdgcn_fdot2_f32_bf16(v, one, s1[e >> 1], false);
          s2[e >> 1] = __builtin_amdgcn_fdot2_f32_bf16(v, v, s2[e >> 1], false);
        }
        SCHED();
      }
      u32x4 o = {tag, 0u, tag, 0u};
#pragma unroll
      for (int e = 0; e < 2; ++e) {
        const float t1 = sum_rows(s1[e] + dpp_f<0x128>(s1[e])), t2 = sum_rows(s2[e] + dpp_f<0x128>(s2[e]));
        const float mean = t1 * (1.0f / D_);
        const float var = __builtin_fmaxf(t2 * (1.0f / D_) - mean * mean, 0.f);
        o[2 * e + 1] = pack2(mean, __builtin_amdgcn_sqrtf(rbf(rbf(var) + 1e-5f)));
      }
      STAMP(1);
      DRAIN();
      if (l < 8) st16(rs, WS_ST1 + (unsigned)T * 128u + l16, o);
      STAMP(3);
    }
    if (job < 60) {
      const int grp = job < 10 ? 40 + job : (job < 50 ? job - 10 : job);
      if (job >= 50 && T < 0) __builtin_amdgcn_s_sleep(LATE_SLEEP);
      if (job >= 10 && T < 0) __builtin_amdgcn_s_sleep(AUX_SLEEP);
      const unsigned cv = *(const unsigned*)(p.cvec + 80 * w + 2 * (l < 40 ? l : 0));
      u32x4 pre[10];
#pragma unroll
      for (int pp = 0; pp < 10; ++pp) pre[pp] = *(const u32x4*)(p.w_mod + (size_t)(80 * w + pp * 8 + (l >> 3)) * 3840 + grp * 64 + (l & 7) * 8);
      SCHED();
      STAMP(34);
      tag = __builtin_amdgcn_readfirstlane(epv) * 2654435761u + 0x9E3779B9u;
      if (l < 40) {
        const float c0 = bflo(cv), c1 = bfhi(cv);
        *(f32x2*)(tab + w * 80 + 2 * l) = (f32x2){rbf(c0 * __builtin_amdgcn_rcpf(1.0f + __expf(-c0))), rbf(c1 * __builtin_amdgcn_rcpf(1.0f + __expf(-c1)))};
      }
      asm volatile("s_waitcnt lgkmcnt(0)" ::: "memory");
      f32x2 acc[4];
#pragma unroll
      for (int e = 0; e < 4; ++e) acc[e] = (f32x2){0.f, 0.f};
#pragma unroll
      for (int pp = 0; pp < 10; ++pp) {
        const float sv = tab[w * 80 + pp * 8 + (l >> 3)];
#pragma unroll
        for (int e = 0; e < 4; ++e) acc[e] = __builtin_elementwise_fma((f32x2){sv, sv}, bf2(pre[pp][e]), acc[e]);
      }
      STAMP(35);
      asm volatile("" : "+v"(acc[0]), "+v"(acc[1]), "+v"(acc[2]), "+v"(acc[3]));
#pragma unroll
      for (int e = 0; e < 8; ++e) { float a = acc[e >> 1][e & 1]; a += dpp_f<0x128>(a); acc[e >> 1][e & 1] = sum_rows(a); }
      if (l < 8) {
        *(f32x4*)(red + w * 64 + l * 8) = (f32x4){acc[0][0], acc[0][1], acc[1][0], acc[1][1]};
        *(f32x4*)(red + w * 64 + l * 8 + 4) = (f32x4){acc[2][0], acc[2][1], acc[3][0], acc[3][1]};
      }
      if (w == 7) __builtin_amdgcn_s_setprio(2);
      BAR();
      if (w == 7) {
        float t = 0.f;
#pragma unroll
        for (int r = 0; r < 8; ++r) t += red[r * 64 + l];
        st8(rs, WS_MOD + (unsigned)(grp * 64 + l) * 8u, (u32x2){tag, __float_as_uint(rbf(t))});
        __builtin_amdgcn_s_setprio(0);
      } else if (unit) {
#pragma unroll
        for (int k = 0; k < 5; ++k) issue_unit(w * 5 + k);
      }
    } else if (unit && w >= 3 && w < 7) {
      __builtin_amdgcn_s_sleep(AUX_SLEEP);
#pragma unroll
      for (int k = 0; k < 9; ++k) issue_unit((w - 3) * 9 + k < 35 ? (w - 3) * 9 + k : 34);
    }
    tag = __builtin_amdgcn_readfirstlane(epv) * 2654435761u + 0x9E3779B9u;
    if (unit) {
      auto mod_tab = [&](int which, float add, float* dst) {
        const unsigned o = WS_MOD + (unsigned)(which * D_ + kg * 160) * 8u;
        u32x4 g0, g1;
        _Pragma("unroll 1") for (unsigned spins = 0; spins < SPIN_CAP; ++spins) {
          g0 = ld16(rs, o + l16);
          g1 = ld16(rs, o + 1024u + (l16 & 255u));
          if (__all(g0[0] == tag && g0[2] == tag && g1[0] == tag && g1[2] == tag)) break;
          __builtin_amdgcn_s_sleep(2);
        }
        *(f32x2*)(dst + 2 * l) = (f32x2){rbf(add + __uint_as_float(g0[1])), rbf(add + __uint_as_float(g0[3]))};
        if (l < 16) *(f32x2*)(dst + 128 + 2 * l) = (f32x2){rbf(add + __uint_as_float(g1[1])), rbf(add + __uint_as_float(g1[3]))};
      };
      if (w == 7) {
        STAMP(1);
        if (slot >= 8) __builtin_amdgcn_s_sleep(64);
        mod_tab(4, 1.0f, tab);
        BAR();
        STAMP(2);
        mod_tab(3, 0.0f, tab + 160);
        BAR();
      } else {
        DRAIN();
        STAMP(36);
        BAR();
        STAMP(2);
        const char* bp = lds + L_J3 + (4 * h4 + (n16 >> 2)) * 224 + (n16 & 3) * 8 + w * 32;
        const float* tp = tab + 4 * h4;
        u32x4 f3[5];
        float cs = 0.f, b1 = 0.f;
        const bool pad = w == 6 && n16 >= 12;
        const unsigned padv = (n16 == 12 && ucb >= 8) ? 0x3f803f80u : 0u;
#pragma unroll
        for (int k = 0; k < 5; ++k) f3[k] = trfrag(bp + k * 7168, 16 * 224);
#pragma unroll
        for (int k = 0; k < 5; ++k) {
          const f32x4 ca = *(const f32x4*)(tp + k * 32), cb4 = *(const f32x4*)(tp + k * 32 + 16);
          u32x4 o;
#pragma unroll
          for (int e = 0; e < 4; ++e) {
            const float c0 = e < 2 ? ca[2 * e] : cb4[2 * e - 4], c1 = e < 2 ? ca[2 * e + 1] : cb4[2 * e - 3];
            const float p0 = bflo(f3[k][e]) * c0, p1 = bfhi(f3[k][e]) * c1;
            cs += p0 + p1;
            o[e] = pad ? padv : pack2(p0, p1);
          }
          st16(rs, WS_WINF + (unsigned)(ucb * 7 + w) * 20480u + (unsigned)(kg * 5 + k) * 1024u + l16, o);
        }
        cs = sum_rows(cs);
        DRAIN();
        if (l == 0) st4(rs, WS_FW1 + (unsigned)((ucb * 8 + w) * 4 + kg) * 128u, tag);
        STAMP(5);
        BAR();
#pragma unroll
        for (int k = 0; k < 5; ++k) {
          const f32x4 sa = *(const f32x4*)(tp + 160 + k * 32), sb4 = *(const f32x4*)(tp + 160 + k * 32 + 16);
#pragma unroll
          for (int e = 0; e < 4; ++e) {
            const float s0 = e < 2 ? sa[2 * e] : sb4[2 * e - 4], s1 = e < 2 ? sa[2 * e + 1] : sb4[2 * e - 3];
            b1 = __builtin_fmaf(s0, bflo(f3[k][e]), __builtin_fmaf(s1, bfhi(f3[k][e]), b1));
          }
        }
        b1 = sum_rows(b1);
        if (pad) { cs = padv ? 160.0f : 0.0f; b1 = 0.0f; }
        if (l < 16) st16(rs, WS_CS + (unsigned)((ucb * 4 + kg) * 112 + w * 16 + l) * 16u, (u32x4){tag, __float_as_uint(cs), tag, __float_as_uint(b1)});
      }
    } else {
      if (job >= 60 && T < 0) __builtin_amdgcn_s_sleep(100);
      if (slot < 2 && w >= 2 && w < 6) {
        const int c5 = slot * 4 + w - 2, c2 = xcd;
        char* st = lds + L_J5 + (w - 2) * 18432;
#pragma unroll
        for (int r = 0; r < 18; ++r) {
          const int g = r * 64 + l;
          __builtin_amdgcn_raw_ptr_buffer_load_lds(rwo, (LAS void*)(st + r * 1024), 16, (unsigned)((g / 10) * (D_ * 2) + (g % 10) * 16),
                                                   (unsigned)(xcd * CB * D_ * 2 + c5 * 160), 0, 0);
        }
        DRAIN();
        const char* bp = st + (4 * h4 + (n16 >> 2)) * 160 + (n16 & 3) * 8;
        const unsigned base = WS_WOUTF + (unsigned)c5 * WBLK;
#pragma unroll
        for (int dj = 0; dj < 5; ++dj) {
          st16(rs, base + (unsigned)c2 * 10240u + (unsigned)dj * 1024u + l16, trfrag(bp + dj * 32, 32 * 160));
          st16(rs, base + (unsigned)c2 * 10240u + (unsigned)(5 + dj) * 1024u + l16, trfrag(bp + 16 * 160 + dj * 32, 32 * 160));
          st16(rs, base + 81920u + (unsigned)c2 * 5120u + (unsigned)dj * 1024u + l16, trfrag(bp + 64 * 160 + dj * 32, 16 * 160));
          st8(rs, base + 122880u + (unsigned)((c2 >> 1) * 5 + dj) * 1024u + (unsigned)(c2 & 1) * 512u + (unsigned)l * 8u, trd(bp + 96 * 160 + dj * 32));
        }
        fw2 = WS_FW2 + (unsigned)(c5 * 8 + xcd) * 128u;
      }
    }
    STAMP(4);
#pragma unroll
    for (int i = 0; i < NRAW; ++i) LDW(i);
    bs_raw = p.b_sp[trow];
    e_fw1 = ~tag;
    asm volatile(".Lmega_aux_end:");
    asm volatile("" ::"v"(cpf));
    STAMP(8);
  }

  LAS char* xa = (LAS char*)(lds + L_XI) + l * 16;
  LAS char* xb = xa + 65536;
  asm volatile("" : "+v"(xb));
#define LDA(ks, rt) (*(const volatile LAS u32x4*)((ks) < 16 ? xa + ((ks) * 4 + (rt)) * 1024 : xb + (((ks) - 16) * 4 + (rt)) * 1024))
  auto lda1 = [&](int ks, u32x4* A) {
#pragma unroll
    for (int rt = 0; rt < 4; ++rt) A[rt] = LDA(ks, rt);
  };
  const LAS char* tb4 = (const LAS char*)(lds + L_MISC) + h4 * 16;
  const LAS char* tbn = (const LAS char*)(lds + L_MISC) + n16 * 4;
  asm volatile("" : "+v"(tb4), "+v"(tbn));
  u32x4 xr[5][2];
  auto ld_x1 = [&]() {
#pragma unroll
    for (int m = 0; m < 5; ++m) {
      if (m == 0) __builtin_amdgcn_s_setprio(2);
      if (m == 1) __builtin_amdgcn_s_setprio(1);
      if (m == 3) __builtin_amdgcn_s_setprio(0);
#pragma unroll
      for (int e = 0; e < 2; ++e)
        xr[m][e] = ld16(rs, l16, WS_X1B + (unsigned)(R * 2 + (w & 1)) * WX1P + (unsigned)(4 * m + (w >> 1)) * 2048u + (unsigned)e * 1024u);
      SCHED();
    }
  };
  LAS char* xw = xa + w * 2048;
  auto put_x1 = [&](int m) {
    LAS char* d = m < 4 ? xw + m * 16384 : xb + w * 2048;
#pragma unroll
    for (int e = 0; e < 2; ++e) {
      *(volatile LAS u32x2*)(d + 8 * e) = (u32x2){xr[m][e][0], xr[m][e][1]};
      *(volatile LAS u32x2*)(d + 1024 + 8 * e) = (u32x2){xr[m][e][2], xr[m][e][3]};
    }
  };
  auto poison_u = [&]() {
#pragma unroll
    for (int k = 0; k < 2; ++k) {
      const unsigned o = (unsigned)(wj * 2 + k) * 1024u + l16;
      const unsigned rt = o / 3584u, r = o - rt * 3584u;
      st16(rs, WS_UF + ((unsigned)(R * 4) + rt) * 8u * 3584u + (unsigned)c * 3584u + r, (u32x4){~0u, ~0u, ~0u, ~0u});
    }
  };
  const unsigned wob = WS_WOUTF + (unsigned)c * WBLK;
  const LAS char* wrd = (const LAS char*)(lds + L_WS) + w * 8192 + n16 * 256 + ((h4 ^ ((4 - (n16 >> 2)) & 3)) * 64);
#define DMA_W(g, q) __builtin_amdgcn_raw_ptr_buffer_load_lds(rsp, (LAS void*)(lds + L_WS + w * 8192 + (((g) + 1) & 1) * 4096 + (q) * 1024), 16, WVO(q), WSO(g, q), 0, 0)
#define RD_W(g) do { _Pragma("unroll") for (int i_ = 0; i_ < 4; ++i_) \
                      wsp[(g) & 3][((g) >> 2) * 4 + i_] = *(const volatile LAS u32x4*)(wrd + (((g) + 1) & 1) * 4096 + ((i_ ^ (n16 & 3)) * 16)); } while (0)
  auto park = [&]() {
    LAS char* d = (LAS char*)(lds + L_WS) + w * 8192 + l * 16;
#pragma unroll
    for (int g = 0; g < NRAW / 4; ++g) {
#pragma unroll
      for (int q = 0; q < 4; ++q) *(volatile LAS u32x4*)(d + ((g + 1) & 1) * 4096 + q * 1024) = raw[4 * g + q];
      RD_W(g);
    }
    asm volatile("s_waitcnt lgkmcnt(0)" ::"v"(etv) : "memory");
  };
#define DMA_AB(k) dma1k(rs, lds + L_AB + ((k) * 8 + w) * 1024, l16, wob + (unsigned)((k) * 8 + w) * 1024u)
  const int rto = w & 3, cls = w >> 2;
  u32x2 xp[3];
  constexpr int W = G2W;
  const unsigned zb = WS_Z2F + (unsigned)c * 458752u + (unsigned)w * 8192u;
  const unsigned fzo = WS_FZ + (unsigned)(c * 32 + w * 4 + (l & 3)) * 128u;
  u32x4 fr[W];
#define LDZ(i) fr[(i) % W] = ld16(rs, l16, zb + (unsigned)((i) >> 3) * 65536u + (unsigned)((i) & 7) * 1024u)
  u32x4 cr[5];
#define LDCR(k) cr[k] = ld16(rs, l16, wob + 81920u + (unsigned)(w * 5 + (k)) * 1024u)
  auto rd_xp = [&]() {
#pragma unroll
    for (int j = 0; j < 3; ++j) {
      const int q4 = 5 * c + (cls ? (3 + (j < 2 ? j : 1)) : j);
      xp[j] = *(const volatile LAS u32x2*)(xa + ((q4 >> 1) * 4 + rto) * 1024 + (q4 & 1) * 8);
    }
  };
  if (w < 7) {
    u32x4 B2[20], B1[20];
    const unsigned b2o = WS_WINF + (unsigned)((8 + c) * 7 + w) * 20480u, b1o = WS_WINF + (unsigned)(c * 7 + w) * 20480u;
#define LDB2(ks) B2[ks] = ld16(rs, l16, b2o + (unsigned)(ks) * 1024u)
#define LDB1(ks) B1[ks] = ld16(rs, l16, b1o + (unsigned)(ks) * 1024u)
    const bool early = __all(e_fw1 == tag);
    asm volatile("" ::: "memory");
    {
      const unsigned le = early ? l16 : OOB;
#pragma unroll
      for (int ks = 0; ks < NB2A; ++ks) B2[ks] = ld16(rs, le, b2o + (unsigned)ks * 1024u);
      SCHED();
      if (is_conv) VMW(NB2A);
    }
    STAMP(33);
    BAR();
    STAMP(37);
    if (!early) {
      wait_flag<4>(rs, fw1o, tag);
      DRAIN();
      st4(rs, l == 0 ? fw2 : OOB, tag);
#pragma unroll
      for (int ks = 0; ks < NB2A; ++ks) LDB2(ks);
    }
#pragma unroll
    for (int ks = NB2A; ks < 20; ++ks) {
      LDB2(ks);
      if (ks == 15) { SCHED(); park(); SCHED(); }
      if (ks == 16) { SCHED(); __builtin_amdgcn_s_barrier(); SCHED(); }
    }
#pragma unroll
    for (int q = 0; q < 4; ++q) DMA_W(2, q);
    STAMP(9);
    BAR();
    STAMP(10);
    ld_x1();
    SCHED();
#pragma unroll
    for (int m = 0; m < 3; ++m) put_x1(m);
    if (w == 6) __builtin_amdgcn_s_setprio(2);
    BAR();
    STAMP(11);
    f32x4 acc[4];
    u32x4 A[2][4];
    lda1(0, A[0]);
    poison_u();
#pragma unroll
    for (int ks = 0; ks < 20; ++ks) {
      if (ks == 6) put_x1(3);
      if (ks == 8) put_x1(4);
      if (ks == 11) BAR();
      if (ks >= 4) LDB1(ks - 4);
      if (ks >= 16) LDB1(ks);
      if (ks == 12) RD_W(2);
      SCHED();
#pragma unroll
      for (int rt = 0; rt < 4; ++rt) {
        if (ks < 19) A[(ks + 1) & 1][rt] = LDA(ks + 1, rt);
        SCHED();
        acc[rt] = MFMA16(A[ks & 1][rt], B2[ks], ks ? acc[rt] : ZERO4);
        SCHED();
      }
    }
    STAMP(12);
    if (w == 6) {
      if (n16 == 12) {
#pragma unroll
        for (int rt = 0; rt < 4; ++rt) *(f32x4*)(lds + L_S1 + (rt * 16 + 4 * h4) * 4) = acc[rt];
      }
      asm volatile("" ::: "memory");
      *(volatile LAS unsigned*)(lds + L_S1 + 256) = tag;
      __builtin_amdgcn_s_setprio(0);
    }
    VMW(20);
    BAR();
    STAMP(13);
    f32x2 zz[8];
    f32x4 cs1a, cs1b;
    {
      f32x4 rs4[4], nm4[4];
#pragma unroll
      for (int rt = 0; rt < 4; ++rt) {
        rs4[rt] = *(const LAS f32x4*)(tb4 + (L_T2 - L_MISC) + rt * 64);
        nm4[rt] = *(const LAS f32x4*)(tb4 + (L_T2 - L_MISC) + 256 + rt * 64);
      }
      const f32x2 cs2 = *(const f32x2*)(lds + L_CS1 + (112 + 16 * w + n16) * 8);
      const f32x2 csv = {cs2[0], cs2[0]}, b1v = {cs2[1], cs2[1]};
      float v1[16], v2[16];
#pragma unroll
      for (int i = 0; i < 8; ++i) {
        const int rt = i >> 1;
        const f32x2 a2 = i & 1 ? HI2(acc[rt]) : LO2(acc[rt]), n2 = i & 1 ? HI2(nm4[rt]) : LO2(nm4[rt]), r2 = i & 1 ? HI2(rs4[rt]) : LO2(rs4[rt]);
        zz[i] = gelu2(__builtin_elementwise_fma(__builtin_elementwise_fma(n2, csv, a2), r2, b1v));
        asm volatile("" : "+v"(zz[i]));
        DMA_W(3 + (i >> 2), i & 3);
        const f32x2 sq = zz[i] * zz[i];
        v1[2 * i] = zz[i][0]; v1[2 * i + 1] = zz[i][1]; v2[2 * i] = sq[0]; v2[2 * i + 1] = sq[1];
      }
      const float t1 = tr_sum16(v1, l);
      const float t2 = tr_sum16(v2, l);
      *(f32x2*)(lds + L_Z1S + (w * 64 + (n16 >> 2) * 16 + 4 * h4 + (n16 & 3)) * 8) = (f32x2){t1, t2};
      lda1(0, A[0]);
      cs1a = *(const f32x4*)(lds + L_CS1 + (16 * w + 4 * h4) * 8);
      cs1b = *(const f32x4*)(lds + L_CS1 + (16 * w + 4 * h4) * 8 + 16);
    }
    BAR();
    STAMP(14);
#pragma unroll
    for (int ks = 0; ks < 20; ++ks) {
      if (ks == Z1SPLIT) {
        STAMP(15);
        BAR();
        STAMP(16);
        f32x4 ra[4], rb[4];
#pragma unroll
        for (int rt = 0; rt < 4; ++rt) {
          ra[rt] = *(const LAS f32x4*)(tb4 + (L_ST2 - L_MISC) + rt * 64);
          rb[rt] = *(const LAS f32x4*)(tb4 + (L_ST2 - L_MISC) + 256 + rt * 64);
        }
#pragma unroll
        for (int i = 0; i < 8; ++i) {
          zz[i] = __builtin_elementwise_fma(zz[i], i & 1 ? HI2(ra[i >> 1]) : LO2(ra[i >> 1]), i & 1 ? HI2(rb[i >> 1]) : LO2(rb[i >> 1]));
          asm volatile("" : "+v"(zz[i]));
        }
        const u32x4 o0 = {pack2(zz[0][0], zz[2][0]), pack2(zz[0][1], zz[2][1]), pack2(zz[1][0], zz[3][0]), pack2(zz[1][1], zz[3][1])};
        const u32x4 o1 = {pack2(zz[4][0], zz[6][0]), pack2(zz[4][1], zz[6][1]), pack2(zz[5][0], zz[7][0]), pack2(zz[5][1], zz[7][1])};
        const unsigned zo = WS_Z2F + (unsigned)((c * 7 + w) * 64 + 4 * (R >> 1) + h4) * 1024u + (unsigned)(R & 1) * 512u + (unsigned)n16 * 16u;
        st16(rs, zo, o0);
        st16(rs, zo + 256u, o1);
        SCHED();
      }
      if (ks == 0) { VMW(4); RD_W(3); }
      if (ks == 2) { VMW(0); RD_W(4); }
      if (ks == 8) { VMW(4 + 2); RD_W(5); }
      if (ks == 11) rd_xp();
      if (ks == 13) { VMW(4 + 5 + 2); RD_W(6); }
      if (ks >= 13 && ks < 19) DMA_AB(ks - 13);
      if (ks >= 2 && ks < 6) { DMA_W(5 + ((ks - 2) >> 1), 2 * (ks & 1)); DMA_W(5 + ((ks - 2) >> 1), 2 * (ks & 1) + 1); }
      if (ks >= 6 && ks < 9) LDCR(ks - 6);
      if (ks == 12) { LDCR(3); LDCR(4); }
      if (ks == 10 || ks == 11) { DMA_W(7, 2 * (ks & 1)); DMA_W(7, 2 * (ks & 1) + 1); }
      SCHED();
#pragma unroll
      for (int rt = 0; rt < 4; ++rt) {
        if (ks < 19) A[(ks + 1) & 1][rt] = LDA(ks + 1, rt);
        SCHED();
        acc[rt] = MFMA16(B1[ks], A[ks & 1][rt], ks ? acc[rt] : ZERO4);
        SCHED();
      }
    }
    STAMP(18);
    VMW(2 + 2 + 6);
    RD_W(7);
    const f32x2 c01 = {cs1a[0], cs1a[2]}, b01 = {cs1a[1], cs1a[3]}, c23 = {cs1b[0], cs1b[2]}, b23 = {cs1b[1], cs1b[3]};
    u32x2 g1[4];
    auto z1g = [&](int rt) {
      const float r1 = *(const LAS float*)(tbn + (L_T2 - L_MISC) + rt * 64), n1 = *(const LAS float*)(tbn + (L_T2 - L_MISC) + 256 + rt * 64);
      const f32x2 r2 = {r1, r1}, n2 = {n1, n1};
      const f32x2 a = gelu2(__builtin_elementwise_fma(__builtin_elementwise_fma(n2, c01, LO2(acc[rt])), r2, b01));
      const f32x2 b = gelu2(__builtin_elementwise_fma(__builtin_elementwise_fma(n2, c23, HI2(acc[rt])), r2, b23));
      g1[rt] = (u32x2){pack2(a[0], a[1]), pack2(b[0], b[1])};
      asm volatile("" : "+v"(g1[rt]));
    };
    SCHED();
    VMW(2 + 6);
    BAR();
    STAMP(17);
#pragma unroll
    for (int rt = 0; rt < 4; ++rt) {
      if (rt < 2) { DMA_AB(6 + 2 * rt); DMA_AB(7 + 2 * rt); }
      z1g(rt);
      *(u32x2*)(lds + L_Z1S + ((w * 4 + rt) * 64 + l) * 8) = g1[rt];
    }
  } else {
    __builtin_amdgcn_s_setprio(3);
    DRAIN();
    BAR();
    if (is_conv && l == 0) st4(rs, WS_FX + (unsigned)quad * 128u, tag);
    STAMP(9);
    *(volatile LAS unsigned*)(lds + L_S1 + 256) = ~tag;
    u32x4 gv, cv[2][2][4];
    const unsigned gvo = WS_MOD + (unsigned)(5 * D_ + 80 * c) * 8u + (l < 40 ? l16 : 0u);
    auto ld_consts = [&]() {
      gv = ld16(rs, gvo);
#pragma unroll
      for (int i = 0; i < 16; ++i) cv[i >> 3][(i >> 2) & 1][i & 3] = ld16(rs, l < 56 ? l16 : 0u, WS_CS + (unsigned)(((c + 8 * (i >> 3)) * 4 + (i & 3)) * 112 + 56 * ((i >> 2) & 1)) * 16u);
    };
    auto ok_consts = [&]() {
      unsigned x = (gv[0] ^ tag) | (gv[2] ^ tag);
#pragma unroll
      for (int i = 0; i < 16; ++i) x |= (cv[i >> 3][(i >> 2) & 1][i & 3][0] ^ tag) | (cv[i >> 3][(i >> 2) & 1][i & 3][2] ^ tag);
      asm volatile("" : "+v"(x));
      return __all(x == 0u);
    };
    {
#pragma unroll
      for (int k = 0; k < 3; ++k) cpf ^= ld4(rs, k * 64 + l < 140 ? (unsigned)l * 128u : OOB, WS_WINF + (unsigned)c * WBLK + (unsigned)(idx & 7) * 17920u + (unsigned)k * 8192u);
    }
    park();
#pragma unroll
    for (int q = 0; q < 4; ++q) DMA_W(2, q);
    __builtin_amdgcn_s_barrier();
    ld_consts();
    STAMP(31);
    _Pragma("unroll 1") for (unsigned spins = 0; spins < SPIN_CAP; ++spins) {
      const unsigned f0 = ld4(rs, WS_FX + (unsigned)l * 128u), f1 = ld4(rs, WS_FX + 8192u + (unsigned)l * 128u),
                     f2 = ld4(rs, l < 32 ? WS_FX + 16384u + (unsigned)l * 128u : WS_FW1 + (unsigned)(c * 32 + (l < 60 ? l - 32 : 0)) * 128u);
      if (__all(f0 == tag && f1 == tag && f2 == tag)) break;
      __builtin_amdgcn_s_sleep(POLL_SLEEP);
    }
    BAR();
    STAMP(10);
    ld_x1();
    __builtin_amdgcn_s_setprio(3);
    if (!ok_consts()) {
      _Pragma("unroll 1") for (unsigned spins = 0; spins < SPIN_CAP; ++spins) {
        ld_consts();
        if (ok_consts()) break;
        __builtin_amdgcn_s_sleep(8);
      }
    }
    if (l < 40) *(f32x2*)(lds + L_GB + l * 8) = (f32x2){__uint_as_float(gv[1]), __uint_as_float(gv[3])};
    if (l < 56) {
#pragma unroll
      for (int i = 0; i < 4; ++i) {
        const u32x4* q4 = cv[i >> 1][i & 1];
        *(f32x2*)(lds + L_CS1 + (56 * i + l) * 8) =
            (f32x2){(__uint_as_float(q4[0][1]) + __uint_as_float(q4[1][1])) + (__uint_as_float(q4[2][1]) + __uint_as_float(q4[3][1])),
                    (__uint_as_float(q4[0][3]) + __uint_as_float(q4[1][3])) + (__uint_as_float(q4[2][3]) + __uint_as_float(q4[3][3]))};
      }
    }
    SCHED();
#pragma unroll
    for (int m = 0; m < 3; ++m) put_x1(m);
    BAR();
    f32x4 acc[4];
    u32x4 A[2][4];
    lda1(0, A[0]);
    auto touch_w = [&](int k) {
      cpf ^= __builtin_amdgcn_raw_buffer_load_b32(rsp, (unsigned)(h4 * 4096 + (n16 >> 1) * 512 + (n16 & 1) * 128),
                                                  (unsigned)(R * 64 + (c & 3) * 16 + k * 4) * 4096u + 256u, 0);
    };
#pragma unroll
    for (int ks = 0; ks < 20; ++ks) {
      if (ks == 6) put_x1(3);
      if (ks == 8) put_x1(4);
      if (ks == 11) BAR();
      if (ks == 4) RD_W(2);
      if (ks >= DK && ks < DK + 8) DMA_W(3 + ((ks - DK) >> 2), (ks - DK) & 3);
      if (!ET && ks >= TK && ks < TK + 4) touch_w(ks - TK);
      if (ks < 19) lda1(ks + 1, A[(ks + 1) & 1]);
      SCHED();
#pragma unroll
      for (int rt = 0; rt < 4; ++rt) acc[rt] = MFMA16(A[ks & 1][rt], A[ks & 1][rt], ks ? acc[rt] : ZERO4);
      SCHED();
    }
    {
      if ((n16 >> 2) == h4) {
#pragma unroll
        for (int rt = 0; rt < 4; ++rt) {
          const float lo = (l & 1) ? acc[rt][1] : acc[rt][0], hi = (l & 1) ? acc[rt][3] : acc[rt][2];
          *(volatile LAS float*)(tbn + (L_T2 - L_MISC) + rt * 64) = (l & 2) ? hi : lo;
        }
      }
      const float s2 = *(volatile LAS float*)(lds + L_T2 + l * 4) * (1.0f / D_);
      float s1;
      _Pragma("unroll 1") for (unsigned spins = 0; spins < SPIN_CAP * 16; ++spins) {
        const unsigned f = *(volatile LAS unsigned*)(lds + L_S1 + 256);
        s1 = *(volatile LAS float*)(lds + L_S1 + l * 4);
        if (f == tag) break;
      }
      const float nm = s1 * (-1.0f / D_);
      *(float*)(lds + L_T2 + l * 4) = __builtin_amdgcn_rsqf(__builtin_fmaxf(s2 - nm * nm, 0.f) + 1e-5f);
      *(float*)(lds + L_T2 + 256 + l * 4) = nm;
    }
    if (TK == 20) { touch_w(0); touch_w(1); touch_w(2); touch_w(3); }
    BAR();
    if (TK == 21) { touch_w(0); touch_w(1); touch_w(2); touch_w(3); }
    __builtin_amdgcn_s_setprio(0);
    asm volatile("" ::"v"(cpf));
    DRAIN();
    RD_W(3); RD_W(4);
    rd_xp();
    asm volatile("s_waitcnt lgkmcnt(0)" ::: "memory");
#pragma unroll
    for (int i = 0; i < 8; ++i) DMA_W(5 + (i >> 2), i & 3);
    const unsigned e_fw2 = ld4(rs, WS_FW2 + (unsigned)(c * 8 + (l & 7)) * 128u);
    if (!__all(e_fw2 == tag)) wait_flag<2>(rs, WS_FW2 + (unsigned)(c * 8 + (l & 7)) * 128u, tag);
    __builtin_amdgcn_s_setprio(3);
    BAR();
    {
      f32x2 t = {0.f, 0.f};
#pragma unroll
      for (int k = 0; k < 7; ++k) t += *(const f32x2*)(lds + L_Z1S + (k * 64 + l) * 8);
      st16(rs, WS_PART + (unsigned)(R * 8 + c) * 1024u + l16, (u32x4){tag, __float_as_uint(t[0]), tag, __float_as_uint(t[1])});
    }
    STAMP(14);
    {
      u32x4 v[8];
      const unsigned po = WS_PART + (unsigned)R * 8192u + l16;
      __builtin_amdgcn_s_sleep(16);
      _Pragma("unroll 1") for (unsigned spins = 0; spins < SPIN_CAP; ++spins) {
#pragma unroll
        for (int k = 0; k < 8; ++k) v[k] = ld16(rs, po + (unsigned)k * 1024u);
        unsigned x = 0;
#pragma unroll
        for (int k = 0; k < 8; ++k) x |= (v[k][0] ^ tag) | (v[k][2] ^ tag);
        asm volatile("" : "+v"(x));
        if (__all(x == 0u)) break;
        __builtin_amdgcn_s_sleep(1);
      }
      float t1 = 0.f, t2 = 0.f;
#pragma unroll
      for (int k = 0; k < 8; ++k) { t1 += __uint_as_float(v[k][1]); t2 += __uint_as_float(v[k][3]); }
      const float mean = t1 * (1.0f / FH);
      const float var = __builtin_fmaxf(t2 * (1.0f / FH) - mean * mean, 0.f);
      const float rstd = __builtin_amdgcn_rsqf(var + 1e-5f);
      *(float*)(lds + L_ST2 + l * 4) = rstd;
      *(float*)(lds + L_ST2 + 256 + l * 4) = -mean * rstd;
    }
    STAMP(15);
    BAR();
    RD_W(5); RD_W(6);
    asm volatile("s_waitcnt lgkmcnt(0)" ::: "memory");
#pragma unroll
    for (int q = 0; q < 4; ++q) DMA_W(7, q);
    DRAIN();
    RD_W(7);
    BAR();
    if (l == 0) st4(rs, WS_FZ + (unsigned)(c * 32 + R) * 128u, tag);
    STAMP(17);
    __builtin_amdgcn_s_setprio(0);
#pragma unroll
    for (int k = 0; k < 10; ++k) DMA_AB(k);
#pragma unroll
    for (int k = 0; k < 5; ++k) LDCR(k);
  }
  const rsrc_t rout = __builtin_amdgcn_make_buffer_rsrc((void*)p.out, 0, S_ * D_ * 2, 0x00020000);
  STAMP(19);
  u32x2 z1o[4];
  wait_flag<POLL_SLEEP>(rs, fzo, tag);
#pragma unroll
  for (int i = 0; i < W; ++i) { LDZ(i); SCHED(); }
  STAMP(20);

  const unsigned ub = WS_UF + (unsigned)((R * 4 + rto) * 8 + c) * 3584u;
  const float bs = bflo(bs_raw);
  const unsigned ur = WS_UF + (unsigned)((R * 4 + rto) * 8 + cls) * 3584u;
  u32x4 uf[4][2];
  f32x4 acc[2][4];
  f32x4* const bufw = (f32x4*)(lds + L_BUF) + w * 256 + l;
#define BUFW(slot, rt) bufw[(slot) * 2048 + (rt) * 64]
  {
    const f32x4* const rb = (const f32x4*)(lds + L_BUF) + cls * 2048 + rto * 64 + l;
    f32x4 rdv[4], s;
    u32x4 uo;
#pragma unroll
    for (int j = 0; j < 7; ++j) {
#pragma unroll
      for (int ks = 0; ks < 8; ++ks) {
        const int i = 8 * j + ks;
#pragma unroll
        for (int rt = 0; rt < 4; ++rt) acc[j & 1][rt] = MFMA16(fr[i % W], wsp[rt][ks], ks ? acc[j & 1][rt] : ZERO4);
        if (i == 0) STAMP(21);
        if (i == 24) STAMP(22);
        SCHED();
        if (i + W < 56) LDZ(i + W);
        if (j > 0) {
          if (ks == 0 && (j & 1)) BAR();
          if (j == 1 && ks == 0) {
#pragma unroll
            for (int k = 0; k < 4; ++k) z1o[k] = *(const u32x2*)(lds + L_Z1S + (((k < 3 ? 2 * k + cls : 6) * 4 + rto) * 64 + l) * 8);
          }
          if (i >= (56 - G2W) && i < 53) {
#pragma unroll
            for (int k = 0; k < 8; ++k)
              if ((56 - G2W) + k * (53 - (56 - G2W)) / 8 == i) uf[k >> 1][k & 1] = ld16(rs, l16, ur + (unsigned)(k >> 1) * 7168u + (unsigned)(k & 1) * 1024u);
            SCHED();
          }
          const bool fast = j == 6;
          if (!fast && ks < 4) BUFW((j - 1) & 1, ks) = acc[(j - 1) & 1][ks];
          if (fast && ks < 2) { BUFW(1, 2 * ks) = acc[1][2 * ks]; BUFW(1, 2 * ks + 1) = acc[1][2 * ks + 1]; }
          if (!(j & 1)) {
            const int k0 = fast ? 2 : 5;
            if (ks == k0) {
              BAR();
#pragma unroll
              for (int k = 0; k < 4; ++k) rdv[k] = rb[k * 256];
            }
            if (ks == k0 + 1) {
              s = (rdv[0] + rdv[1]) + (rdv[2] + rdv[3]);
#pragma unroll
              for (int k = 0; k < 4; ++k) rdv[k] = rb[(4 + k) * 256];
            }
            if (ks == k0 + 2) {
              s += (rdv[0] + rdv[1]) + (rdv[2] + rdv[3]);
              const u32x2 z = z1o[(j >> 1) - 1];
              const int e = j == 4 ? 2 : 0;
              uo[e] = pack2(bflo(z[0]) * (s[0] + bs), bfhi(z[0]) * (s[1] + bs));
              uo[e + 1] = pack2(bflo(z[1]) * (s[2] + bs), bfhi(z[1]) * (s[3] + bs));
              if (j == 4) st16(rs, ub + (unsigned)cls * 1024u + l16, uo);
              if (j == 6) st16(rs, l < 32 ? ub + 2048u + (unsigned)cls * 512u + l16 : OOB, half_swap((u32x4){uo[0], uo[1], uo[0], uo[1]}));
            }
            if (j == 6 && ks == 4) BAR();
            if (j == 6 && ks >= 4 && ks < 7) {
#pragma unroll
              for (int k = 0; k < 5; ++k)
                if (k / 2 == ks - 4) *(u32x4*)(lds + (w < 6 ? L_BUF + 32768 + w * 5120 : L_Z1S + (w - 6) * 5120) + k * 1024 + l16) = cr[k];
            }
          }
        }
        SCHED();
      }
    }
  }
  STAMP(23);

  {
    u32x4 uc[4], uh[4];
    const unsigned uho = (unsigned)(l >> 5) * 3584u + (unsigned)(l & 31) * 16u;
#define LD_C(cc) uc[cc] = ld16(rs, l16, ur + (unsigned)(cc) * 7168u + 2048u)
#define LD_H(cc) uh[cc] = ld16(rs, uho, WS_UF + (unsigned)((R * 4 + rto) * 8) * 3584u + (unsigned)(cc) * 7168u + 3072u)
    auto mx = [](unsigned a, unsigned b) { return __builtin_elementwise_max(a, b); };
    auto ok4 = [&](const u32x4* v, int n) {
      unsigned m = 0;
#pragma unroll
      for (int i = 0; i < 8; ++i)
        if (i < n) m = mx(m, mx(mx(v[i][0], v[i][1]), mx(v[i][2], v[i][3])));
      return __all(m != ~0u);
    };
#pragma unroll
    for (int rt = 0; rt < 4; ++rt) BUFW(0, rt) = acc[0][rt];
    BAR();
    f32x4 a3[5];
    u32x4 af[2][5];
    const LAS char* wb[2] = {(const LAS char*)(lds + L_AB) + cls * (10240 + 3072) + l16, (const LAS char*)(lds + L_AB) + cls * (10240 - 2048) + l16};
    const LAS char* wb2[2] = {wb[0] + 40960, wb[1] + 40960};
    asm volatile("" : "+v"(wb[0]), "+v"(wb2[0]), "+v"(wb[1]), "+v"(wb2[1]));
    u32x4 ho = {0u, 0u, 0u, 0u};
    if (w < 4) {
      __builtin_amdgcn_s_setprio(2);
      const f32x4* const rb = (const f32x4*)(lds + L_BUF) + rto * 64 + l;
      f32x4 r8[8];
#pragma unroll
      for (int k = 0; k < 8; ++k) r8[k] = rb[k * 256];
      const f32x4 s = ((r8[0] + r8[1]) + (r8[2] + r8[3])) + ((r8[4] + r8[5]) + (r8[6] + r8[7]));
      const u32x2 z = z1o[3];
      const unsigned o0 = pack2(bflo(z[0]) * (s[0] + bs), bfhi(z[0]) * (s[1] + bs)), o1 = pack2(bflo(z[1]) * (s[2] + bs), bfhi(z[1]) * (s[3] + bs));
      ho = half_swap((u32x4){o0, o1, o0, o1});
    }
    st16(rs, tid < 256 && l < 32 ? ub + 3072u + l16 : OOB, ho);
    SCHED();
    __builtin_amdgcn_s_setprio(0);
#pragma unroll
    for (int dj = 0; dj < 5; ++dj) af[0][dj] = *(const LAS u32x4*)(wb[dj >= 2] + dj * 1024);
    const bool ab_ok = ok4(&uf[0][0], 8);
    STAMP(24);
    if (!ab_ok) {
      _Pragma("unroll 1") for (unsigned spins = 0; spins < SPIN_CAP; ++spins) {
#pragma unroll
        for (int i = 0; i < 8; ++i) uf[i >> 1][i & 1] = ld16(rs, l16, ur + (unsigned)(i >> 1) * 7168u + (unsigned)(i & 1) * 1024u);
        if (ok4(&uf[0][0], 8)) break;
        __builtin_amdgcn_s_sleep(1);
      }
    }
    STAMP(25);
    const LAS char* cb0[2] = {(const LAS char*)(lds + L_BUF + 32768) + cls * (5120 + 3072) + l16, (const LAS char*)(lds + L_BUF + 32768) + cls * (5120 - 2048) + l16};
    u32x4 ah[4][3];
    const LAS char* hb = (const LAS char*)(lds + L_BUF) + cls * 12288 + l * 8;
    f32x4 gb[3], xo3[3], xf[3];
    LAS char *otw, *xw3;
    const LAS char *ota, *otb, *xr3;
    unsigned oa_o, ob_o;
#pragma unroll
    for (int st = 0; st < 12; ++st) {
      if (st < 2) { LD_C(2 * st); LD_C(2 * st + 1); }
      else if (st < 7 && w < 4) dma1k(rs, lds + L_BUF + ((st - 2) * 4 + w) * 1024, l16, wob + 122880u + (unsigned)(w * 5 + st - 2) * 1024u);
      if (st == 2) {
        LAS char* ot = (LAS char*)(lds + L_AB + 32768) + w * 2048;
        otw = ot + n16 * 112 + h4 * 8;
        ota = ot + (l >> 2) * 112 + (l & 3) * 16;
        otb = ot + ((l >> 1) & 15) * 112 + 64 + (l & 1) * 16;
        asm volatile("" : "+v"(otw), "+v"(ota), "+v"(otb));
      }
      if (st == 3) {
        oa_o = (unsigned)(l >> 2) * 2560u + (unsigned)(l & 3) * 16u;
        ob_o = l < 32 ? (unsigned)(l >> 1) * 2560u + 64u + (unsigned)(l & 1) * 16u : OOB;
        xw3 = (LAS char*)(lds + L_AB) + w * 3072 + l16;
        xr3 = (const LAS char*)(lds + L_AB) + (w ^ 4) * 3072 + cls * 1024 + l16;
        asm volatile("" : "+v"(oa_o), "+v"(ob_o), "+v"(xw3), "+v"(xr3));
      }
      if (st >= 4 && st < 7) {
        const int j = st - 4;
        gb[j] = *(const f32x4*)(lds + L_GB + (48 * cls + 16 * (cls && j == 2 ? 1 : j) + 4 * h4) * 4);
        xf[j] = (f32x4){bflo(xp[j][0]), bfhi(xp[j][0]), bflo(xp[j][1]), bfhi(xp[j][1])};
        asm volatile("" : "+v"(xf[j]));
      }
      if (st == 8) {
        STAMP(26);
        if (!ok4(uc, 4)) {
          _Pragma("unroll 1") for (unsigned spins = 0; spins < SPIN_CAP; ++spins) {
            LD_C(0); LD_C(1); LD_C(2); LD_C(3);
            if (ok4(uc, 4)) break;
            __builtin_amdgcn_s_sleep(1);
          }
        }
        LD_H(0); LD_H(1); LD_H(2); LD_H(3);
#pragma unroll
        for (int cc = 0; cc < 4; ++cc) uc[cc] = half_swap(uc[cc]);
        VMW(4);
        __builtin_amdgcn_s_barrier();
        asm volatile("" ::: "memory");
        STAMP(27);
      }
      SCHED();
#pragma unroll
      for (int dj = 0; dj < 5; ++dj) {
        const int sn = st + 1;
        if (sn < 8) af[sn & 1][dj] = *(const LAS u32x4*)((sn < 4 ? wb : wb2)[dj >= 2] + ((sn >> 1) & 1) * 20480 + ((sn & 1) * 5 + dj) * 1024);
        else if (sn < 12) af[sn & 1][dj] = *(const LAS u32x4*)(cb0[dj >= 2] + (sn < 11 ? (sn - 8) * 10240 : L_Z1S - L_BUF - 32768) + dj * 1024);
        if (st >= 8) {
#pragma unroll
          for (int k = 0; k < 2; ++k) {
            const int h = st < 11 ? (k ? 99 : (st - 8) * 5 + dj) : 15 + 2 * dj + k;
            if (h < 24) {
              const u32x2 v = *(const volatile LAS u32x2*)(hb + (((h >> 1) % 3) * 4 + (h >> 1) / 3) * 1024 + (h & 1) * 512);
              ah[(h >> 1) / 3][(h >> 1) % 3][2 * (h & 1)] = v[0]; ah[(h >> 1) / 3][(h >> 1) % 3][2 * (h & 1) + 1] = v[1];
            }
          }
        }
        SCHED();
        a3[dj] = MFMA16(af[st & 1][dj], st < 8 ? uf[(st >> 1) & 3][st & 1] : uc[st & 3], st ? a3[dj] : ZERO4);
        SCHED();
      }
    }
#pragma unroll
    for (int j = 0; j < 3; ++j) *(volatile LAS f32x4*)(xw3 + j * 1024) = a3[2 + j];
    BAR();
#pragma unroll
    for (int j = 0; j < 3; ++j) xo3[j] = *(const volatile LAS f32x4*)(xr3 + j * 1024);
    SCHED();
    if (!ok4(uh, 4)) {
      _Pragma("unroll 1") for (unsigned spins = 0; spins < SPIN_CAP; ++spins) {
        LD_H(0); LD_H(1); LD_H(2); LD_H(3);
        if (ok4(uh, 4)) break;
        __builtin_amdgcn_s_sleep(1);
      }
    }
#pragma unroll
    for (int cc = 0; cc < 4; ++cc) {
      const u32x4 b = half_swap(uh[cc]);
#pragma unroll
      for (int j = 0; j < 3; ++j) a3[j] = MFMA16(ah[cc][j], b, a3[j]);
      if (cc >= 1) {
        const int j = cc - 1;
        xo3[j] = __builtin_elementwise_fma(gb[j], xo3[j], xf[j]);
        asm volatile("" : "+v"(xo3[j]));
      }
      SCHED();
    }
#pragma unroll
    for (int j = 0; j < 3; ++j) {
      const f32x4 r = __builtin_elementwise_fma(gb[j], a3[j], xo3[j]);
      *(volatile LAS u32x2*)(otw + j * 32) = (u32x2){pack2(r[0], r[1]), pack2(r[2], r[3])};
    }
    const u32x4 oa = *(const volatile LAS u32x4*)(ota);
    const u32x4 ob = *(const volatile LAS u32x4*)(otb);
    const unsigned obase = (unsigned)((R * 64 + 32 * ((w >> 1) & 1) + (w & 1)) * D_ + 80 * c + 48 * cls) * 2u;
    __builtin_amdgcn_raw_buffer_store_b128(oa, rout, oa_o, obase, OAUX);
    if (cls == 0) __builtin_amdgcn_raw_buffer_store_b128(ob, rout, ob_o, obase, OAUX);
    if (bid == 0 && tid == 0) st4(rs, WS_EPOCH, tag);
  }
  STAMP(30);
  asm volatile(".Lmega_end:");
}

extern "C" void kernel_launch(void* const* d_in, const int* in_sizes, int n_in, void* d_out, int out_size,
                              void* d_ws, size_t ws_size, hipStream_t stream) {
  Params p;
  p.q = (const u16*)d_in[0];
  p.k = (const u16*)d_in[1];
  p.v = (const u16*)d_in[2];
  p.shift_k = (const u16*)d_in[3];
  p.ssm_k = (const u16*)d_in[4];
  p.ssm_D = (const u16*)d_in[5];
  p.w_in = (const u16*)d_in[7];
  p.w_sp = (const u16*)d_in[8];
  p.b_sp = (const u16*)d_in[9];
  p.w_out = (const u16*)d_in[10];
  p.cvec = (const u16*)d_in[11];
  p.w_mod = (const u16*)d_in[12];
  p.out = (u16*)d_out;
  p.ws = (char*)d_ws;
  mega<<<256, 512, 0, stream>>>(p);
}
```

```cpp
#include <hip/hip_runtime.h>
#include <stdint.h>

typedef unsigned short u16;
typedef __attribute__((ext_vector_type(8))) short bf16x8;
typedef __attribute__((ext_vector_type(4))) float f32x4;
typedef __attribute__((ext_vector_type(2))) float f32x2;
typedef __attribute__((ext_vector_type(4))) unsigned int u32x4;
typedef __attribute__((ext_vector_type(2))) unsigned int u32x2;
#define LAS __attribute__((address_space(3)))
#define CAS __attribute__((address_space(4)))
#define GAS __attribute__((address_space(1)))
typedef __amdgpu_buffer_rsrc_t rsrc_t;
typedef __attribute__((ext_vector_type(4))) short s16x4;

constexpr int S_ = 2048, D_ = 640, F_ = 1728, FH = 864, CB = 108;

#define X1PAD 256
constexpr unsigned WS_EPOCH = 0;
constexpr unsigned WS_FX = 4096;
constexpr unsigned WS_FZ = WS_FX + 24576;
constexpr unsigned WS_FW2 = WS_FZ + 32768;
constexpr unsigned WS_FW1 = WS_FW2 + 8192;
constexpr unsigned WS_CS = WS_FW1 + 65536;
constexpr unsigned WS_PART = WS_CS + 131072;
constexpr unsigned WS_MOD = WS_PART + 262144;
constexpr unsigned WS_ST1 = WS_MOD + 32768;
constexpr unsigned WS_X1B = WS_ST1 + 16384;
constexpr unsigned WX1P = 40960 + X1PAD;
constexpr unsigned WBLK = 143360;
constexpr unsigned WS_QB = WS_X1B + 2686976;
constexpr unsigned WS_WINF = WS_QB + 2686976;
constexpr unsigned WS_WOUTF = WS_WINF + 16 * WBLK;
constexpr unsigned WS_Z2F = WS_WOUTF + 8 * WBLK;
constexpr unsigned WS_UF = WS_Z2F + 8 * 64 * 7168;
constexpr unsigned WS_KVT = WS_UF + 32 * 4 * 8 * 3584;
constexpr unsigned WS_FK = WS_KVT + 32 * 81920;
constexpr unsigned WS_STAMP = WS_FK + 20480;
constexpr unsigned WS_END = WS_STAMP + 256 * 40 * 8;

constexpr int U_BYTES = (256 + S_) * 2;
constexpr int G_COPY = 4160;
constexpr int CH_BYTES = U_BYTES + 4 * G_COPY;
constexpr int L_YP = 4 * CH_BYTES;
constexpr int L_A0 = 98304;
constexpr int L_RED = 0;
constexpr int L_TAB = 2048;
constexpr int L_J3 = 8192;
constexpr int L_QT = 49152;
constexpr int L_J5 = L_QT;
constexpr int L_XI = 0;
constexpr int L_BUF = 81920;
constexpr int L_AB = 0;
constexpr int L_MISC = 147456;
constexpr int L_Z1S = L_MISC;
constexpr int L_S1 = L_MISC + 14336;
constexpr int L_T2 = L_S1 + 512;
constexpr int L_ST2 = L_T2 + 512;
constexpr int L_GB = L_ST2 + 512;
constexpr int L_CS1 = L_Z1S + 4096;
constexpr int L_WS = 81920;
constexpr int LDS_TOTAL = 163840;

constexpr int POLL_SLEEP = 1;
#define ET 0
#define TK 1
#define DK 6
#define PM 1
#define AT 3
#define NRA 12
#define D3E 0
#define P1B 16
#define PK 15
#define KX 1
#define RAWD1 3
#define NR1 12
#define AT_AT 0
#define RAW2 40
#define CDIST 4
#define PREP_AT 123
#define SV_AT 96
#define RAWD 3
#define P_SLEEP 0
#define F_SLEEP 0
#define OAUX 0
#define AUX_SLEEP 36
#define Q_SLEEP 0
#define LATE_SLEEP 110
constexpr int NRAW = 12;
#define NB2A 6
constexpr int G2W = 7;
constexpr int Z1SPLIT = 12;
constexpr int SC1 = 16;
constexpr unsigned SPIN_CAP = 1u << 16;
constexpr unsigned OOB = 0x80000000u;

__device__ __forceinline__ float bflo(unsigned x) { return __uint_as_float(x << 16); }
__device__ __forceinline__ float bfhi(unsigned x) { return __uint_as_float(x & 0xffff0000u); }
__device__ __forceinline__ f32x2 bf2(unsigned x) { return (f32x2){bflo(x), bfhi(x)}; }
__device__ __forceinline__ unsigned f2bf(float x) {
  __bf16 b = (__bf16)x;
  return (unsigned)__builtin_bit_cast(unsigned short, b);
}
__device__ __forceinline__ float rbf(float x) { return bflo(f2bf(x)); }
typedef __attribute__((ext_vector_type(2))) __bf16 bf2_t;
__device__ __forceinline__ unsigned pack2(float lo, float hi) {
  f32x2 v = {lo, hi};
  return __builtin_bit_cast(unsigned, __builtin_convertvector(v, bf2_t));
}
template <int CTRL>
__device__ __forceinline__ float dpp_f(float v) {
  return __int_as_float(__builtin_amdgcn_update_dpp(0, __float_as_int(v), CTRL, 0xf, 0xf, true));
}
__device__ __forceinline__ float sum_rows(float v) {
  auto a = __builtin_amdgcn_permlane16_swap(__float_as_uint(v), __float_as_uint(v), false, false);
  v = __uint_as_float(a[0]) + __uint_as_float(a[1]);
  auto b = __builtin_amdgcn_permlane32_swap(__float_as_uint(v), __float_as_uint(v), false, false);
  return __uint_as_float(b[0]) + __uint_as_float(b[1]);
}
__device__ __forceinline__ f32x2 gelu2(f32x2 x) {
  const float c0 = -2.0f * 0.7978845608028654f * 1.4426950408889634f;
  const float c1 = c0 * 0.044715f;
  const f32x2 t = x * __builtin_elementwise_fma(x * x, (f32x2){c1, c1}, (f32x2){c0, c0});
  const f32x2 d = (f32x2){__builtin_amdgcn_exp2f(t[0]), __builtin_amdgcn_exp2f(t[1])} + (f32x2){1.0f, 1.0f};
  return x * (f32x2){__builtin_amdgcn_rcpf(d[0]), __builtin_amdgcn_rcpf(d[1])};
}
#define LO2(v) __builtin_shufflevector(v, v, 0, 1)
#define HI2(v) __builtin_shufflevector(v, v, 2, 3)
__device__ __forceinline__ float tr_sum16(const float* v, int l) {
  float x[8], y[4];
  asm volatile(
      "s_nop 1\n\t"
      "v_add_f32_dpp %0, %8, %8 row_ror:8 row_mask:0xf bank_mask:0x3\n\t"
      "v_add_f32_dpp %1, %9, %9 row_ror:8 row_mask:0xf bank_mask:0x3\n\t"
      "v_add_f32_dpp %2, %10, %10 row_ror:8 row_mask:0xf bank_mask:0x3\n\t"
      "v_add_f32_dpp %3, %11, %11 row_ror:8 row_mask:0xf bank_mask:0x3\n\t"
      "v_add_f32_dpp %4, %12, %12 row_ror:8 row_mask:0xf bank_mask:0x3\n\t"
      "v_add_f32_dpp %5, %13, %13 row_ror:8 row_mask:0xf bank_mask:0x3\n\t"
      "v_add_f32_dpp %6, %14, %14 row_ror:8 row_mask:0xf bank_mask:0x3\n\t"
      "v_add_f32_dpp %7, %15, %15 row_ror:8 row_mask:0xf bank_mask:0x3\n\t"
      "v_add_f32_dpp %0, %16, %16 row_ror:8 row_mask:0xf bank_mask:0xc\n\t"
      "v_add_f32_dpp %1, %17, %17 row_ror:8 row_mask:0xf bank_mask:0xc\n\t"
      "v_add_f32_dpp %2, %18, %18 row_ror:8 row_mask:0xf bank_mask:0xc\n\t"
      "v_add_f32_dpp %3, %19, %19 row_ror:8 row_mask:0xf bank_mask:0xc\n\t"
      "v_add_f32_dpp %4, %20, %20 row_ror:8 row_mask:0xf bank_mask:0xc\n\t"
      "v_add_f32_dpp %5, %21, %21 row_ror:8 row_mask:0xf bank_mask:0xc\n\t"
      "v_add_f32_dpp %6, %22, %22 row_ror:8 row_mask:0xf bank_mask:0xc\n\t"
      "v_add_f32_dpp %7, %23, %23 row_ror:8 row_mask:0xf bank_mask:0xc"
      : "=&v"(x[0]), "=&v"(x[1]), "=&v"(x[2]), "=&v"(x[3]), "=&v"(x[4]), "=&v"(x[5]), "=&v"(x[6]), "=&v"(x[7])
      : "v"(v[0]), "v"(v[1]), "v"(v[2]), "v"(v[3]), "v"(v[4]), "v"(v[5]), "v"(v[6]), "v"(v[7]), "v"(v[8]), "v"(v[9]), "v"(v[10]),
        "v"(v[11]), "v"(v[12]), "v"(v[13]), "v"(v[14]), "v"(v[15]));
  asm volatile(
      "s_nop 1\n\t"
      "v_add_f32_dpp %0, %4, %4 row_shl:4 row_mask:0xf bank_mask:0x5\n\t"
      "v_add_f32_dpp %1, %5, %5 row_shl:4 row_mask:0xf bank_mask:0x5\n\t"
      "v_add_f32_dpp %2, %6, %6 row_shl:4 row_mask:0xf bank_mask:0x5\n\t"
      "v_add_f32_dpp %3, %7, %7 row_shl:4 row_mask:0xf bank_mask:0x5\n\t"
      "v_add_f32_dpp %0, %8, %8 row_shr:4 row_mask:0xf bank_mask:0xa\n\t"
      "v_add_f32_dpp %1, %9, %9 row_shr:4 row_mask:0xf bank_mask:0xa\n\t"
      "v_add_f32_dpp %2, %10, %10 row_shr:4 row_mask:0xf bank_mask:0xa\n\t"
      "v_add_f32_dpp %3, %11, %11 row_shr:4 row_mask:0xf bank_mask:0xa\n\t"
      "s_nop 1"
      : "=&v"(y[0]), "=&v"(y[1]), "=&v"(y[2]), "=&v"(y[3])
      : "v"(x[0]), "v"(x[1]), "v"(x[2]), "v"(x[3]), "v"(x[4]), "v"(x[5]), "v"(x[6]), "v"(x[7]));
  const bool b1 = l & 2, b0 = l & 1;
  const float z0 = (b1 ? y[2] : y[0]) + dpp_f<0x4E>(b1 ? y[0] : y[2]);
  const float z1 = (b1 ? y[3] : y[1]) + dpp_f<0x4E>(b1 ? y[1] : y[3]);
  return (b0 ? z1 : z0) + dpp_f<0xB1>(b0 ? z0 : z1);
}
__device__ __forceinline__ u32x4 half_swap(u32x4 v) {
  const auto a = __builtin_amdgcn_permlane32_swap(v[0], v[2], false, false);
  const auto b = __builtin_amdgcn_permlane32_swap(v[1], v[3], false, false);
  return (u32x4){a[0], b[0], a[1], b[1]};
}
#define MFMA16(a, b, c) __builtin_amdgcn_mfma_f32_16x16x32_bf16(__builtin_bit_cast(bf16x8, a), __builtin_bit_cast(bf16x8, b), c, 0, 0, 0)
__device__ __forceinline__ u32x2 trd(const char* p) {
  return __builtin_bit_cast(u32x2, __builtin_amdgcn_ds_read_tr16_b64_v4i16((LAS s16x4*)p));
}
__device__ __forceinline__ u32x4 trfrag(const char* p, int second) {
  u32x2 a = trd(p), b = trd(p + second);
  return (u32x4){a[0], a[1], b[0], b[1]};
}
#define BAR() asm volatile("s_waitcnt lgkmcnt(0)\n\ts_barrier" ::: "memory")
#define DRAIN() do { __builtin_amdgcn_s_waitcnt(0x0F70); asm volatile("" ::: "memory"); } while (0)
#define VMW(n) do { __builtin_amdgcn_s_waitcnt(0x0F70 | ((n) & 15) | (((n) >> 4) << 14)); asm volatile("" ::: "memory"); } while (0)
#define SCHED() __builtin_amdgcn_sched_barrier(0)
#define ZERO4 ((f32x4){0.f, 0.f, 0.f, 0.f})

__device__ __forceinline__ u32x4 ld16(rsrc_t r, unsigned off, unsigned so = 0) { return __builtin_amdgcn_raw_buffer_load_b128(r, off, so, SC1); }
__device__ __forceinline__ u32x2 ld8(rsrc_t r, unsigned off, unsigned so = 0) { return __builtin_amdgcn_raw_buffer_load_b64(r, off, so, SC1); }
__device__ __forceinline__ unsigned ld4(rsrc_t r, unsigned off, unsigned so = 0) { return __builtin_amdgcn_raw_buffer_load_b32(r, off, so, SC1); }
__device__ __forceinline__ void st16(rsrc_t r, unsigned off, u32x4 v) { __builtin_amdgcn_raw_buffer_store_b128(v, r, off, 0, SC1); }
__device__ __forceinline__ void st8(rsrc_t r, unsigned off, u32x2 v) { __builtin_amdgcn_raw_buffer_store_b64(v, r, off, 0, SC1); }
__device__ __forceinline__ void st4(rsrc_t r, unsigned off, unsigned v) { __builtin_amdgcn_raw_buffer_store_b32(v, r, off, 0, SC1); }
__device__ __forceinline__ void dma1k(rsrc_t r, char* lds_dst, unsigned l16, unsigned soff) {
  __builtin_amdgcn_raw_ptr_buffer_load_lds(r, (LAS void*)lds_dst, 16, l16, soff, 0, SC1);
}

#define STAMP(i)

template <int SLEEP>
__device__ __forceinline__ void wait_flag(rsrc_t r, unsigned off, unsigned tag) {
  _Pragma("unroll 1") for (unsigned spins = 0; spins < SPIN_CAP; ++spins) {
    if (__all(ld4(r, off) == tag)) break;
    __builtin_amdgcn_s_sleep(SLEEP);
  }
  asm volatile("" ::: "memory");
}

struct ConvSched {
  signed char pp[144], m[144], rd[144];
  int pre;
};
constexpr ConvSched conv_sched(int dist) {
  ConvSched s = {};
  int need[96] = {}, i = 0;
  for (int r = 0; r < 4; ++r)
    for (int pp = 0; pp < 8; ++pp) {
      const int j = 24 * r + (pp ? 8 + 2 * pp : 0);
      need[j] = need[j + 1] = i;
      for (int m = 7 - pp; m >= 0; --m) {
        if (pp == 0) need[24 * r + 9 - m] = i;
        s.pp[i] = pp; s.m[i] = m; s.rd[i] = -1;
        ++i;
      }
    }
  int next = 144;
  for (int j = 95; j >= 0; --j) {
    next = need[j] - dist < next - 1 ? need[j] - dist : next - 1;
    if (next >= 0) s.rd[next] = j;
    else if (s.pre == 0) s.pre = j + 1;
  }
  return s;
}

template <int I> struct IC { static constexpr int v = I; };
template <int I0, int I1, class F>
__device__ __forceinline__ void static_for(F&& f) {
  if constexpr (I0 < I1) { f(IC<I0>{}); static_for<I0 + 1, I1>(f); }
}

struct Params {
  const u16 *q, *k, *v, *shift_k, *ssm_k, *ssm_D, *w_in, *w_sp, *b_sp, *w_out, *cvec, *w_mod;
  u16* out;
  char* ws;
};

__global__ __launch_bounds__(512, 2) void mega(Params p) {
  __shared__ __attribute__((aligned(16))) char lds[LDS_TOTAL];
  const int tid = threadIdx.x, l = tid & 63;
  const int w = __builtin_amdgcn_readfirstlane(tid >> 6);
  const int bid = blockIdx.x, xcd = bid & 7, idx = bid >> 3;
  char* const ws = p.ws;
  asm volatile("" ::"s"(p.q), "s"(p.k), "s"(p.v), "s"(p.shift_k), "s"(p.ssm_k), "s"(p.ssm_D), "s"(p.w_in), "s"(p.w_sp), "s"(p.b_sp),
               "s"(p.w_out), "s"(p.cvec), "s"(p.w_mod), "s"(p.out), "s"(p.ws));
  const rsrc_t rs = __builtin_amdgcn_make_buffer_rsrc(ws, 0, WS_END, 0x00020000);
  const unsigned l16 = (unsigned)l * 16u;
  STAMP(0);
  const unsigned epv = ld4(rs, WS_EPOCH);
  const int dg = ((idx & 7) + 2 * (idx >> 3)) & 7;
  const bool is_conv = dg >= 3;
  const int slot = 3 * (idx >> 3) + dg;
  const int cidx = 5 * (idx >> 3) + dg - 3;
  unsigned cpf = 0;
  if (!is_conv) {
    unsigned long long pc;
    unsigned len, len2;
    asm volatile("s_getpc_b64 %0\n.Lmega_here:\n\ts_mov_b32 %1, .Lmega_end-.Lmega_here\n\ts_mov_b32 %2, .Lmega_aux_end-.Lmega_here" : "=s"(pc), "=s"(len), "=s"(len2));
    len = len > len2 ? len : len2;
    unsigned o = (unsigned)((slot * 8 + w) * 1024 + (l & 7) * 128);
    o = o + 16u <= len ? o : 0u;
    cpf = *(const GAS unsigned*)((pc & ~15ull) + o);
  }

  const int quad = xcd * 20 + cidx;
  const int R = (xcd & 3) * 8 + (idx & 7);
  const int c = (xcd >> 2) * 4 + (idx >> 3);
  const int n16 = l & 15, h4 = l >> 4;
  const rsrc_t rq = __builtin_amdgcn_make_buffer_rsrc((void*)p.q, 0, S_ * D_ * 2, 0x00020000);
  const rsrc_t rsp = __builtin_amdgcn_make_buffer_rsrc((void*)p.w_sp, 0, S_ * S_ * 2, 0x00020000);
  const int wj = w < 7 ? w : 0;
  u32x4 wsp[4][8];
  const int wk = w ^ (KX && (dg == 5 || dg == 6) ? 4 : 0);
  const unsigned wso = (unsigned)(R * 64) * 4096u + (unsigned)wk * 512u;
  const unsigned wdo = (unsigned)(h4 * 8192 + ((n16 ^ h4) * 16));
#define WVO(q) (wdo ^ (unsigned)(((4 - (q)) & 3) * 64))
#define WSO(g, q) (wso + (unsigned)((g) >> 2) * 256u + (unsigned)(32 * (((g) >> 1) & 1) + ((g) & 1) + 8 * (q)) * 4096u)
  u32x4 raw[NRAW];
#define LDW(i) raw[i] = __builtin_amdgcn_raw_buffer_load_b128(rsp, WVO((i) & 3), WSO((i) >> 2, (i) & 3), 0)
#define LDWQ(k) do { _Pragma("unroll") for (int i_ = (k) * (NRAW / 4); i_ < ((k) + 1) * (NRAW / 4); ++i_) LDW(i_); } while (0)
  const int trow = R * 64 + 32 * ((w >> 1) & 1) + 2 * n16 + (w & 1);
  unsigned bs_raw;
  unsigned tag;
  unsigned etv = 0;
  auto etouch = [&]() {
    if (ET) etv = __builtin_amdgcn_raw_buffer_load_b32(rsp, l < 32 ? (unsigned)(((c & 3) * 16 + (l >> 1)) * 4096 + 256 + (l & 1) * 128)
                                                         : (l < 48 ? (unsigned)((32 + (c & 3) * 8 + ((l >> 1) & 7)) * 4096 + (l & 1) * 128) : OOB), wso, 0);
    SCHED();
  };
  unsigned fw2 = OOB;
  unsigned e_fw1;
  const unsigned fw1o = WS_FW1 + (unsigned)(((8 + c) * 8 + wj) * 4 + (l & 3)) * 128u;

  if (__builtin_expect(is_conv, 1)) {
    const rsrc_t rk = __builtin_amdgcn_make_buffer_rsrc((void*)p.k, 0, S_ * D_ * 2, 0x00020000);
    const rsrc_t rv = __builtin_amdgcn_make_buffer_rsrc((void*)p.v, 0, S_ * D_ * 2, 0x00020000);
    const rsrc_t rh = __builtin_amdgcn_make_buffer_rsrc((void*)p.shift_k, 0, D_ * 8, 0x00020000);
    const rsrc_t rf = __builtin_amdgcn_make_buffer_rsrc((void*)p.ssm_k, 0, S_ * D_ * 2, 0x00020000);
    STAMP(32);
    const int blk = (quad / 5) * 8 + w, sl = quad % 5;
    {
      const unsigned g = (unsigned)h4 * 1280u + (unsigned)((n16 ^ (4 * (h4 >> 1))) * 16);
      const unsigned so = (unsigned)blk * 10240u + (unsigned)sl * 256u;
      char* const la = lds + L_A0 + w * 6144;
      __builtin_amdgcn_raw_ptr_buffer_load_lds(rk, (LAS void*)(la + 1024), 16, g ^ 128u, so, 0, 0);
      __builtin_amdgcn_raw_ptr_buffer_load_lds(rk, (LAS void*)(la + 2048), 16, g, so + 5120u, 0, 0);
      __builtin_amdgcn_raw_ptr_buffer_load_lds(rv, (LAS void*)(la + 3072), 16, g, so, 0, 0);
      __builtin_amdgcn_raw_ptr_buffer_load_lds(rv, (LAS void*)(la + 4096), 16, g ^ 128u, so + 5120u, 0, 0);
      __builtin_amdgcn_raw_ptr_buffer_load_lds(rk, (LAS void*)(la), 16, g + (h4 ? 0u : 1280u), blk ? so - 5120u : so, 0, 0);
      __builtin_amdgcn_raw_ptr_buffer_load_lds(rh, (LAS void*)(la + 5120), 16, l16, (unsigned)sl * 1024u, 0, 0);
      SCHED();
    }
    const u32x2 dd2 = *(const CAS u32x2*)((const CAS char*)p.ssm_D + quad * 8);
    {
      const u32x4 z = {0u, 0u, 0u, 0u};
      if (tid < 128) *(u32x4*)(lds + (tid >> 5) * CH_BYTES + (tid & 31) * 16) = z;
      else if (tid < 152) {
        const int i = (tid - 128) >> 2;
        *(u32x4*)(lds + (tid & 3) * CH_BYTES + U_BYTES + (i < 4 ? i * G_COPY : (i & 1 ? 4128 : 16))) = z;
      }
      const LAS char* a0r = (const LAS char*)(lds + L_A0) + w * 6144 + (l & 3) * 512;
#define A0R(k) (a0r + ((((l >> 2) ^ (4 * ((l + (k)) & 3))) * 16)))
      unsigned go = WS_KVT + (unsigned)(quad / 5) * 81920u + (unsigned)sl * 16384u + (unsigned)(l >> 2) * 1024u + (unsigned)w * 64u + (unsigned)(l & 3) * 16u;
      const LAS char* r0 = A0R(0), * r1 = A0R(1), * r2 = A0R(2);
      const LAS char* rsh = (const LAS char*)(lds + L_A0) + w * 6144 + 5120 + (l >> 2) * 64;
      asm volatile("" : "+v"(go), "+v"(r0), "+v"(r1), "+v"(r2), "+v"(rsh));
      SCHED();
      __builtin_amdgcn_s_sleep(F_SLEEP);
#pragma unroll
      for (int j = 0; j < 2; ++j)
        __builtin_amdgcn_raw_ptr_buffer_load_lds(rf, (LAS void*)(lds + (w >> 1) * CH_BYTES + U_BYTES + 32 + (2 * (w & 1) + j) * 1024), 16, l16,
                                                 (unsigned)quad * 16384u + (unsigned)(2 * w + j) * 1024u, 0, 0);
      STAMP(34);
      tag = __builtin_amdgcn_readfirstlane(epv) * 2654435761u + 0x9E3779B9u;
      SCHED();
      VMW(2);
      STAMP(35);
      u32x4 sh[4], ka[5], va[2];
      sh[0] = *(const volatile LAS u32x4*)(rsh);
      ka[0] = *(const volatile LAS u32x4*)(r0 + 256);
      ka[1] = *(const volatile LAS u32x4*)(r1 + 512);
      ka[2] = *(const volatile LAS u32x4*)(r1 + 768);
      ka[3] = *(const volatile LAS u32x4*)(r2 + 1024);
      ka[4] = *(const volatile LAS u32x4*)(r2 + 1280);
      va[0] = *(const volatile LAS u32x4*)(r0 + 3072);
      va[1] = *(const volatile LAS u32x4*)(r0 + 3328);
#pragma unroll
      for (int e = 1; e < 4; ++e) sh[e] = *(const volatile LAS u32x4*)(rsh + 16 * e);
      SCHED();
      if (blk == 0) {
        if ((l & 3) < 2) ka[0] = z;
        if ((l & 3) == 0) { ka[1] = z; ka[2] = z; }
      }
      u32x4 ko;
#pragma unroll
      for (int e = 0; e < 4; ++e) {
        const f32x2 w0 = {bflo(sh[e][0]), bflo(sh[e][2])}, w1 = {bfhi(sh[e][0]), bfhi(sh[e][2])}, w2 = {bflo(sh[e][1]), bflo(sh[e][3])}, w3 = {bfhi(sh[e][1]), bfhi(sh[e][3])};
        const f32x2 km3 = bf2(ka[0][e]), km2 = bf2(ka[1][e]), km1 = bf2(ka[2][e]), k0 = bf2(ka[3][e]), k1 = bf2(ka[4][e]);
        const f32x2 s0 = __builtin_elementwise_fma(k0, w0, __builtin_elementwise_fma(km1, w1, __builtin_elementwise_fma(km2, w2, km3 * w3)));
        const f32x2 s1 = __builtin_elementwise_fma(k1, w0, __builtin_elementwise_fma(k0, w1, __builtin_elementwise_fma(km1, w2, km2 * w3)));
        const f32x2 o0 = s0 * bf2(va[0][e]), o1 = s1 * bf2(va[1][e]);
        ko[2 * (e & 1)] = pack2(o1[0], o0[0]); ko[2 * (e & 1) + 1] = pack2(o1[1], o0[1]);
        if (e & 1) st16(rs, go + (unsigned)(e >> 1) * 512u, ko);
        SCHED();
      }
    }
    STAMP(1);
    DRAIN();
    BAR();
    st4(rs, tid == 0 ? WS_FK + (unsigned)quad * 128u : OOB, tag);
    unsigned atv = 0;
    auto atouch = [&]() {
      const int i = w < 5 ? (cidx * 5 + w) * 20 + (l < 19 ? l : 19) : (cidx * 3 + w - 5) * 19 + (l < 18 ? l : 18);
      const int r = i / 3;
      const unsigned o1 = (unsigned)((r < 639 ? r : 639) * (F_ * 2) + xcd * CB * 2 + (i - 3 * r) * 108);
      const unsigned o2 = (unsigned)(xcd * CB * D_ * 2 + (i < 1079 ? i : 1079) * 128);
      atv = *(const GAS unsigned*)((const GAS char*)(w < 5 ? p.w_in : p.w_out) + (w < 5 ? o1 : o2));
      SCHED();
    };
    if (AT == 2) atouch();
    {
      u32x4 d[3];
      u32x2 n[3];
#pragma unroll
      for (int j = 0; j < 3; ++j) {
        char* G = j < 2 ? lds + ((tid >> 8) + 2 * j) * CH_BYTES + U_BYTES + ((tid & 255) + 2) * 16 : lds + (tid & 3) * CH_BYTES + U_BYTES + 16;
        d[j] = *(const u32x4*)G;
        n[j] = *(const u32x2*)(G + 16);
      }
#pragma unroll
      for (int j = 0; j < 3; ++j) {
        char* G = j < 2 ? lds + ((tid >> 8) + 2 * j) * CH_BYTES + U_BYTES + ((tid & 255) + 2) * 16 : lds + (tid & 3) * CH_BYTES + U_BYTES + 16;
        const unsigned a0 = __builtin_amdgcn_alignbit(d[j][1], d[j][0], 16), a1 = __builtin_amdgcn_alignbit(d[j][2], d[j][1], 16),
                       a2 = __builtin_amdgcn_alignbit(d[j][3], d[j][2], 16), a3 = __builtin_amdgcn_alignbit(n[j][0], d[j][3], 16),
                       a4 = __builtin_amdgcn_alignbit(n[j][1], n[j][0], 16);
        if (j < 2 || tid < 4) {
          *(u32x4*)(G + G_COPY) = (u32x4){a0, a1, a2, a3};
          *(u32x4*)(G + 2 * G_COPY) = (u32x4){d[j][1], d[j][2], d[j][3], n[j][0]};
          *(u32x4*)(G + 3 * G_COPY) = (u32x4){a1, a2, a3, a4};
        }
      }
    }
    STAMP(36);
    {
      __builtin_amdgcn_s_sleep(P_SLEEP);
      wait_flag<1>(rs, WS_FK + (unsigned)(5 * (4 * w + (l & 3)) + (quad >> 5)) * 128u, tag);
      STAMP(29);
      const unsigned gp = WS_KVT + (unsigned)(4 * w + (l >> 5)) * 81920u + (unsigned)quad * 512u + (unsigned)(l & 31) * 16u;
      const u32x4 g0 = ld16(rs, gp), g1 = ld16(rs, gp + 163840u);
      SCHED();
      if (AT == 1) atouch();
      char* Uw = lds + 512 + (128 * w + (l & ~3) + 3 - (l & 3)) * 4;
#pragma unroll
      for (int i = 0; i < 4; ++i) *(unsigned*)(Uw + i * CH_BYTES) = g0[i];
#pragma unroll
      for (int i = 0; i < 4; ++i) *(unsigned*)(Uw + 256 + i * CH_BYTES) = g1[i];
    }
    STAMP(28);
    BAR();
    STAMP(2);
    const int te = 2 * tid;
    u32x4 qa[2];
    u32x4 sv[2];
    u32x2 mg;
    const unsigned xo = (unsigned)(tid >> 4) * WX1P + (unsigned)quad * 256u + (unsigned)(tid & 15) * 16u;
    const unsigned mgo = WS_MOD + (unsigned)((l < 12 ? (l >> 2) * D_ + (l & 3) : 0) + quad * 4) * 8u;
    const int ch = w & 3, hf = w >> 2;
    auto ld_sv = [&]() {
      sv[0] = ld16(rs, WS_ST1 + (unsigned)tid * 16u);
      sv[1] = ld16(rs, WS_ST1 + (unsigned)tid * 16u + 8192u);
    };
    auto ld_q = [&]() {
      qa[0] = ld16(rs, WS_QB + xo);
      qa[1] = ld16(rs, WS_QB + xo + 32u * WX1P);
    };
    f32x2 hh[2][2][2], base[2][2][2];
    auto prep = [&]() {
      const LAS char* ur = (const LAS char*)(lds + 512) + ((te & ~7) + 6 - (te & 7)) * 2;
      unsigned uu[2][4];
#pragma unroll
      for (int i = 0; i < 8; ++i) uu[i >> 2][i & 3] = *(const volatile LAS unsigned*)(ur + (i >> 2) * 2048 + (i & 3) * CH_BYTES);
      if (!__all(((sv[0][0] ^ tag) | (sv[0][2] ^ tag) | (sv[1][0] ^ tag) | (sv[1][2] ^ tag)) == 0u)) {
        _Pragma("unroll 1") for (unsigned spins = 0; spins < SPIN_CAP; ++spins) {
          ld_sv();
          if (__all(sv[0][0] == tag && sv[0][2] == tag && sv[1][0] == tag && sv[1][2] == tag)) break;
          __builtin_amdgcn_s_sleep(4);
        }
        asm volatile("" ::: "memory");
        ld_q();
        mg = ld8(rs, mgo);
        DRAIN();
      }
      if (!__all(mg[0] == tag)) {
        _Pragma("unroll 1") for (unsigned spins = 0; spins < SPIN_CAP; ++spins) {
          mg = ld8(rs, mgo);
          if (__all(mg[0] == tag)) break;
          __builtin_amdgcn_s_sleep(2);
        }
      }
      f32x2 cc2[2], ss2[2], dv2[2];
      float cch[4], sch[4];
#pragma unroll
      for (int i = 0; i < 4; ++i) {
        const float sa = __uint_as_float(__builtin_amdgcn_readlane(mg[1], i)), ca = __uint_as_float(__builtin_amdgcn_readlane(mg[1], 4 + i)),
                    ga = __uint_as_float(__builtin_amdgcn_readlane(mg[1], 8 + i));
        cch[i] = ga * rbf(1.0f + ca);
        sch[i] = ga * sa;
      }
      cc2[0] = (f32x2){cch[0], cch[1]}; cc2[1] = (f32x2){cch[2], cch[3]};
      ss2[0] = (f32x2){sch[0], sch[1]}; ss2[1] = (f32x2){sch[2], sch[3]};
      dv2[0] = bf2(dd2[0]); dv2[1] = bf2(dd2[1]);
#pragma unroll
      for (int H = 0; H < 2; ++H)
#pragma unroll
        for (int e = 0; e < 2; ++e) {
          const unsigned st = sv[H][1 + 2 * e];
          const float a = __builtin_amdgcn_rcpf(bfhi(st)), b = -bflo(st) * a;
#pragma unroll
          for (int i = 0; i < 2; ++i) {
            const f32x2 q2 = bf2(qa[H][2 * e + i]);
            const f32x2 u2 = e ? (f32x2){bflo(uu[H][2 * i]), bflo(uu[H][2 * i + 1])} : (f32x2){bfhi(uu[H][2 * i]), bfhi(uu[H][2 * i + 1])};
            const f32x2 n1 = __builtin_elementwise_fma(q2, (f32x2){a, a}, (f32x2){b, b});
            hh[H][e][i] = __builtin_elementwise_fma(n1, cc2[i], ss2[i]);
            base[H][e][i] = __builtin_elementwise_fma(hh[H][e][i], u2 * dv2[i], q2);
            asm volatile("" : "+v"(hh[H][e][i]), "+v"(base[H][e][i]));
          }
        }
    };
    auto HF = [&]() { int x = hf; asm volatile("" : "+s"(x)); return x; };
    {
      const char* U = lds + ch * CH_BYTES;
      const char* G = U + U_BYTES;
      const int a = n16, jj = h4 >> 1, e2 = h4 & 1;
      const int sg = (a + 1) & 3;
      const LAS char* bp = (const LAS char*)U + 512 + 32 * (a - jj) + 16 * e2 - 256 * hf - 192;
      const LAS char* ap = (const LAS char*)G + sg * G_COPY + 2 * (16 * jj + a - 8 * e2 + 9 - sg) + 256 * hf;
      char* yp = lds + L_YP + (hf * 4 + ch) * 8192 + (h4 * 16 + ((a + 4 * h4) & 15)) * 16;
      f32x4 acc[8];
      u32x4 A[2][8], B[2][8];
      auto rd = [&](int k) {
        const int r = k / 24, j = k % 24;
        if (j >= 2 && j < 10) B[r & 1][9 - j] = *(const volatile LAS u32x4*)(bp + 512 * (9 - j) + 64 * (3 - r));
        else {
          const int pp = j < 2 ? 0 : (j - 8) >> 1;
          const u32x2 v = *(const volatile LAS u32x2*)(ap + 512 * pp + 64 * r + 8 * (j & 1));
          A[r & 1][pp][2 * (j & 1)] = v[0]; A[r & 1][pp][2 * (j & 1) + 1] = v[1];
        }
      };
#pragma unroll
      for (int k = 0; k < IC<conv_sched(CDIST).pre>::v; ++k) rd(k);
      SCHED();
      auto step = [&](auto ic) {
        constexpr int i = decltype(ic)::v, r = i / 36, pp = conv_sched(CDIST).pp[i], m = conv_sched(CDIST).m[i], k = conv_sched(CDIST).rd[i];
        acc[m + pp] = MFMA16(A[r & 1][pp], B[r & 1][m], i < 8 ? ZERO4 : acc[m + pp]);
        SCHED();
        if (k >= 0) rd(k);
        if (i == AT_AT && AT == 3) atouch();
        if (i >= 2 && (i - 2) % (2 * RAWD1) == 0 && (i - 2) / RAWD1 < NR1) {
          if (!HF()) { LDW((i - 2) / RAWD1); LDW((i - 2) / RAWD1 + 1); }
        }
        if (i >= RAW2 && (i - RAW2) % (2 * RAWD) == 0 && (i - RAW2) / RAWD < NRAW) {
          if (HF()) { LDW((i - RAW2) / RAWD); LDW((i - RAW2) / RAWD + 1); }
        }
        if (i == SV_AT) { ld_sv(); bs_raw = p.b_sp[trow]; }
        if (r == 3 && pp >= 1 && m == 7 - pp) *(f32x4*)(yp + (pp - 1) * 1024) = acc[pp - 1];
        SCHED();
      };
      static_for<0, PREP_AT>(step);
      STAMP(3);
      DRAIN();
      asm volatile("" ::"v"(atv));
      ld_q();
      mg = ld8(rs, mgo);
      SCHED();
      if (PM == 0) {
        if (HF()) __builtin_amdgcn_s_setprio(1);
        if (!HF()) prep();
      }
      STAMP(4);
      static_for<PREP_AT, 144>(step);
      *(f32x4*)(yp + 7 * 1024) = acc[7];
      if (NR1 < NRAW && !HF()) {
#pragma unroll
        for (int i = NR1; i < NRAW; ++i) LDW(i);
      }
      STAMP(5);
    }
    if (PM == 0) { if (HF()) { prep(); __builtin_amdgcn_s_setprio(0); } }
    else prep();
    e_fw1 = ld4(rs, fw1o);
    STAMP(6);
    const LAS char* yr = (const LAS char*)(lds + L_YP) + (te >> 8) * 1024 + (((te >> 2) & 3) * 16 + ((((te >> 4) & 15) + 4 * ((te >> 2) & 3)) & 15)) * 16 + (te & 3) * 4;
    asm volatile("" : "+v"(yr));
    BAR();
    STAMP(7);
    f32x2 y[2][8];
#pragma unroll
    for (int i = 0; i < 16; ++i) y[i >> 3][i & 7] = *(const volatile LAS f32x2*)(yr + (i >> 3) * 4096 + (i & 7) * 8192);
#pragma unroll
    for (int H = 0; H < 2; ++H) {
      u32x4 o;
#pragma unroll
      for (int e = 0; e < 2; ++e)
#pragma unroll
        for (int i = 0; i < 2; ++i) {
          const float x0 = __builtin_fmaf(hh[H][e][i][0], y[H][2 * i][e] + y[H][4 + 2 * i][e], base[H][e][i][0]);
          const float x1 = __builtin_fmaf(hh[H][e][i][1], y[H][2 * i + 1][e] + y[H][5 + 2 * i][e], base[H][e][i][1]);
          o[2 * e + i] = pack2(x0, x1);
        }
      st16(rs, WS_X1B + (unsigned)(32 * H) * WX1P + xo, o);
    }
    STAMP(8);
  } else {
    const rsrc_t rwi = __builtin_amdgcn_make_buffer_rsrc((void*)p.w_in, 0, D_ * F_ * 2, 0x00020000);
    const rsrc_t rwo = __builtin_amdgcn_make_buffer_rsrc((void*)p.w_out, 0, FH * D_ * 2, 0x00020000);
    STAMP(32);
    const int job = xcd + 8 * slot;
    const int T = job >= 50 && w < 3 && job - 50 + 46 * w < 128 ? job - 50 + 46 * w : -1;
    const bool unit = slot >= 8 || (slot >= 2 && slot < 6);
    const int ucb = slot >= 8 ? 8 + xcd : xcd, kg = (slot >= 8 ? slot : slot - 2) & 3;
    float* tab = (float*)(lds + L_TAB);
    float* red = (float*)(lds + L_RED);
    auto issue_unit = [&](int i) {
      const int g = i * 64 + l;
      __builtin_amdgcn_raw_ptr_buffer_load_lds(rwi, (LAS void*)(lds + L_J3 + i * 1024), 16, (unsigned)((g / 14) * (F_ * 2) + (g % 14) * 16),
                                               (unsigned)(kg * 160 * F_ * 2 + ucb * CB * 2), 0, 0);
    };
    if (T >= 0) {
      __builtin_amdgcn_s_sleep(Q_SLEEP);
      char* qt = lds + L_QT + w * 20480;
      const unsigned so = (unsigned)(16 * T) * 1280u;
#pragma unroll
      for (int n = 0; n < 16; ++n)
        __builtin_amdgcn_raw_ptr_buffer_load_lds(rq, (LAS void*)(qt + n * 1024), 16, l16 ^ (unsigned)(n * 16), so + (unsigned)(n * 1280), 0, 0);
#pragma unroll
      for (int j = 0; j < 4; ++j)
        __builtin_amdgcn_raw_ptr_buffer_load_lds(rq, (LAS void*)(qt + 16384 + j * 1024), 16, (unsigned)(h4 * 1280 + ((n16 ^ (4 * j + h4)) * 16)), so + (unsigned)(4 * j * 1280 + 1024), 0, 0);
      tag = __builtin_amdgcn_readfirstlane(epv) * 2654435761u + 0x9E3779B9u;
      STAMP(34);
      DRAIN();
      STAMP(35);
      const bf2_t one = {(__bf16)1.0f, (__bf16)1.0f};
      float s1[2] = {0.f, 0.f}, s2[2] = {0.f, 0.f};
      const int n0 = 2 * (l & 7);
      u32x4 qv[20];
#pragma unroll
      for (int i = 0; i < 20; ++i)
#pragma unroll
        for (int e = 0; e < 2; ++e) {
          const int n = n0 + e, ck = 4 * (i & 15) + h4;
          const u32x2 v = *(const volatile LAS u32x2*)(qt + (i < 16 ? n * 1024 : 16384 + n * 256) + ((ck ^ n) * 16) + (l & 8));
          qv[i][2 * e] = v[0]; qv[i][2 * e + 1] = v[1];
        }
#pragma unroll
      for (int i = 0; i < 20; ++i) {
        st16(rs, WS_QB + (unsigned)(T >> 1) * WX1P + (unsigned)i * 2048u + (unsigned)((l >> 3) * 256 + (T & 1) * 128 + (l & 7) * 16), qv[i]);
#pragma unroll
        for (int e = 0; e < 4; ++e) {
          const unsigned x = qv[i][e];
          const bf2_t v = __builtin_bit_cast(bf2_t, x);
          s1[e >> 1] = __builtin_amdgcn_fdot2_f32_bf16(v, one, s1[e >> 1], false);
          s2[e >> 1] = __builtin_amdgcn_fdot2_f32_bf16(v, v, s2[e >> 1], false);
        }
        SCHED();
      }
      u32x4 o = {tag, 0u, tag, 0u};
#pragma unroll
      for (int e = 0; e < 2; ++e) {
        const float t1 = sum_rows(s1[e] + dpp_f<0x128>(s1[e])), t2 = sum_rows(s2[e] + dpp_f<0x128>(s2[e]));
        const float mean = t1 * (1.0f / D_);
        const float var = __builtin_fmaxf(t2 * (1.0f / D_) - mean * mean, 0.f);
        o[2 * e + 1] = pack2(mean, __builtin_amdgcn_sqrtf(rbf(rbf(var) + 1e-5f)));
      }
      STAMP(1);
      DRAIN();
      if (l < 8) st16(rs, WS_ST1 + (unsigned)T * 128u + l16, o);
      STAMP(3);
    }
    if (job < 60) {
      const int grp = job < 10 ? 40 + job : (job < 50 ? job - 10 : job);
      if (job >= 50 && T < 0) __builtin_amdgcn_s_sleep(LATE_SLEEP);
      if (job >= 10 && T < 0) __builtin_amdgcn_s_sleep(AUX_SLEEP);
      const unsigned cv = *(const unsigned*)(p.cvec + 80 * w + 2 * (l < 40 ? l : 0));
      u32x4 pre[10];
#pragma unroll
      for (int pp = 0; pp < 10; ++pp) pre[pp] = *(const u32x4*)(p.w_mod + (size_t)(80 * w + pp * 8 + (l >> 3)) * 3840 + grp * 64 + (l & 7) * 8);
      SCHED();
      STAMP(34);
      tag = __builtin_amdgcn_readfirstlane(epv) * 2654435761u + 0x9E3779B9u;
      if (l < 40) {
        const float c0 = bflo(cv), c1 = bfhi(cv);
        *(f32x2*)(tab + w * 80 + 2 * l) = (f32x2){rbf(c0 * __builtin_amdgcn_rcpf(1.0f + __expf(-c0))), rbf(c1 * __builtin_amdgcn_rcpf(1.0f + __expf(-c1)))};
      }
      asm volatile("s_waitcnt lgkmcnt(0)" ::: "memory");
      f32x2 acc[4];
#pragma unroll
      for (int e = 0; e < 4; ++e) acc[e] = (f32x2){0.f, 0.f};
#pragma unroll
      for (int pp = 0; pp < 10; ++pp) {
        const float sv = tab[w * 80 + pp * 8 + (l >> 3)];
#pragma unroll
        for (int e = 0; e < 4; ++e) acc[e] = __builtin_elementwise_fma((f32x2){sv, sv}, bf2(pre[pp][e]), acc[e]);
      }
      STAMP(35);
      asm volatile("" : "+v"(acc[0]), "+v"(acc[1]), "+v"(acc[2]), "+v"(acc[3]));
#pragma unroll
      for (int e = 0; e < 8; ++e) { float a = acc[e >> 1][e & 1]; a += dpp_f<0x128>(a); acc[e >> 1][e & 1] = sum_rows(a); }
      if (l < 8) {
        *(f32x4*)(red + w * 64 + l * 8) = (f32x4){acc[0][0], acc[0][1], acc[1][0], acc[1][1]};
        *(f32x4*)(red + w * 64 + l * 8 + 4) = (f32x4){acc[2][0], acc[2][1], acc[3][0], acc[3][1]};
      }
      if (w == 7) __builtin_amdgcn_s_setprio(2);
      BAR();
      if (w == 7) {
        float t = 0.f;
#pragma unroll
        for (int r = 0; r < 8; ++r) t += red[r * 64 + l];
        st8(rs, WS_MOD + (unsigned)(grp * 64 + l) * 8u, (u32x2){tag, __float_as_uint(rbf(t))});
        __builtin_amdgcn_s_setprio(0);
      } else if (unit) {
#pragma unroll
        for (int k = 0; k < 5; ++k) issue_unit(w * 5 + k);
      }
    } else if (unit && w >= 3 && w < 7) {
      __builtin_amdgcn_s_sleep(AUX_SLEEP);
#pragma unroll
      for (int k = 0; k < 9; ++k) issue_unit((w - 3) * 9 + k < 35 ? (w - 3) * 9 + k : 34);
    }
    tag = __builtin_amdgcn_readfirstlane(epv) * 2654435761u + 0x9E3779B9u;
    if (unit) {
      auto mod_tab = [&](int which, float add, float* dst) {
        const unsigned o = WS_MOD + (unsigned)(which * D_ + kg * 160) * 8u;
        u32x4 g0, g1;
        _Pragma("unroll 1") for (unsigned spins = 0; spins < SPIN_CAP; ++spins) {
          g0 = ld16(rs, o + l16);
          g1 = ld16(rs, o + 1024u + (l16 & 255u));
          if (__all(g0[0] == tag && g0[2] == tag && g1[0] == tag && g1[2] == tag)) break;
          __builtin_amdgcn_s_sleep(2);
        }
        *(f32x2*)(dst + 2 * l) = (f32x2){rbf(add + __uint_as_float(g0[1])), rbf(add + __uint_as_float(g0[3]))};
        if (l < 16) *(f32x2*)(dst + 128 + 2 * l) = (f32x2){rbf(add + __uint_as_float(g1[1])), rbf(add + __uint_as_float(g1[3]))};
      };
      if (w == 7) {
        STAMP(1);
        if (slot >= 8) __builtin_amdgcn_s_sleep(64);
        mod_tab(4, 1.0f, tab);
        BAR();
        STAMP(2);
        mod_tab(3, 0.0f, tab + 160);
        BAR();
      } else {
        DRAIN();
        STAMP(36);
        BAR();
        STAMP(2);
        const char* bp = lds + L_J3 + (4 * h4 + (n16 >> 2)) * 224 + (n16 & 3) * 8 + w * 32;
        const float* tp = tab + 4 * h4;
        u32x4 f3[5];
        float cs = 0.f, b1 = 0.f;
        const bool pad = w == 6 && n16 >= 12;
        const unsigned padv = (n16 == 12 && ucb >= 8) ? 0x3f803f80u : 0u;
#pragma unroll
        for (int k = 0; k < 5; ++k) f3[k] = trfrag(bp + k * 7168, 16 * 224);
#pragma unroll
        for (int k = 0; k < 5; ++k) {
          const f32x4 ca = *(const f32x4*)(tp + k * 32), cb4 = *(const f32x4*)(tp + k * 32 + 16);
          u32x4 o;
#pragma unroll
          for (int e = 0; e < 4; ++e) {
            const float c0 = e < 2 ? ca[2 * e] : cb4[2 * e - 4], c1 = e < 2 ? ca[2 * e + 1] : cb4[2 * e - 3];
            const float p0 = bflo(f3[k][e]) * c0, p1 = bfhi(f3[k][e]) * c1;
            cs += p0 + p1;
            o[e] = pad ? padv : pack2(p0, p1);
          }
          st16(rs, WS_WINF + (unsigned)(ucb * 7 + w) * 20480u + (unsigned)(kg * 5 + k) * 1024u + l16, o);
        }
        cs = sum_rows(cs);
        DRAIN();
        if (l == 0) st4(rs, WS_FW1 + (unsigned)((ucb * 8 + w) * 4 + kg) * 128u, tag);
        STAMP(5);
        BAR();
#pragma unroll
        for (int k = 0; k < 5; ++k) {
          const f32x4 sa = *(const f32x4*)(tp + 160 + k * 32), sb4 = *(const f32x4*)(tp + 160 + k * 32 + 16);
#pragma unroll
          for (int e = 0; e < 4; ++e) {
            const float s0 = e < 2 ? sa[2 * e] : sb4[2 * e - 4], s1 = e < 2 ? sa[2 * e + 1] : sb4[2 * e - 3];
            b1 = __builtin_fmaf(s0, bflo(f3[k][e]), __builtin_fmaf(s1, bfhi(f3[k][e]), b1));
          }
        }
        b1 = sum_rows(b1);
        if (pad) { cs = padv ? 160.0f : 0.0f; b1 = 0.0f; }
        if (l < 16) st16(rs, WS_CS + (unsigned)((ucb * 4 + kg) * 112 + w * 16 + l) * 16u, (u32x4){tag, __float_as_uint(cs), tag, __float_as_uint(b1)});
      }
    } else {
      if (job >= 60 && T < 0) __builtin_amdgcn_s_sleep(100);
      if (slot < 2 && w >= 2 && w < 6) {
        const int c5 = slot * 4 + w - 2, c2 = xcd;
        char* st = lds + L_J5 + (w - 2) * 18432;
#pragma unroll
        for (int r = 0; r < 18; ++r) {
          const int g = r * 64 + l;
          __builtin_amdgcn_raw_ptr_buffer_load_lds(rwo, (LAS void*)(st + r * 1024), 16, (unsigned)((g / 10) * (D_ * 2) + (g % 10) * 16),
                                                   (unsigned)(xcd * CB * D_ * 2 + c5 * 160), 0, 0);
        }
        DRAIN();
        const char* bp = st + (4 * h4 + (n16 >> 2)) * 160 + (n16 & 3) * 8;
        const unsigned base = WS_WOUTF + (unsigned)c5 * WBLK;
#pragma unroll
        for (int dj = 0; dj < 5; ++dj) {
          st16(rs, base + (unsigned)c2 * 10240u + (unsigned)dj * 1024u + l16, trfrag(bp + dj * 32, 32 * 160));
          st16(rs, base + (unsigned)c2 * 10240u + (unsigned)(5 + dj) * 1024u + l16, trfrag(bp + 16 * 160 + dj * 32, 32 * 160));
          st16(rs, base + 81920u + (unsigned)c2 * 5120u + (unsigned)dj * 1024u + l16, trfrag(bp + 64 * 160 + dj * 32, 16 * 160));
          st8(rs, base + 122880u + (unsigned)((c2 >> 1) * 5 + dj) * 1024u + (unsigned)(c2 & 1) * 512u + (unsigned)l * 8u, trd(bp + 96 * 160 + dj * 32));
        }
        fw2 = WS_FW2 + (unsigned)(c5 * 8 + xcd) * 128u;
      }
    }
    STAMP(4);
#pragma unroll
    for (int i = 0; i < NRAW; ++i) LDW(i);
    bs_raw = p.b_sp[trow];
    e_fw1 = ~tag;
    asm volatile(".Lmega_aux_end:");
    asm volatile("" ::"v"(cpf));
    STAMP(8);
  }

  LAS char* xa = (LAS char*)(lds + L_XI) + l * 16;
  LAS char* xb = xa + 65536;
  asm volatile("" : "+v"(xb));
#define LDA(ks, rt) (*(const volatile LAS u32x4*)((ks) < 16 ? xa + ((ks) * 4 + (rt)) * 1024 : xb + (((ks) - 16) * 4 + (rt)) * 1024))
  auto lda1 = [&](int ks, u32x4* A) {
#pragma unroll
    for (int rt = 0; rt < 4; ++rt) A[rt] = LDA(ks, rt);
  };
  const LAS char* tb4 = (const LAS char*)(lds + L_MISC) + h4 * 16;
  const LAS char* tbn = (const LAS char*)(lds + L_MISC) + n16 * 4;
  asm volatile("" : "+v"(tb4), "+v"(tbn));
  u32x4 xr[5][2];
  auto ld_x1 = [&]() {
#pragma unroll
    for (int m = 0; m < 5; ++m) {
      if (m == 0) __builtin_amdgcn_s_setprio(2);
      if (m == 1) __builtin_amdgcn_s_setprio(1);
      if (m == 3) __builtin_amdgcn_s_setprio(0);
#pragma unroll
      for (int e = 0; e < 2; ++e)
        xr[m][e] = ld16(rs, l16, WS_X1B + (unsigned)(R * 2 + (w & 1)) * WX1P + (unsigned)(4 * m + (w >> 1)) * 2048u + (unsigned)e * 1024u);
      SCHED();
    }
  };
  LAS char* xw = xa + w * 2048;
  auto put_x1 = [&](int m) {
    LAS char* d = m < 4 ? xw + m * 16384 : xb + w * 2048;
#pragma unroll
    for (int e = 0; e < 2; ++e) {
      *(volatile LAS u32x2*)(d + 8 * e) = (u32x2){xr[m][e][0], xr[m][e][1]};
      *(volatile LAS u32x2*)(d + 1024 + 8 * e) = (u32x2){xr[m][e][2], xr[m][e][3]};
    }
  };
  auto poison_u = [&]() {
#pragma unroll
    for (int k = 0; k < 2; ++k) {
      const unsigned o = (unsigned)(wj * 2 + k) * 1024u + l16;
      const unsigned rt = o / 3584u, r = o - rt * 3584u;
      st16(rs, WS_UF + ((unsigned)(R * 4) + rt) * 8u * 3584u + (unsigned)c * 3584u + r, (u32x4){~0u, ~0u, ~0u, ~0u});
    }
  };
  const unsigned wob = WS_WOUTF + (unsigned)c * WBLK;
  const LAS char* wrd = (const LAS char*)(lds + L_WS) + w * 8192 + n16 * 256 + ((h4 ^ ((4 - (n16 >> 2)) & 3)) * 64);
#define DMA_W(g, q) __builtin_amdgcn_raw_ptr_buffer_load_lds(rsp, (LAS void*)(lds + L_WS + w * 8192 + (((g) + 1) & 1) * 4096 + (q) * 1024), 16, WVO(q), WSO(g, q), 0, 0)
#define RD_W(g) do { _Pragma("unroll") for (int i_ = 0; i_ < 4; ++i_) \
                      wsp[(g) & 3][((g) >> 2) * 4 + i_] = *(const volatile LAS u32x4*)(wrd + (((g) + 1) & 1) * 4096 + ((i_ ^ (n16 & 3)) * 16)); } while (0)
  auto park = [&]() {
    LAS char* d = (LAS char*)(lds + L_WS) + w * 8192 + l * 16;
#pragma unroll
    for (int g = 0; g < NRAW / 4; ++g) {
#pragma unroll
      for (int q = 0; q < 4; ++q) *(volatile LAS u32x4*)(d + ((g + 1) & 1) * 4096 + q * 1024) = raw[4 * g + q];
      if (g < 2) RD_W(g);
    }
    asm volatile("s_waitcnt lgkmcnt(0)" ::"v"(etv) : "memory");
  };
#define DMA_AB(k) dma1k(rs, lds + L_AB + ((k) * 8 + w) * 1024, l16, wob + (unsigned)((k) * 8 + w) * 1024u)
  const int rto = w & 3, cls = w >> 2;
  u32x2 xp[3];
  constexpr int W = G2W;
  const unsigned zb = WS_Z2F + (unsigned)c * 458752u + (unsigned)wk * 8192u;
  const unsigned fzo = WS_FZ + (unsigned)(c * 32 + wk * 4 + (l & 3)) * 128u;
  u32x4 fr[W];
#define LDZ(i) fr[(i) % W] = ld16(rs, l16, zb + (unsigned)((i) >> 3) * 65536u + (unsigned)((i) & 7) * 1024u)
  u32x4 cr[5];
#define LDCR(k) cr[k] = ld16(rs, l16, wob + 81920u + (unsigned)(w * 5 + (k)) * 1024u)
  auto rd_xp = [&]() {
#pragma unroll
    for (int j = 0; j < 3; ++j) {
      const int q4 = 5 * c + (cls ? (3 + (j < 2 ? j : 1)) : j);
      xp[j] = *(const volatile LAS u32x2*)(xa + ((q4 >> 1) * 4 + rto) * 1024 + (q4 & 1) * 8);
    }
  };
  if (w < 7) {
    u32x4 B2[20], B1[20];
    const unsigned b2o = WS_WINF + (unsigned)((8 + c) * 7 + w) * 20480u, b1o = WS_WINF + (unsigned)(c * 7 + w) * 20480u;
#define LDB2(ks) B2[ks] = ld16(rs, l16, b2o + (unsigned)(ks) * 1024u)
#define LDB1(ks) B1[ks] = ld16(rs, l16, b1o + (unsigned)(ks) * 1024u)
    const bool early = __all(e_fw1 == tag);
    asm volatile("" ::: "memory");
    {
      const unsigned le = early ? l16 : OOB;
#pragma unroll
      for (int ks = 0; ks < NB2A; ++ks) B2[ks] = ld16(rs, le, b2o + (unsigned)ks * 1024u);
      SCHED();
      if (is_conv) VMW(NB2A);
    }
    STAMP(33);
    BAR();
    STAMP(37);
    if (!early) {
      wait_flag<4>(rs, fw1o, tag);
      DRAIN();
      st4(rs, l == 0 ? fw2 : OOB, tag);
#pragma unroll
      for (int ks = 0; ks < NB2A; ++ks) LDB2(ks);
    }
#pragma unroll
    for (int ks = NB2A; ks < 20; ++ks) {
      LDB2(ks);
      if (ks == PK) { SCHED(); park(); SCHED(); }
      if (ks == P1B) { SCHED(); __builtin_amdgcn_s_barrier(); SCHED(); }
    }
    if (D3E) {
#pragma unroll
      for (int q = 0; q < 4; ++q) DMA_W(3, q);
    }
    STAMP(9);
    BAR();
    STAMP(10);
    ld_x1();
    SCHED();
#pragma unroll
    for (int m = 0; m < 3; ++m) put_x1(m);
    if (w == 6) __builtin_amdgcn_s_setprio(2);
    BAR();
    STAMP(11);
    f32x4 acc[4];
    u32x4 A[2][4];
    lda1(0, A[0]);
    poison_u();
#pragma unroll
    for (int ks = 0; ks < 20; ++ks) {
      if (ks == 6) put_x1(3);
      if (ks == 8) put_x1(4);
      if (ks == 11) BAR();
      if (ks >= 4) LDB1(ks - 4);
      if (ks >= 16) LDB1(ks);
      if (ks == 12) RD_W(2);
      SCHED();
#pragma unroll
      for (int rt = 0; rt < 4; ++rt) {
        if (ks < 19) A[(ks + 1) & 1][rt] = LDA(ks + 1, rt);
        SCHED();
        acc[rt] = MFMA16(A[ks & 1][rt], B2[ks], ks ? acc[rt] : ZERO4);
        SCHED();
      }
    }
    STAMP(12);
    if (w == 6) {
      if (n16 == 12) {
#pragma unroll
        for (int rt = 0; rt < 4; ++rt) *(f32x4*)(lds + L_S1 + (rt * 16 + 4 * h4) * 4) = acc[rt];
      }
      asm volatile("" ::: "memory");
      *(volatile LAS unsigned*)(lds + L_S1 + 256) = tag;
      __builtin_amdgcn_s_setprio(0);
    }
    VMW(20);
    BAR();
    STAMP(13);
    f32x2 zz[8];
    f32x4 cs1a, cs1b;
    {
      f32x4 rs4[4], nm4[4];
#pragma unroll
      for (int rt = 0; rt < 4; ++rt) {
        rs4[rt] = *(const LAS f32x4*)(tb4 + (L_T2 - L_MISC) + rt * 64);
        nm4[rt] = *(const LAS f32x4*)(tb4 + (L_T2 - L_MISC) + 256 + rt * 64);
      }
      const f32x2 cs2 = *(const f32x2*)(lds + L_CS1 + (112 + 16 * w + n16) * 8);
      const f32x2 csv = {cs2[0], cs2[0]}, b1v = {cs2[1], cs2[1]};
      float v1[16], v2[16];
#pragma unroll
      for (int i = 0; i < 8; ++i) {
        const int rt = i >> 1;
        const f32x2 a2 = i & 1 ? HI2(acc[rt]) : LO2(acc[rt]), n2 = i & 1 ? HI2(nm4[rt]) : LO2(nm4[rt]), r2 = i & 1 ? HI2(rs4[rt]) : LO2(rs4[rt]);
        zz[i] = gelu2(__builtin_elementwise_fma(__builtin_elementwise_fma(n2, csv, a2), r2, b1v));
        asm volatile("" : "+v"(zz[i]));
        if (!D3E) DMA_W(3 + (i >> 2), i & 3);
        else if (i & 1) DMA_W(4, i >> 1);
        const f32x2 sq = zz[i] * zz[i];
        v1[2 * i] = zz[i][0]; v1[2 * i + 1] = zz[i][1]; v2[2 * i] = sq[0]; v2[2 * i + 1] = sq[1];
      }
      const float t1 = tr_sum16(v1, l);
      const float t2 = tr_sum16(v2, l);
      *(f32x2*)(lds + L_Z1S + (w * 64 + (n16 >> 2) * 16 + 4 * h4 + (n16 & 3)) * 8) = (f32x2){t1, t2};
      lda1(0, A[0]);
      cs1a = *(const f32x4*)(lds + L_CS1 + (16 * w + 4 * h4) * 8);
      cs1b = *(const f32x4*)(lds + L_CS1 + (16 * w + 4 * h4) * 8 + 16);
    }
    BAR();
    STAMP(14);
#pragma unroll
    for (int ks = 0; ks < 20; ++ks) {
      if (ks == Z1SPLIT) {
        STAMP(15);
        BAR();
        STAMP(16);
        f32x4 ra[4], rb[4];
#pragma unroll
        for (int rt = 0; rt < 4; ++rt) {
          ra[rt] = *(const LAS f32x4*)(tb4 + (L_ST2 - L_MISC) + rt * 64);
          rb[rt] = *(const LAS f32x4*)(tb4 + (L_ST2 - L_MISC) + 256 + rt * 64);
        }
#pragma unroll
        for (int i = 0; i < 8; ++i) {
          zz[i] = __builtin_elementwise_fma(zz[i], i & 1 ? HI2(ra[i >> 1]) : LO2(ra[i >> 1]), i & 1 ? HI2(rb[i >> 1]) : LO2(rb[i >> 1]));
          asm volatile("" : "+v"(zz[i]));
        }
        const u32x4 o0 = {pack2(zz[0][0], zz[2][0]), pack2(zz[0][1], zz[2][1]), pack2(zz[1][0], zz[3][0]), pack2(zz[1][1], zz[3][1])};
        const u32x4 o1 = {pack2(zz[4][0], zz[6][0]), pack2(zz[4][1], zz[6][1]), pack2(zz[5][0], zz[7][0]), pack2(zz[5][1], zz[7][1])};
        const unsigned zo = WS_Z2F + (unsigned)((c * 7 + w) * 64 + 4 * (R >> 1) + h4) * 1024u + (unsigned)(R & 1) * 512u + (unsigned)n16 * 16u;
        st16(rs, zo, o0);
        st16(rs, zo + 256u, o1);
        SCHED();
      }
      if (ks == 0) { VMW(4); RD_W(3); }
      if (ks == 2) { VMW(0); RD_W(4); }
      if (ks == 8) { VMW(4 + 2); RD_W(5); }
      if (ks == 11) rd_xp();
      if (ks == 13) { VMW(4 + 5 + 2); RD_W(6); }
      if (ks >= 13 && ks < 19) DMA_AB(ks - 13);
      if (ks >= 2 && ks < 6) { DMA_W(5 + ((ks - 2) >> 1), 2 * (ks & 1)); DMA_W(5 + ((ks - 2) >> 1), 2 * (ks & 1) + 1); }
      if (ks >= 6 && ks < 9) LDCR(ks - 6);
      if (ks == 12) { LDCR(3); LDCR(4); }
      if (ks == 10 || ks == 11) { DMA_W(7, 2 * (ks & 1)); DMA_W(7, 2 * (ks & 1) + 1); }
      SCHED();
#pragma unroll
      for (int rt = 0; rt < 4; ++rt) {
        if (ks < 19) A[(ks + 1) & 1][rt] = LDA(ks + 1, rt);
        SCHED();
        acc[rt] = MFMA16(B1[ks], A[ks & 1][rt], ks ? acc[rt] : ZERO4);
        SCHED();
      }
    }
    STAMP(18);
    VMW(2 + 2 + 6);
    RD_W(7);
    const f32x2 c01 = {cs1a[0], cs1a[2]}, b01 = {cs1a[1], cs1a[3]}, c23 = {cs1b[0], cs1b[2]}, b23 = {cs1b[1], cs1b[3]};
    u32x2 g1[4];
    auto z1g = [&](int rt) {
      const float r1 = *(const LAS float*)(tbn + (L_T2 - L_MISC) + rt * 64), n1 = *(const LAS float*)(tbn + (L_T2 - L_MISC) + 256 + rt * 64);
      const f32x2 r2 = {r1, r1}, n2 = {n1, n1};
      const f32x2 a = gelu2(__builtin_elementwise_fma(__builtin_elementwise_fma(n2, c01, LO2(acc[rt])), r2, b01));
      const f32x2 b = gelu2(__builtin_elementwise_fma(__builtin_elementwise_fma(n2, c23, HI2(acc[rt])), r2, b23));
      g1[rt] = (u32x2){pack2(a[0], a[1]), pack2(b[0], b[1])};
      asm volatile("" : "+v"(g1[rt]));
    };
    SCHED();
    VMW(2 + 6);
    BAR();
    STAMP(17);
#pragma unroll
    for (int rt = 0; rt < 4; ++rt) {
      if (rt < 2) { DMA_AB(6 + 2 * rt); DMA_AB(7 + 2 * rt); }
      z1g(rt);
      *(u32x2*)(lds + L_Z1S + ((w * 4 + rt) * 64 + l) * 8) = g1[rt];
    }
  } else {
    __builtin_amdgcn_s_setprio(3);
    DRAIN();
    BAR();
    if (is_conv && l == 0) st4(rs, WS_FX + (unsigned)quad * 128u, tag);
    STAMP(9);
    *(volatile LAS unsigned*)(lds + L_S1 + 256) = ~tag;
    u32x4 gv, cv[2][2][4];
    const unsigned gvo = WS_MOD + (unsigned)(5 * D_ + 80 * c) * 8u + (l < 40 ? l16 : 0u);
    auto ld_consts = [&]() {
      gv = ld16(rs, gvo);
#pragma unroll
      for (int i = 0; i < 16; ++i) cv[i >> 3][(i >> 2) & 1][i & 3] = ld16(rs, l < 56 ? l16 : 0u, WS_CS + (unsigned)(((c + 8 * (i >> 3)) * 4 + (i & 3)) * 112 + 56 * ((i >> 2) & 1)) * 16u);
    };
    auto ok_consts = [&]() {
      unsigned x = (gv[0] ^ tag) | (gv[2] ^ tag);
#pragma unroll
      for (int i = 0; i < 16; ++i) x |= (cv[i >> 3][(i >> 2) & 1][i & 3][0] ^ tag) | (cv[i >> 3][(i >> 2) & 1][i & 3][2] ^ tag);
      asm volatile("" : "+v"(x));
      return __all(x == 0u);
    };
    {
#pragma unroll
      for (int k = 0; k < 3; ++k) cpf ^= ld4(rs, k * 64 + l < 140 ? (unsigned)l * 128u : OOB, WS_WINF + (unsigned)c * WBLK + (unsigned)(idx & 7) * 17920u + (unsigned)k * 8192u);
    }
    park();
    __builtin_amdgcn_s_barrier();
    ld_consts();
    STAMP(31);
    _Pragma("unroll 1") for (unsigned spins = 0; spins < SPIN_CAP; ++spins) {
      const unsigned f0 = ld4(rs, WS_FX + (unsigned)l * 128u), f1 = ld4(rs, WS_FX + 8192u + (unsigned)l * 128u),
                     f2 = ld4(rs, l < 32 ? WS_FX + 16384u + (unsigned)l * 128u : WS_FW1 + (unsigned)(c * 32 + (l < 60 ? l - 32 : 0)) * 128u);
      if (__all(f0 == tag && f1 == tag && f2 == tag)) break;
      __builtin_amdgcn_s_sleep(POLL_SLEEP);
    }
    BAR();
    STAMP(10);
    ld_x1();
    __builtin_amdgcn_s_setprio(3);
    if (!ok_consts()) {
      _Pragma("unroll 1") for (unsigned spins = 0; spins < SPIN_CAP; ++spins) {
        ld_consts();
        if (ok_consts()) break;
        __builtin_amdgcn_s_sleep(8);
      }
    }
    if (l < 40) *(f32x2*)(lds + L_GB + l * 8) = (f32x2){__uint_as_float(gv[1]), __uint_as_float(gv[3])};
    if (l < 56) {
#pragma unroll
      for (int i = 0; i < 4; ++i) {
        const u32x4* q4 = cv[i >> 1][i & 1];
        *(f32x2*)(lds + L_CS1 + (56 * i + l) * 8) =
            (f32x2){(__uint_as_float(q4[0][1]) + __uint_as_float(q4[1][1])) + (__uint_as_float(q4[2][1]) + __uint_as_float(q4[3][1])),
                    (__uint_as_float(q4[0][3]) + __uint_as_float(q4[1][3])) + (__uint_as_float(q4[2][3]) + __uint_as_float(q4[3][3]))};
      }
    }
    SCHED();
#pragma unroll
    for (int m = 0; m < 3; ++m) put_x1(m);
    BAR();
    f32x4 acc[4];
    u32x4 A[2][4];
    lda1(0, A[0]);
    auto touch_w = [&](int k) {
      cpf ^= __builtin_amdgcn_raw_buffer_load_b32(rsp, (unsigned)(h4 * 4096 + (n16 >> 1) * 512 + (n16 & 1) * 128),
                                                  (unsigned)(R * 64 + (c & 3) * 16 + k * 4) * 4096u + 256u, 0);
    };
#pragma unroll
    for (int ks = 0; ks < 20; ++ks) {
      if (ks == 6) put_x1(3);
      if (ks == 8) put_x1(4);
      if (ks == 11) BAR();
      if (ks == 4) RD_W(2);
      if (ks >= DK && ks < DK + 8) DMA_W(3 + ((ks - DK) >> 2), (ks - DK) & 3);
      if (!ET && ks >= TK && ks < TK + 4) touch_w(ks - TK);
      if (ks < 19) lda1(ks + 1, A[(ks + 1) & 1]);
      SCHED();
#pragma unroll
      for (int rt = 0; rt < 4; ++rt) acc[rt] = MFMA16(A[ks & 1][rt], A[ks & 1][rt], ks ? acc[rt] : ZERO4);
      SCHED();
    }
    {
      if ((n16 >> 2) == h4) {
#pragma unroll
        for (int rt = 0; rt < 4; ++rt) {
          const float lo = (l & 1) ? acc[rt][1] : acc[rt][0], hi = (l & 1) ? acc[rt][3] : acc[rt][2];
          *(volatile LAS float*)(tbn + (L_T2 - L_MISC) + rt * 64) = (l & 2) ? hi : lo;
        }
      }
      const float s2 = *(volatile LAS float*)(lds + L_T2 + l * 4) * (1.0f / D_);
      float s1;
      _Pragma("unroll 1") for (unsigned spins = 0; spins < SPIN_CAP * 16; ++spins) {
        const unsigned f = *(volatile LAS unsigned*)(lds + L_S1 + 256);
        s1 = *(volatile LAS float*)(lds + L_S1 + l * 4);
        if (f == tag) break;
      }
      const float nm = s1 * (-1.0f / D_);
      *(float*)(lds + L_T2 + l * 4) = __builtin_amdgcn_rsqf(__builtin_fmaxf(s2 - nm * nm, 0.f) + 1e-5f);
      *(float*)(lds + L_T2 + 256 + l * 4) = nm;
    }
    if (TK == 20) { touch_w(0); touch_w(1); touch_w(2); touch_w(3); }
    BAR();
    if (TK == 21) { touch_w(0); touch_w(1); touch_w(2); touch_w(3); }
    __builtin_amdgcn_s_setprio(0);
    asm volatile("" ::"v"(cpf));
    DRAIN();
    RD_W(3); RD_W(4);
    rd_xp();
    asm volatile("s_waitcnt lgkmcnt(0)" ::: "memory");
#pragma unroll
    for (int i = 0; i < 8; ++i) DMA_W(5 + (i >> 2), i & 3);
    const unsigned e_fw2 = ld4(rs, WS_FW2 + (unsigned)(c * 8 + (l & 7)) * 128u);
    if (!__all(e_fw2 == tag)) wait_flag<2>(rs, WS_FW2 + (unsigned)(c * 8 + (l & 7)) * 128u, tag);
    __builtin_amdgcn_s_setprio(3);
    BAR();
    {
      f32x2 t = {0.f, 0.f};
#pragma unroll
      for (int k = 0; k < 7; ++k) t += *(const f32x2*)(lds + L_Z1S + (k * 64 + l) * 8);
      st16(rs, WS_PART + (unsigned)(R * 8 + c) * 1024u + l16, (u32x4){tag, __float_as_uint(t[0]), tag, __float_as_uint(t[1])});
    }
    STAMP(14);
    {
      u32x4 v[8];
      const unsigned po = WS_PART + (unsigned)R * 8192u + l16;
      __builtin_amdgcn_s_sleep(16);
      _Pragma("unroll 1") for (unsigned spins = 0; spins < SPIN_CAP; ++spins) {
#pragma unroll
        for (int k = 0; k < 8; ++k) v[k] = ld16(rs, po + (unsigned)k * 1024u);
        unsigned x = 0;
#pragma unroll
        for (int k = 0; k < 8; ++k) x |= (v[k][0] ^ tag) | (v[k][2] ^ tag);
        asm volatile("" : "+v"(x));
        if (__all(x == 0u)) break;
        __builtin_amdgcn_s_sleep(1);
      }
      float t1 = 0.f, t2 = 0.f;
#pragma unroll
      for (int k = 0; k < 8; ++k) { t1 += __uint_as_float(v[k][1]); t2 += __uint_as_float(v[k][3]); }
      const float mean = t1 * (1.0f / FH);
      const float var = __builtin_fmaxf(t2 * (1.0f / FH) - mean * mean, 0.f);
      const float rstd = __builtin_amdgcn_rsqf(var + 1e-5f);
      *(float*)(lds + L_ST2 + l * 4) = rstd;
      *(float*)(lds + L_ST2 + 256 + l * 4) = -mean * rstd;
    }
    STAMP(15);
    BAR();
    RD_W(5); RD_W(6);
    asm volatile("s_waitcnt lgkmcnt(0)" ::: "memory");
#pragma unroll
    for (int q = 0; q < 4; ++q) DMA_W(7, q);
    DRAIN();
    RD_W(7);
    BAR();
    if (l == 0) st4(rs, WS_FZ + (unsigned)(c * 32 + R) * 128u, tag);
    STAMP(17);
    __builtin_amdgcn_s_setprio(0);
#pragma unroll
    for (int k = 0; k < 10; ++k) DMA_AB(k);
#pragma unroll
    for (int k = 0; k < 5; ++k) LDCR(k);
  }
  const rsrc_t rout = __builtin_amdgcn_make_buffer_rsrc((void*)p.out, 0, S_ * D_ * 2, 0x00020000);
  STAMP(19);
  u32x2 z1o[4];
  wait_flag<POLL_SLEEP>(rs, fzo, tag);
#pragma unroll
  for (int i = 0; i < W; ++i) { LDZ(i); SCHED(); }
  STAMP(20);

  const unsigned ub = WS_UF + (unsigned)((R * 4 + rto) * 8 + c) * 3584u;
  const float bs = bflo(bs_raw);
  const unsigned ur = WS_UF + (unsigned)((R * 4 + rto) * 8 + cls) * 3584u;
  u32x4 uf[4][2];
  f32x4 acc[2][4];
  f32x4* const bufw = (f32x4*)(lds + L_BUF) + w * 256 + l;
#define BUFW(slot, rt) bufw[(slot) * 2048 + (rt) * 64]
  {
    const f32x4* const rb = (const f32x4*)(lds + L_BUF) + cls * 2048 + rto * 64 + l;
    f32x4 rdv[4], s;
    u32x4 uo;
#pragma unroll
    for (int j = 0; j < 7; ++j) {
#pragma unroll
      for (int ks = 0; ks < 8; ++ks) {
        const int i = 8 * j + ks;
#pragma unroll
        for (int rt = 0; rt < 4; ++rt) acc[j & 1][rt] = MFMA16(fr[i % W], wsp[rt][ks], ks ? acc[j & 1][rt] : ZERO4);
        if (i == 0) STAMP(21);
        if (i == 24) STAMP(22);
        SCHED();
        if (i + W < 56) LDZ(i + W);
        if (j > 0) {
          if (ks == 0 && (j & 1)) BAR();
          if (j == 1 && ks == 0) {
#pragma unroll
            for (int k = 0; k < 4; ++k) z1o[k] = *(const u32x2*)(lds + L_Z1S + (((k < 3 ? 2 * k + cls : 6) * 4 + rto) * 64 + l) * 8);
          }
          if (i >= (56 - G2W) && i < 53) {
#pragma unroll
            for (int k = 0; k < 8; ++k)
              if ((56 - G2W) + k * (53 - (56 - G2W)) / 8 == i) uf[k >> 1][k & 1] = ld16(rs, l16, ur + (unsigned)(k >> 1) * 7168u + (unsigned)(k & 1) * 1024u);
            SCHED();
          }
          const bool fast = j == 6;
          if (!fast && ks < 4) BUFW((j - 1) & 1, ks) = acc[(j - 1) & 1][ks];
          if (fast && ks < 2) { BUFW(1, 2 * ks) = acc[1][2 * ks]; BUFW(1, 2 * ks + 1) = acc[1][2 * ks + 1]; }
          if (!(j & 1)) {
            const int k0 = fast ? 2 : 5;
            if (ks == k0) {
              BAR();
#pragma unroll
              for (int k = 0; k < 4; ++k) rdv[k] = rb[k * 256];
            }
            if (ks == k0 + 1) {
              s = (rdv[0] + rdv[1]) + (rdv[2] + rdv[3]);
#pragma unroll
              for (int k = 0; k < 4; ++k) rdv[k] = rb[(4 + k) * 256];
            }
            if (ks == k0 + 2) {
              s += (rdv[0] + rdv[1]) + (rdv[2] + rdv[3]);
              const u32x2 z = z1o[(j >> 1) - 1];
              const int e = j == 4 ? 2 : 0;
              uo[e] = pack2(bflo(z[0]) * (s[0] + bs), bfhi(z[0]) * (s[1] + bs));
              uo[e + 1] = pack2(bflo(z[1]) * (s[2] + bs), bfhi(z[1]) * (s[3] + bs));
              if (j == 4) st16(rs, ub + (unsigned)cls * 1024u + l16, uo);
              if (j == 6) st16(rs, l < 32 ? ub + 2048u + (unsigned)cls * 512u + l16 : OOB, half_swap((u32x4){uo[0], uo[1], uo[0], uo[1]}));
            }
            if (j == 6 && ks == 4) BAR();
            if (j == 6 && ks >= 4 && ks < 7) {
#pragma unroll
              for (int k = 0; k < 5; ++k)
                if (k / 2 == ks - 4) *(u32x4*)(lds + (w < 6 ? L_BUF + 32768 + w * 5120 : L_Z1S + (w - 6) * 5120) + k * 1024 + l16) = cr[k];
            }
          }
        }
        SCHED();
      }
    }
  }
  STAMP(23);

  {
    u32x4 uc[4], uh[4];
    const unsigned uho = (unsigned)(l >> 5) * 3584u + (unsigned)(l & 31) * 16u;
#define LD_C(cc) uc[cc] = ld16(rs, l16, ur + (unsigned)(cc) * 7168u + 2048u)
#define LD_H(cc) uh[cc] = ld16(rs, uho, WS_UF + (unsigned)((R * 4 + rto) * 8) * 3584u + (unsigned)(cc) * 7168u + 3072u)
    auto mx = [](unsigned a, unsigned b) { return __builtin_elementwise_max(a, b); };
    auto ok4 = [&](const u32x4* v, int n) {
      unsigned m = 0;
#pragma unroll
      for (int i = 0; i < 8; ++i)
        if (i < n) m = mx(m, mx(mx(v[i][0], v[i][1]), mx(v[i][2], v[i][3])));
      return __all(m != ~0u);
    };
#pragma unroll
    for (int rt = 0; rt < 4; ++rt) BUFW(0, rt) = acc[0][rt];
    BAR();
    f32x4 a3[5];
    u32x4 af[2][5];
    const LAS char* wb[2] = {(const LAS char*)(lds + L_AB) + cls * (10240 + 3072) + l16, (const LAS char*)(lds + L_AB) + cls * (10240 - 2048) + l16};
    const LAS char* wb2[2] = {wb[0] + 40960, wb[1] + 40960};
    asm volatile("" : "+v"(wb[0]), "+v"(wb2[0]), "+v"(wb[1]), "+v"(wb2[1]));
    u32x4 ho = {0u, 0u, 0u, 0u};
    if (w < 4) {
      __builtin_amdgcn_s_setprio(2);
      const f32x4* const rb = (const f32x4*)(lds + L_BUF) + rto * 64 + l;
      f32x4 r8[8];
#pragma unroll
      for (int k = 0; k < 8; ++k) r8[k] = rb[k * 256];
      const f32x4 s = ((r8[0] + r8[1]) + (r8[2] + r8[3])) + ((r8[4] + r8[5]) + (r8[6] + r8[7]));
      const u32x2 z = z1o[3];
      const unsigned o0 = pack2(bflo(z[0]) * (s[0] + bs), bfhi(z[0]) * (s[1] + bs)), o1 = pack2(bflo(z[1]) * (s[2] + bs), bfhi(z[1]) * (s[3] + bs));
      ho = half_swap((u32x4){o0, o1, o0, o1});
    }
    st16(rs, tid < 256 && l < 32 ? ub + 3072u + l16 : OOB, ho);
    SCHED();
    __builtin_amdgcn_s_setprio(0);
#pragma unroll
    for (int dj = 0; dj < 5; ++dj) af[0][dj] = *(const LAS u32x4*)(wb[dj >= 2] + dj * 1024);
    const bool ab_ok = ok4(&uf[0][0], 8);
    STAMP(24);
    if (!ab_ok) {
      _Pragma("unroll 1") for (unsigned spins = 0; spins < SPIN_CAP; ++spins) {
#pragma unroll
        for (int i = 0; i < 8; ++i) uf[i >> 1][i & 1] = ld16(rs, l16, ur + (unsigned)(i >> 1) * 7168u + (unsigned)(i & 1) * 1024u);
        if (ok4(&uf[0][0], 8)) break;
        __builtin_amdgcn_s_sleep(1);
      }
    }
    STAMP(25);
    const LAS char* cb0[2] = {(const LAS char*)(lds + L_BUF + 32768) + cls * (5120 + 3072) + l16, (const LAS char*)(lds + L_BUF + 32768) + cls * (5120 - 2048) + l16};
    u32x4 ah[4][3];
    const LAS char* hb = (const LAS char*)(lds + L_BUF) + cls * 12288 + l * 8;
    f32x4 gb[3], xo3[3], xf[3];
    LAS char *otw, *xw3;
    const LAS char *ota, *otb, *xr3;
    unsigned oa_o, ob_o;
#pragma unroll
    for (int st = 0; st < 12; ++st) {
      if (st < 2) { LD_C(2 * st); LD_C(2 * st + 1); }
      else if (st < 7 && w < 4) dma1k(rs, lds + L_BUF + ((st - 2) * 4 + w) * 1024, l16, wob + 122880u + (unsigned)(w * 5 + st - 2) * 1024u);
      if (st == 2) {
        LAS char* ot = (LAS char*)(lds + L_AB + 32768) + w * 2048;
        otw = ot + n16 * 112 + h4 * 8;
        ota = ot + (l >> 2) * 112 + (l & 3) * 16;
        otb = ot + ((l >> 1) & 15) * 112 + 64 + (l & 1) * 16;
        asm volatile("" : "+v"(otw), "+v"(ota), "+v"(otb));
      }
      if (st == 3) {
        oa_o = (unsigned)(l >> 2) * 2560u + (unsigned)(l & 3) * 16u;
        ob_o = l < 32 ? (unsigned)(l >> 1) * 2560u + 64u + (unsigned)(l & 1) * 16u : OOB;
        xw3 = (LAS char*)(lds + L_AB) + w * 3072 + l16;
        xr3 = (const LAS char*)(lds + L_AB) + (w ^ 4) * 3072 + cls * 1024 + l16;
        asm volatile("" : "+v"(oa_o), "+v"(ob_o), "+v"(xw3), "+v"(xr3));
      }
      if (st >= 4 && st < 7) {
        const int j = st - 4;
        gb[j] = *(const f32x4*)(lds + L_GB + (48 * cls + 16 * (cls && j == 2 ? 1 : j) + 4 * h4) * 4);
        xf[j] = (f32x4){bflo(xp[j][0]), bfhi(xp[j][0]), bflo(xp[j][1]), bfhi(xp[j][1])};
        asm volatile("" : "+v"(xf[j]));
      }
      if (st == 8) {
        STAMP(26);
        if (!ok4(uc, 4)) {
          _Pragma("unroll 1") for (unsigned spins = 0; spins < SPIN_CAP; ++spins) {
            LD_C(0); LD_C(1); LD_C(2); LD_C(3);
            if (ok4(uc, 4)) break;
            __builtin_amdgcn_s_sleep(1);
          }
        }
        LD_H(0); LD_H(1); LD_H(2); LD_H(3);
#pragma unroll
        for (int cc = 0; cc < 4; ++cc) uc[cc] = half_swap(uc[cc]);
        VMW(4);
        __builtin_amdgcn_s_barrier();
        asm volatile("" ::: "memory");
        STAMP(27);
      }
      SCHED();
#pragma unroll
      for (int dj = 0; dj < 5; ++dj) {
        const int sn = st + 1;
        if (sn < 8) af[sn & 1][dj] = *(const LAS u32x4*)((sn < 4 ? wb : wb2)[dj >= 2] + ((sn >> 1) & 1) * 20480 + ((sn & 1) * 5 + dj) * 1024);
        else if (sn < 12) af[sn & 1][dj] = *(const LAS u32x4*)(cb0[dj >= 2] + (sn < 11 ? (sn - 8) * 10240 : L_Z1S - L_BUF - 32768) + dj * 1024);
        if (st >= 8) {
#pragma unroll
          for (int k = 0; k < 2; ++k) {
            const int h = st < 11 ? (k ? 99 : (st - 8) * 5 + dj) : 15 + 2 * dj + k;
            if (h < 24) {
              const u32x2 v = *(const volatile LAS u32x2*)(hb + (((h >> 1) % 3) * 4 + (h >> 1) / 3) * 1024 + (h & 1) * 512);
              ah[(h >> 1) / 3][(h >> 1) % 3][2 * (h & 1)] = v[0]; ah[(h >> 1) / 3][(h >> 1) % 3][2 * (h & 1) + 1] = v[1];
            }
          }
        }
        SCHED();
        a3[dj] = MFMA16(af[st & 1][dj], st < 8 ? uf[(st >> 1) & 3][st & 1] : uc[st & 3], st ? a3[dj] : ZERO4);
        SCHED();
      }
    }
#pragma unroll
    for (int j = 0; j < 3; ++j) *(volatile LAS f32x4*)(xw3 + j * 1024) = a3[2 + j];
    BAR();
#pragma unroll
    for (int j = 0; j < 3; ++j) xo3[j] = *(const volatile LAS f32x4*)(xr3 + j * 1024);
    SCHED();
    if (!ok4(uh, 4)) {
      _Pragma("unroll 1") for (unsigned spins = 0; spins < SPIN_CAP; ++spins) {
        LD_H(0); LD_H(1); LD_H(2); LD_H(3);
        if (ok4(uh, 4)) break;
        __builtin_amdgcn_s_sleep(1);
      }
    }
#pragma unroll
    for (int cc = 0; cc < 4; ++cc) {
      const u32x4 b = half_swap(uh[cc]);
#pragma unroll
      for (int j = 0; j < 3; ++j) a3[j] = MFMA16(ah[cc][j], b, a3[j]);
      if (cc >= 1) {
        const int j = cc - 1;
        xo3[j] = __builtin_elementwise_fma(gb[j], xo3[j], xf[j]);
        asm volatile("" : "+v"(xo3[j]));
      }
      SCHED();
    }
#pragma unroll
    for (int j = 0; j < 3; ++j) {
      const f32x4 r = __builtin_elementwise_fma(gb[j], a3[j], xo3[j]);
      *(volatile LAS u32x2*)(otw + j * 32) = (u32x2){pack2(r[0], r[1]), pack2(r[2], r[3])};
    }
    const u32x4 oa = *(const volatile LAS u32x4*)(ota);
    const u32x4 ob = *(const volatile LAS u32x4*)(otb);
    const unsigned obase = (unsigned)((R * 64 + 32 * ((w >> 1) & 1) + (w & 1)) * D_ + 80 * c + 48 * cls) * 2u;
    __builtin_amdgcn_raw_buffer_store_b128(oa, rout, oa_o, obase, OAUX);
    if (cls == 0) __builtin_amdgcn_raw_buffer_store_b128(ob, rout, ob_o, obase, OAUX);
    if (bid == 0 && tid == 0) st4(rs, WS_EPOCH, tag);
  }
  STAMP(30);
  asm volatile(".Lmega_end:");
}

extern "C" void kernel_launch(void* const* d_in, const int* in_sizes, int n_in, void* d_out, int out_size,
                              void* d_ws, size_t ws_size, hipStream_t stream) {
  Params p;
  p.q = (const u16*)d_in[0];
  p.k = (const u16*)d_in[1];
  p.v = (const u16*)d_in[2];
  p.shift_k = (const u16*)d_in[3];
  p.ssm_k = (const u16*)d_in[4];
  p.ssm_D = (const u16*)d_in[5];
  p.w_in = (const u16*)d_in[7];
  p.w_sp = (const u16*)d_in[8];
  p.b_sp = (const u16*)d_in[9];
  p.w_out = (const u16*)d_in[10];
  p.cvec = (const u16*)d_in[11];
  p.w_mod = (const u16*)d_in[12];
  p.out = (u16*)d_out;
  p.ws = (char*)d_ws;
  mega<<<256, 512, 0, stream>>>(p);
}
```

```cpp
#include <hip/hip_runtime.h>
#include <stdint.h>

typedef unsigned short u16;
typedef __attribute__((ext_vector_type(8))) short bf16x8;
typedef __attribute__((ext_vector_type(4))) float f32x4;
typedef __attribute__((ext_vector_type(2))) float f32x2;
typedef __attribute__((ext_vector_type(4))) unsigned int u32x4;
typedef __attribute__((ext_vector_type(2))) unsigned int u32x2;
#define LAS __attribute__((address_space(3)))
#define CAS __attribute__((address_space(4)))
#define GAS __attribute__((address_space(1)))
typedef __amdgpu_buffer_rsrc_t rsrc_t;
typedef __attribute__((ext_vector_type(4))) short s16x4;

constexpr int S_ = 2048, D_ = 640, F_ = 1728, FH = 864, CB = 108;

#define X1PAD 256
constexpr unsigned WS_EPOCH = 0;
constexpr unsigned WS_FX = 4096;
constexpr unsigned WS_FZ = WS_FX + 24576;
constexpr unsigned WS_FW2 = WS_FZ + 32768;
constexpr unsigned WS_FW1 = WS_FW2 + 8192;
constexpr unsigned WS_CS = WS_FW1 + 65536;
constexpr unsigned WS_PART = WS_CS + 131072;
constexpr unsigned WS_MOD = WS_PART + 262144;
constexpr unsigned WS_ST1 = WS_MOD + 32768;
constexpr unsigned WS_X1B = WS_ST1 + 16384;
constexpr unsigned WX1P = 40960 + X1PAD;
constexpr unsigned WBLK = 143360;
constexpr unsigned WS_QB = WS_X1B + 2686976;
constexpr unsigned WS_WINF = WS_QB + 2686976;
constexpr unsigned WS_WOUTF = WS_WINF + 16 * WBLK;
constexpr unsigned WS_Z2F = WS_WOUTF + 8 * WBLK;
constexpr unsigned WS_UF = WS_Z2F + 8 * 64 * 7168;
constexpr unsigned WS_KVT = WS_UF + 32 * 4 * 8 * 3584;
constexpr unsigned WS_FK = WS_KVT + 32 * 81920;
constexpr unsigned WS_STAMP = WS_FK + 20480;
constexpr unsigned WS_END = WS_STAMP + 256 * 40 * 8;

constexpr int U_BYTES = (256 + S_) * 2;
constexpr int G_COPY = 4160;
constexpr int CH_BYTES = U_BYTES + 4 * G_COPY;
constexpr int L_YP = 4 * CH_BYTES;
constexpr int L_A0 = 98304;
constexpr int L_RED = 0;
constexpr int L_TAB = 2048;
constexpr int L_J3 = 8192;
constexpr int L_QT = 49152;
constexpr int L_J5 = L_QT;
constexpr int L_XI = 0;
constexpr int L_BUF = 81920;
constexpr int L_AB = 0;
constexpr int L_MISC = 147456;
constexpr int L_Z1S = L_MISC;
constexpr int L_S1 = L_MISC + 14336;
constexpr int L_T2 = L_S1 + 512;
constexpr int L_ST2 = L_T2 + 512;
constexpr int L_GB = L_ST2 + 512;
constexpr int L_CS1 = L_Z1S + 4096;
constexpr int L_WS = 81920;
constexpr int LDS_TOTAL = 163840;

constexpr int POLL_SLEEP = 1;
constexpr int AUX_SLEEP = 36, LATE_SLEEP = 96;
constexpr int CDIST = 4;
constexpr int NRAW = 12;
constexpr int RAW2 = 40;
constexpr int SV_AT = 96, Q_AT = 123;
constexpr int NB2A = 6;
constexpr int G2W = 7;
constexpr int Z1SPLIT = 12;
constexpr int SC1 = 16;
constexpr unsigned SPIN_CAP = 1u << 16;
constexpr unsigned OOB = 0x80000000u;

__device__ __forceinline__ float bflo(unsigned x) { return __uint_as_float(x << 16); }
__device__ __forceinline__ float bfhi(unsigned x) { return __uint_as_float(x & 0xffff0000u); }
__device__ __forceinline__ f32x2 bf2(unsigned x) { return (f32x2){bflo(x), bfhi(x)}; }
__device__ __forceinline__ unsigned f2bf(float x) {
  __bf16 b = (__bf16)x;
  return (unsigned)__builtin_bit_cast(unsigned short, b);
}
__device__ __forceinline__ float rbf(float x) { return bflo(f2bf(x)); }
typedef __attribute__((ext_vector_type(2))) __bf16 bf2_t;
__device__ __forceinline__ unsigned pack2(float lo, float hi) {
  f32x2 v = {lo, hi};
  return __builtin_bit_cast(unsigned, __builtin_convertvector(v, bf2_t));
}
template <int CTRL>
__device__ __forceinline__ float dpp_f(float v) {
  return __int_as_float(__builtin_amdgcn_update_dpp(0, __float_as_int(v), CTRL, 0xf, 0xf, true));
}
__device__ __forceinline__ float sum_rows(float v) {
  auto a = __builtin_amdgcn_permlane16_swap(__float_as_uint(v), __float_as_uint(v), false, false);
  v = __uint_as_float(a[0]) + __uint_as_float(a[1]);
  auto b = __builtin_amdgcn_permlane32_swap(__float_as_uint(v), __float_as_uint(v), false, false);
  return __uint_as_float(b[0]) + __uint_as_float(b[1]);
}
__device__ __forceinline__ f32x2 gelu2(f32x2 x) {
  const float c0 = -2.0f * 0.7978845608028654f * 1.4426950408889634f;
  const float c1 = c0 * 0.044715f;
  const f32x2 t = x * __builtin_elementwise_fma(x * x, (f32x2){c1, c1}, (f32x2){c0, c0});
  const f32x2 d = (f32x2){__builtin_amdgcn_exp2f(t[0]), __builtin_amdgcn_exp2f(t[1])} + (f32x2){1.0f, 1.0f};
  return x * (f32x2){__builtin_amdgcn_rcpf(d[0]), __builtin_amdgcn_rcpf(d[1])};
}
#define LO2(v) __builtin_shufflevector(v, v, 0, 1)
#define HI2(v) __builtin_shufflevector(v, v, 2, 3)
__device__ __forceinline__ float tr_sum16(const float* v, int l) {
  float x[8], y[4];
  asm volatile(
      "s_nop 1\n\t"
      "v_add_f32_dpp %0, %8, %8 row_ror:8 row_mask:0xf bank_mask:0x3\n\t"
      "v_add_f32_dpp %1, %9, %9 row_ror:8 row_mask:0xf bank_mask:0x3\n\t"
      "v_add_f32_dpp %2, %10, %10 row_ror:8 row_mask:0xf bank_mask:0x3\n\t"
      "v_add_f32_dpp %3, %11, %11 row_ror:8 row_mask:0xf bank_mask:0x3\n\t"
      "v_add_f32_dpp %4, %12, %12 row_ror:8 row_mask:0xf bank_mask:0x3\n\t"
      "v_add_f32_dpp %5, %13, %13 row_ror:8 row_mask:0xf bank_mask:0x3\n\t"
      "v_add_f32_dpp %6, %14, %14 row_ror:8 row_mask:0xf bank_mask:0x3\n\t"
      "v_add_f32_dpp %7, %15, %15 row_ror:8 row_mask:0xf bank_mask:0x3\n\t"
      "v_add_f32_dpp %0, %16, %16 row_ror:8 row_mask:0xf bank_mask:0xc\n\t"
      "v_add_f32_dpp %1, %17, %17 row_ror:8 row_mask:0xf bank_mask:0xc\n\t"
      "v_add_f32_dpp %2, %18, %18 row_ror:8 row_mask:0xf bank_mask:0xc\n\t"
      "v_add_f32_dpp %3, %19, %19 row_ror:8 row_mask:0xf bank_mask:0xc\n\t"
      "v_add_f32_dpp %4, %20, %20 row_ror:8 row_mask:0xf bank_mask:0xc\n\t"
      "v_add_f32_dpp %5, %21, %21 row_ror:8 row_mask:0xf bank_mask:0xc\n\t"
      "v_add_f32_dpp %6, %22, %22 row_ror:8 row_mask:0xf bank_mask:0xc\n\t"
      "v_add_f32_dpp %7, %23, %23 row_ror:8 row_mask:0xf bank_mask:0xc"
      : "=&v"(x[0]), "=&v"(x[1]), "=&v"(x[2]), "=&v"(x[3]), "=&v"(x[4]), "=&v"(x[5]), "=&v"(x[6]), "=&v"(x[7])
      : "v"(v[0]), "v"(v[1]), "v"(v[2]), "v"(v[3]), "v"(v[4]), "v"(v[5]), "v"(v[6]), "v"(v[7]), "v"(v[8]), "v"(v[9]), "v"(v[10]),
        "v"(v[11]), "v"(v[12]), "v"(v[13]), "v"(v[14]), "v"(v[15]));
  asm volatile(
      "s_nop 1\n\t"
      "v_add_f32_dpp %0, %4, %4 row_shl:4 row_mask:0xf bank_mask:0x5\n\t"
      "v_add_f32_dpp %1, %5, %5 row_shl:4 row_mask:0xf bank_mask:0x5\n\t"
      "v_add_f32_dpp %2, %6, %6 row_shl:4 row_mask:0xf bank_mask:0x5\n\t"
      "v_add_f32_dpp %3, %7, %7 row_shl:4 row_mask:0xf bank_mask:0x5\n\t"
      "v_add_f32_dpp %0, %8, %8 row_shr:4 row_mask:0xf bank_mask:0xa\n\t"
      "v_add_f32_dpp %1, %9, %9 row_shr:4 row_mask:0xf bank_mask:0xa\n\t"
      "v_add_f32_dpp %2, %10, %10 row_shr:4 row_mask:0xf bank_mask:0xa\n\t"
      "v_add_f32_dpp %3, %11, %11 row_shr:4 row_mask:0xf bank_mask:0xa\n\t"
      "s_nop 1"
      : "=&v"(y[0]), "=&v"(y[1]), "=&v"(y[2]), "=&v"(y[3])
      : "v"(x[0]), "v"(x[1]), "v"(x[2]), "v"(x[3]), "v"(x[4]), "v"(x[5]), "v"(x[6]), "v"(x[7]));
  const bool b1 = l & 2, b0 = l & 1;
  const float z0 = (b1 ? y[2] : y[0]) + dpp_f<0x4E>(b1 ? y[0] : y[2]);
  const float z1 = (b1 ? y[3] : y[1]) + dpp_f<0x4E>(b1 ? y[1] : y[3]);
  return (b0 ? z1 : z0) + dpp_f<0xB1>(b0 ? z0 : z1);
}
__device__ __forceinline__ u32x4 half_swap(u32x4 v) {
  const auto a = __builtin_amdgcn_permlane32_swap(v[0], v[2], false, false);
  const auto b = __builtin_amdgcn_permlane32_swap(v[1], v[3], false, false);
  return (u32x4){a[0], b[0], a[1], b[1]};
}
#define MFMA16(a, b, c) __builtin_amdgcn_mfma_f32_16x16x32_bf16(__builtin_bit_cast(bf16x8, a), __builtin_bit_cast(bf16x8, b), c, 0, 0, 0)
__device__ __forceinline__ u32x2 trd(const char* p) {
  return __builtin_bit_cast(u32x2, __builtin_amdgcn_ds_read_tr16_b64_v4i16((LAS s16x4*)p));
}
__device__ __forceinline__ u32x4 trfrag(const char* p, int second) {
  u32x2 a = trd(p), b = trd(p + second);
  return (u32x4){a[0], a[1], b[0], b[1]};
}
#define BAR() asm volatile("s_waitcnt lgkmcnt(0)\n\ts_barrier" ::: "memory")
#define DRAIN() do { __builtin_amdgcn_s_waitcnt(0x0F70); asm volatile("" ::: "memory"); } while (0)
#define VMW(n) do { __builtin_amdgcn_s_waitcnt(0x0F70 | ((n) & 15) | (((n) >> 4) << 14)); asm volatile("" ::: "memory"); } while (0)
#define SCHED() __builtin_amdgcn_sched_barrier(0)
#define ZERO4 ((f32x4){0.f, 0.f, 0.f, 0.f})

__device__ __forceinline__ u32x4 ld16(rsrc_t r, unsigned off, unsigned so = 0) { return __builtin_amdgcn_raw_buffer_load_b128(r, off, so, SC1); }
__device__ __forceinline__ u32x2 ld8(rsrc_t r, unsigned off, unsigned so = 0) { return __builtin_amdgcn_raw_buffer_load_b64(r, off, so, SC1); }
__device__ __forceinline__ unsigned ld4(rsrc_t r, unsigned off, unsigned so = 0) { return __builtin_amdgcn_raw_buffer_load_b32(r, off, so, SC1); }
__device__ __forceinline__ void st16(rsrc_t r, unsigned off, u32x4 v) { __builtin_amdgcn_raw_buffer_store_b128(v, r, off, 0, SC1); }
__device__ __forceinline__ void st8(rsrc_t r, unsigned off, u32x2 v) { __builtin_amdgcn_raw_buffer_store_b64(v, r, off, 0, SC1); }
__device__ __forceinline__ void st4(rsrc_t r, unsigned off, unsigned v) { __builtin_amdgcn_raw_buffer_store_b32(v, r, off, 0, SC1); }
__device__ __forceinline__ void dma1k(rsrc_t r, char* lds_dst, unsigned l16, unsigned soff) {
  __builtin_amdgcn_raw_ptr_buffer_load_lds(r, (LAS void*)lds_dst, 16, l16, soff, 0, SC1);
}

#define STAMP(i)

template <int SLEEP>
__device__ __forceinline__ void wait_flag(rsrc_t r, unsigned off, unsigned tag) {
  _Pragma("unroll 1") for (unsigned spins = 0; spins < SPIN_CAP; ++spins) {
    if (__all(ld4(r, off) == tag)) break;
    __builtin_amdgcn_s_sleep(SLEEP);
  }
  asm volatile("" ::: "memory");
}

struct ConvSched {
  signed char pp[144], m[144], rd[144];
  int pre;
};
constexpr ConvSched conv_sched(int dist) {
  ConvSched s = {};
  int need[96] = {}, i = 0;
  for (int r = 0; r < 4; ++r)
    for (int pp = 0; pp < 8; ++pp) {
      const int j = 24 * r + (pp ? 8 + 2 * pp : 0);
      need[j] = need[j + 1] = i;
      for (int m = 7 - pp; m >= 0; --m) {
        if (pp == 0) need[24 * r + 9 - m] = i;
        s.pp[i] = pp; s.m[i] = m; s.rd[i] = -1;
        ++i;
      }
    }
  int next = 144;
  for (int j = 95; j >= 0; --j) {
    next = need[j] - dist < next - 1 ? need[j] - dist : next - 1;
    if (next >= 0) s.rd[next] = j;
    else if (s.pre == 0) s.pre = j + 1;
  }
  return s;
}

template <int I> struct IC { static constexpr int v = I; };
template <int I0, int I1, class F>
__device__ __forceinline__ void static_for(F&& f) {
  if constexpr (I0 < I1) { f(IC<I0>{}); static_for<I0 + 1, I1>(f); }
}

struct Params {
  const u16 *q, *k, *v, *shift_k, *ssm_k, *ssm_D, *w_in, *w_sp, *b_sp, *w_out, *cvec, *w_mod;
  u16* out;
  char* ws;
};

__global__ __launch_bounds__(512, 2) void mega(Params p) {
  __shared__ __attribute__((aligned(16))) char lds[LDS_TOTAL];
  unsigned ka0, ka1;
  asm volatile("s_load_dword %0, %2, 0x0\n\ts_load_dword %1, %2, 0x40" : "=&s"(ka0), "=&s"(ka1) : "s"(__builtin_amdgcn_kernarg_segment_ptr()));
  SCHED();
  const int tid = threadIdx.x, l = tid & 63;
  const int w = __builtin_amdgcn_readfirstlane(tid >> 6);
  const int bid = blockIdx.x, xcd = bid & 7, idx = bid >> 3;
  const int dg = ((idx & 7) + 2 * (idx >> 3)) & 7;
  const bool is_conv = dg >= 3;
  const int slot = 3 * (idx >> 3) + dg;
  const int cidx = 5 * (idx >> 3) + dg - 3;
  const int quad = xcd * 20 + cidx;
  const unsigned l16 = (unsigned)l * 16u;
  const int blk = (quad / 5) * 8 + w, sl = quad % 5;
  unsigned a0g = (unsigned)(l >> 4) * 1280u + (unsigned)(((l & 15) ^ (4 * (l >> 5))) * 16), a0x = a0g ^ 128u, a0h = a0g + (l < 16 ? 1280u : 0u);
  unsigned a0s = (unsigned)blk * 10240u + (unsigned)sl * 256u, a0p = blk ? a0s - 5120u : a0s, a0l = L_A0 + w * 6144;
  asm volatile("" : "+v"(a0g), "+v"(a0x), "+v"(a0h));
  asm volatile("" : "+s"(a0s), "+s"(a0p), "+s"(a0l));
  char* const ws = p.ws;
  asm volatile("s_waitcnt lgkmcnt(0)" ::"s"(ka0), "s"(ka1), "s"(p.q), "s"(p.k), "s"(p.v), "s"(p.shift_k), "s"(p.ssm_k), "s"(p.ssm_D), "s"(p.w_in), "s"(p.w_sp), "s"(p.b_sp),
               "s"(p.w_out), "s"(p.cvec), "s"(p.w_mod), "s"(p.out), "s"(p.ws));
  STAMP(0);
  unsigned cpf = 0;
  if (!is_conv) {
    unsigned long long pc;
    unsigned len, len2;
    asm volatile("s_getpc_b64 %0\n.Lmega_here:\n\ts_mov_b32 %1, .Lmega_end-.Lmega_here\n\ts_mov_b32 %2, .Lmega_aux_end-.Lmega_here" : "=s"(pc), "=s"(len), "=s"(len2));
    len = len > len2 ? len : len2;
    unsigned o = (unsigned)((slot * 8 + w) * 1024 + (l & 7) * 128);
    o = o + 16u <= len ? o : 0u;
    cpf = *(const GAS unsigned*)((pc & ~15ull) + o);
  }
  const rsrc_t rs = __builtin_amdgcn_make_buffer_rsrc(ws, 0, WS_END, 0x00020000);
  const unsigned epv = ld4(rs, WS_EPOCH);
  SCHED();
  u32x2 dd2;
  {
    int cq = dg;
    asm volatile("" : "+s"(cq));
    if (cq >= 3) {
      const rsrc_t rk = __builtin_amdgcn_make_buffer_rsrc((void*)p.k, 0, S_ * D_ * 2, 0x00020000);
      const rsrc_t rv = __builtin_amdgcn_make_buffer_rsrc((void*)p.v, 0, S_ * D_ * 2, 0x00020000);
      const rsrc_t rh = __builtin_amdgcn_make_buffer_rsrc((void*)p.shift_k, 0, D_ * 8, 0x00020000);
      char* const la = lds + a0l;
      __builtin_amdgcn_raw_ptr_buffer_load_lds(rh, (LAS void*)(la + 5120), 16, l16, (unsigned)sl * 1024u, 0, 0);
      __builtin_amdgcn_raw_ptr_buffer_load_lds(rk, (LAS void*)(la + 1024), 16, a0x, a0s, 0, 0);
      __builtin_amdgcn_raw_ptr_buffer_load_lds(rk, (LAS void*)(la + 2048), 16, a0g, a0s + 5120u, 0, 0);
      __builtin_amdgcn_raw_ptr_buffer_load_lds(rv, (LAS void*)(la + 3072), 16, a0g, a0s, 0, 0);
      __builtin_amdgcn_raw_ptr_buffer_load_lds(rv, (LAS void*)(la + 4096), 16, a0x, a0s + 5120u, 0, 0);
      __builtin_amdgcn_raw_ptr_buffer_load_lds(rk, (LAS void*)(la), 16, a0h, a0p, 0, 0);
      SCHED();
      const rsrc_t rf = __builtin_amdgcn_make_buffer_rsrc((void*)p.ssm_k, 0, S_ * D_ * 2, 0x00020000);
      dd2 = *(const CAS u32x2*)((const CAS char*)p.ssm_D + quad * 8);
      STAMP(32);
      {
        const u32x4 z = {0u, 0u, 0u, 0u};
        if (tid < 128) *(u32x4*)(lds + (tid >> 5) * CH_BYTES + (tid & 31) * 16) = z;
        else if (tid < 152) {
          const int i = (tid - 128) >> 2;
          *(u32x4*)(lds + (tid & 3) * CH_BYTES + U_BYTES + (i < 4 ? i * G_COPY : (i & 1 ? 4128 : 16))) = z;
        }
        const LAS char* a0r = (const LAS char*)(lds + L_A0) + w * 6144 + (l & 3) * 512;
#define A0R(k) (a0r + ((((l >> 2) ^ (4 * ((l + (k)) & 3))) * 16)))
        unsigned go = WS_KVT + (unsigned)(quad / 5) * 81920u + (unsigned)sl * 16384u + (unsigned)(l >> 2) * 1024u + (unsigned)w * 64u + (unsigned)(l & 3) * 16u;
        const LAS char* r0 = A0R(0), * r1 = A0R(1), * r2 = A0R(2);
        const LAS char* rsh = (const LAS char*)(lds + L_A0) + w * 6144 + 5120 + (l >> 2) * 64;
        asm volatile("" : "+v"(go), "+v"(r0), "+v"(r1), "+v"(r2), "+v"(rsh));
        SCHED();
#pragma unroll
        for (int j = 0; j < 2; ++j)
          __builtin_amdgcn_raw_ptr_buffer_load_lds(rf, (LAS void*)(lds + (w >> 1) * CH_BYTES + U_BYTES + 32 + (2 * (w & 1) + j) * 1024), 16, l16,
                                                   (unsigned)quad * 16384u + (unsigned)(2 * w + j) * 1024u, 0, 0);
        STAMP(34);
        SCHED();
        VMW(7);
        f32x2 wt[4][4];
#pragma unroll
        for (int e = 0; e < 4; ++e) {
          const u32x4 sh = *(const volatile LAS u32x4*)(rsh + 16 * e);
          wt[e][0] = (f32x2){bflo(sh[0]), bflo(sh[2])}; wt[e][1] = (f32x2){bfhi(sh[0]), bfhi(sh[2])};
          wt[e][2] = (f32x2){bflo(sh[1]), bflo(sh[3])}; wt[e][3] = (f32x2){bfhi(sh[1]), bfhi(sh[3])};
        }
#pragma unroll
        for (int e = 0; e < 4; ++e) asm volatile("" : "+v"(wt[e][0]), "+v"(wt[e][1]), "+v"(wt[e][2]), "+v"(wt[e][3]));
        asm volatile("" : "+s"(dd2));
        SCHED();
        VMW(2);
        STAMP(35);
        u32x4 ka[5], va[2];
        ka[0] = *(const volatile LAS u32x4*)(r0 + 256);
        ka[1] = *(const volatile LAS u32x4*)(r1 + 512);
        ka[2] = *(const volatile LAS u32x4*)(r1 + 768);
        ka[3] = *(const volatile LAS u32x4*)(r2 + 1024);
        ka[4] = *(const volatile LAS u32x4*)(r2 + 1280);
        va[0] = *(const volatile LAS u32x4*)(r0 + 3072);
        va[1] = *(const volatile LAS u32x4*)(r0 + 3328);
        SCHED();
        if (blk == 0) {
          if ((l & 3) < 2) ka[0] = z;
          if ((l & 3) == 0) { ka[1] = z; ka[2] = z; }
        }
        u32x4 ko;
#pragma unroll
        for (int e = 0; e < 4; ++e) {
          const f32x2 w0 = wt[e][0], w1 = wt[e][1], w2 = wt[e][2], w3 = wt[e][3];
          const f32x2 km3 = bf2(ka[0][e]), km2 = bf2(ka[1][e]), km1 = bf2(ka[2][e]), k0 = bf2(ka[3][e]), k1 = bf2(ka[4][e]);
          const f32x2 s0 = __builtin_elementwise_fma(k0, w0, __builtin_elementwise_fma(km1, w1, __builtin_elementwise_fma(km2, w2, km3 * w3)));
          const f32x2 s1 = __builtin_elementwise_fma(k1, w0, __builtin_elementwise_fma(k0, w1, __builtin_elementwise_fma(km1, w2, km2 * w3)));
          const f32x2 o0 = s0 * bf2(va[0][e]), o1 = s1 * bf2(va[1][e]);
          ko[2 * (e & 1)] = pack2(o1[0], o0[0]); ko[2 * (e & 1) + 1] = pack2(o1[1], o0[1]);
          if (e & 1) st16(rs, go + (unsigned)(e >> 1) * 512u, ko);
          SCHED();
        }
      }
    }
  }
  const int R = (xcd & 3) * 8 + (idx & 7);
  const int c = (xcd >> 2) * 4 + (idx >> 3);
  const int n16 = l & 15, h4 = l >> 4;
  const rsrc_t rq = __builtin_amdgcn_make_buffer_rsrc((void*)p.q, 0, S_ * D_ * 2, 0x00020000);
  const rsrc_t rsp = __builtin_amdgcn_make_buffer_rsrc((void*)p.w_sp, 0, S_ * S_ * 2, 0x00020000);
  const int wj = w < 7 ? w : 0;
  u32x4 wsp[4][8];
  const int wk = w ^ (dg == 5 || dg == 6 ? 4 : 0);
  const unsigned wso = (unsigned)(R * 64) * 4096u + (unsigned)wk * 512u;
  const unsigned wdo = (unsigned)(h4 * 8192 + ((n16 ^ h4) * 16));
#define WVO(q) (wdo ^ (unsigned)(((4 - (q)) & 3) * 64))
#define WSO(g, q) (wso + (unsigned)((g) >> 2) * 256u + (unsigned)(32 * (((g) >> 1) & 1) + ((g) & 1) + 8 * (q)) * 4096u)
  u32x4 raw[NRAW];
#define LDW(i) raw[i] = __builtin_amdgcn_raw_buffer_load_b128(rsp, WVO((i) & 3), WSO((i) >> 2, (i) & 3), 0)
  const int trow = R * 64 + 32 * ((w >> 1) & 1) + 2 * n16 + (w & 1);
  unsigned bs_raw;
  unsigned tag;
  unsigned fw2 = OOB;
  unsigned e_fw1;
  const unsigned fw1o = WS_FW1 + (unsigned)(((8 + c) * 8 + wj) * 4 + (l & 3)) * 128u;

  if (__builtin_expect(is_conv, 1)) {
    STAMP(1);
    DRAIN();
    tag = __builtin_amdgcn_readfirstlane(epv) * 2654435761u + 0x9E3779B9u;
    BAR();
    st4(rs, tid == 0 ? WS_FK + (unsigned)quad * 128u : OOB, tag);
    unsigned atv = 0;
    auto atouch = [&]() {
      const int i = w < 5 ? (cidx * 5 + w) * 20 + (l < 19 ? l : 19) : (cidx * 3 + w - 5) * 19 + (l < 18 ? l : 18);
      const int r = i / 3;
      const unsigned o1 = (unsigned)((r < 639 ? r : 639) * (F_ * 2) + xcd * CB * 2 + (i - 3 * r) * 108);
      const unsigned o2 = (unsigned)(xcd * CB * D_ * 2 + (i < 1079 ? i : 1079) * 128);
      atv = *(const GAS unsigned*)((const GAS char*)(w < 5 ? p.w_in : p.w_out) + (w < 5 ? o1 : o2));
      SCHED();
    };
    {
      u32x4 d[3];
      u32x2 n[3];
#pragma unroll
      for (int j = 0; j < 3; ++j) {
        char* G = j < 2 ? lds + ((tid >> 8) + 2 * j) * CH_BYTES + U_BYTES + ((tid & 255) + 2) * 16 : lds + (tid & 3) * CH_BYTES + U_BYTES + 16;
        d[j] = *(const u32x4*)G;
        n[j] = *(const u32x2*)(G + 16);
      }
#pragma unroll
      for (int j = 0; j < 3; ++j) {
        char* G = j < 2 ? lds + ((tid >> 8) + 2 * j) * CH_BYTES + U_BYTES + ((tid & 255) + 2) * 16 : lds + (tid & 3) * CH_BYTES + U_BYTES + 16;
        const unsigned a0 = __builtin_amdgcn_alignbit(d[j][1], d[j][0], 16), a1 = __builtin_amdgcn_alignbit(d[j][2], d[j][1], 16),
                       a2 = __builtin_amdgcn_alignbit(d[j][3], d[j][2], 16), a3 = __builtin_amdgcn_alignbit(n[j][0], d[j][3], 16),
                       a4 = __builtin_amdgcn_alignbit(n[j][1], n[j][0], 16);
        if (j < 2 || tid < 4) {
          *(u32x4*)(G + G_COPY) = (u32x4){a0, a1, a2, a3};
          *(u32x4*)(G + 2 * G_COPY) = (u32x4){d[j][1], d[j][2], d[j][3], n[j][0]};
          *(u32x4*)(G + 3 * G_COPY) = (u32x4){a1, a2, a3, a4};
        }
      }
    }
    STAMP(36);
    {
      wait_flag<1>(rs, WS_FK + (unsigned)(5 * (4 * w + (l & 3)) + (quad >> 5)) * 128u, tag);
      STAMP(29);
      const unsigned gp = WS_KVT + (unsigned)(4 * w + (l >> 5)) * 81920u + (unsigned)quad * 512u + (unsigned)(l & 31) * 16u;
      const u32x4 g0 = ld16(rs, gp), g1 = ld16(rs, gp + 163840u);
      SCHED();
      char* Uw = lds + 512 + (128 * w + (l & ~3) + 3 - (l & 3)) * 4;
#pragma unroll
      for (int i = 0; i < 4; ++i) *(unsigned*)(Uw + i * CH_BYTES) = g0[i];
#pragma unroll
      for (int i = 0; i < 4; ++i) *(unsigned*)(Uw + 256 + i * CH_BYTES) = g1[i];
    }
    STAMP(28);
    BAR();
    STAMP(2);
    const int te = 2 * tid;
    u32x4 qa[2];
    u32x4 sv[2];
    u32x2 mg;
    const unsigned xo = (unsigned)(tid >> 4) * WX1P + (unsigned)quad * 256u + (unsigned)(tid & 15) * 16u;
    const unsigned mgo = WS_MOD + (unsigned)((l < 12 ? (l >> 2) * D_ + (l & 3) : 0) + quad * 4) * 8u;
    const int ch = w & 3, hf = w >> 2;
    auto ld_sv = [&]() {
      sv[0] = ld16(rs, WS_ST1 + (unsigned)tid * 16u);
      sv[1] = ld16(rs, WS_ST1 + (unsigned)tid * 16u + 8192u);
    };
    auto ld_q = [&]() {
      qa[0] = ld16(rs, WS_QB + xo);
      qa[1] = ld16(rs, WS_QB + xo + 32u * WX1P);
    };
    f32x2 hh[2][2][2], base[2][2][2];
    auto prep = [&]() {
      const LAS char* ur = (const LAS char*)(lds + 512) + ((te & ~7) + 6 - (te & 7)) * 2;
      unsigned uu[2][4];
#pragma unroll
      for (int i = 0; i < 8; ++i) uu[i >> 2][i & 3] = *(const volatile LAS unsigned*)(ur + (i >> 2) * 2048 + (i & 3) * CH_BYTES);
      if (!__all(((sv[0][0] ^ tag) | (sv[0][2] ^ tag) | (sv[1][0] ^ tag) | (sv[1][2] ^ tag)) == 0u)) {
        _Pragma("unroll 1") for (unsigned spins = 0; spins < SPIN_CAP; ++spins) {
          ld_sv();
          if (__all(sv[0][0] == tag && sv[0][2] == tag && sv[1][0] == tag && sv[1][2] == tag)) break;
          __builtin_amdgcn_s_sleep(4);
        }
        asm volatile("" ::: "memory");
        ld_q();
        mg = ld8(rs, mgo);
        DRAIN();
      }
      if (!__all(mg[0] == tag)) {
        _Pragma("unroll 1") for (unsigned spins = 0; spins < SPIN_CAP; ++spins) {
          mg = ld8(rs, mgo);
          if (__all(mg[0] == tag)) break;
          __builtin_amdgcn_s_sleep(2);
        }
      }
      f32x2 cc2[2], ss2[2], dv2[2];
      float cch[4], sch[4];
#pragma unroll
      for (int i = 0; i < 4; ++i) {
        const float sa = __uint_as_float(__builtin_amdgcn_readlane(mg[1], i)), ca = __uint_as_float(__builtin_amdgcn_readlane(mg[1], 4 + i)),
                    ga = __uint_as_float(__builtin_amdgcn_readlane(mg[1], 8 + i));
        cch[i] = ga * rbf(1.0f + ca);
        sch[i] = ga * sa;
      }
      cc2[0] = (f32x2){cch[0], cch[1]}; cc2[1] = (f32x2){cch[2], cch[3]};
      ss2[0] = (f32x2){sch[0], sch[1]}; ss2[1] = (f32x2){sch[2], sch[3]};
      dv2[0] = bf2(dd2[0]); dv2[1] = bf2(dd2[1]);
#pragma unroll
      for (int H = 0; H < 2; ++H)
#pragma unroll
        for (int e = 0; e < 2; ++e) {
          const unsigned st = sv[H][1 + 2 * e];
          const float a = __builtin_amdgcn_rcpf(bfhi(st)), b = -bflo(st) * a;
#pragma unroll
          for (int i = 0; i < 2; ++i) {
            const f32x2 q2 = bf2(qa[H][2 * e + i]);
            const f32x2 u2 = e ? (f32x2){bflo(uu[H][2 * i]), bflo(uu[H][2 * i + 1])} : (f32x2){bfhi(uu[H][2 * i]), bfhi(uu[H][2 * i + 1])};
            const f32x2 n1 = __builtin_elementwise_fma(q2, (f32x2){a, a}, (f32x2){b, b});
            hh[H][e][i] = __builtin_elementwise_fma(n1, cc2[i], ss2[i]);
            base[H][e][i] = __builtin_elementwise_fma(hh[H][e][i], u2 * dv2[i], q2);
            asm volatile("" : "+v"(hh[H][e][i]), "+v"(base[H][e][i]));
          }
        }
    };
    auto HF = [&]() { int x = hf; asm volatile("" : "+s"(x)); return x; };
    {
      const char* U = lds + ch * CH_BYTES;
      const char* G = U + U_BYTES;
      const int a = n16, jj = h4 >> 1, e2 = h4 & 1;
      const int sg = (a + 1) & 3;
      const LAS char* bp = (const LAS char*)U + 512 + 32 * (a - jj) + 16 * e2 - 256 * hf - 192;
      const LAS char* ap = (const LAS char*)G + sg * G_COPY + 2 * (16 * jj + a - 8 * e2 + 9 - sg) + 256 * hf;
      char* yp = lds + L_YP + (hf * 4 + ch) * 8192 + (h4 * 16 + ((a + 4 * h4) & 15)) * 16;
      f32x4 acc[8];
      u32x4 A[2][8], B[2][8];
      auto rd = [&](int k) {
        const int r = k / 24, j = k % 24;
        if (j >= 2 && j < 10) B[r & 1][9 - j] = *(const volatile LAS u32x4*)(bp + 512 * (9 - j) + 64 * (3 - r));
        else {
          const int pp = j < 2 ? 0 : (j - 8) >> 1;
          const u32x2 v = *(const volatile LAS u32x2*)(ap + 512 * pp + 64 * r + 8 * (j & 1));
          A[r & 1][pp][2 * (j & 1)] = v[0]; A[r & 1][pp][2 * (j & 1) + 1] = v[1];
        }
      };
#pragma unroll
      for (int k = 0; k < IC<conv_sched(CDIST).pre>::v; ++k) rd(k);
      SCHED();
      auto step = [&](auto ic) {
        constexpr int i = decltype(ic)::v, r = i / 36, pp = conv_sched(CDIST).pp[i], m = conv_sched(CDIST).m[i], k = conv_sched(CDIST).rd[i];
        acc[m + pp] = MFMA16(A[r & 1][pp], B[r & 1][m], i < 8 ? ZERO4 : acc[m + pp]);
        SCHED();
        if (k >= 0) rd(k);
        if (i == 0) atouch();
        if (i >= 2 && (i - 2) % 6 == 0 && (i - 2) / 3 < NRAW) {
          if (!HF()) { LDW((i - 2) / 3); LDW((i - 2) / 3 + 1); }
        }
        if (i >= RAW2 && (i - RAW2) % 6 == 0 && (i - RAW2) / 3 < NRAW) {
          if (HF()) { LDW((i - RAW2) / 3); LDW((i - RAW2) / 3 + 1); }
        }
        if (i == SV_AT) { ld_sv(); bs_raw = p.b_sp[trow]; }
        if (r == 3 && pp >= 1 && m == 7 - pp) *(f32x4*)(yp + (pp - 1) * 1024) = acc[pp - 1];
        SCHED();
      };
      static_for<0, Q_AT>(step);
      STAMP(3);
      DRAIN();
      asm volatile("" ::"v"(atv));
      ld_q();
      mg = ld8(rs, mgo);
      SCHED();
      STAMP(4);
      static_for<Q_AT, 144>(step);
      *(f32x4*)(yp + 7 * 1024) = acc[7];
      STAMP(5);
    }
    prep();
    e_fw1 = ld4(rs, fw1o);
    STAMP(6);
    const LAS char* yr = (const LAS char*)(lds + L_YP) + (te >> 8) * 1024 + (((te >> 2) & 3) * 16 + ((((te >> 4) & 15) + 4 * ((te >> 2) & 3)) & 15)) * 16 + (te & 3) * 4;
    asm volatile("" : "+v"(yr));
    BAR();
    STAMP(7);
    f32x2 y[2][8];
#pragma unroll
    for (int i = 0; i < 16; ++i) y[i >> 3][i & 7] = *(const volatile LAS f32x2*)(yr + (i >> 3) * 4096 + (i & 7) * 8192);
#pragma unroll
    for (int H = 0; H < 2; ++H) {
      u32x4 o;
#pragma unroll
      for (int e = 0; e < 2; ++e)
#pragma unroll
        for (int i = 0; i < 2; ++i) {
          const float x0 = __builtin_fmaf(hh[H][e][i][0], y[H][2 * i][e] + y[H][4 + 2 * i][e], base[H][e][i][0]);
          const float x1 = __builtin_fmaf(hh[H][e][i][1], y[H][2 * i + 1][e] + y[H][5 + 2 * i][e], base[H][e][i][1]);
          o[2 * e + i] = pack2(x0, x1);
        }
      st16(rs, WS_X1B + (unsigned)(32 * H) * WX1P + xo, o);
    }
    STAMP(8);
  } else {
    const rsrc_t rwi = __builtin_amdgcn_make_buffer_rsrc((void*)p.w_in, 0, D_ * F_ * 2, 0x00020000);
    const rsrc_t rwo = __builtin_amdgcn_make_buffer_rsrc((void*)p.w_out, 0, FH * D_ * 2, 0x00020000);
    STAMP(32);
    const int job = xcd + 8 * slot;
    const int T = job >= 50 && w < 3 && job - 50 + 46 * w < 128 ? job - 50 + 46 * w : -1;
    const bool unit = slot >= 8 || (slot >= 2 && slot < 6);
    const int ucb = slot >= 8 ? 8 + xcd : xcd, kg = (slot >= 8 ? slot : slot - 2) & 3;
    float* tab = (float*)(lds + L_TAB);
    float* red = (float*)(lds + L_RED);
    auto issue_unit = [&](int i) {
      const int g = i * 64 + l;
      __builtin_amdgcn_raw_ptr_buffer_load_lds(rwi, (LAS void*)(lds + L_J3 + i * 1024), 16, (unsigned)((g / 14) * (F_ * 2) + (g % 14) * 16),
                                               (unsigned)(kg * 160 * F_ * 2 + ucb * CB * 2), 0, 0);
    };
    if (T >= 0) {
      char* qt = lds + L_QT + w * 20480;
      const unsigned so = (unsigned)(16 * T) * 1280u;
#pragma unroll
      for (int n = 0; n < 16; ++n)
        __builtin_amdgcn_raw_ptr_buffer_load_lds(rq, (LAS void*)(qt + n * 1024), 16, l16 ^ (unsigned)(n * 16), so + (unsigned)(n * 1280), 0, 0);
#pragma unroll
      for (int j = 0; j < 4; ++j)
        __builtin_amdgcn_raw_ptr_buffer_load_lds(rq, (LAS void*)(qt + 16384 + j * 1024), 16, (unsigned)(h4 * 1280 + ((n16 ^ (4 * j + h4)) * 16)), so + (unsigned)(4 * j * 1280 + 1024), 0, 0);
      tag = __builtin_amdgcn_readfirstlane(epv) * 2654435761u + 0x9E3779B9u;
      STAMP(34);
      DRAIN();
      STAMP(35);
      const bf2_t one = {(__bf16)1.0f, (__bf16)1.0f};
      float s1[2] = {0.f, 0.f}, s2[2] = {0.f, 0.f};
      const int n0 = 2 * (l & 7);
      u32x4 qv[20];
#pragma unroll
      for (int i = 0; i < 20; ++i)
#pragma unroll
        for (int e = 0; e < 2; ++e) {
          const int n = n0 + e, ck = 4 * (i & 15) + h4;
          const u32x2 v = *(const volatile LAS u32x2*)(qt + (i < 16 ? n * 1024 : 16384 + n * 256) + ((ck ^ n) * 16) + (l & 8));
          qv[i][2 * e] = v[0]; qv[i][2 * e + 1] = v[1];
        }
#pragma unroll
      for (int i = 0; i < 20; ++i) {
        st16(rs, WS_QB + (unsigned)(T >> 1) * WX1P + (unsigned)i * 2048u + (unsigned)((l >> 3) * 256 + (T & 1) * 128 + (l & 7) * 16), qv[i]);
#pragma unroll
        for (int e = 0; e < 4; ++e) {
          const unsigned x = qv[i][e];
          const bf2_t v = __builtin_bit_cast(bf2_t, x);
          s1[e >> 1] = __builtin_amdgcn_fdot2_f32_bf16(v, one, s1[e >> 1], false);
          s2[e >> 1] = __builtin_amdgcn_fdot2_f32_bf16(v, v, s2[e >> 1], false);
        }
        SCHED();
      }
      u32x4 o = {tag, 0u, tag, 0u};
#pragma unroll
      for (int e = 0; e < 2; ++e) {
        const float t1 = sum_rows(s1[e] + dpp_f<0x128>(s1[e])), t2 = sum_rows(s2[e] + dpp_f<0x128>(s2[e]));
        const float mean = t1 * (1.0f / D_);
        const float var = __builtin_fmaxf(t2 * (1.0f / D_) - mean * mean, 0.f);
        o[2 * e + 1] = pack2(mean, __builtin_amdgcn_sqrtf(rbf(rbf(var) + 1e-5f)));
      }
      STAMP(1);
      DRAIN();
      if (l < 8) st16(rs, WS_ST1 + (unsigned)T * 128u + l16, o);
      STAMP(3);
    }
    if (job < 60) {
      const int grp = job < 10 ? 40 + job : (job < 50 ? job - 10 : job);
      if (job >= 50 && T < 0) __builtin_amdgcn_s_sleep(LATE_SLEEP);
      if (job >= 10 && T < 0) __builtin_amdgcn_s_sleep(AUX_SLEEP);
      const unsigned cv = *(const unsigned*)(p.cvec + 80 * w + 2 * (l < 40 ? l : 0));
      u32x4 pre[10];
#pragma unroll
      for (int pp = 0; pp < 10; ++pp) pre[pp] = *(const u32x4*)(p.w_mod + (size_t)(80 * w + pp * 8 + (l >> 3)) * 3840 + grp * 64 + (l & 7) * 8);
      SCHED();
      STAMP(34);
      tag = __builtin_amdgcn_readfirstlane(epv) * 2654435761u + 0x9E3779B9u;
      if (l < 40) {
        const float c0 = bflo(cv), c1 = bfhi(cv);
        *(f32x2*)(tab + w * 80 + 2 * l) = (f32x2){rbf(c0 * __builtin_amdgcn_rcpf(1.0f + __expf(-c0))), rbf(c1 * __builtin_amdgcn_rcpf(1.0f + __expf(-c1)))};
      }
      asm volatile("s_waitcnt lgkmcnt(0)" ::: "memory");
      f32x2 acc[4];
#pragma unroll
      for (int e = 0; e < 4; ++e) acc[e] = (f32x2){0.f, 0.f};
#pragma unroll
      for (int pp = 0; pp < 10; ++pp) {
        const float sv = tab[w * 80 + pp * 8 + (l >> 3)];
#pragma unroll
        for (int e = 0; e < 4; ++e) acc[e] = __builtin_elementwise_fma((f32x2){sv, sv}, bf2(pre[pp][e]), acc[e]);
      }
      STAMP(35);
      asm volatile("" : "+v"(acc[0]), "+v"(acc[1]), "+v"(acc[2]), "+v"(acc[3]));
#pragma unroll
      for (int e = 0; e < 8; ++e) { float a = acc[e >> 1][e & 1]; a += dpp_f<0x128>(a); acc[e >> 1][e & 1] = sum_rows(a); }
      if (l < 8) {
        *(f32x4*)(red + w * 64 + l * 8) = (f32x4){acc[0][0], acc[0][1], acc[1][0], acc[1][1]};
        *(f32x4*)(red + w * 64 + l * 8 + 4) = (f32x4){acc[2][0], acc[2][1], acc[3][0], acc[3][1]};
      }
      if (w == 7) __builtin_amdgcn_s_setprio(2);
      BAR();
      if (w == 7) {
        float t = 0.f;
#pragma unroll
        for (int r = 0; r < 8; ++r) t += red[r * 64 + l];
        st8(rs, WS_MOD + (unsigned)(grp * 64 + l) * 8u, (u32x2){tag, __float_as_uint(rbf(t))});
        __builtin_amdgcn_s_setprio(0);
      } else if (unit) {
#pragma unroll
        for (int k = 0; k < 5; ++k) issue_unit(w * 5 + k);
      }
    } else if (unit && w >= 3 && w < 7) {
      __builtin_amdgcn_s_sleep(AUX_SLEEP);
#pragma unroll
      for (int k = 0; k < 9; ++k) issue_unit((w - 3) * 9 + k < 35 ? (w - 3) * 9 + k : 34);
    }
    tag = __builtin_amdgcn_readfirstlane(epv) * 2654435761u + 0x9E3779B9u;
    if (unit) {
      auto mod_tab = [&](int which, float add, float* dst) {
        const unsigned o = WS_MOD + (unsigned)(which * D_ + kg * 160) * 8u;
        u32x4 g0, g1;
        _Pragma("unroll 1") for (unsigned spins = 0; spins < SPIN_CAP; ++spins) {
          g0 = ld16(rs, o + l16);
          g1 = ld16(rs, o + 1024u + (l16 & 255u));
          if (__all(g0[0] == tag && g0[2] == tag && g1[0] == tag && g1[2] == tag)) break;
          __builtin_amdgcn_s_sleep(2);
        }
        *(f32x2*)(dst + 2 * l) = (f32x2){rbf(add + __uint_as_float(g0[1])), rbf(add + __uint_as_float(g0[3]))};
        if (l < 16) *(f32x2*)(dst + 128 + 2 * l) = (f32x2){rbf(add + __uint_as_float(g1[1])), rbf(add + __uint_as_float(g1[3]))};
      };
      if (w == 7) {
        STAMP(1);
        if (slot >= 8) __builtin_amdgcn_s_sleep(64);
        mod_tab(4, 1.0f, tab);
        BAR();
        STAMP(2);
        mod_tab(3, 0.0f, tab + 160);
        BAR();
      } else {
        DRAIN();
        STAMP(36);
        BAR();
        STAMP(2);
        const char* bp = lds + L_J3 + (4 * h4 + (n16 >> 2)) * 224 + (n16 & 3) * 8 + w * 32;
        const float* tp = tab + 4 * h4;
        u32x4 f3[5];
        float cs = 0.f, b1 = 0.f;
        const bool pad = w == 6 && n16 >= 12;
        const unsigned padv = (n16 == 12 && ucb >= 8) ? 0x3f803f80u : 0u;
#pragma unroll
        for (int k = 0; k < 5; ++k) f3[k] = trfrag(bp + k * 7168, 16 * 224);
#pragma unroll
        for (int k = 0; k < 5; ++k) {
          const f32x4 ca = *(const f32x4*)(tp + k * 32), cb4 = *(const f32x4*)(tp + k * 32 + 16);
          u32x4 o;
#pragma unroll
          for (int e = 0; e < 4; ++e) {
            const float c0 = e < 2 ? ca[2 * e] : cb4[2 * e - 4], c1 = e < 2 ? ca[2 * e + 1] : cb4[2 * e - 3];
            const float p0 = bflo(f3[k][e]) * c0, p1 = bfhi(f3[k][e]) * c1;
            cs += p0 + p1;
            o[e] = pad ? padv : pack2(p0, p1);
          }
          st16(rs, WS_WINF + (unsigned)(ucb * 7 + w) * 20480u + (unsigned)(kg * 5 + k) * 1024u + l16, o);
        }
        cs = sum_rows(cs);
        DRAIN();
        if (l == 0) st4(rs, WS_FW1 + (unsigned)((ucb * 8 + w) * 4 + kg) * 128u, tag);
        STAMP(5);
        BAR();
#pragma unroll
        for (int k = 0; k < 5; ++k) {
          const f32x4 sa = *(const f32x4*)(tp + 160 + k * 32), sb4 = *(const f32x4*)(tp + 160 + k * 32 + 16);
#pragma unroll
          for (int e = 0; e < 4; ++e) {
            const float s0 = e < 2 ? sa[2 * e] : sb4[2 * e - 4], s1 = e < 2 ? sa[2 * e + 1] : sb4[2 * e - 3];
            b1 = __builtin_fmaf(s0, bflo(f3[k][e]), __builtin_fmaf(s1, bfhi(f3[k][e]), b1));
          }
        }
        b1 = sum_rows(b1);
        if (pad) { cs = padv ? 160.0f : 0.0f; b1 = 0.0f; }
        if (l < 16) st16(rs, WS_CS + (unsigned)((ucb * 4 + kg) * 112 + w * 16 + l) * 16u, (u32x4){tag, __float_as_uint(cs), tag, __float_as_uint(b1)});
      }
    } else {
      if (job >= 60 && T < 0) __builtin_amdgcn_s_sleep(100);
      if (slot < 2 && w >= 2 && w < 6) {
        const int c5 = slot * 4 + w - 2, c2 = xcd;
        char* st = lds + L_J5 + (w - 2) * 18432;
#pragma unroll
        for (int r = 0; r < 18; ++r) {
          const int g = r * 64 + l;
          __builtin_amdgcn_raw_ptr_buffer_load_lds(rwo, (LAS void*)(st + r * 1024), 16, (unsigned)((g / 10) * (D_ * 2) + (g % 10) * 16),
                                                   (unsigned)(xcd * CB * D_ * 2 + c5 * 160), 0, 0);
        }
        DRAIN();
        const char* bp = st + (4 * h4 + (n16 >> 2)) * 160 + (n16 & 3) * 8;
        const unsigned base = WS_WOUTF + (unsigned)c5 * WBLK;
#pragma unroll
        for (int dj = 0; dj < 5; ++dj) {
          st16(rs, base + (unsigned)c2 * 10240u + (unsigned)dj * 1024u + l16, trfrag(bp + dj * 32, 32 * 160));
          st16(rs, base + (unsigned)c2 * 10240u + (unsigned)(5 + dj) * 1024u + l16, trfrag(bp + 16 * 160 + dj * 32, 32 * 160));
          st16(rs, base + 81920u + (unsigned)c2 * 5120u + (unsigned)dj * 1024u + l16, trfrag(bp + 64 * 160 + dj * 32, 16 * 160));
          st8(rs, base + 122880u + (unsigned)((c2 >> 1) * 5 + dj) * 1024u + (unsigned)(c2 & 1) * 512u + (unsigned)l * 8u, trd(bp + 96 * 160 + dj * 32));
        }
        fw2 = WS_FW2 + (unsigned)(c5 * 8 + xcd) * 128u;
      }
    }
    STAMP(4);
#pragma unroll
    for (int i = 0; i < NRAW; ++i) LDW(i);
    bs_raw = p.b_sp[trow];
    e_fw1 = ~tag;
    asm volatile(".Lmega_aux_end:");
    asm volatile("" ::"v"(cpf));
    STAMP(8);
  }

  LAS char* xa = (LAS char*)(lds + L_XI) + l * 16;
  LAS char* xb = xa + 65536;
  asm volatile("" : "+v"(xb));
#define LDA(ks, rt) (*(const volatile LAS u32x4*)((ks) < 16 ? xa + ((ks) * 4 + (rt)) * 1024 : xb + (((ks) - 16) * 4 + (rt)) * 1024))
  auto lda1 = [&](int ks, u32x4* A) {
#pragma unroll
    for (int rt = 0; rt < 4; ++rt) A[rt] = LDA(ks, rt);
  };
  const LAS char* tb4 = (const LAS char*)(lds + L_MISC) + h4 * 16;
  const LAS char* tbn = (const LAS char*)(lds + L_MISC) + n16 * 4;
  asm volatile("" : "+v"(tb4), "+v"(tbn));
  u32x4 xr[5][2];
  auto ld_x1 = [&]() {
#pragma unroll
    for (int m = 0; m < 5; ++m) {
      if (m == 0) __builtin_amdgcn_s_setprio(2);
      if (m == 1) __builtin_amdgcn_s_setprio(1);
      if (m == 3) __builtin_amdgcn_s_setprio(0);
#pragma unroll
      for (int e = 0; e < 2; ++e)
        xr[m][e] = ld16(rs, l16, WS_X1B + (unsigned)(R * 2 + (w & 1)) * WX1P + (unsigned)(4 * m + (w >> 1)) * 2048u + (unsigned)e * 1024u);
      SCHED();
    }
  };
  LAS char* xw = xa + w * 2048;
  auto put_x1 = [&](int m) {
    LAS char* d = m < 4 ? xw + m * 16384 : xb + w * 2048;
#pragma unroll
    for (int e = 0; e < 2; ++e) {
      *(volatile LAS u32x2*)(d + 8 * e) = (u32x2){xr[m][e][0], xr[m][e][1]};
      *(volatile LAS u32x2*)(d + 1024 + 8 * e) = (u32x2){xr[m][e][2], xr[m][e][3]};
    }
  };
  auto poison_u = [&]() {
#pragma unroll
    for (int k = 0; k < 2; ++k) {
      const unsigned o = (unsigned)(wj * 2 + k) * 1024u + l16;
      const unsigned rt = o / 3584u, r = o - rt * 3584u;
      st16(rs, WS_UF + ((unsigned)(R * 4) + rt) * 8u * 3584u + (unsigned)c * 3584u + r, (u32x4){~0u, ~0u, ~0u, ~0u});
    }
  };
  const unsigned wob = WS_WOUTF + (unsigned)c * WBLK;
  const LAS char* wrd = (const LAS char*)(lds + L_WS) + w * 8192 + n16 * 256 + ((h4 ^ ((4 - (n16 >> 2)) & 3)) * 64);
#define DMA_W(g, q) __builtin_amdgcn_raw_ptr_buffer_load_lds(rsp, (LAS void*)(lds + L_WS + w * 8192 + (((g) + 1) & 1) * 4096 + (q) * 1024), 16, WVO(q), WSO(g, q), 0, 0)
#define RD_W(g) do { _Pragma("unroll") for (int i_ = 0; i_ < 4; ++i_) \
                      wsp[(g) & 3][((g) >> 2) * 4 + i_] = *(const volatile LAS u32x4*)(wrd + (((g) + 1) & 1) * 4096 + ((i_ ^ (n16 & 3)) * 16)); } while (0)
  auto park = [&]() {
    LAS char* d = (LAS char*)(lds + L_WS) + w * 8192 + l * 16;
#pragma unroll
    for (int g = 0; g < NRAW / 4; ++g) {
#pragma unroll
      for (int q = 0; q < 4; ++q) *(volatile LAS u32x4*)(d + ((g + 1) & 1) * 4096 + q * 1024) = raw[4 * g + q];
      if (g < 2) RD_W(g);
    }
    asm volatile("s_waitcnt lgkmcnt(0)" ::: "memory");
  };
#define DMA_AB(k) dma1k(rs, lds + L_AB + ((k) * 8 + w) * 1024, l16, wob + (unsigned)((k) * 8 + w) * 1024u)
  const int rto = w & 3, cls = w >> 2;
  u32x2 xp[3];
  constexpr int W = G2W;
  const unsigned zb = WS_Z2F + (unsigned)c * 458752u + (unsigned)wk * 8192u;
  const unsigned fzo = WS_FZ + (unsigned)(c * 32 + wk * 4 + (l & 3)) * 128u;
  u32x4 fr[W];
#define LDZ(i) fr[(i) % W] = ld16(rs, l16, zb + (unsigned)((i) >> 3) * 65536u + (unsigned)((i) & 7) * 1024u)
  u32x4 cr[5];
#define LDCR(k) cr[k] = ld16(rs, l16, wob + 81920u + (unsigned)(w * 5 + (k)) * 1024u)
  auto rd_xp = [&]() {
#pragma unroll
    for (int j = 0; j < 3; ++j) {
      const int q4 = 5 * c + (cls ? (3 + (j < 2 ? j : 1)) : j);
      xp[j] = *(const volatile LAS u32x2*)(xa + ((q4 >> 1) * 4 + rto) * 1024 + (q4 & 1) * 8);
    }
  };
  if (w < 7) {
    u32x4 B2[20], B1[20];
    const unsigned b2o = WS_WINF + (unsigned)((8 + c) * 7 + w) * 20480u, b1o = WS_WINF + (unsigned)(c * 7 + w) * 20480u;
#define LDB2(ks) B2[ks] = ld16(rs, l16, b2o + (unsigned)(ks) * 1024u)
#define LDB1(ks) B1[ks] = ld16(rs, l16, b1o + (unsigned)(ks) * 1024u)
    const bool early = __all(e_fw1 == tag);
    asm volatile("" ::: "memory");
    {
      const unsigned le = early ? l16 : OOB;
#pragma unroll
      for (int ks = 0; ks < NB2A; ++ks) B2[ks] = ld16(rs, le, b2o + (unsigned)ks * 1024u);
      SCHED();
      if (is_conv) VMW(NB2A);
    }
    STAMP(33);
    BAR();
    STAMP(37);
    if (!early) {
      wait_flag<4>(rs, fw1o, tag);
      DRAIN();
      st4(rs, l == 0 ? fw2 : OOB, tag);
#pragma unroll
      for (int ks = 0; ks < NB2A; ++ks) LDB2(ks);
    }
#pragma unroll
    for (int ks = NB2A; ks < 20; ++ks) {
      LDB2(ks);
      if (ks == 12) { SCHED(); park(); SCHED(); }
      if (ks == 13) { SCHED(); __builtin_amdgcn_s_barrier(); SCHED(); }
    }
    STAMP(9);
    BAR();
    STAMP(10);
    ld_x1();
    SCHED();
    poison_u();
    SCHED();
#pragma unroll
    for (int m = 0; m < 3; ++m) put_x1(m);
    if (w == 6) __builtin_amdgcn_s_setprio(2);
    BAR();
    STAMP(11);
    f32x4 acc[4];
    u32x4 A[2][4];
    lda1(0, A[0]);
#pragma unroll
    for (int ks = 0; ks < 20; ++ks) {
      if (ks == 6) put_x1(3);
      if (ks == 8) put_x1(4);
      if (ks == 11) BAR();
      if (ks >= 4) LDB1(ks - 4);
      if (ks >= 16) LDB1(ks);
      if (ks == 12) RD_W(2);
      SCHED();
#pragma unroll
      for (int rt = 0; rt < 4; ++rt) {
        if (ks < 19) A[(ks + 1) & 1][rt] = LDA(ks + 1, rt);
        SCHED();
        acc[rt] = MFMA16(A[ks & 1][rt], B2[ks], ks ? acc[rt] : ZERO4);
        SCHED();
      }
    }
    STAMP(12);
    if (w == 6) {
      if (n16 == 12) {
#pragma unroll
        for (int rt = 0; rt < 4; ++rt) *(f32x4*)(lds + L_S1 + (rt * 16 + 4 * h4) * 4) = acc[rt];
      }
      asm volatile("" ::: "memory");
      *(volatile LAS unsigned*)(lds + L_S1 + 256) = tag;
      __builtin_amdgcn_s_setprio(0);
    }
    VMW(20);
    BAR();
    STAMP(13);
    f32x2 zz[8];
    f32x4 cs1a, cs1b;
    {
      f32x4 rs4[4], nm4[4];
#pragma unroll
      for (int rt = 0; rt < 4; ++rt) {
        rs4[rt] = *(const LAS f32x4*)(tb4 + (L_T2 - L_MISC) + rt * 64);
        nm4[rt] = *(const LAS f32x4*)(tb4 + (L_T2 - L_MISC) + 256 + rt * 64);
      }
      const f32x2 cs2 = *(const f32x2*)(lds + L_CS1 + (112 + 16 * w + n16) * 8);
      const f32x2 csv = {cs2[0], cs2[0]}, b1v = {cs2[1], cs2[1]};
      float v1[16], v2[16];
#pragma unroll
      for (int i = 0; i < 8; ++i) {
        const int rt = i >> 1;
        const f32x2 a2 = i & 1 ? HI2(acc[rt]) : LO2(acc[rt]), n2 = i & 1 ? HI2(nm4[rt]) : LO2(nm4[rt]), r2 = i & 1 ? HI2(rs4[rt]) : LO2(rs4[rt]);
        zz[i] = gelu2(__builtin_elementwise_fma(__builtin_elementwise_fma(n2, csv, a2), r2, b1v));
        asm volatile("" : "+v"(zz[i]));
        DMA_W(3 + (i >> 2), i & 3);
        const f32x2 sq = zz[i] * zz[i];
        v1[2 * i] = zz[i][0]; v1[2 * i + 1] = zz[i][1]; v2[2 * i] = sq[0]; v2[2 * i + 1] = sq[1];
      }
      const float t1 = tr_sum16(v1, l);
      const float t2 = tr_sum16(v2, l);
      *(f32x2*)(lds + L_Z1S + (w * 64 + (n16 >> 2) * 16 + 4 * h4 + (n16 & 3)) * 8) = (f32x2){t1, t2};
      lda1(0, A[0]);
      cs1a = *(const f32x4*)(lds + L_CS1 + (16 * w + 4 * h4) * 8);
      cs1b = *(const f32x4*)(lds + L_CS1 + (16 * w + 4 * h4) * 8 + 16);
    }
    BAR();
    STAMP(14);
#pragma unroll
    for (int ks = 0; ks < 20; ++ks) {
      if (ks == Z1SPLIT) {
        STAMP(15);
        BAR();
        STAMP(16);
        f32x4 ra[4], rb[4];
#pragma unroll
        for (int rt = 0; rt < 4; ++rt) {
          ra[rt] = *(const LAS f32x4*)(tb4 + (L_ST2 - L_MISC) + rt * 64);
          rb[rt] = *(const LAS f32x4*)(tb4 + (L_ST2 - L_MISC) + 256 + rt * 64);
        }
#pragma unroll
        for (int i = 0; i < 8; ++i) {
          zz[i] = __builtin_elementwise_fma(zz[i], i & 1 ? HI2(ra[i >> 1]) : LO2(ra[i >> 1]), i & 1 ? HI2(rb[i >> 1]) : LO2(rb[i >> 1]));
          asm volatile("" : "+v"(zz[i]));
        }
        const u32x4 o0 = {pack2(zz[0][0], zz[2][0]), pack2(zz[0][1], zz[2][1]), pack2(zz[1][0], zz[3][0]), pack2(zz[1][1], zz[3][1])};
        const u32x4 o1 = {pack2(zz[4][0], zz[6][0]), pack2(zz[4][1], zz[6][1]), pack2(zz[5][0], zz[7][0]), pack2(zz[5][1], zz[7][1])};
        const unsigned zo = WS_Z2F + (unsigned)((c * 7 + w) * 64 + 4 * (R >> 1) + h4) * 1024u + (unsigned)(R & 1) * 512u + (unsigned)n16 * 16u;
        st16(rs, zo, o0);
        st16(rs, zo + 256u, o1);
        SCHED();
      }
      if (ks == 0) { VMW(4); RD_W(3); }
      if (ks == 2) { VMW(0); RD_W(4); }
      if (ks == 8) { VMW(4 + 2); RD_W(5); }
      if (ks == 11) rd_xp();
      if (ks == 13) { VMW(4 + 5 + 2); RD_W(6); }
      if (ks >= 13 && ks < 19) DMA_AB(ks - 13);
      if (ks >= 2 && ks < 6) { DMA_W(5 + ((ks - 2) >> 1), 2 * (ks & 1)); DMA_W(5 + ((ks - 2) >> 1), 2 * (ks & 1) + 1); }
      if (ks >= 6 && ks < 9) LDCR(ks - 6);
      if (ks == 12) { LDCR(3); LDCR(4); }
      if (ks == 10 || ks == 11) { DMA_W(7, 2 * (ks & 1)); DMA_W(7, 2 * (ks & 1) + 1); }
      SCHED();
#pragma unroll
      for (int rt = 0; rt < 4; ++rt) {
        if (ks < 19) A[(ks + 1) & 1][rt] = LDA(ks + 1, rt);
        SCHED();
        acc[rt] = MFMA16(B1[ks], A[ks & 1][rt], ks ? acc[rt] : ZERO4);
        SCHED();
      }
    }
    STAMP(18);
    VMW(2 + 2 + 6);
    RD_W(7);
    const f32x2 c01 = {cs1a[0], cs1a[2]}, b01 = {cs1a[1], cs1a[3]}, c23 = {cs1b[0], cs1b[2]}, b23 = {cs1b[1], cs1b[3]};
    u32x2 g1[4];
    auto z1g = [&](int rt) {
      const float r1 = *(const LAS float*)(tbn + (L_T2 - L_MISC) + rt * 64), n1 = *(const LAS float*)(tbn + (L_T2 - L_MISC) + 256 + rt * 64);
      const f32x2 r2 = {r1, r1}, n2 = {n1, n1};
      const f32x2 a = gelu2(__builtin_elementwise_fma(__builtin_elementwise_fma(n2, c01, LO2(acc[rt])), r2, b01));
      const f32x2 b = gelu2(__builtin_elementwise_fma(__builtin_elementwise_fma(n2, c23, HI2(acc[rt])), r2, b23));
      g1[rt] = (u32x2){pack2(a[0], a[1]), pack2(b[0], b[1])};
      asm volatile("" : "+v"(g1[rt]));
    };
    SCHED();
    VMW(2 + 6);
    BAR();
    STAMP(17);
#pragma unroll
    for (int rt = 0; rt < 4; ++rt) {
      if (rt < 2) { DMA_AB(6 + 2 * rt); DMA_AB(7 + 2 * rt); }
      z1g(rt);
      *(u32x2*)(lds + L_Z1S + ((w * 4 + rt) * 64 + l) * 8) = g1[rt];
    }
  } else {
    __builtin_amdgcn_s_setprio(3);
    DRAIN();
    BAR();
    if (is_conv && l == 0) st4(rs, WS_FX + (unsigned)quad * 128u, tag);
    STAMP(9);
    *(volatile LAS unsigned*)(lds + L_S1 + 256) = ~tag;
    u32x4 gv, cv[2][2][4];
    const unsigned gvo = WS_MOD + (unsigned)(5 * D_ + 80 * c) * 8u + (l < 40 ? l16 : 0u);
    auto ld_consts = [&]() {
      gv = ld16(rs, gvo);
#pragma unroll
      for (int i = 0; i < 16; ++i) cv[i >> 3][(i >> 2) & 1][i & 3] = ld16(rs, l < 56 ? l16 : 0u, WS_CS + (unsigned)(((c + 8 * (i >> 3)) * 4 + (i & 3)) * 112 + 56 * ((i >> 2) & 1)) * 16u);
    };
    auto ok_consts = [&]() {
      unsigned x = (gv[0] ^ tag) | (gv[2] ^ tag);
#pragma unroll
      for (int i = 0; i < 16; ++i) x |= (cv[i >> 3][(i >> 2) & 1][i & 3][0] ^ tag) | (cv[i >> 3][(i >> 2) & 1][i & 3][2] ^ tag);
      asm volatile("" : "+v"(x));
      return __all(x == 0u);
    };
    auto touch_w = [&](int k) {
      cpf ^= __builtin_amdgcn_raw_buffer_load_b32(rsp, (unsigned)(h4 * 4096 + (n16 >> 1) * 512 + (n16 & 1) * 128),
                                                  (unsigned)(R * 64 + (c & 3) * 16 + k * 4) * 4096u + 256u, 0);
    };
    park();
    __builtin_amdgcn_s_barrier();
    ld_consts();
    STAMP(31);
    _Pragma("unroll 1") for (unsigned spins = 0; spins < SPIN_CAP; ++spins) {
      const unsigned f0 = ld4(rs, WS_FX + (unsigned)l * 128u), f1 = ld4(rs, WS_FX + 8192u + (unsigned)l * 128u),
                     f2 = ld4(rs, l < 32 ? WS_FX + 16384u + (unsigned)l * 128u : WS_FW1 + (unsigned)(c * 32 + (l < 60 ? l - 32 : 0)) * 128u);
      if (__all(f0 == tag && f1 == tag && f2 == tag)) break;
      __builtin_amdgcn_s_sleep(POLL_SLEEP);
    }
    BAR();
    STAMP(10);
    ld_x1();
    __builtin_amdgcn_s_setprio(3);
    if (!ok_consts()) {
      _Pragma("unroll 1") for (unsigned spins = 0; spins < SPIN_CAP; ++spins) {
        ld_consts();
        if (ok_consts()) break;
        __builtin_amdgcn_s_sleep(8);
      }
    }
    if (l < 40) *(f32x2*)(lds + L_GB + l * 8) = (f32x2){__uint_as_float(gv[1]), __uint_as_float(gv[3])};
    if (l < 56) {
#pragma unroll
      for (int i = 0; i < 4; ++i) {
        const u32x4* q4 = cv[i >> 1][i & 1];
        *(f32x2*)(lds + L_CS1 + (56 * i + l) * 8) =
            (f32x2){(__uint_as_float(q4[0][1]) + __uint_as_float(q4[1][1])) + (__uint_as_float(q4[2][1]) + __uint_as_float(q4[3][1])),
                    (__uint_as_float(q4[0][3]) + __uint_as_float(q4[1][3])) + (__uint_as_float(q4[2][3]) + __uint_as_float(q4[3][3]))};
      }
    }
    SCHED();
#pragma unroll
    for (int m = 0; m < 3; ++m) put_x1(m);
    BAR();
    f32x4 acc[4];
    u32x4 A[2][4];
    lda1(0, A[0]);
#pragma unroll
    for (int ks = 0; ks < 20; ++ks) {
      if (ks == 6) put_x1(3);
      if (ks == 8) put_x1(4);
      if (ks == 11) BAR();
      if (ks == 4) RD_W(2);
      if (ks >= 6 && ks < 14) DMA_W(3 + ((ks - 6) >> 2), (ks - 6) & 3);
      if (ks >= 1 && ks < 5) touch_w(ks - 1);
      if (ks < 19) lda1(ks + 1, A[(ks + 1) & 1]);
      SCHED();
#pragma unroll
      for (int rt = 0; rt < 4; ++rt) acc[rt] = MFMA16(A[ks & 1][rt], A[ks & 1][rt], ks ? acc[rt] : ZERO4);
      SCHED();
    }
    {
      if ((n16 >> 2) == h4) {
#pragma unroll
        for (int rt = 0; rt < 4; ++rt) {
          const float lo = (l & 1) ? acc[rt][1] : acc[rt][0], hi = (l & 1) ? acc[rt][3] : acc[rt][2];
          *(volatile LAS float*)(tbn + (L_T2 - L_MISC) + rt * 64) = (l & 2) ? hi : lo;
        }
      }
      const float s2 = *(volatile LAS float*)(lds + L_T2 + l * 4) * (1.0f / D_);
      float s1;
      _Pragma("unroll 1") for (unsigned spins = 0; spins < SPIN_CAP * 16; ++spins) {
        const unsigned f = *(volatile LAS unsigned*)(lds + L_S1 + 256);
        s1 = *(volatile LAS float*)(lds + L_S1 + l * 4);
        if (f == tag) break;
      }
      const float nm = s1 * (-1.0f / D_);
      *(float*)(lds + L_T2 + l * 4) = __builtin_amdgcn_rsqf(__builtin_fmaxf(s2 - nm * nm, 0.f) + 1e-5f);
      *(float*)(lds + L_T2 + 256 + l * 4) = nm;
    }
    BAR();
    __builtin_amdgcn_s_setprio(0);
    asm volatile("" ::"v"(cpf));
    DRAIN();
    RD_W(3); RD_W(4);
    rd_xp();
    asm volatile("s_waitcnt lgkmcnt(0)" ::: "memory");
#pragma unroll
    for (int i = 0; i < 8; ++i) DMA_W(5 + (i >> 2), i & 3);
    const unsigned e_fw2 = ld4(rs, WS_FW2 + (unsigned)(c * 8 + (l & 7)) * 128u);
    if (!__all(e_fw2 == tag)) wait_flag<2>(rs, WS_FW2 + (unsigned)(c * 8 + (l & 7)) * 128u, tag);
    __builtin_amdgcn_s_setprio(3);
    BAR();
    {
      f32x2 t = {0.f, 0.f};
#pragma unroll
      for (int k = 0; k < 7; ++k) t += *(const f32x2*)(lds + L_Z1S + (k * 64 + l) * 8);
      st16(rs, WS_PART + (unsigned)(R * 8 + c) * 1024u + l16, (u32x4){tag, __float_as_uint(t[0]), tag, __float_as_uint(t[1])});
    }
    STAMP(14);
    {
      u32x4 v[8];
      const unsigned po = WS_PART + (unsigned)R * 8192u + l16;
      __builtin_amdgcn_s_sleep(16);
      _Pragma("unroll 1") for (unsigned spins = 0; spins < SPIN_CAP; ++spins) {
#pragma unroll
        for (int k = 0; k < 8; ++k) v[k] = ld16(rs, po + (unsigned)k * 1024u);
        unsigned x = 0;
#pragma unroll
        for (int k = 0; k < 8; ++k) x |= (v[k][0] ^ tag) | (v[k][2] ^ tag);
        asm volatile("" : "+v"(x));
        if (__all(x == 0u)) break;
        __builtin_amdgcn_s_sleep(1);
      }
      float t1 = 0.f, t2 = 0.f;
#pragma unroll
      for (int k = 0; k < 8; ++k) { t1 += __uint_as_float(v[k][1]); t2 += __uint_as_float(v[k][3]); }
      const float mean = t1 * (1.0f / FH);
      const float var = __builtin_fmaxf(t2 * (1.0f / FH) - mean * mean, 0.f);
      const float rstd = __builtin_amdgcn_rsqf(var + 1e-5f);
      *(float*)(lds + L_ST2 + l * 4) = rstd;
      *(float*)(lds + L_ST2 + 256 + l * 4) = -mean * rstd;
    }
    STAMP(15);
    BAR();
    RD_W(5); RD_W(6);
    asm volatile("s_waitcnt lgkmcnt(0)" ::: "memory");
#pragma unroll
    for (int q = 0; q < 4; ++q) DMA_W(7, q);
    DRAIN();
    RD_W(7);
    BAR();
    if (l == 0) st4(rs, WS_FZ + (unsigned)(c * 32 + R) * 128u, tag);
    STAMP(17);
    __builtin_amdgcn_s_setprio(0);
#pragma unroll
    for (int k = 0; k < 10; ++k) DMA_AB(k);
#pragma unroll
    for (int k = 0; k < 5; ++k) LDCR(k);
  }
  const rsrc_t rout = __builtin_amdgcn_make_buffer_rsrc((void*)p.out, 0, S_ * D_ * 2, 0x00020000);
  STAMP(19);
  u32x2 z1o[4];
  wait_flag<POLL_SLEEP>(rs, fzo, tag);
#pragma unroll
  for (int i = 0; i < W; ++i) { LDZ(i); SCHED(); }
  STAMP(20);

  const unsigned ub = WS_UF + (unsigned)((R * 4 + rto) * 8 + c) * 3584u;
  const float bs = bflo(bs_raw);
  const unsigned ur = WS_UF + (unsigned)((R * 4 + rto) * 8 + cls) * 3584u;
  u32x4 uf[4][2];
  f32x4 acc[2][4];
  f32x4* const bufw = (f32x4*)(lds + L_BUF) + w * 256 + l;
#define BUFW(slot, rt) bufw[(slot) * 2048 + (rt) * 64]
  {
    const f32x4* const rb = (const f32x4*)(lds + L_BUF) + cls * 2048 + rto * 64 + l;
    f32x4 rdv[4], s;
    u32x4 uo;
#pragma unroll
    for (int j = 0; j < 7; ++j) {
#pragma unroll
      for (int ks = 0; ks < 8; ++ks) {
        const int i = 8 * j + ks;
#pragma unroll
        for (int rt = 0; rt < 4; ++rt) acc[j & 1][rt] = MFMA16(fr[i % W], wsp[rt][ks], ks ? acc[j & 1][rt] : ZERO4);
        if (i == 0) STAMP(21);
        if (i == 24) STAMP(22);
        SCHED();
        if (i + W < 56) LDZ(i + W);
        if (j > 0) {
          if (ks == 0 && (j & 1)) BAR();
          if (j == 1 && ks == 0) {
#pragma unroll
            for (int k = 0; k < 4; ++k) z1o[k] = *(const u32x2*)(lds + L_Z1S + (((k < 3 ? 2 * k + cls : 6) * 4 + rto) * 64 + l) * 8);
          }
          if (i >= (56 - G2W) && i < 53) {
#pragma unroll
            for (int k = 0; k < 8; ++k)
              if ((56 - G2W) + k * (53 - (56 - G2W)) / 8 == i) uf[k >> 1][k & 1] = ld16(rs, l16, ur + (unsigned)(k >> 1) * 7168u + (unsigned)(k & 1) * 1024u);
            SCHED();
          }
          const bool fast = j == 6;
          if (!fast && ks < 4) BUFW((j - 1) & 1, ks) = acc[(j - 1) & 1][ks];
          if (fast && ks < 2) { BUFW(1, 2 * ks) = acc[1][2 * ks]; BUFW(1, 2 * ks + 1) = acc[1][2 * ks + 1]; }
          if (!(j & 1)) {
            const int k0 = fast ? 2 : 5;
            if (ks == k0) {
              BAR();
#pragma unroll
              for (int k = 0; k < 4; ++k) rdv[k] = rb[k * 256];
            }
            if (ks == k0 + 1) {
              s = (rdv[0] + rdv[1]) + (rdv[2] + rdv[3]);
#pragma unroll
              for (int k = 0; k < 4; ++k) rdv[k] = rb[(4 + k) * 256];
            }
            if (ks == k0 + 2) {
              s += (rdv[0] + rdv[1]) + (rdv[2] + rdv[3]);
              const u32x2 z = z1o[(j >> 1) - 1];
              const int e = j == 4 ? 2 : 0;
              uo[e] = pack2(bflo(z[0]) * (s[0] + bs), bfhi(z[0]) * (s[1] + bs));
              uo[e + 1] = pack2(bflo(z[1]) * (s[2] + bs), bfhi(z[1]) * (s[3] + bs));
              if (j == 4) st16(rs, ub + (unsigned)cls * 1024u + l16, uo);
              if (j == 6) st16(rs, l < 32 ? ub + 2048u + (unsigned)cls * 512u + l16 : OOB, half_swap((u32x4){uo[0], uo[1], uo[0], uo[1]}));
            }
            if (j == 6 && ks == 4) BAR();
            if (j == 6 && ks >= 4 && ks < 7) {
#pragma unroll
              for (int k = 0; k < 5; ++k)
                if (k / 2 == ks - 4) *(u32x4*)(lds + (w < 6 ? L_BUF + 32768 + w * 5120 : L_Z1S + (w - 6) * 5120) + k * 1024 + l16) = cr[k];
            }
          }
        }
        SCHED();
      }
    }
  }
  STAMP(23);

  {
    u32x4 uc[4], uh[4];
    const unsigned uho = (unsigned)(l >> 5) * 3584u + (unsigned)(l & 31) * 16u;
#define LD_C(cc) uc[cc] = ld16(rs, l16, ur + (unsigned)(cc) * 7168u + 2048u)
#define LD_H(cc) uh[cc] = ld16(rs, uho, WS_UF + (unsigned)((R * 4 + rto) * 8) * 3584u + (unsigned)(cc) * 7168u + 3072u)
    auto mx = [](unsigned a, unsigned b) { return __builtin_elementwise_max(a, b); };
    auto ok4 = [&](const u32x4* v, int n) {
      unsigned m = 0;
#pragma unroll
      for (int i = 0; i < 8; ++i)
        if (i < n) m = mx(m, mx(mx(v[i][0], v[i][1]), mx(v[i][2], v[i][3])));
      return __all(m != ~0u);
    };
#pragma unroll
    for (int rt = 0; rt < 4; ++rt) BUFW(0, rt) = acc[0][rt];
    BAR();
    f32x4 a3[5];
    u32x4 af[2][5];
    const LAS char* wb[2] = {(const LAS char*)(lds + L_AB) + cls * (10240 + 3072) + l16, (const LAS char*)(lds + L_AB) + cls * (10240 - 2048) + l16};
    const LAS char* wb2[2] = {wb[0] + 40960, wb[1] + 40960};
    asm volatile("" : "+v"(wb[0]), "+v"(wb2[0]), "+v"(wb[1]), "+v"(wb2[1]));
    u32x4 ho = {0u, 0u, 0u, 0u};
    if (w < 4) {
      __builtin_amdgcn_s_setprio(2);
      const f32x4* const rb = (const f32x4*)(lds + L_BUF) + rto * 64 + l;
      f32x4 r8[8];
#pragma unroll
      for (int k = 0; k < 8; ++k) r8[k] = rb[k * 256];
      const f32x4 s = ((r8[0] + r8[1]) + (r8[2] + r8[3])) + ((r8[4] + r8[5]) + (r8[6] + r8[7]));
      const u32x2 z = z1o[3];
      const unsigned o0 = pack2(bflo(z[0]) * (s[0] + bs), bfhi(z[0]) * (s[1] + bs)), o1 = pack2(bflo(z[1]) * (s[2] + bs), bfhi(z[1]) * (s[3] + bs));
      ho = half_swap((u32x4){o0, o1, o0, o1});
    }
    st16(rs, tid < 256 && l < 32 ? ub + 3072u + l16 : OOB, ho);
    SCHED();
    __builtin_amdgcn_s_setprio(0);
#pragma unroll
    for (int dj = 0; dj < 5; ++dj) af[0][dj] = *(const LAS u32x4*)(wb[dj >= 2] + dj * 1024);
    const bool ab_ok = ok4(&uf[0][0], 8);
    STAMP(24);
    if (!ab_ok) {
      _Pragma("unroll 1") for (unsigned spins = 0; spins < SPIN_CAP; ++spins) {
#pragma unroll
        for (int i = 0; i < 8; ++i) uf[i >> 1][i & 1] = ld16(rs, l16, ur + (unsigned)(i >> 1) * 7168u + (unsigned)(i & 1) * 1024u);
        if (ok4(&uf[0][0], 8)) break;
        __builtin_amdgcn_s_sleep(1);
      }
    }
    STAMP(25);
    const LAS char* cb0[2] = {(const LAS char*)(lds + L_BUF + 32768) + cls * (5120 + 3072) + l16, (const LAS char*)(lds + L_BUF + 32768) + cls * (5120 - 2048) + l16};
    u32x4 ah[4][3];
    const LAS char* hb = (const LAS char*)(lds + L_BUF) + cls * 12288 + l * 8;
    f32x4 gb[3], xo3[3], xf[3];
    LAS char *otw, *xw3;
    const LAS char *ota, *otb, *xr3;
    unsigned oa_o, ob_o;
#pragma unroll
    for (int st = 0; st < 12; ++st) {
      if (st < 2) { LD_C(2 * st); LD_C(2 * st + 1); }
      else if (st < 7 && w < 4) dma1k(rs, lds + L_BUF + ((st - 2) * 4 + w) * 1024, l16, wob + 122880u + (unsigned)(w * 5 + st - 2) * 1024u);
      if (st == 2) {
        LAS char* ot = (LAS char*)(lds + L_AB + 32768) + w * 2048;
        otw = ot + n16 * 112 + h4 * 8;
        ota = ot + (l >> 2) * 112 + (l & 3) * 16;
        otb = ot + ((l >> 1) & 15) * 112 + 64 + (l & 1) * 16;
        asm volatile("" : "+v"(otw), "+v"(ota), "+v"(otb));
      }
      if (st == 3) {
        oa_o = (unsigned)(l >> 2) * 2560u + (unsigned)(l & 3) * 16u;
        ob_o = l < 32 ? (unsigned)(l >> 1) * 2560u + 64u + (unsigned)(l & 1) * 16u : OOB;
        xw3 = (LAS char*)(lds + L_AB) + w * 3072 + l16;
        xr3 = (const LAS char*)(lds + L_AB) + (w ^ 4) * 3072 + cls * 1024 + l16;
        asm volatile("" : "+v"(oa_o), "+v"(ob_o), "+v"(xw3), "+v"(xr3));
      }
      if (st >= 4 && st < 7) {
        const int j = st - 4;
        gb[j] = *(const f32x4*)(lds + L_GB + (48 * cls + 16 * (cls && j == 2 ? 1 : j) + 4 * h4) * 4);
        xf[j] = (f32x4){bflo(xp[j][0]), bfhi(xp[j][0]), bflo(xp[j][1]), bfhi(xp[j][1])};
        asm volatile("" : "+v"(xf[j]));
      }
      if (st == 8) {
        STAMP(26);
        if (!ok4(uc, 4)) {
          _Pragma("unroll 1") for (unsigned spins = 0; spins < SPIN_CAP; ++spins) {
            LD_C(0); LD_C(1); LD_C(2); LD_C(3);
            if (ok4(uc, 4)) break;
            __builtin_amdgcn_s_sleep(1);
          }
        }
        LD_H(0); LD_H(1); LD_H(2); LD_H(3);
#pragma unroll
        for (int cc = 0; cc < 4; ++cc) uc[cc] = half_swap(uc[cc]);
        VMW(4);
        __builtin_amdgcn_s_barrier();
        asm volatile("" ::: "memory");
        STAMP(27);
      }
      SCHED();
#pragma unroll
      for (int dj = 0; dj < 5; ++dj) {
        const int sn = st + 1;
        if (sn < 8) af[sn & 1][dj] = *(const LAS u32x4*)((sn < 4 ? wb : wb2)[dj >= 2] + ((sn >> 1) & 1) * 20480 + ((sn & 1) * 5 + dj) * 1024);
        else if (sn < 12) af[sn & 1][dj] = *(const LAS u32x4*)(cb0[dj >= 2] + (sn < 11 ? (sn - 8) * 10240 : L_Z1S - L_BUF - 32768) + dj * 1024);
        if (st >= 8) {
#pragma unroll
          for (int k = 0; k < 2; ++k) {
            const int h = st < 11 ? (k ? 99 : (st - 8) * 5 + dj) : 15 + 2 * dj + k;
            if (h < 24) {
              const u32x2 v = *(const volatile LAS u32x2*)(hb + (((h >> 1) % 3) * 4 + (h >> 1) / 3) * 1024 + (h & 1) * 512);
              ah[(h >> 1) / 3][(h >> 1) % 3][2 * (h & 1)] = v[0]; ah[(h >> 1) / 3][(h >> 1) % 3][2 * (h & 1) + 1] = v[1];
            }
          }
        }
        SCHED();
        a3[dj] = MFMA16(af[st & 1][dj], st < 8 ? uf[(st >> 1) & 3][st & 1] : uc[st & 3], st ? a3[dj] : ZERO4);
        SCHED();
      }
    }
#pragma unroll
    for (int j = 0; j < 3; ++j) *(volatile LAS f32x4*)(xw3 + j * 1024) = a3[2 + j];
    BAR();
#pragma unroll
    for (int j = 0; j < 3; ++j) xo3[j] = *(const volatile LAS f32x4*)(xr3 + j * 1024);
    SCHED();
    if (!ok4(uh, 4)) {
      _Pragma("unroll 1") for (unsigned spins = 0; spins < SPIN_CAP; ++spins) {
        LD_H(0); LD_H(1); LD_H(2); LD_H(3);
        if (ok4(uh, 4)) break;
        __builtin_amdgcn_s_sleep(1);
      }
    }
#pragma unroll
    for (int cc = 0; cc < 4; ++cc) {
      const u32x4 b = half_swap(uh[cc]);
#pragma unroll
      for (int j = 0; j < 3; ++j) a3[j] = MFMA16(ah[cc][j], b, a3[j]);
      if (cc >= 1) {
        const int j = cc - 1;
        xo3[j] = __builtin_elementwise_fma(gb[j], xo3[j], xf[j]);
        asm volatile("" : "+v"(xo3[j]));
      }
      SCHED();
    }
#pragma unroll
    for (int j = 0; j < 3; ++j) {
      const f32x4 r = __builtin_elementwise_fma(gb[j], a3[j], xo3[j]);
      *(volatile LAS u32x2*)(otw + j * 32) = (u32x2){pack2(r[0], r[1]), pack2(r[2], r[3])};
    }
    const u32x4 oa = *(const volatile LAS u32x4*)(ota);
    const u32x4 ob = *(const volatile LAS u32x4*)(otb);
    const unsigned obase = (unsigned)((R * 64 + 32 * ((w >> 1) & 1) + (w & 1)) * D_ + 80 * c + 48 * cls) * 2u;
    __builtin_amdgcn_raw_buffer_store_b128(oa, rout, oa_o, obase, 0);
    if (cls == 0) __builtin_amdgcn_raw_buffer_store_b128(ob, rout, ob_o, obase, 0);
    if (bid == 0 && tid == 0) st4(rs, WS_EPOCH, tag);
  }
  STAMP(30);
  asm volatile(".Lmega_end:");
}

extern "C" void kernel_launch(void* const* d_in, const int* in_sizes, int n_in, void* d_out, int out_size,
                              void* d_ws, size_t ws_size, hipStream_t stream) {
  Params p;
  p.q = (const u16*)d_in[0];
  p.k = (const u16*)d_in[1];
  p.v = (const u16*)d_in[2];
  p.shift_k = (const u16*)d_in[3];
  p.ssm_k = (const u16*)d_in[4];
  p.ssm_D = (const u16*)d_in[5];
  p.w_in = (const u16*)d_in[7];
  p.w_sp = (const u16*)d_in[8];
  p.b_sp = (const u16*)d_in[9];
  p.w_out = (const u16*)d_in[10];
  p.cvec = (const u16*)d_in[11];
  p.w_mod = (const u16*)d_in[12];
  p.out = (u16*)d_out;
  p.ws = (char*)d_ws;
  mega<<<256, 512, 0, stream>>>(p);
}
```

```cpp
#include <hip/hip_runtime.h>
#include <stdint.h>

typedef unsigned short u16;
typedef __attribute__((ext_vector_type(8))) short bf16x8;
typedef __attribute__((ext_vector_type(4))) float f32x4;
typedef __attribute__((ext_vector_type(2))) float f32x2;
typedef __attribute__((ext_vector_type(4))) unsigned int u32x4;
typedef __attribute__((ext_vector_type(2))) unsigned int u32x2;
#define LAS __attribute__((address_space(3)))
#define CAS __attribute__((address_space(4)))
#define GAS __attribute__((address_space(1)))
typedef __amdgpu_buffer_rsrc_t rsrc_t;
typedef __attribute__((ext_vector_type(4))) short s16x4;

constexpr int S_ = 2048, D_ = 640, F_ = 1728, FH = 864, CB = 108;

#define X1PAD 256
constexpr unsigned WS_EPOCH = 0;
constexpr unsigned WS_FX = 4096;
constexpr unsigned WS_FZ = WS_FX + 24576;
constexpr unsigned WS_FW2 = WS_FZ + 32768;
constexpr unsigned WS_FW1 = WS_FW2 + 8192;
constexpr unsigned WS_CS = WS_FW1 + 65536;
constexpr unsigned WS_PART = WS_CS + 131072;
constexpr unsigned WS_MOD = WS_PART + 262144;
constexpr unsigned WS_ST1 = WS_MOD + 32768;
constexpr unsigned WS_X1B = WS_ST1 + 16384;
constexpr unsigned WX1P = 40960 + X1PAD;
constexpr unsigned WBLK = 143360;
constexpr unsigned WS_QB = WS_X1B + 2686976;
constexpr unsigned WS_WINF = WS_QB + 2686976;
constexpr unsigned WS_WOUTF = WS_WINF + 16 * WBLK;
constexpr unsigned WS_Z2F = WS_WOUTF + 8 * WBLK;
constexpr unsigned WS_UF = WS_Z2F + 8 * 64 * 7168;
constexpr unsigned WS_KVT = WS_UF + 32 * 4 * 8 * 3584;
constexpr unsigned WS_FK = WS_KVT + 32 * 81920;
constexpr unsigned WS_STAMP = WS_FK + 20480;
constexpr unsigned WS_END = WS_STAMP + 256 * 40 * 8;

constexpr int U_BYTES = (256 + S_) * 2;
constexpr int G_COPY = 4160;
constexpr int CH_BYTES = U_BYTES + 4 * G_COPY;
constexpr int L_YP = 4 * CH_BYTES;
constexpr int L_A0 = 98304;
constexpr int L_RED = 0;
constexpr int L_TAB = 2048;
constexpr int L_J3 = 8192;
constexpr int L_QT = 49152;
constexpr int L_J5 = L_QT;
constexpr int L_XI = 0;
constexpr int L_BUF = 81920;
constexpr int L_AB = 0;
constexpr int L_MISC = 147456;
constexpr int L_Z1S = L_MISC;
constexpr int L_S1 = L_MISC + 14336;
constexpr int L_T2 = L_S1 + 512;
constexpr int L_ST2 = L_T2 + 512;
constexpr int L_GB = L_ST2 + 512;
constexpr int L_CS1 = L_Z1S + 4096;
constexpr int L_WS = 81920;
constexpr int LDS_TOTAL = 163840;

constexpr int POLL_SLEEP = 1;
constexpr int AUX_SLEEP = 36, LATE_SLEEP = 96;
constexpr int CDIST = 4;
constexpr int NRAW = 12;
constexpr int SV_AT = 108, Q_AT = 130;
constexpr int NB2A = 6;
constexpr int G2W = 7;
constexpr int Z1SPLIT = 12;
constexpr int SC1 = 16;
constexpr unsigned SPIN_CAP = 1u << 16;
constexpr unsigned OOB = 0x80000000u;

__device__ __forceinline__ float bflo(unsigned x) { return __uint_as_float(x << 16); }
__device__ __forceinline__ float bfhi(unsigned x) { return __uint_as_float(x & 0xffff0000u); }
__device__ __forceinline__ f32x2 bf2(unsigned x) { return (f32x2){bflo(x), bfhi(x)}; }
__device__ __forceinline__ unsigned f2bf(float x) {
  __bf16 b = (__bf16)x;
  return (unsigned)__builtin_bit_cast(unsigned short, b);
}
__device__ __forceinline__ float rbf(float x) { return bflo(f2bf(x)); }
typedef __attribute__((ext_vector_type(2))) __bf16 bf2_t;
__device__ __forceinline__ unsigned pack2(float lo, float hi) {
  f32x2 v = {lo, hi};
  return __builtin_bit_cast(unsigned, __builtin_convertvector(v, bf2_t));
}
template <int CTRL>
__device__ __forceinline__ float dpp_f(float v) {
  return __int_as_float(__builtin_amdgcn_update_dpp(0, __float_as_int(v), CTRL, 0xf, 0xf, true));
}
__device__ __forceinline__ float sum_rows(float v) {
  auto a = __builtin_amdgcn_permlane16_swap(__float_as_uint(v), __float_as_uint(v), false, false);
  v = __uint_as_float(a[0]) + __uint_as_float(a[1]);
  auto b = __builtin_amdgcn_permlane32_swap(__float_as_uint(v), __float_as_uint(v), false, false);
  return __uint_as_float(b[0]) + __uint_as_float(b[1]);
}
__device__ __forceinline__ f32x2 gelu2(f32x2 x) {
  const float c0 = -2.0f * 0.7978845608028654f * 1.4426950408889634f;
  const float c1 = c0 * 0.044715f;
  const f32x2 t = x * __builtin_elementwise_fma(x * x, (f32x2){c1, c1}, (f32x2){c0, c0});
  const f32x2 d = (f32x2){__builtin_amdgcn_exp2f(t[0]), __builtin_amdgcn_exp2f(t[1])} + (f32x2){1.0f, 1.0f};
  return x * (f32x2){__builtin_amdgcn_rcpf(d[0]), __builtin_amdgcn_rcpf(d[1])};
}
#define LO2(v) __builtin_shufflevector(v, v, 0, 1)
#define HI2(v) __builtin_shufflevector(v, v, 2, 3)
__device__ __forceinline__ float tr_sum16(const float* v, int l) {
  float x[8], y[4];
  asm volatile(
      "s_nop 1\n\t"
      "v_add_f32_dpp %0, %8, %8 row_ror:8 row_mask:0xf bank_mask:0x3\n\t"
      "v_add_f32_dpp %1, %9, %9 row_ror:8 row_mask:0xf bank_mask:0x3\n\t"
      "v_add_f32_dpp %2, %10, %10 row_ror:8 row_mask:0xf bank_mask:0x3\n\t"
      "v_add_f32_dpp %3, %11, %11 row_ror:8 row_mask:0xf bank_mask:0x3\n\t"
      "v_add_f32_dpp %4, %12, %12 row_ror:8 row_mask:0xf bank_mask:0x3\n\t"
      "v_add_f32_dpp %5, %13, %13 row_ror:8 row_mask:0xf bank_mask:0x3\n\t"
      "v_add_f32_dpp %6, %14, %14 row_ror:8 row_mask:0xf bank_mask:0x3\n\t"
      "v_add_f32_dpp %7, %15, %15 row_ror:8 row_mask:0xf bank_mask:0x3\n\t"
      "v_add_f32_dpp %0, %16, %16 row_ror:8 row_mask:0xf bank_mask:0xc\n\t"
      "v_add_f32_dpp %1, %17, %17 row_ror:8 row_mask:0xf bank_mask:0xc\n\t"
      "v_add_f32_dpp %2, %18, %18 row_ror:8 row_mask:0xf bank_mask:0xc\n\t"
      "v_add_f32_dpp %3, %19, %19 row_ror:8 row_mask:0xf bank_mask:0xc\n\t"
      "v_add_f32_dpp %4, %20, %20 row_ror:8 row_mask:0xf bank_mask:0xc\n\t"
      "v_add_f32_dpp %5, %21, %21 row_ror:8 row_mask:0xf bank_mask:0xc\n\t"
      "v_add_f32_dpp %6, %22, %22 row_ror:8 row_mask:0xf bank_mask:0xc\n\t"
      "v_add_f32_dpp %7, %23, %23 row_ror:8 row_mask:0xf bank_mask:0xc"
      : "=&v"(x[0]), "=&v"(x[1]), "=&v"(x[2]), "=&v"(x[3]), "=&v"(x[4]), "=&v"(x[5]), "=&v"(x[6]), "=&v"(x[7])
      : "v"(v[0]), "v"(v[1]), "v"(v[2]), "v"(v[3]), "v"(v[4]), "v"(v[5]), "v"(v[6]), "v"(v[7]), "v"(v[8]), "v"(v[9]), "v"(v[10]),
        "v"(v[11]), "v"(v[12]), "v"(v[13]), "v"(v[14]), "v"(v[15]));
  asm volatile(
      "s_nop 1\n\t"
      "v_add_f32_dpp %0, %4, %4 row_shl:4 row_mask:0xf bank_mask:0x5\n\t"
      "v_add_f32_dpp %1, %5, %5 row_shl:4 row_mask:0xf bank_mask:0x5\n\t"
      "v_add_f32_dpp %2, %6, %6 row_shl:4 row_mask:0xf bank_mask:0x5\n\t"
      "v_add_f32_dpp %3, %7, %7 row_shl:4 row_mask:0xf bank_mask:0x5\n\t"
      "v_add_f32_dpp %0, %8, %8 row_shr:4 row_mask:0xf bank_mask:0xa\n\t"
      "v_add_f32_dpp %1, %9, %9 row_shr:4 row_mask:0xf bank_mask:0xa\n\t"
      "v_add_f32_dpp %2, %10, %10 row_shr:4 row_mask:0xf bank_mask:0xa\n\t"
      "v_add_f32_dpp %3, %11, %11 row_shr:4 row_mask:0xf bank_mask:0xa\n\t"
      "s_nop 1"
      : "=&v"(y[0]), "=&v"(y[1]), "=&v"(y[2]), "=&v"(y[3])
      : "v"(x[0]), "v"(x[1]), "v"(x[2]), "v"(x[3]), "v"(x[4]), "v"(x[5]), "v"(x[6]), "v"(x[7]));
  const bool b1 = l & 2, b0 = l & 1;
  const float z0 = (b1 ? y[2] : y[0]) + dpp_f<0x4E>(b1 ? y[0] : y[2]);
  const float z1 = (b1 ? y[3] : y[1]) + dpp_f<0x4E>(b1 ? y[1] : y[3]);
  return (b0 ? z1 : z0) + dpp_f<0xB1>(b0 ? z0 : z1);
}
__device__ __forceinline__ u32x4 half_swap(u32x4 v) {
  const auto a = __builtin_amdgcn_permlane32_swap(v[0], v[2], false, false);
  const auto b = __builtin_amdgcn_permlane32_swap(v[1], v[3], false, false);
  return (u32x4){a[0], b[0], a[1], b[1]};
}
#define MFMA16(a, b, c) __builtin_amdgcn_mfma_f32_16x16x32_bf16(__builtin_bit_cast(bf16x8, a), __builtin_bit_cast(bf16x8, b), c, 0, 0, 0)
__device__ __forceinline__ u32x2 trd(const char* p) {
  return __builtin_bit_cast(u32x2, __builtin_amdgcn_ds_read_tr16_b64_v4i16((LAS s16x4*)p));
}
__device__ __forceinline__ u32x4 trfrag(const char* p, int second) {
  u32x2 a = trd(p), b = trd(p + second);
  return (u32x4){a[0], a[1], b[0], b[1]};
}
#define BAR() asm volatile("s_waitcnt lgkmcnt(0)\n\ts_barrier" ::: "memory")
#define DRAIN() do { __builtin_amdgcn_s_waitcnt(0x0F70); asm volatile("" ::: "memory"); } while (0)
#define VMW(n) do { __builtin_amdgcn_s_waitcnt(0x0F70 | ((n) & 15) | (((n) >> 4) << 14)); asm volatile("" ::: "memory"); } while (0)
#define SCHED() __builtin_amdgcn_sched_barrier(0)
#define ZERO4 ((f32x4){0.f, 0.f, 0.f, 0.f})

__device__ __forceinline__ u32x4 ld16(rsrc_t r, unsigned off, unsigned so = 0) { return __builtin_amdgcn_raw_buffer_load_b128(r, off, so, SC1); }
__device__ __forceinline__ u32x2 ld8(rsrc_t r, unsigned off, unsigned so = 0) { return __builtin_amdgcn_raw_buffer_load_b64(r, off, so, SC1); }
__device__ __forceinline__ unsigned ld4(rsrc_t r, unsigned off, unsigned so = 0) { return __builtin_amdgcn_raw_buffer_load_b32(r, off, so, SC1); }
__device__ __forceinline__ void st16(rsrc_t r, unsigned off, u32x4 v) { __builtin_amdgcn_raw_buffer_store_b128(v, r, off, 0, SC1); }
__device__ __forceinline__ void st8(rsrc_t r, unsigned off, u32x2 v) { __builtin_amdgcn_raw_buffer_store_b64(v, r, off, 0, SC1); }
__device__ __forceinline__ void st4(rsrc_t r, unsigned off, unsigned v) { __builtin_amdgcn_raw_buffer_store_b32(v, r, off, 0, SC1); }
__device__ __forceinline__ void dma1k(rsrc_t r, char* lds_dst, unsigned l16, unsigned soff) {
  __builtin_amdgcn_raw_ptr_buffer_load_lds(r, (LAS void*)lds_dst, 16, l16, soff, 0, SC1);
}

#define STAMP(i)

template <int SLEEP>
__device__ __forceinline__ void wait_flag(rsrc_t r, unsigned off, unsigned tag) {
  _Pragma("unroll 1") for (unsigned spins = 0; spins < SPIN_CAP; ++spins) {
    if (__all(ld4(r, off) == tag)) break;
    __builtin_amdgcn_s_sleep(SLEEP);
  }
  asm volatile("" ::: "memory");
}

struct ConvSched {
  signed char pp[144], m[144], rd[144];
  int pre;
};
constexpr ConvSched conv_sched(int dist) {
  ConvSched s = {};
  int need[96] = {}, i = 0;
  for (int r = 0; r < 4; ++r)
    for (int pp = 0; pp < 8; ++pp) {
      const int j = 24 * r + (pp ? 8 + 2 * pp : 0);
      need[j] = need[j + 1] = i;
      for (int m = 7 - pp; m >= 0; --m) {
        if (pp == 0) need[24 * r + 9 - m] = i;
        s.pp[i] = pp; s.m[i] = m; s.rd[i] = -1;
        ++i;
      }
    }
  int next = 144;
  for (int j = 95; j >= 0; --j) {
    next = need[j] - dist < next - 1 ? need[j] - dist : next - 1;
    if (next >= 0) s.rd[next] = j;
    else if (s.pre == 0) s.pre = j + 1;
  }
  return s;
}

template <int I> struct IC { static constexpr int v = I; };
template <int I0, int I1, class F>
__device__ __forceinline__ void static_for(F&& f) {
  if constexpr (I0 < I1) { f(IC<I0>{}); static_for<I0 + 1, I1>(f); }
}

struct Params {
  const u16 *q, *k, *v, *shift_k, *ssm_k, *ssm_D, *w_in, *w_sp, *b_sp, *w_out, *cvec, *w_mod;
  u16* out;
  char* ws;
};

__global__ __launch_bounds__(512, 2) void mega(Params) {
  __shared__ __attribute__((aligned(16))) char lds[LDS_TOTAL];
  const CAS Params* kp = (const CAS Params*)__builtin_amdgcn_kernarg_segment_ptr();
  unsigned ka0, ka1;
  asm volatile("s_load_dword %0, %2, 0x0\n\ts_load_dword %1, %2, 0x40" : "=&s"(ka0), "=&s"(ka1) : "s"(kp));
  SCHED();
  const int tid = threadIdx.x, l = tid & 63;
  const int w = __builtin_amdgcn_readfirstlane(tid >> 6);
  const int bid = blockIdx.x, xcd = bid & 7, idx = bid >> 3;
  const int dg = ((idx & 7) + 2 * (idx >> 3)) & 7;
  const bool is_conv = dg >= 3;
  const int slot = 3 * (idx >> 3) + dg;
  const int cidx = 5 * (idx >> 3) + dg - 3;
  const int quad = xcd * 20 + cidx;
  const unsigned l16 = (unsigned)l * 16u;
  const int blk = (quad / 5) * 8 + w, sl = quad % 5;
  unsigned a0g = (unsigned)(l >> 4) * 1280u + (unsigned)(((l & 15) ^ (4 * (l >> 5))) * 16), a0x = a0g ^ 128u, a0h = a0g + (l < 16 ? 1280u : 0u);
  unsigned a0s = (unsigned)blk * 10240u + (unsigned)sl * 256u, a0p = blk ? a0s - 5120u : a0s, a0l = L_A0 + w * 6144;
  asm volatile("" : "+v"(a0g), "+v"(a0x), "+v"(a0h));
  asm volatile("" : "+s"(a0s), "+s"(a0p), "+s"(a0l));
  SCHED();
  asm volatile("" : "+s"(kp));
  Params p = {kp->q, kp->k, kp->v, kp->shift_k, kp->ssm_k, kp->ssm_D, kp->w_in, kp->w_sp, kp->b_sp, kp->w_out, kp->cvec, kp->w_mod, kp->out, kp->ws};
  asm volatile("s_waitcnt lgkmcnt(0)" : "+s"(p.q), "+s"(p.k), "+s"(p.v), "+s"(p.shift_k), "+s"(p.ssm_k), "+s"(p.ssm_D), "+s"(p.w_in), "+s"(p.w_sp),
               "+s"(p.b_sp), "+s"(p.w_out), "+s"(p.cvec), "+s"(p.w_mod), "+s"(p.out), "+s"(p.ws) : "s"(ka0), "s"(ka1));
  char* const ws = p.ws;
  STAMP(0);
  unsigned cpf = 0;
  if (!is_conv) {
    unsigned long long pc;
    unsigned len, len2;
    asm volatile("s_getpc_b64 %0\n.Lmega_here:\n\ts_mov_b32 %1, .Lmega_end-.Lmega_here\n\ts_mov_b32 %2, .Lmega_aux_end-.Lmega_here" : "=s"(pc), "=s"(len), "=s"(len2));
    len = len > len2 ? len : len2;
    unsigned o = (unsigned)((slot * 8 + w) * 1024 + (l & 7) * 128);
    o = o + 16u <= len ? o : 0u;
    cpf = *(const GAS unsigned*)((pc & ~15ull) + o);
  }
  const rsrc_t rs = __builtin_amdgcn_make_buffer_rsrc(ws, 0, WS_END, 0x00020000);
  const unsigned epv = ld4(rs, WS_EPOCH);
  SCHED();
  u32x2 dd2;
  {
    int cq = dg;
    asm volatile("" : "+s"(cq));
    if (cq >= 3) {
      const rsrc_t rk = __builtin_amdgcn_make_buffer_rsrc((void*)p.k, 0, S_ * D_ * 2, 0x00020000);
      const rsrc_t rv = __builtin_amdgcn_make_buffer_rsrc((void*)p.v, 0, S_ * D_ * 2, 0x00020000);
      const rsrc_t rh = __builtin_amdgcn_make_buffer_rsrc((void*)p.shift_k, 0, D_ * 8, 0x00020000);
      char* const la = lds + a0l;
      __builtin_amdgcn_raw_ptr_buffer_load_lds(rh, (LAS void*)(la + 5120), 16, l16, (unsigned)sl * 1024u, 0, 0);
      __builtin_amdgcn_raw_ptr_buffer_load_lds(rk, (LAS void*)(la + 1024), 16, a0x, a0s, 0, 0);
      __builtin_amdgcn_raw_ptr_buffer_load_lds(rk, (LAS void*)(la + 2048), 16, a0g, a0s + 5120u, 0, 0);
      __builtin_amdgcn_raw_ptr_buffer_load_lds(rv, (LAS void*)(la + 3072), 16, a0g, a0s, 0, 0);
      __builtin_amdgcn_raw_ptr_buffer_load_lds(rv, (LAS void*)(la + 4096), 16, a0x, a0s + 5120u, 0, 0);
      __builtin_amdgcn_raw_ptr_buffer_load_lds(rk, (LAS void*)(la), 16, a0h, a0p, 0, 0);
      SCHED();
      const rsrc_t rf = __builtin_amdgcn_make_buffer_rsrc((void*)p.ssm_k, 0, S_ * D_ * 2, 0x00020000);
      dd2 = *(const CAS u32x2*)((const CAS char*)p.ssm_D + quad * 8);
      STAMP(32);
      {
        const u32x4 z = {0u, 0u, 0u, 0u};
        if (tid < 128) *(u32x4*)(lds + (tid >> 5) * CH_BYTES + (tid & 31) * 16) = z;
        else if (tid < 152) {
          const int i = (tid - 128) >> 2;
          *(u32x4*)(lds + (tid & 3) * CH_BYTES + U_BYTES + (i < 4 ? i * G_COPY : (i & 1 ? 4128 : 16))) = z;
        }
        const LAS char* a0r = (const LAS char*)(lds + L_A0) + w * 6144 + (l & 3) * 512;
#define A0R(k) (a0r + ((((l >> 2) ^ (4 * ((l + (k)) & 3))) * 16)))
        unsigned go = WS_KVT + (unsigned)(quad / 5) * 81920u + (unsigned)sl * 16384u + (unsigned)(l >> 2) * 1024u + (unsigned)w * 64u + (unsigned)(l & 3) * 16u;
        const LAS char* r0 = A0R(0), * r1 = A0R(1), * r2 = A0R(2);
        const LAS char* rsh = (const LAS char*)(lds + L_A0) + w * 6144 + 5120 + (l >> 2) * 64;
        asm volatile("" : "+v"(go), "+v"(r0), "+v"(r1), "+v"(r2), "+v"(rsh));
        SCHED();
#pragma unroll
        for (int j = 0; j < 2; ++j)
          __builtin_amdgcn_raw_ptr_buffer_load_lds(rf, (LAS void*)(lds + (w >> 1) * CH_BYTES + U_BYTES + 32 + (2 * (w & 1) + j) * 1024), 16, l16,
                                                   (unsigned)quad * 16384u + (unsigned)(2 * w + j) * 1024u, 0, 0);
        STAMP(34);
        SCHED();
        VMW(7);
        f32x2 wt[4][4];
#pragma unroll
        for (int e = 0; e < 4; ++e) {
          const u32x4 sh = *(const volatile LAS u32x4*)(rsh + 16 * e);
          wt[e][0] = (f32x2){bflo(sh[0]), bflo(sh[2])}; wt[e][1] = (f32x2){bfhi(sh[0]), bfhi(sh[2])};
          wt[e][2] = (f32x2){bflo(sh[1]), bflo(sh[3])}; wt[e][3] = (f32x2){bfhi(sh[1]), bfhi(sh[3])};
        }
#pragma unroll
        for (int e = 0; e < 4; ++e) asm volatile("" : "+v"(wt[e][0]), "+v"(wt[e][1]), "+v"(wt[e][2]), "+v"(wt[e][3]));
        asm volatile("" : "+s"(dd2));
        SCHED();
        VMW(2);
        STAMP(35);
        u32x4 ka[5], va[2];
        ka[0] = *(const volatile LAS u32x4*)(r0 + 256);
        ka[1] = *(const volatile LAS u32x4*)(r1 + 512);
        ka[2] = *(const volatile LAS u32x4*)(r1 + 768);
        ka[3] = *(const volatile LAS u32x4*)(r2 + 1024);
        ka[4] = *(const volatile LAS u32x4*)(r2 + 1280);
        va[0] = *(const volatile LAS u32x4*)(r0 + 3072);
        va[1] = *(const volatile LAS u32x4*)(r0 + 3328);
        SCHED();
        if (blk == 0) {
          if ((l & 3) < 2) ka[0] = z;
          if ((l & 3) == 0) { ka[1] = z; ka[2] = z; }
        }
        u32x4 ko;
#pragma unroll
        for (int e = 0; e < 4; ++e) {
          const f32x2 w0 = wt[e][0], w1 = wt[e][1], w2 = wt[e][2], w3 = wt[e][3];
          const f32x2 km3 = bf2(ka[0][e]), km2 = bf2(ka[1][e]), km1 = bf2(ka[2][e]), k0 = bf2(ka[3][e]), k1 = bf2(ka[4][e]);
          const f32x2 s0 = __builtin_elementwise_fma(k0, w0, __builtin_elementwise_fma(km1, w1, __builtin_elementwise_fma(km2, w2, km3 * w3)));
          const f32x2 s1 = __builtin_elementwise_fma(k1, w0, __builtin_elementwise_fma(k0, w1, __builtin_elementwise_fma(km1, w2, km2 * w3)));
          const f32x2 o0 = s0 * bf2(va[0][e]), o1 = s1 * bf2(va[1][e]);
          ko[2 * (e & 1)] = pack2(o1[0], o0[0]); ko[2 * (e & 1) + 1] = pack2(o1[1], o0[1]);
          if (e & 1) st16(rs, go + (unsigned)(e >> 1) * 512u, ko);
          SCHED();
        }
      }
    }
  }
  const int R = (xcd & 3) * 8 + (idx & 7);
  const int c = (xcd >> 2) * 4 + (idx >> 3);
  const int n16 = l & 15, h4 = l >> 4;
  const rsrc_t rq = __builtin_amdgcn_make_buffer_rsrc((void*)p.q, 0, S_ * D_ * 2, 0x00020000);
  const rsrc_t rsp = __builtin_amdgcn_make_buffer_rsrc((void*)p.w_sp, 0, S_ * S_ * 2, 0x00020000);
  const int wj = w < 7 ? w : 0;
  u32x4 wsp[4][8];
  const int wk = w ^ (dg == 5 || dg == 6 ? 4 : 0);
  const unsigned wso = (unsigned)(R * 64) * 4096u + (unsigned)wk * 512u;
  const unsigned wdo = (unsigned)(h4 * 8192 + ((n16 ^ h4) * 16));
#define WVO(q) (wdo ^ (unsigned)(((4 - (q)) & 3) * 64))
#define WSO(g, q) (wso + (unsigned)((g) >> 2) * 256u + (unsigned)(32 * (((g) >> 1) & 1) + ((g) & 1) + 8 * (q)) * 4096u)
  u32x4 raw[NRAW];
#define LDW(i) raw[i] = __builtin_amdgcn_raw_buffer_load_b128(rsp, WVO((i) & 3), WSO((i) >> 2, (i) & 3), 0)
  const int trow = R * 64 + 32 * ((w >> 1) & 1) + 2 * n16 + (w & 1);
  unsigned bs_raw;
  unsigned tag;
  unsigned fw2 = OOB;
  unsigned e_fw1;
  const unsigned fw1o = WS_FW1 + (unsigned)(((8 + c) * 8 + wj) * 4 + (l & 3)) * 128u;

  if (__builtin_expect(is_conv, 1)) {
    STAMP(1);
    DRAIN();
    tag = __builtin_amdgcn_readfirstlane(epv) * 2654435761u + 0x9E3779B9u;
    BAR();
    st4(rs, tid == 0 ? WS_FK + (unsigned)quad * 128u : OOB, tag);
    unsigned atv, ato;
    const unsigned long long atb = (unsigned long long)(w < 5 ? p.w_in : p.w_out);
    {
      const int i = w < 5 ? (cidx * 5 + w) * 20 + (l < 19 ? l : 19) : (cidx * 3 + w - 5) * 19 + (l < 18 ? l : 18);
      const int r = i / 3;
      ato = w < 5 ? (unsigned)((r < 639 ? r : 639) * (F_ * 2) + xcd * CB * 2 + (i - 3 * r) * 108) : (unsigned)(xcd * CB * D_ * 2 + (i < 1079 ? i : 1079) * 128);
      asm volatile("" : "+v"(ato));
    }
    {
      u32x4 d[3];
      u32x2 n[3];
#pragma unroll
      for (int j = 0; j < 3; ++j) {
        char* G = j < 2 ? lds + ((tid >> 8) + 2 * j) * CH_BYTES + U_BYTES + ((tid & 255) + 2) * 16 : lds + (tid & 3) * CH_BYTES + U_BYTES + 16;
        d[j] = *(const u32x4*)G;
        n[j] = *(const u32x2*)(G + 16);
      }
#pragma unroll
      for (int j = 0; j < 3; ++j) {
        char* G = j < 2 ? lds + ((tid >> 8) + 2 * j) * CH_BYTES + U_BYTES + ((tid & 255) + 2) * 16 : lds + (tid & 3) * CH_BYTES + U_BYTES + 16;
        const unsigned a0 = __builtin_amdgcn_alignbit(d[j][1], d[j][0], 16), a1 = __builtin_amdgcn_alignbit(d[j][2], d[j][1], 16),
                       a2 = __builtin_amdgcn_alignbit(d[j][3], d[j][2], 16), a3 = __builtin_amdgcn_alignbit(n[j][0], d[j][3], 16),
                       a4 = __builtin_amdgcn_alignbit(n[j][1], n[j][0], 16);
        if (j < 2 || tid < 4) {
          *(u32x4*)(G + G_COPY) = (u32x4){a0, a1, a2, a3};
          *(u32x4*)(G + 2 * G_COPY) = (u32x4){d[j][1], d[j][2], d[j][3], n[j][0]};
          *(u32x4*)(G + 3 * G_COPY) = (u32x4){a1, a2, a3, a4};
        }
      }
    }
    const int ch = w & 3, hf = w >> 2;
    const LAS char *ap, *bp;
    {
      const char* U = lds + ch * CH_BYTES;
      const char* G = U + U_BYTES;
      const int a = n16, jj = h4 >> 1, e2 = h4 & 1;
      const int sg = (a + 1) & 3;
      bp = (const LAS char*)U + 512 + 32 * (a - jj) + 16 * e2 - 256 * hf - 192;
      ap = (const LAS char*)G + sg * G_COPY + 2 * (16 * jj + a - 8 * e2 + 9 - sg) + 256 * hf;
      asm volatile("" : "+v"(ap), "+v"(bp));
    }
    STAMP(36);
    {
      wait_flag<1>(rs, WS_FK + (unsigned)(5 * (4 * w + (l & 3)) + (quad >> 5)) * 128u, tag);
      STAMP(29);
      const unsigned gp = WS_KVT + (unsigned)(4 * w + (l >> 5)) * 81920u + (unsigned)quad * 512u + (unsigned)(l & 31) * 16u;
      const u32x4 g0 = ld16(rs, gp), g1 = ld16(rs, gp + 163840u);
      SCHED();
      char* Uw = lds + 512 + (128 * w + (l & ~3) + 3 - (l & 3)) * 4;
#pragma unroll
      for (int i = 0; i < 4; ++i) *(unsigned*)(Uw + i * CH_BYTES) = g0[i];
#pragma unroll
      for (int i = 0; i < 4; ++i) *(unsigned*)(Uw + 256 + i * CH_BYTES) = g1[i];
    }
    STAMP(28);
    BAR();
    STAMP(2);
    const int te = 2 * tid;
    u32x4 qa[2];
    u32x4 sv[2];
    u32x2 mg;
    const unsigned xo = (unsigned)(tid >> 4) * WX1P + (unsigned)quad * 256u + (unsigned)(tid & 15) * 16u;
    const unsigned mgo = WS_MOD + (unsigned)((l < 12 ? (l >> 2) * D_ + (l & 3) : 0) + quad * 4) * 8u;
    auto ld_sv = [&]() {
      sv[0] = ld16(rs, WS_ST1 + (unsigned)tid * 16u);
      sv[1] = ld16(rs, WS_ST1 + (unsigned)tid * 16u + 8192u);
    };
    auto ld_q = [&]() {
      qa[0] = ld16(rs, WS_QB + xo);
      qa[1] = ld16(rs, WS_QB + xo + 32u * WX1P);
    };
    f32x2 hh[2][2][2], base[2][2][2];
    auto prep = [&]() {
      const LAS char* ur = (const LAS char*)(lds + 512) + ((te & ~7) + 6 - (te & 7)) * 2;
      unsigned uu[2][4];
#pragma unroll
      for (int i = 0; i < 8; ++i) uu[i >> 2][i & 3] = *(const volatile LAS unsigned*)(ur + (i >> 2) * 2048 + (i & 3) * CH_BYTES);
      if (!__all(((sv[0][0] ^ tag) | (sv[0][2] ^ tag) | (sv[1][0] ^ tag) | (sv[1][2] ^ tag)) == 0u)) {
        _Pragma("unroll 1") for (unsigned spins = 0; spins < SPIN_CAP; ++spins) {
          ld_sv();
          if (__all(sv[0][0] == tag && sv[0][2] == tag && sv[1][0] == tag && sv[1][2] == tag)) break;
          __builtin_amdgcn_s_sleep(4);
        }
        asm volatile("" ::: "memory");
        ld_q();
        mg = ld8(rs, mgo);
        DRAIN();
      }
      if (!__all(mg[0] == tag)) {
        _Pragma("unroll 1") for (unsigned spins = 0; spins < SPIN_CAP; ++spins) {
          mg = ld8(rs, mgo);
          if (__all(mg[0] == tag)) break;
          __builtin_amdgcn_s_sleep(2);
        }
      }
      f32x2 cc2[2], ss2[2], dv2[2];
      float cch[4], sch[4];
#pragma unroll
      for (int i = 0; i < 4; ++i) {
        const float sa = __uint_as_float(__builtin_amdgcn_readlane(mg[1], i)), ca = __uint_as_float(__builtin_amdgcn_readlane(mg[1], 4 + i)),
                    ga = __uint_as_float(__builtin_amdgcn_readlane(mg[1], 8 + i));
        cch[i] = ga * rbf(1.0f + ca);
        sch[i] = ga * sa;
      }
      cc2[0] = (f32x2){cch[0], cch[1]}; cc2[1] = (f32x2){cch[2], cch[3]};
      ss2[0] = (f32x2){sch[0], sch[1]}; ss2[1] = (f32x2){sch[2], sch[3]};
      dv2[0] = bf2(dd2[0]); dv2[1] = bf2(dd2[1]);
#pragma unroll
      for (int H = 0; H < 2; ++H)
#pragma unroll
        for (int e = 0; e < 2; ++e) {
          const unsigned st = sv[H][1 + 2 * e];
          const float a = __builtin_amdgcn_rcpf(bfhi(st)), b = -bflo(st) * a;
#pragma unroll
          for (int i = 0; i < 2; ++i) {
            const f32x2 q2 = bf2(qa[H][2 * e + i]);
            const f32x2 u2 = e ? (f32x2){bflo(uu[H][2 * i]), bflo(uu[H][2 * i + 1])} : (f32x2){bfhi(uu[H][2 * i]), bfhi(uu[H][2 * i + 1])};
            const f32x2 n1 = __builtin_elementwise_fma(q2, (f32x2){a, a}, (f32x2){b, b});
            hh[H][e][i] = __builtin_elementwise_fma(n1, cc2[i], ss2[i]);
            base[H][e][i] = __builtin_elementwise_fma(hh[H][e][i], u2 * dv2[i], q2);
            asm volatile("" : "+v"(hh[H][e][i]), "+v"(base[H][e][i]));
          }
        }
    };
    auto HF = [&]() { int x = hf; asm volatile("" : "+s"(x)); return x; };
    {
      const int a = n16;
      char* yp = lds + L_YP + (hf * 4 + ch) * 8192 + (h4 * 16 + ((a + 4 * h4) & 15)) * 16;
      f32x4 acc[8];
      u32x4 A[2][8], B[2][8];
      auto rd = [&](int k) {
        const int r = k / 24, j = k % 24;
        if (j >= 2 && j < 10) B[r & 1][9 - j] = *(const volatile LAS u32x4*)(bp + 512 * (9 - j) + 64 * (3 - r));
        else {
          const int pp = j < 2 ? 0 : (j - 8) >> 1;
          const u32x2 v = *(const volatile LAS u32x2*)(ap + 512 * pp + 64 * r + 8 * (j & 1));
          A[r & 1][pp][2 * (j & 1)] = v[0]; A[r & 1][pp][2 * (j & 1) + 1] = v[1];
        }
      };
#pragma unroll
      for (int k = 0; k < IC<conv_sched(CDIST).pre>::v; ++k) rd(k);
      SCHED();
      auto step = [&](auto ic) {
        constexpr int i = decltype(ic)::v, r = i / 36, pp = conv_sched(CDIST).pp[i], m = conv_sched(CDIST).m[i], k = conv_sched(CDIST).rd[i];
        if (i == 0) __builtin_amdgcn_s_setprio(3);
        if (i == 60) __builtin_amdgcn_s_setprio(2);
        if (i == 100) __builtin_amdgcn_s_setprio(1);
        if (i == 126) __builtin_amdgcn_s_setprio(0);
        acc[m + pp] = MFMA16(A[r & 1][pp], B[r & 1][m], i < 8 ? ZERO4 : acc[m + pp]);
        SCHED();
        if (k >= 0) rd(k);
        if (i == 0) atv = *(const GAS unsigned*)(atb + ato);
        if (i % 6 == 2 && i / 3 < NRAW) {
          if (!HF()) { LDW(i / 3); LDW(i / 3 + 1); }
        }
        if (i > Q_AT && (i - Q_AT) % 2 == 1 && i - Q_AT < NRAW) {
          if (HF()) { LDW(i - Q_AT - 1); LDW(i - Q_AT); }
        }
        if (i == SV_AT) { ld_sv(); bs_raw = ((const GAS u16*)p.b_sp)[trow]; }
        if (r == 3 && pp >= 1 && m == 7 - pp) *(f32x4*)(yp + (pp - 1) * 1024) = acc[pp - 1];
        SCHED();
      };
      static_for<0, Q_AT>(step);
      STAMP(3);
      DRAIN();
      asm volatile("" ::"v"(atv));
      ld_q();
      mg = ld8(rs, mgo);
      SCHED();
      STAMP(4);
      static_for<Q_AT, 144>(step);
      *(f32x4*)(yp + 7 * 1024) = acc[7];
      STAMP(5);
    }
    prep();
    e_fw1 = ld4(rs, fw1o);
    STAMP(6);
    const LAS char* yr = (const LAS char*)(lds + L_YP) + (te >> 8) * 1024 + (((te >> 2) & 3) * 16 + ((((te >> 4) & 15) + 4 * ((te >> 2) & 3)) & 15)) * 16 + (te & 3) * 4;
    asm volatile("" : "+v"(yr));
    BAR();
    STAMP(7);
    f32x2 y[2][8];
#pragma unroll
    for (int i = 0; i < 16; ++i) y[i >> 3][i & 7] = *(const volatile LAS f32x2*)(yr + (i >> 3) * 4096 + (i & 7) * 8192);
#pragma unroll
    for (int H = 0; H < 2; ++H) {
      u32x4 o;
#pragma unroll
      for (int e = 0; e < 2; ++e)
#pragma unroll
        for (int i = 0; i < 2; ++i) {
          const float x0 = __builtin_fmaf(hh[H][e][i][0], y[H][2 * i][e] + y[H][4 + 2 * i][e], base[H][e][i][0]);
          const float x1 = __builtin_fmaf(hh[H][e][i][1], y[H][2 * i + 1][e] + y[H][5 + 2 * i][e], base[H][e][i][1]);
          o[2 * e + i] = pack2(x0, x1);
        }
      st16(rs, WS_X1B + (unsigned)(32 * H) * WX1P + xo, o);
    }
    STAMP(8);
  } else {
    const rsrc_t rwi = __builtin_amdgcn_make_buffer_rsrc((void*)p.w_in, 0, D_ * F_ * 2, 0x00020000);
    const rsrc_t rwo = __builtin_amdgcn_make_buffer_rsrc((void*)p.w_out, 0, FH * D_ * 2, 0x00020000);
    STAMP(32);
    const int job = xcd + 8 * slot;
    const int T = job >= 50 && w < 3 && job - 50 + 46 * w < 128 ? job - 50 + 46 * w : -1;
    const bool unit = slot >= 8 || (slot >= 2 && slot < 6);
    const int ucb = slot >= 8 ? 8 + xcd : xcd, kg = (slot >= 8 ? slot : slot - 2) & 3;
    float* tab = (float*)(lds + L_TAB);
    float* red = (float*)(lds + L_RED);
    auto issue_unit = [&](int i) {
      const int g = i * 64 + l;
      __builtin_amdgcn_raw_ptr_buffer_load_lds(rwi, (LAS void*)(lds + L_J3 + i * 1024), 16, (unsigned)((g / 14) * (F_ * 2) + (g % 14) * 16),
                                               (unsigned)(kg * 160 * F_ * 2 + ucb * CB * 2), 0, 0);
    };
    if (T >= 0) {
      char* qt = lds + L_QT + w * 20480;
      const unsigned so = (unsigned)(16 * T) * 1280u;
#pragma unroll
      for (int n = 0; n < 16; ++n)
        __builtin_amdgcn_raw_ptr_buffer_load_lds(rq, (LAS void*)(qt + n * 1024), 16, l16 ^ (unsigned)(n * 16), so + (unsigned)(n * 1280), 0, 0);
#pragma unroll
      for (int j = 0; j < 4; ++j)
        __builtin_amdgcn_raw_ptr_buffer_load_lds(rq, (LAS void*)(qt + 16384 + j * 1024), 16, (unsigned)(h4 * 1280 + ((n16 ^ (4 * j + h4)) * 16)), so + (unsigned)(4 * j * 1280 + 1024), 0, 0);
      tag = __builtin_amdgcn_readfirstlane(epv) * 2654435761u + 0x9E3779B9u;
      STAMP(34);
      DRAIN();
      STAMP(35);
      const bf2_t one = {(__bf16)1.0f, (__bf16)1.0f};
      float s1[2] = {0.f, 0.f}, s2[2] = {0.f, 0.f};
      const int n0 = 2 * (l & 7);
      u32x4 qv[20];
#pragma unroll
      for (int i = 0; i < 20; ++i)
#pragma unroll
        for (int e = 0; e < 2; ++e) {
          const int n = n0 + e, ck = 4 * (i & 15) + h4;
          const u32x2 v = *(const volatile LAS u32x2*)(qt + (i < 16 ? n * 1024 : 16384 + n * 256) + ((ck ^ n) * 16) + (l & 8));
          qv[i][2 * e] = v[0]; qv[i][2 * e + 1] = v[1];
        }
#pragma unroll
      for (int i = 0; i < 20; ++i) {
        st16(rs, WS_QB + (unsigned)(T >> 1) * WX1P + (unsigned)i * 2048u + (unsigned)((l >> 3) * 256 + (T & 1) * 128 + (l & 7) * 16), qv[i]);
#pragma unroll
        for (int e = 0; e < 4; ++e) {
          const unsigned x = qv[i][e];
          const bf2_t v = __builtin_bit_cast(bf2_t, x);
          s1[e >> 1] = __builtin_amdgcn_fdot2_f32_bf16(v, one, s1[e >> 1], false);
          s2[e >> 1] = __builtin_amdgcn_fdot2_f32_bf16(v, v, s2[e >> 1], false);
        }
        SCHED();
      }
      u32x4 o = {tag, 0u, tag, 0u};
#pragma unroll
      for (int e = 0; e < 2; ++e) {
        const float t1 = sum_rows(s1[e] + dpp_f<0x128>(s1[e])), t2 = sum_rows(s2[e] + dpp_f<0x128>(s2[e]));
        const float mean = t1 * (1.0f / D_);
        const float var = __builtin_fmaxf(t2 * (1.0f / D_) - mean * mean, 0.f);
        o[2 * e + 1] = pack2(mean, __builtin_amdgcn_sqrtf(rbf(rbf(var) + 1e-5f)));
      }
      STAMP(1);
      DRAIN();
      if (l < 8) st16(rs, WS_ST1 + (unsigned)T * 128u + l16, o);
      STAMP(3);
    }
    if (job < 60) {
      const int grp = job < 10 ? 40 + job : (job < 50 ? job - 10 : job);
      if (job >= 50 && T < 0) __builtin_amdgcn_s_sleep(LATE_SLEEP);
      if (job >= 10 && T < 0) __builtin_amdgcn_s_sleep(AUX_SLEEP);
      const unsigned cv = *(const GAS unsigned*)((const GAS u16*)p.cvec + 80 * w + 2 * (l < 40 ? l : 0));
      u32x4 pre[10];
#pragma unroll
      for (int pp = 0; pp < 10; ++pp) pre[pp] = *(const GAS u32x4*)((const GAS u16*)p.w_mod + (size_t)(80 * w + pp * 8 + (l >> 3)) * 3840 + grp * 64 + (l & 7) * 8);
      SCHED();
      STAMP(34);
      tag = __builtin_amdgcn_readfirstlane(epv) * 2654435761u + 0x9E3779B9u;
      if (l < 40) {
        const float c0 = bflo(cv), c1 = bfhi(cv);
        *(f32x2*)(tab + w * 80 + 2 * l) = (f32x2){rbf(c0 * __builtin_amdgcn_rcpf(1.0f + __expf(-c0))), rbf(c1 * __builtin_amdgcn_rcpf(1.0f + __expf(-c1)))};
      }
      asm volatile("s_waitcnt lgkmcnt(0)" ::: "memory");
      f32x2 acc[4];
#pragma unroll
      for (int e = 0; e < 4; ++e) acc[e] = (f32x2){0.f, 0.f};
#pragma unroll
      for (int pp = 0; pp < 10; ++pp) {
        const float sv = tab[w * 80 + pp * 8 + (l >> 3)];
#pragma unroll
        for (int e = 0; e < 4; ++e) acc[e] = __builtin_elementwise_fma((f32x2){sv, sv}, bf2(pre[pp][e]), acc[e]);
      }
      STAMP(35);
      asm volatile("" : "+v"(acc[0]), "+v"(acc[1]), "+v"(acc[2]), "+v"(acc[3]));
#pragma unroll
      for (int e = 0; e < 8; ++e) { float a = acc[e >> 1][e & 1]; a += dpp_f<0x128>(a); acc[e >> 1][e & 1] = sum_rows(a); }
      if (l < 8) {
        *(f32x4*)(red + w * 64 + l * 8) = (f32x4){acc[0][0], acc[0][1], acc[1][0], acc[1][1]};
        *(f32x4*)(red + w * 64 + l * 8 + 4) = (f32x4){acc[2][0], acc[2][1], acc[3][0], acc[3][1]};
      }
      if (w == 7) __builtin_amdgcn_s_setprio(2);
      BAR();
      if (w == 7) {
        float t = 0.f;
#pragma unroll
        for (int r = 0; r < 8; ++r) t += red[r * 64 + l];
        st8(rs, WS_MOD + (unsigned)(grp * 64 + l) * 8u, (u32x2){tag, __float_as_uint(rbf(t))});
        __builtin_amdgcn_s_setprio(0);
      } else if (unit) {
#pragma unroll
        for (int k = 0; k < 5; ++k) issue_unit(w * 5 + k);
      }
    } else if (unit && w >= 3 && w < 7) {
      __builtin_amdgcn_s_sleep(AUX_SLEEP);
#pragma unroll
      for (int k = 0; k < 9; ++k) issue_unit((w - 3) * 9 + k < 35 ? (w - 3) * 9 + k : 34);
    }
    tag = __builtin_amdgcn_readfirstlane(epv) * 2654435761u + 0x9E3779B9u;
    if (unit) {
      auto mod_tab = [&](int which, float add, float* dst) {
        const unsigned o = WS_MOD + (unsigned)(which * D_ + kg * 160) * 8u;
        u32x4 g0, g1;
        _Pragma("unroll 1") for (unsigned spins = 0; spins < SPIN_CAP; ++spins) {
          g0 = ld16(rs, o + l16);
          g1 = ld16(rs, o + 1024u + (l16 & 255u));
          if (__all(g0[0] == tag && g0[2] == tag && g1[0] == tag && g1[2] == tag)) break;
          __builtin_amdgcn_s_sleep(2);
        }
        *(f32x2*)(dst + 2 * l) = (f32x2){rbf(add + __uint_as_float(g0[1])), rbf(add + __uint_as_float(g0[3]))};
        if (l < 16) *(f32x2*)(dst + 128 + 2 * l) = (f32x2){rbf(add + __uint_as_float(g1[1])), rbf(add + __uint_as_float(g1[3]))};
      };
      if (w == 7) {
        STAMP(1);
        if (slot >= 8) __builtin_amdgcn_s_sleep(64);
        mod_tab(4, 1.0f, tab);
        BAR();
        STAMP(2);
        mod_tab(3, 0.0f, tab + 160);
        BAR();
      } else {
        DRAIN();
        STAMP(36);
        BAR();
        STAMP(2);
        const char* bp = lds + L_J3 + (4 * h4 + (n16 >> 2)) * 224 + (n16 & 3) * 8 + w * 32;
        const float* tp = tab + 4 * h4;
        u32x4 f3[5];
        float cs = 0.f, b1 = 0.f;
        const bool pad = w == 6 && n16 >= 12;
        const unsigned padv = (n16 == 12 && ucb >= 8) ? 0x3f803f80u : 0u;
#pragma unroll
        for (int k = 0; k < 5; ++k) f3[k] = trfrag(bp + k * 7168, 16 * 224);
#pragma unroll
        for (int k = 0; k < 5; ++k) {
          const f32x4 ca = *(const f32x4*)(tp + k * 32), cb4 = *(const f32x4*)(tp + k * 32 + 16);
          u32x4 o;
#pragma unroll
          for (int e = 0; e < 4; ++e) {
            const float c0 = e < 2 ? ca[2 * e] : cb4[2 * e - 4], c1 = e < 2 ? ca[2 * e + 1] : cb4[2 * e - 3];
            const float p0 = bflo(f3[k][e]) * c0, p1 = bfhi(f3[k][e]) * c1;
            cs += p0 + p1;
            o[e] = pad ? padv : pack2(p0, p1);
          }
          st16(rs, WS_WINF + (unsigned)(ucb * 7 + w) * 20480u + (unsigned)(kg * 5 + k) * 1024u + l16, o);
        }
        cs = sum_rows(cs);
        DRAIN();
        if (l == 0) st4(rs, WS_FW1 + (unsigned)((ucb * 8 + w) * 4 + kg) * 128u, tag);
        STAMP(5);
        BAR();
#pragma unroll
        for (int k = 0; k < 5; ++k) {
          const f32x4 sa = *(const f32x4*)(tp + 160 + k * 32), sb4 = *(const f32x4*)(tp + 160 + k * 32 + 16);
#pragma unroll
          for (int e = 0; e < 4; ++e) {
            const float s0 = e < 2 ? sa[2 * e] : sb4[2 * e - 4], s1 = e < 2 ? sa[2 * e + 1] : sb4[2 * e - 3];
            b1 = __builtin_fmaf(s0, bflo(f3[k][e]), __builtin_fmaf(s1, bfhi(f3[k][e]), b1));
          }
        }
        b1 = sum_rows(b1);
        if (pad) { cs = padv ? 160.0f : 0.0f; b1 = 0.0f; }
        if (l < 16) st16(rs, WS_CS + (unsigned)((ucb * 4 + kg) * 112 + w * 16 + l) * 16u, (u32x4){tag, __float_as_uint(cs), tag, __float_as_uint(b1)});
      }
    } else {
      if (job >= 60 && T < 0) __builtin_amdgcn_s_sleep(100);
      if (slot < 2 && w >= 2 && w < 6) {
        const int c5 = slot * 4 + w - 2, c2 = xcd;
        char* st = lds + L_J5 + (w - 2) * 18432;
#pragma unroll
        for (int r = 0; r < 18; ++r) {
          const int g = r * 64 + l;
          __builtin_amdgcn_raw_ptr_buffer_load_lds(rwo, (LAS void*)(st + r * 1024), 16, (unsigned)((g / 10) * (D_ * 2) + (g % 10) * 16),
                                                   (unsigned)(xcd * CB * D_ * 2 + c5 * 160), 0, 0);
        }
        DRAIN();
        const char* bp = st + (4 * h4 + (n16 >> 2)) * 160 + (n16 & 3) * 8;
        const unsigned base = WS_WOUTF + (unsigned)c5 * WBLK;
#pragma unroll
        for (int dj = 0; dj < 5; ++dj) {
          st16(rs, base + (unsigned)c2 * 10240u + (unsigned)dj * 1024u + l16, trfrag(bp + dj * 32, 32 * 160));
          st16(rs, base + (unsigned)c2 * 10240u + (unsigned)(5 + dj) * 1024u + l16, trfrag(bp + 16 * 160 + dj * 32, 32 * 160));
          st16(rs, base + 81920u + (unsigned)c2 * 5120u + (unsigned)dj * 1024u + l16, trfrag(bp + 64 * 160 + dj * 32, 16 * 160));
          st8(rs, base + 122880u + (unsigned)((c2 >> 1) * 5 + dj) * 1024u + (unsigned)(c2 & 1) * 512u + (unsigned)l * 8u, trd(bp + 96 * 160 + dj * 32));
        }
        fw2 = WS_FW2 + (unsigned)(c5 * 8 + xcd) * 128u;
      }
    }
    STAMP(4);
#pragma unroll
    for (int i = 0; i < NRAW; ++i) LDW(i);
    bs_raw = ((const GAS u16*)p.b_sp)[trow];
    e_fw1 = ~tag;
    asm volatile(".Lmega_aux_end:");
    asm volatile("" ::"v"(cpf));
    STAMP(8);
  }

  LAS char* xa = (LAS char*)(lds + L_XI) + l * 16;
  LAS char* xb = xa + 65536;
  asm volatile("" : "+v"(xb));
#define LDA(ks, rt) (*(const volatile LAS u32x4*)((ks) < 16 ? xa + ((ks) * 4 + (rt)) * 1024 : xb + (((ks) - 16) * 4 + (rt)) * 1024))
  auto lda1 = [&](int ks, u32x4* A) {
#pragma unroll
    for (int rt = 0; rt < 4; ++rt) A[rt] = LDA(ks, rt);
  };
  const LAS char* tb4 = (const LAS char*)(lds + L_MISC) + h4 * 16;
  const LAS char* tbn = (const LAS char*)(lds + L_MISC) + n16 * 4;
  asm volatile("" : "+v"(tb4), "+v"(tbn));
  u32x4 xr[5][2];
  auto ld_x1 = [&]() {
#pragma unroll
    for (int m = 0; m < 5; ++m) {
      if (m == 0) __builtin_amdgcn_s_setprio(2);
      if (m == 1) __builtin_amdgcn_s_setprio(1);
      if (m == 3) __builtin_amdgcn_s_setprio(0);
#pragma unroll
      for (int e = 0; e < 2; ++e)
        xr[m][e] = ld16(rs, l16, WS_X1B + (unsigned)(R * 2 + (w & 1)) * WX1P + (unsigned)(4 * m + (w >> 1)) * 2048u + (unsigned)e * 1024u);
      SCHED();
    }
  };
  LAS char* xw = xa + w * 2048;
  auto put_x1 = [&](int m) {
    LAS char* d = m < 4 ? xw + m * 16384 : xb + w * 2048;
#pragma unroll
    for (int e = 0; e < 2; ++e) {
      *(volatile LAS u32x2*)(d + 8 * e) = (u32x2){xr[m][e][0], xr[m][e][1]};
      *(volatile LAS u32x2*)(d + 1024 + 8 * e) = (u32x2){xr[m][e][2], xr[m][e][3]};
    }
  };
  auto poison_u = [&]() {
#pragma unroll
    for (int k = 0; k < 2; ++k) {
      const unsigned o = (unsigned)(wj * 2 + k) * 1024u + l16;
      const unsigned rt = o / 3584u, r = o - rt * 3584u;
      st16(rs, WS_UF + ((unsigned)(R * 4) + rt) * 8u * 3584u + (unsigned)c * 3584u + r, (u32x4){~0u, ~0u, ~0u, ~0u});
    }
  };
  const unsigned wob = WS_WOUTF + (unsigned)c * WBLK;
  const LAS char* wrd = (const LAS char*)(lds + L_WS) + w * 8192 + n16 * 256 + ((h4 ^ ((4 - (n16 >> 2)) & 3)) * 64);
#define DMA_W(g, q) __builtin_amdgcn_raw_ptr_buffer_load_lds(rsp, (LAS void*)(lds + L_WS + w * 8192 + (((g) + 1) & 1) * 4096 + (q) * 1024), 16, WVO(q), WSO(g, q), 0, 0)
#define RD_W(g) do { _Pragma("unroll") for (int i_ = 0; i_ < 4; ++i_) \
                      wsp[(g) & 3][((g) >> 2) * 4 + i_] = *(const volatile LAS u32x4*)(wrd + (((g) + 1) & 1) * 4096 + ((i_ ^ (n16 & 3)) * 16)); } while (0)
  auto park = [&]() {
    LAS char* d = (LAS char*)(lds + L_WS) + w * 8192 + l * 16;
#pragma unroll
    for (int g = 0; g < NRAW / 4; ++g) {
#pragma unroll
      for (int q = 0; q < 4; ++q) *(volatile LAS u32x4*)(d + ((g + 1) & 1) * 4096 + q * 1024) = raw[4 * g + q];
      if (g < 2) RD_W(g);
    }
    asm volatile("s_waitcnt lgkmcnt(0)" ::: "memory");
  };
#define DMA_AB(k) dma1k(rs, lds + L_AB + ((k) * 8 + w) * 1024, l16, wob + (unsigned)((k) * 8 + w) * 1024u)
  const int rto = w & 3, cls = w >> 2;
  u32x2 xp[3];
  constexpr int W = G2W;
  const unsigned zb = WS_Z2F + (unsigned)c * 458752u + (unsigned)wk * 8192u;
  const unsigned fzo = WS_FZ + (unsigned)(c * 32 + wk * 4 + (l & 3)) * 128u;
  u32x4 fr[W];
#define LDZ(i) fr[(i) % W] = ld16(rs, l16, zb + (unsigned)((i) >> 3) * 65536u + (unsigned)((i) & 7) * 1024u)
  u32x4 cr[5];
#define LDCR(k) cr[k] = ld16(rs, l16, wob + 81920u + (unsigned)(w * 5 + (k)) * 1024u)
  auto rd_xp = [&]() {
#pragma unroll
    for (int j = 0; j < 3; ++j) {
      const int q4 = 5 * c + (cls ? (3 + (j < 2 ? j : 1)) : j);
      xp[j] = *(const volatile LAS u32x2*)(xa + ((q4 >> 1) * 4 + rto) * 1024 + (q4 & 1) * 8);
    }
  };
  if (w < 7) {
    u32x4 B2[20], B1[20];
    const unsigned b2o = WS_WINF + (unsigned)((8 + c) * 7 + w) * 20480u, b1o = WS_WINF + (unsigned)(c * 7 + w) * 20480u;
#define LDB2(ks) B2[ks] = ld16(rs, l16, b2o + (unsigned)(ks) * 1024u)
#define LDB1(ks) B1[ks] = ld16(rs, l16, b1o + (unsigned)(ks) * 1024u)
    const bool early = __all(e_fw1 == tag);
    asm volatile("" ::: "memory");
    {
      const unsigned le = early ? l16 : OOB;
#pragma unroll
      for (int ks = 0; ks < NB2A; ++ks) B2[ks] = ld16(rs, le, b2o + (unsigned)ks * 1024u);
      SCHED();
      if (is_conv) VMW(NB2A);
    }
    STAMP(33);
    BAR();
    STAMP(37);
    if (!early) {
      wait_flag<4>(rs, fw1o, tag);
      DRAIN();
      st4(rs, l == 0 ? fw2 : OOB, tag);
#pragma unroll
      for (int ks = 0; ks < NB2A; ++ks) LDB2(ks);
    }
#pragma unroll
    for (int ks = NB2A; ks < 20; ++ks) {
      LDB2(ks);
      if (ks == 12) { SCHED(); park(); SCHED(); }
      if (ks == 13) { SCHED(); __builtin_amdgcn_s_barrier(); SCHED(); }
    }
    STAMP(9);
    BAR();
    STAMP(10);
    ld_x1();
    SCHED();
    poison_u();
    SCHED();
#pragma unroll
    for (int m = 0; m < 3; ++m) put_x1(m);
    if (w == 6) __builtin_amdgcn_s_setprio(2);
    BAR();
    STAMP(11);
    f32x4 acc[4];
    u32x4 A[2][4];
    lda1(0, A[0]);
#pragma unroll
    for (int ks = 0; ks < 20; ++ks) {
      if (ks == 6) put_x1(3);
      if (ks == 8) put_x1(4);
      if (ks == 11) BAR();
      if (ks >= 4) LDB1(ks - 4);
      if (ks >= 16) LDB1(ks);
      if (ks == 12) RD_W(2);
      SCHED();
#pragma unroll
      for (int rt = 0; rt < 4; ++rt) {
        if (ks < 19) A[(ks + 1) & 1][rt] = LDA(ks + 1, rt);
        SCHED();
        acc[rt] = MFMA16(A[ks & 1][rt], B2[ks], ks ? acc[rt] : ZERO4);
        SCHED();
      }
    }
    STAMP(12);
    if (w == 6) {
      if (n16 == 12) {
#pragma unroll
        for (int rt = 0; rt < 4; ++rt) *(f32x4*)(lds + L_S1 + (rt * 16 + 4 * h4) * 4) = acc[rt];
      }
      asm volatile("" ::: "memory");
      *(volatile LAS unsigned*)(lds + L_S1 + 256) = tag;
      __builtin_amdgcn_s_setprio(0);
    }
    VMW(20);
    BAR();
    STAMP(13);
    f32x2 zz[8];
    f32x4 cs1a, cs1b;
    {
      f32x4 rs4[4], nm4[4];
#pragma unroll
      for (int rt = 0; rt < 4; ++rt) {
        rs4[rt] = *(const LAS f32x4*)(tb4 + (L_T2 - L_MISC) + rt * 64);
        nm4[rt] = *(const LAS f32x4*)(tb4 + (L_T2 - L_MISC) + 256 + rt * 64);
      }
      const f32x2 cs2 = *(const f32x2*)(lds + L_CS1 + (112 + 16 * w + n16) * 8);
      const f32x2 csv = {cs2[0], cs2[0]}, b1v = {cs2[1], cs2[1]};
      float v1[16], v2[16];
#pragma unroll
      for (int i = 0; i < 8; ++i) {
        const int rt = i >> 1;
        const f32x2 a2 = i & 1 ? HI2(acc[rt]) : LO2(acc[rt]), n2 = i & 1 ? HI2(nm4[rt]) : LO2(nm4[rt]), r2 = i & 1 ? HI2(rs4[rt]) : LO2(rs4[rt]);
        zz[i] = gelu2(__builtin_elementwise_fma(__builtin_elementwise_fma(n2, csv, a2), r2, b1v));
        asm volatile("" : "+v"(zz[i]));
        DMA_W(3 + (i >> 2), i & 3);
        const f32x2 sq = zz[i] * zz[i];
        v1[2 * i] = zz[i][0]; v1[2 * i + 1] = zz[i][1]; v2[2 * i] = sq[0]; v2[2 * i + 1] = sq[1];
      }
      const float t1 = tr_sum16(v1, l);
      const float t2 = tr_sum16(v2, l);
      *(f32x2*)(lds + L_Z1S + (w * 64 + (n16 >> 2) * 16 + 4 * h4 + (n16 & 3)) * 8) = (f32x2){t1, t2};
      lda1(0, A[0]);
      cs1a = *(const f32x4*)(lds + L_CS1 + (16 * w + 4 * h4) * 8);
      cs1b = *(const f32x4*)(lds + L_CS1 + (16 * w + 4 * h4) * 8 + 16);
    }
    BAR();
    STAMP(14);
#pragma unroll
    for (int ks = 0; ks < 20; ++ks) {
      if (ks == Z1SPLIT) {
        STAMP(15);
        BAR();
        STAMP(16);
        f32x4 ra[4], rb[4];
#pragma unroll
        for (int rt = 0; rt < 4; ++rt) {
          ra[rt] = *(const LAS f32x4*)(tb4 + (L_ST2 - L_MISC) + rt * 64);
          rb[rt] = *(const LAS f32x4*)(tb4 + (L_ST2 - L_MISC) + 256 + rt * 64);
        }
#pragma unroll
        for (int i = 0; i < 8; ++i) {
          zz[i] = __builtin_elementwise_fma(zz[i], i & 1 ? HI2(ra[i >> 1]) : LO2(ra[i >> 1]), i & 1 ? HI2(rb[i >> 1]) : LO2(rb[i >> 1]));
          asm volatile("" : "+v"(zz[i]));
        }
        const u32x4 o0 = {pack2(zz[0][0], zz[2][0]), pack2(zz[0][1], zz[2][1]), pack2(zz[1][0], zz[3][0]), pack2(zz[1][1], zz[3][1])};
        const u32x4 o1 = {pack2(zz[4][0], zz[6][0]), pack2(zz[4][1], zz[6][1]), pack2(zz[5][0], zz[7][0]), pack2(zz[5][1], zz[7][1])};
        const unsigned zo = WS_Z2F + (unsigned)((c * 7 + w) * 64 + 4 * (R >> 1) + h4) * 1024u + (unsigned)(R & 1) * 512u + (unsigned)n16 * 16u;
        st16(rs, zo, o0);
        st16(rs, zo + 256u, o1);
        SCHED();
      }
      if (ks == 0) { VMW(4); RD_W(3); }
      if (ks == 2) { VMW(0); RD_W(4); }
      if (ks == 8) { VMW(4 + 2); RD_W(5); }
      if (ks == 11) rd_xp();
      if (ks == 13) { VMW(4 + 5 + 2); RD_W(6); }
      if (ks >= 13 && ks < 19) DMA_AB(ks - 13);
      if (ks >= 2 && ks < 6) { DMA_W(5 + ((ks - 2) >> 1), 2 * (ks & 1)); DMA_W(5 + ((ks - 2) >> 1), 2 * (ks & 1) + 1); }
      if (ks >= 6 && ks < 9) LDCR(ks - 6);
      if (ks == 12) { LDCR(3); LDCR(4); }
      if (ks == 10 || ks == 11) { DMA_W(7, 2 * (ks & 1)); DMA_W(7, 2 * (ks & 1) + 1); }
      SCHED();
#pragma unroll
      for (int rt = 0; rt < 4; ++rt) {
        if (ks < 19) A[(ks + 1) & 1][rt] = LDA(ks + 1, rt);
        SCHED();
        acc[rt] = MFMA16(B1[ks], A[ks & 1][rt], ks ? acc[rt] : ZERO4);
        SCHED();
      }
    }
    STAMP(18);
    VMW(2 + 2 + 6);
    RD_W(7);
    const f32x2 c01 = {cs1a[0], cs1a[2]}, b01 = {cs1a[1], cs1a[3]}, c23 = {cs1b[0], cs1b[2]}, b23 = {cs1b[1], cs1b[3]};
    u32x2 g1[4];
    auto z1g = [&](int rt) {
      const float r1 = *(const LAS float*)(tbn + (L_T2 - L_MISC) + rt * 64), n1 = *(const LAS float*)(tbn + (L_T2 - L_MISC) + 256 + rt * 64);
      const f32x2 r2 = {r1, r1}, n2 = {n1, n1};
      const f32x2 a = gelu2(__builtin_elementwise_fma(__builtin_elementwise_fma(n2, c01, LO2(acc[rt])), r2, b01));
      const f32x2 b = gelu2(__builtin_elementwise_fma(__builtin_elementwise_fma(n2, c23, HI2(acc[rt])), r2, b23));
      g1[rt] = (u32x2){pack2(a[0], a[1]), pack2(b[0], b[1])};
      asm volatile("" : "+v"(g1[rt]));
    };
    SCHED();
    VMW(2 + 6);
    BAR();
    STAMP(17);
#pragma unroll
    for (int rt = 0; rt < 4; ++rt) {
      if (rt < 2) { DMA_AB(6 + 2 * rt); DMA_AB(7 + 2 * rt); }
      z1g(rt);
      *(u32x2*)(lds + L_Z1S + ((w * 4 + rt) * 64 + l) * 8) = g1[rt];
    }
  } else {
    __builtin_amdgcn_s_setprio(3);
    DRAIN();
    BAR();
    if (is_conv && l == 0) st4(rs, WS_FX + (unsigned)quad * 128u, tag);
    STAMP(9);
    *(volatile LAS unsigned*)(lds + L_S1 + 256) = ~tag;
    u32x4 gv, cv[2][2][4];
    const unsigned gvo = WS_MOD + (unsigned)(5 * D_ + 80 * c) * 8u + (l < 40 ? l16 : 0u);
    auto ld_consts = [&]() {
      gv = ld16(rs, gvo);
#pragma unroll
      for (int i = 0; i < 16; ++i) cv[i >> 3][(i >> 2) & 1][i & 3] = ld16(rs, l < 56 ? l16 : 0u, WS_CS + (unsigned)(((c + 8 * (i >> 3)) * 4 + (i & 3)) * 112 + 56 * ((i >> 2) & 1)) * 16u);
    };
    auto ok_consts = [&]() {
      unsigned x = (gv[0] ^ tag) | (gv[2] ^ tag);
#pragma unroll
      for (int i = 0; i < 16; ++i) x |= (cv[i >> 3][(i >> 2) & 1][i & 3][0] ^ tag) | (cv[i >> 3][(i >> 2) & 1][i & 3][2] ^ tag);
      asm volatile("" : "+v"(x));
      return __all(x == 0u);
    };
    auto touch_w = [&](int k) {
      cpf ^= __builtin_amdgcn_raw_buffer_load_b32(rsp, (unsigned)(h4 * 4096 + (n16 >> 1) * 512 + (n16 & 1) * 128),
                                                  (unsigned)(R * 64 + (c & 3) * 16 + k * 4) * 4096u + 256u, 0);
    };
    park();
    __builtin_amdgcn_s_barrier();
    ld_consts();
    STAMP(31);
    _Pragma("unroll 1") for (unsigned spins = 0; spins < SPIN_CAP; ++spins) {
      const unsigned f0 = ld4(rs, WS_FX + (unsigned)l * 128u), f1 = ld4(rs, WS_FX + 8192u + (unsigned)l * 128u),
                     f2 = ld4(rs, l < 32 ? WS_FX + 16384u + (unsigned)l * 128u : WS_FW1 + (unsigned)(c * 32 + (l < 60 ? l - 32 : 0)) * 128u);
      if (__all(f0 == tag && f1 == tag && f2 == tag)) break;
      __builtin_amdgcn_s_sleep(POLL_SLEEP);
    }
    BAR();
    STAMP(10);
    ld_x1();
    __builtin_amdgcn_s_setprio(3);
    if (!ok_consts()) {
      _Pragma("unroll 1") for (unsigned spins = 0; spins < SPIN_CAP; ++spins) {
        ld_consts();
        if (ok_consts()) break;
        __builtin_amdgcn_s_sleep(8);
      }
    }
    if (l < 40) *(f32x2*)(lds + L_GB + l * 8) = (f32x2){__uint_as_float(gv[1]), __uint_as_float(gv[3])};
    if (l < 56) {
#pragma unroll
      for (int i = 0; i < 4; ++i) {
        const u32x4* q4 = cv[i >> 1][i & 1];
        *(f32x2*)(lds + L_CS1 + (56 * i + l) * 8) =
            (f32x2){(__uint_as_float(q4[0][1]) + __uint_as_float(q4[1][1])) + (__uint_as_float(q4[2][1]) + __uint_as_float(q4[3][1])),
                    (__uint_as_float(q4[0][3]) + __uint_as_float(q4[1][3])) + (__uint_as_float(q4[2][3]) + __uint_as_float(q4[3][3]))};
      }
    }
    SCHED();
#pragma unroll
    for (int m = 0; m < 3; ++m) put_x1(m);
    BAR();
    f32x4 acc[4];
    u32x4 A[2][4];
    lda1(0, A[0]);
#pragma unroll
    for (int ks = 0; ks < 20; ++ks) {
      if (ks == 6) put_x1(3);
      if (ks == 8) put_x1(4);
      if (ks == 11) BAR();
      if (ks == 4) RD_W(2);
      if (ks >= 6 && ks < 14) DMA_W(3 + ((ks - 6) >> 2), (ks - 6) & 3);
      if (ks >= 1 && ks < 5) touch_w(ks - 1);
      if (ks < 19) lda1(ks + 1, A[(ks + 1) & 1]);
      SCHED();
#pragma unroll
      for (int rt = 0; rt < 4; ++rt) acc[rt] = MFMA16(A[ks & 1][rt], A[ks & 1][rt], ks ? acc[rt] : ZERO4);
      SCHED();
    }
    {
      if ((n16 >> 2) == h4) {
#pragma unroll
        for (int rt = 0; rt < 4; ++rt) {
          const float lo = (l & 1) ? acc[rt][1] : acc[rt][0], hi = (l & 1) ? acc[rt][3] : acc[rt][2];
          *(volatile LAS float*)(tbn + (L_T2 - L_MISC) + rt * 64) = (l & 2) ? hi : lo;
        }
      }
      const float s2 = *(volatile LAS float*)(lds + L_T2 + l * 4) * (1.0f / D_);
      float s1;
      _Pragma("unroll 1") for (unsigned spins = 0; spins < SPIN_CAP * 16; ++spins) {
        const unsigned f = *(volatile LAS unsigned*)(lds + L_S1 + 256);
        s1 = *(volatile LAS float*)(lds + L_S1 + l * 4);
        if (f == tag) break;
      }
      const float nm = s1 * (-1.0f / D_);
      *(float*)(lds + L_T2 + l * 4) = __builtin_amdgcn_rsqf(__builtin_fmaxf(s2 - nm * nm, 0.f) + 1e-5f);
      *(float*)(lds + L_T2 + 256 + l * 4) = nm;
    }
    BAR();
    __builtin_amdgcn_s_setprio(0);
    asm volatile("" ::"v"(cpf));
    DRAIN();
    RD_W(3); RD_W(4);
    rd_xp();
    asm volatile("s_waitcnt lgkmcnt(0)" ::: "memory");
#pragma unroll
    for (int i = 0; i < 8; ++i) DMA_W(5 + (i >> 2), i & 3);
    const unsigned e_fw2 = ld4(rs, WS_FW2 + (unsigned)(c * 8 + (l & 7)) * 128u);
    if (!__all(e_fw2 == tag)) wait_flag<2>(rs, WS_FW2 + (unsigned)(c * 8 + (l & 7)) * 128u, tag);
    __builtin_amdgcn_s_setprio(3);
    BAR();
    {
      f32x2 t = {0.f, 0.f};
#pragma unroll
      for (int k = 0; k < 7; ++k) t += *(const f32x2*)(lds + L_Z1S + (k * 64 + l) * 8);
      st16(rs, WS_PART + (unsigned)(R * 8 + c) * 1024u + l16, (u32x4){tag, __float_as_uint(t[0]), tag, __float_as_uint(t[1])});
    }
    STAMP(14);
    {
      u32x4 v[8];
      const unsigned po = WS_PART + (unsigned)R * 8192u + l16;
      __builtin_amdgcn_s_sleep(16);
      _Pragma("unroll 1") for (unsigned spins = 0; spins < SPIN_CAP; ++spins) {
#pragma unroll
        for (int k = 0; k < 8; ++k) v[k] = ld16(rs, po + (unsigned)k * 1024u);
        unsigned x = 0;
#pragma unroll
        for (int k = 0; k < 8; ++k) x |= (v[k][0] ^ tag) | (v[k][2] ^ tag);
        asm volatile("" : "+v"(x));
        if (__all(x == 0u)) break;
        __builtin_amdgcn_s_sleep(1);
      }
      float t1 = 0.f, t2 = 0.f;
#pragma unroll
      for (int k = 0; k < 8; ++k) { t1 += __uint_as_float(v[k][1]); t2 += __uint_as_float(v[k][3]); }
      const float mean = t1 * (1.0f / FH);
      const float var = __builtin_fmaxf(t2 * (1.0f / FH) - mean * mean, 0.f);
      const float rstd = __builtin_amdgcn_rsqf(var + 1e-5f);
      *(float*)(lds + L_ST2 + l * 4) = rstd;
      *(float*)(lds + L_ST2 + 256 + l * 4) = -mean * rstd;
    }
    STAMP(15);
    BAR();
    RD_W(5); RD_W(6);
    asm volatile("s_waitcnt lgkmcnt(0)" ::: "memory");
#pragma unroll
    for (int q = 0; q < 4; ++q) DMA_W(7, q);
    DRAIN();
    RD_W(7);
    BAR();
    if (l == 0) st4(rs, WS_FZ + (unsigned)(c * 32 + R) * 128u, tag);
    STAMP(17);
    __builtin_amdgcn_s_setprio(0);
#pragma unroll
    for (int k = 0; k < 10; ++k) DMA_AB(k);
#pragma unroll
    for (int k = 0; k < 5; ++k) LDCR(k);
  }
  const rsrc_t rout = __builtin_amdgcn_make_buffer_rsrc((void*)p.out, 0, S_ * D_ * 2, 0x00020000);
  STAMP(19);
  u32x2 z1o[4];
  wait_flag<POLL_SLEEP>(rs, fzo, tag);
#pragma unroll
  for (int i = 0; i < W; ++i) { LDZ(i); SCHED(); }
  STAMP(20);

  const unsigned ub = WS_UF + (unsigned)((R * 4 + rto) * 8 + c) * 3584u;
  const float bs = bflo(bs_raw);
  const unsigned ur = WS_UF + (unsigned)((R * 4 + rto) * 8 + cls) * 3584u;
  u32x4 uf[4][2];
  f32x4 acc[2][4];
  f32x4* const bufw = (f32x4*)(lds + L_BUF) + w * 256 + l;
#define BUFW(slot, rt) bufw[(slot) * 2048 + (rt) * 64]
  {
    const f32x4* const rb = (const f32x4*)(lds + L_BUF) + cls * 2048 + rto * 64 + l;
    f32x4 rdv[4], s;
    u32x4 uo;
#pragma unroll
    for (int j = 0; j < 7; ++j) {
#pragma unroll
      for (int ks = 0; ks < 8; ++ks) {
        const int i = 8 * j + ks;
#pragma unroll
        for (int rt = 0; rt < 4; ++rt) acc[j & 1][rt] = MFMA16(fr[i % W], wsp[rt][ks], ks ? acc[j & 1][rt] : ZERO4);
        if (i == 0) STAMP(21);
        if (i == 24) STAMP(22);
        SCHED();
        if (i + W < 56) LDZ(i + W);
        if (j > 0) {
          if (ks == 0 && (j & 1)) BAR();
          if (j == 1 && ks == 0) {
#pragma unroll
            for (int k = 0; k < 4; ++k) z1o[k] = *(const u32x2*)(lds + L_Z1S + (((k < 3 ? 2 * k + cls : 6) * 4 + rto) * 64 + l) * 8);
          }
          if (i >= (56 - G2W) && i < 53) {
#pragma unroll
            for (int k = 0; k < 8; ++k)
              if ((56 - G2W) + k * (53 - (56 - G2W)) / 8 == i) uf[k >> 1][k & 1] = ld16(rs, l16, ur + (unsigned)(k >> 1) * 7168u + (unsigned)(k & 1) * 1024u);
            SCHED();
          }
          const bool fast = j == 6;
          if (!fast && ks < 4) BUFW((j - 1) & 1, ks) = acc[(j - 1) & 1][ks];
          if (fast && ks < 2) { BUFW(1, 2 * ks) = acc[1][2 * ks]; BUFW(1, 2 * ks + 1) = acc[1][2 * ks + 1]; }
          if (!(j & 1)) {
            const int k0 = fast ? 2 : 5;
            if (ks == k0) {
              BAR();
#pragma unroll
              for (int k = 0; k < 4; ++k) rdv[k] = rb[k * 256];
            }
            if (ks == k0 + 1) {
              s = (rdv[0] + rdv[1]) + (rdv[2] + rdv[3]);
#pragma unroll
              for (int k = 0; k < 4; ++k) rdv[k] = rb[(4 + k) * 256];
            }
            if (ks == k0 + 2) {
              s += (rdv[0] + rdv[1]) + (rdv[2] + rdv[3]);
              const u32x2 z = z1o[(j >> 1) - 1];
              const int e = j == 4 ? 2 : 0;
              uo[e] = pack2(bflo(z[0]) * (s[0] + bs), bfhi(z[0]) * (s[1] + bs));
              uo[e + 1] = pack2(bflo(z[1]) * (s[2] + bs), bfhi(z[1]) * (s[3] + bs));
              if (j == 4) st16(rs, ub + (unsigned)cls * 1024u + l16, uo);
              if (j == 6) st16(rs, l < 32 ? ub + 2048u + (unsigned)cls * 512u + l16 : OOB, half_swap((u32x4){uo[0], uo[1], uo[0], uo[1]}));
            }
            if (j == 6 && ks == 4) BAR();
            if (j == 6 && ks >= 4 && ks < 7) {
#pragma unroll
              for (int k = 0; k < 5; ++k)
                if (k / 2 == ks - 4) *(u32x4*)(lds + (w < 6 ? L_BUF + 32768 + w * 5120 : L_Z1S + (w - 6) * 5120) + k * 1024 + l16) = cr[k];
            }
          }
        }
        SCHED();
      }
    }
  }
  STAMP(23);

  {
    u32x4 uc[4], uh[4];
    const unsigned uho = (unsigned)(l >> 5) * 3584u + (unsigned)(l & 31) * 16u;
#define LD_C(cc) uc[cc] = ld16(rs, l16, ur + (unsigned)(cc) * 7168u + 2048u)
#define LD_H(cc) uh[cc] = ld16(rs, uho, WS_UF + (unsigned)((R * 4 + rto) * 8) * 3584u + (unsigned)(cc) * 7168u + 3072u)
    auto mx = [](unsigned a, unsigned b) { return __builtin_elementwise_max(a, b); };
    auto ok4 = [&](const u32x4* v, int n) {
      unsigned m = 0;
#pragma unroll
      for (int i = 0; i < 8; ++i)
        if (i < n) m = mx(m, mx(mx(v[i][0], v[i][1]), mx(v[i][2], v[i][3])));
      return __all(m != ~0u);
    };
#pragma unroll
    for (int rt = 0; rt < 4; ++rt) BUFW(0, rt) = acc[0][rt];
    BAR();
    f32x4 a3[5];
    u32x4 af[2][5];
    const LAS char* wb[2] = {(const LAS char*)(lds + L_AB) + cls * (10240 + 3072) + l16, (const LAS char*)(lds + L_AB) + cls * (10240 - 2048) + l16};
    const LAS char* wb2[2] = {wb[0] + 40960, wb[1] + 40960};
    asm volatile("" : "+v"(wb[0]), "+v"(wb2[0]), "+v"(wb[1]), "+v"(wb2[1]));
    u32x4 ho = {0u, 0u, 0u, 0u};
    if (w < 4) {
      __builtin_amdgcn_s_setprio(2);
      const f32x4* const rb = (const f32x4*)(lds + L_BUF) + rto * 64 + l;
      f32x4 r8[8];
#pragma unroll
      for (int k = 0; k < 8; ++k) r8[k] = rb[k * 256];
      const f32x4 s = ((r8[0] + r8[1]) + (r8[2] + r8[3])) + ((r8[4] + r8[5]) + (r8[6] + r8[7]));
      const u32x2 z = z1o[3];
      const unsigned o0 = pack2(bflo(z[0]) * (s[0] + bs), bfhi(z[0]) * (s[1] + bs)), o1 = pack2(bflo(z[1]) * (s[2] + bs), bfhi(z[1]) * (s[3] + bs));
      ho = half_swap((u32x4){o0, o1, o0, o1});
    }
    st16(rs, tid < 256 && l < 32 ? ub + 3072u + l16 : OOB, ho);
    SCHED();
    __builtin_amdgcn_s_setprio(0);
#pragma unroll
    for (int dj = 0; dj < 5; ++dj) af[0][dj] = *(const LAS u32x4*)(wb[dj >= 2] + dj * 1024);
    const bool ab_ok = ok4(&uf[0][0], 8);
    STAMP(24);
    if (!ab_ok) {
      _Pragma("unroll 1") for (unsigned spins = 0; spins < SPIN_CAP; ++spins) {
#pragma unroll
        for (int i = 0; i < 8; ++i) uf[i >> 1][i & 1] = ld16(rs, l16, ur + (unsigned)(i >> 1) * 7168u + (unsigned)(i & 1) * 1024u);
        if (ok4(&uf[0][0], 8)) break;
        __builtin_amdgcn_s_sleep(1);
      }
    }
    STAMP(25);
    const LAS char* cb0[2] = {(const LAS char*)(lds + L_BUF + 32768) + cls * (5120 + 3072) + l16, (const LAS char*)(lds + L_BUF + 32768) + cls * (5120 - 2048) + l16};
    u32x4 ah[4][3];
    const LAS char* hb = (const LAS char*)(lds + L_BUF) + cls * 12288 + l * 8;
    f32x4 gb[3], xo3[3], xf[3];
    LAS char *otw, *xw3;
    const LAS char *ota, *otb, *xr3;
    unsigned oa_o, ob_o;
#pragma unroll
    for (int st = 0; st < 12; ++st) {
      if (st < 2) { LD_C(2 * st); LD_C(2 * st + 1); }
      else if (st < 7 && w < 4) dma1k(rs, lds + L_BUF + ((st - 2) * 4 + w) * 1024, l16, wob + 122880u + (unsigned)(w * 5 + st - 2) * 1024u);
      if (st == 2) {
        LAS char* ot = (LAS char*)(lds + L_AB + 32768) + w * 2048;
        otw = ot + n16 * 112 + h4 * 8;
        ota = ot + (l >> 2) * 112 + (l & 3) * 16;
        otb = ot + ((l >> 1) & 15) * 112 + 64 + (l & 1) * 16;
        asm volatile("" : "+v"(otw), "+v"(ota), "+v"(otb));
      }
      if (st == 3) {
        oa_o = (unsigned)(l >> 2) * 2560u + (unsigned)(l & 3) * 16u;
        ob_o = l < 32 ? (unsigned)(l >> 1) * 2560u + 64u + (unsigned)(l & 1) * 16u : OOB;
        xw3 = (LAS char*)(lds + L_AB) + w * 3072 + l16;
        xr3 = (const LAS char*)(lds + L_AB) + (w ^ 4) * 3072 + cls * 1024 + l16;
        asm volatile("" : "+v"(oa_o), "+v"(ob_o), "+v"(xw3), "+v"(xr3));
      }
      if (st >= 4 && st < 7) {
        const int j = st - 4;
        gb[j] = *(const f32x4*)(lds + L_GB + (48 * cls + 16 * (cls && j == 2 ? 1 : j) + 4 * h4) * 4);
        xf[j] = (f32x4){bflo(xp[j][0]), bfhi(xp[j][0]), bflo(xp[j][1]), bfhi(xp[j][1])};
        asm volatile("" : "+v"(xf[j]));
      }
      if (st == 8) {
        STAMP(26);
        if (!ok4(uc, 4)) {
          _Pragma("unroll 1") for (unsigned spins = 0; spins < SPIN_CAP; ++spins) {
            LD_C(0); LD_C(1); LD_C(2); LD_C(3);
            if (ok4(uc, 4)) break;
            __builtin_amdgcn_s_sleep(1);
          }
        }
        LD_H(0); LD_H(1); LD_H(2); LD_H(3);
#pragma unroll
        for (int cc = 0; cc < 4; ++cc) uc[cc] = half_swap(uc[cc]);
        VMW(4);
        __builtin_amdgcn_s_barrier();
        asm volatile("" ::: "memory");
        STAMP(27);
      }
      SCHED();
#pragma unroll
      for (int dj = 0; dj < 5; ++dj) {
        const int sn = st + 1;
        if (sn < 8) af[sn & 1][dj] = *(const LAS u32x4*)((sn < 4 ? wb : wb2)[dj >= 2] + ((sn >> 1) & 1) * 20480 + ((sn & 1) * 5 + dj) * 1024);
        else if (sn < 12) af[sn & 1][dj] = *(const LAS u32x4*)(cb0[dj >= 2] + (sn < 11 ? (sn - 8) * 10240 : L_Z1S - L_BUF - 32768) + dj * 1024);
        if (st >= 8) {
#pragma unroll
          for (int k = 0; k < 2; ++k) {
            const int h = st < 11 ? (k ? 99 : (st - 8) * 5 + dj) : 15 + 2 * dj + k;
            if (h < 24) {
              const u32x2 v = *(const volatile LAS u32x2*)(hb + (((h >> 1) % 3) * 4 + (h >> 1) / 3) * 1024 + (h & 1) * 512);
              ah[(h >> 1) / 3][(h >> 1) % 3][2 * (h & 1)] = v[0]; ah[(h >> 1) / 3][(h >> 1) % 3][2 * (h & 1) + 1] = v[1];
            }
          }
        }
        SCHED();
        a3[dj] = MFMA16(af[st & 1][dj], st < 8 ? uf[(st >> 1) & 3][st & 1] : uc[st & 3], st ? a3[dj] : ZERO4);
        SCHED();
      }
    }
#pragma unroll
    for (int j = 0; j < 3; ++j) *(volatile LAS f32x4*)(xw3 + j * 1024) = a3[2 + j];
    BAR();
#pragma unroll
    for (int j = 0; j < 3; ++j) xo3[j] = *(const volatile LAS f32x4*)(xr3 + j * 1024);
    SCHED();
    if (!ok4(uh, 4)) {
      _Pragma("unroll 1") for (unsigned spins = 0; spins < SPIN_CAP; ++spins) {
        LD_H(0); LD_H(1); LD_H(2); LD_H(3);
        if (ok4(uh, 4)) break;
        __builtin_amdgcn_s_sleep(1);
      }
    }
#pragma unroll
    for (int cc = 0; cc < 4; ++cc) {
      const u32x4 b = half_swap(uh[cc]);
#pragma unroll
      for (int j = 0; j < 3; ++j) a3[j] = MFMA16(ah[cc][j], b, a3[j]);
      if (cc >= 1) {
        const int j = cc - 1;
        xo3[j] = __builtin_elementwise_fma(gb[j], xo3[j], xf[j]);
        asm volatile("" : "+v"(xo3[j]));
      }
      SCHED();
    }
#pragma unroll
    for (int j = 0; j < 3; ++j) {
      const f32x4 r = __builtin_elementwise_fma(gb[j], a3[j], xo3[j]);
      *(volatile LAS u32x2*)(otw + j * 32) = (u32x2){pack2(r[0], r[1]), pack2(r[2], r[3])};
    }
    const u32x4 oa = *(const volatile LAS u32x4*)(ota);
    const u32x4 ob = *(const volatile LAS u32x4*)(otb);
    const unsigned obase = (unsigned)((R * 64 + 32 * ((w >> 1) & 1) + (w & 1)) * D_ + 80 * c + 48 * cls) * 2u;
    __builtin_amdgcn_raw_buffer_store_b128(oa, rout, oa_o, obase, 0);
    if (cls == 0) __builtin_amdgcn_raw_buffer_store_b128(ob, rout, ob_o, obase, 0);
    if (bid == 0 && tid == 0) st4(rs, WS_EPOCH, tag);
  }
  STAMP(30);
  asm volatile(".Lmega_end:");
}

extern "C" void kernel_launch(void* const* d_in, const int* in_sizes, int n_in, void* d_out, int out_size,
                              void* d_ws, size_t ws_size, hipStream_t stream) {
  Params p;
  p.q = (const u16*)d_in[0];
  p.k = (const u16*)d_in[1];
  p.v = (const u16*)d_in[2];
  p.shift_k = (const u16*)d_in[3];
  p.ssm_k = (const u16*)d_in[4];
  p.ssm_D = (const u16*)d_in[5];
  p.w_in = (const u16*)d_in[7];
  p.w_sp = (const u16*)d_in[8];
  p.b_sp = (const u16*)d_in[9];
  p.w_out = (const u16*)d_in[10];
  p.cvec = (const u16*)d_in[11];
  p.w_mod = (const u16*)d_in[12];
  p.out = (u16*)d_out;
  p.ws = (char*)d_ws;
  mega<<<256, 512, 0, stream>>>(p);
}
```

```cpp
#include <hip/hip_runtime.h>
#include <stdint.h>

typedef unsigned short u16;
typedef __attribute__((ext_vector_type(8))) short bf16x8;
typedef __attribute__((ext_vector_type(4))) float f32x4;
typedef __attribute__((ext_vector_type(2))) float f32x2;
typedef __attribute__((ext_vector_type(4))) unsigned int u32x4;
typedef __attribute__((ext_vector_type(2))) unsigned int u32x2;
#define LAS __attribute__((address_space(3)))
#define CAS __attribute__((address_space(4)))
#define GAS __attribute__((address_space(1)))
typedef __amdgpu_buffer_rsrc_t rsrc_t;
typedef __attribute__((ext_vector_type(4))) short s16x4;

constexpr int S_ = 2048, D_ = 640, F_ = 1728, FH = 864, CB = 108;

#define X1PAD 256
constexpr unsigned WS_EPOCH = 0;
constexpr unsigned WS_FX = 4096;
constexpr unsigned WS_FZ = WS_FX + 24576;
constexpr unsigned WS_FW2 = WS_FZ + 32768;
constexpr unsigned WS_FW1 = WS_FW2 + 8192;
constexpr unsigned WS_CS = WS_FW1 + 65536;
constexpr unsigned WS_PART = WS_CS + 131072;
constexpr unsigned WS_MOD = WS_PART + 262144;
constexpr unsigned WS_ST1 = WS_MOD + 32768;
constexpr unsigned WS_X1B = WS_ST1 + 16384;
constexpr unsigned WX1P = 40960 + X1PAD;
constexpr unsigned WBLK = 143360;
constexpr unsigned WS_QB = WS_X1B + 2686976;
constexpr unsigned WS_WINF = WS_QB + 2686976;
constexpr unsigned WS_WOUTF = WS_WINF + 16 * WBLK;
constexpr unsigned WS_Z2F = WS_WOUTF + 8 * WBLK;
constexpr unsigned WS_UF = WS_Z2F + 8 * 64 * 7168;
constexpr unsigned WS_KVT = WS_UF + 32 * 4 * 8 * 3584;
constexpr unsigned WS_FK = WS_KVT + 32 * 81920;
constexpr unsigned WS_STAMP = WS_FK + 20480;
constexpr unsigned WS_END = WS_STAMP + 256 * 40 * 8;

constexpr int U_BYTES = (256 + S_) * 2;
constexpr int G_COPY = 4160;
constexpr int CH_BYTES = U_BYTES + 4 * G_COPY;
constexpr int L_YP = 4 * CH_BYTES;
constexpr int L_A0 = 98304;
constexpr int L_RED = 0;
constexpr int L_TAB = 2048;
constexpr int L_J3 = 8192;
constexpr int L_QT = 49152;
constexpr int L_J5 = L_QT;
constexpr int L_XI = 0;
constexpr int L_BUF = 81920;
constexpr int L_AB = 0;
constexpr int L_MISC = 147456;
constexpr int L_Z1S = L_MISC;
constexpr int L_S1 = L_MISC + 14336;
constexpr int L_T2 = L_S1 + 512;
constexpr int L_ST2 = L_T2 + 512;
constexpr int L_GB = L_ST2 + 512;
constexpr int L_CS1 = L_Z1S + 4096;
constexpr int L_WS = 81920;
constexpr int LDS_TOTAL = 163840;

constexpr int POLL_SLEEP = 1;
constexpr int AUX_SLEEP = 36, LATE_SLEEP = 96;
constexpr int CDIST = 4;
constexpr int NRAW = 12;
constexpr int SV_AT = 108, Q_AT = 130;
constexpr int NB2A = 6;
constexpr int G2W = 7;
constexpr int Z1SPLIT = 12;
constexpr int SC1 = 16;
constexpr unsigned SPIN_CAP = 1u << 16;
constexpr unsigned OOB = 0x80000000u;

__device__ __forceinline__ float bflo(unsigned x) { return __uint_as_float(x << 16); }
__device__ __forceinline__ float bfhi(unsigned x) { return __uint_as_float(x & 0xffff0000u); }
__device__ __forceinline__ f32x2 bf2(unsigned x) { return (f32x2){bflo(x), bfhi(x)}; }
__device__ __forceinline__ unsigned f2bf(float x) {
  __bf16 b = (__bf16)x;
  return (unsigned)__builtin_bit_cast(unsigned short, b);
}
__device__ __forceinline__ float rbf(float x) { return bflo(f2bf(x)); }
typedef __attribute__((ext_vector_type(2))) __bf16 bf2_t;
__device__ __forceinline__ unsigned pack2(float lo, float hi) {
  f32x2 v = {lo, hi};
  return __builtin_bit_cast(unsigned, __builtin_convertvector(v, bf2_t));
}
template <int CTRL>
__device__ __forceinline__ float dpp_f(float v) {
  return __int_as_float(__builtin_amdgcn_update_dpp(0, __float_as_int(v), CTRL, 0xf, 0xf, true));
}
__device__ __forceinline__ float sum_rows(float v) {
  auto a = __builtin_amdgcn_permlane16_swap(__float_as_uint(v), __float_as_uint(v), false, false);
  v = __uint_as_float(a[0]) + __uint_as_float(a[1]);
  auto b = __builtin_amdgcn_permlane32_swap(__float_as_uint(v), __float_as_uint(v), false, false);
  return __uint_as_float(b[0]) + __uint_as_float(b[1]);
}
__device__ __forceinline__ f32x2 gelu2(f32x2 x) {
  const float c0 = -2.0f * 0.7978845608028654f * 1.4426950408889634f;
  const float c1 = c0 * 0.044715f;
  const f32x2 t = x * __builtin_elementwise_fma(x * x, (f32x2){c1, c1}, (f32x2){c0, c0});
  const f32x2 d = (f32x2){__builtin_amdgcn_exp2f(t[0]), __builtin_amdgcn_exp2f(t[1])} + (f32x2){1.0f, 1.0f};
  return x * (f32x2){__builtin_amdgcn_rcpf(d[0]), __builtin_amdgcn_rcpf(d[1])};
}
#define LO2(v) __builtin_shufflevector(v, v, 0, 1)
#define HI2(v) __builtin_shufflevector(v, v, 2, 3)
__device__ __forceinline__ float tr_sum16(const float* v, int l) {
  float x[8], y[4];
  asm volatile(
      "s_nop 1\n\t"
      "v_add_f32_dpp %0, %8, %8 row_ror:8 row_mask:0xf bank_mask:0x3\n\t"
      "v_add_f32_dpp %1, %9, %9 row_ror:8 row_mask:0xf bank_mask:0x3\n\t"
      "v_add_f32_dpp %2, %10, %10 row_ror:8 row_mask:0xf bank_mask:0x3\n\t"
      "v_add_f32_dpp %3, %11, %11 row_ror:8 row_mask:0xf bank_mask:0x3\n\t"
      "v_add_f32_dpp %4, %12, %12 row_ror:8 row_mask:0xf bank_mask:0x3\n\t"
      "v_add_f32_dpp %5, %13, %13 row_ror:8 row_mask:0xf bank_mask:0x3\n\t"
      "v_add_f32_dpp %6, %14, %14 row_ror:8 row_mask:0xf bank_mask:0x3\n\t"
      "v_add_f32_dpp %7, %15, %15 row_ror:8 row_mask:0xf bank_mask:0x3\n\t"
      "v_add_f32_dpp %0, %16, %16 row_ror:8 row_mask:0xf bank_mask:0xc\n\t"
      "v_add_f32_dpp %1, %17, %17 row_ror:8 row_mask:0xf bank_mask:0xc\n\t"
      "v_add_f32_dpp %2, %18, %18 row_ror:8 row_mask:0xf bank_mask:0xc\n\t"
      "v_add_f32_dpp %3, %19, %19 row_ror:8 row_mask:0xf bank_mask:0xc\n\t"
      "v_add_f32_dpp %4, %20, %20 row_ror:8 row_mask:0xf bank_mask:0xc\n\t"
      "v_add_f32_dpp %5, %21, %21 row_ror:8 row_mask:0xf bank_mask:0xc\n\t"
      "v_add_f32_dpp %6, %22, %22 row_ror:8 row_mask:0xf bank_mask:0xc\n\t"
      "v_add_f32_dpp %7, %23, %23 row_ror:8 row_mask:0xf bank_mask:0xc"
      : "=&v"(x[0]), "=&v"(x[1]), "=&v"(x[2]), "=&v"(x[3]), "=&v"(x[4]), "=&v"(x[5]), "=&v"(x[6]), "=&v"(x[7])
      : "v"(v[0]), "v"(v[1]), "v"(v[2]), "v"(v[3]), "v"(v[4]), "v"(v[5]), "v"(v[6]), "v"(v[7]), "v"(v[8]), "v"(v[9]), "v"(v[10]),
        "v"(v[11]), "v"(v[12]), "v"(v[13]), "v"(v[14]), "v"(v[15]));
  asm volatile(
      "s_nop 1\n\t"
      "v_add_f32_dpp %0, %4, %4 row_shl:4 row_mask:0xf bank_mask:0x5\n\t"
      "v_add_f32_dpp %1, %5, %5 row_shl:4 row_mask:0xf bank_mask:0x5\n\t"
      "v_add_f32_dpp %2, %6, %6 row_shl:4 row_mask:0xf bank_mask:0x5\n\t"
      "v_add_f32_dpp %3, %7, %7 row_shl:4 row_mask:0xf bank_mask:0x5\n\t"
      "v_add_f32_dpp %0, %8, %8 row_shr:4 row_mask:0xf bank_mask:0xa\n\t"
      "v_add_f32_dpp %1, %9, %9 row_shr:4 row_mask:0xf bank_mask:0xa\n\t"
      "v_add_f32_dpp %2, %10, %10 row_shr:4 row_mask:0xf bank_mask:0xa\n\t"
      "v_add_f32_dpp %3, %11, %11 row_shr:4 row_mask:0xf bank_mask:0xa\n\t"
      "s_nop 1"
      : "=&v"(y[0]), "=&v"(y[1]), "=&v"(y[2]), "=&v"(y[3])
      : "v"(x[0]), "v"(x[1]), "v"(x[2]), "v"(x[3]), "v"(x[4]), "v"(x[5]), "v"(x[6]), "v"(x[7]));
  const bool b1 = l & 2, b0 = l & 1;
  const float z0 = (b1 ? y[2] : y[0]) + dpp_f<0x4E>(b1 ? y[0] : y[2]);
  const float z1 = (b1 ? y[3] : y[1]) + dpp_f<0x4E>(b1 ? y[1] : y[3]);
  return (b0 ? z1 : z0) + dpp_f<0xB1>(b0 ? z0 : z1);
}
__device__ __forceinline__ u32x4 half_swap(u32x4 v) {
  const auto a = __builtin_amdgcn_permlane32_swap(v[0], v[2], false, false);
  const auto b = __builtin_amdgcn_permlane32_swap(v[1], v[3], false, false);
  return (u32x4){a[0], b[0], a[1], b[1]};
}
#define MFMA16(a, b, c) __builtin_amdgcn_mfma_f32_16x16x32_bf16(__builtin_bit_cast(bf16x8, a), __builtin_bit_cast(bf16x8, b), c, 0, 0, 0)
__device__ __forceinline__ u32x2 trd(const char* p) {
  return __builtin_bit_cast(u32x2, __builtin_amdgcn_ds_read_tr16_b64_v4i16((LAS s16x4*)p));
}
__device__ __forceinline__ u32x4 trfrag(const char* p, int second) {
  u32x2 a = trd(p), b = trd(p + second);
  return (u32x4){a[0], a[1], b[0], b[1]};
}
#define BAR() asm volatile("s_waitcnt lgkmcnt(0)\n\ts_barrier" ::: "memory")
#define DRAIN() do { __builtin_amdgcn_s_waitcnt(0x0F70); asm volatile("" ::: "memory"); } while (0)
#define VMW(n) do { __builtin_amdgcn_s_waitcnt(0x0F70 | ((n) & 15) | (((n) >> 4) << 14)); asm volatile("" ::: "memory"); } while (0)
#define SCHED() __builtin_amdgcn_sched_barrier(0)
#define ZERO4 ((f32x4){0.f, 0.f, 0.f, 0.f})

__device__ __forceinline__ u32x4 ld16(rsrc_t r, unsigned off, unsigned so = 0) { return __builtin_amdgcn_raw_buffer_load_b128(r, off, so, SC1); }
__device__ __forceinline__ u32x2 ld8(rsrc_t r, unsigned off, unsigned so = 0) { return __builtin_amdgcn_raw_buffer_load_b64(r, off, so, SC1); }
__device__ __forceinline__ unsigned ld4(rsrc_t r, unsigned off, unsigned so = 0) { return __builtin_amdgcn_raw_buffer_load_b32(r, off, so, SC1); }
__device__ __forceinline__ void st16(rsrc_t r, unsigned off, u32x4 v) { __builtin_amdgcn_raw_buffer_store_b128(v, r, off, 0, SC1); }
__device__ __forceinline__ void st8(rsrc_t r, unsigned off, u32x2 v) { __builtin_amdgcn_raw_buffer_store_b64(v, r, off, 0, SC1); }
__device__ __forceinline__ void st4(rsrc_t r, unsigned off, unsigned v) { __builtin_amdgcn_raw_buffer_store_b32(v, r, off, 0, SC1); }
__device__ __forceinline__ void dma1k(rsrc_t r, char* lds_dst, unsigned l16, unsigned soff) {
  __builtin_amdgcn_raw_ptr_buffer_load_lds(r, (LAS void*)lds_dst, 16, l16, soff, 0, SC1);
}

#define STAMP(i)

template <int SLEEP>
__device__ __forceinline__ void wait_flag(rsrc_t r, unsigned off, unsigned tag) {
  _Pragma("unroll 1") for (unsigned spins = 0; spins < SPIN_CAP; ++spins) {
    if (__all(ld4(r, off) == tag)) break;
    __builtin_amdgcn_s_sleep(SLEEP);
  }
  asm volatile("" ::: "memory");
}

struct ConvSched {
  signed char pp[144], m[144], rd[144];
  int pre;
};
constexpr ConvSched conv_sched(int dist) {
  ConvSched s = {};
  int need[96] = {}, i = 0;
  for (int r = 0; r < 4; ++r)
    for (int pp = 0; pp < 8; ++pp) {
      const int j = 24 * r + (pp ? 8 + 2 * pp : 0);
      need[j] = need[j + 1] = i;
      for (int m = 7 - pp; m >= 0; --m) {
        if (pp == 0) need[24 * r + 9 - m] = i;
        s.pp[i] = pp; s.m[i] = m; s.rd[i] = -1;
        ++i;
      }
    }
  int next = 144;
  for (int j = 95; j >= 0; --j) {
    next = need[j] - dist < next - 1 ? need[j] - dist : next - 1;
    if (next >= 0) s.rd[next] = j;
    else if (s.pre == 0) s.pre = j + 1;
  }
  return s;
}

template <int I> struct IC { static constexpr int v = I; };
template <int I0, int I1, class F>
__device__ __forceinline__ void static_for(F&& f) {
  if constexpr (I0 < I1) { f(IC<I0>{}); static_for<I0 + 1, I1>(f); }
}

struct Params {
  const u16 *q, *k, *v, *shift_k, *ssm_k, *ssm_D, *w_in, *w_sp, *b_sp, *w_out, *cvec, *w_mod;
  u16* out;
  char* ws;
};

__global__ __launch_bounds__(512, 2) void mega(Params) {
  __shared__ __attribute__((aligned(16))) char lds[LDS_TOTAL];
  const CAS Params* kp = (const CAS Params*)__builtin_amdgcn_kernarg_segment_ptr();
  unsigned ka0, ka1;
  asm volatile("s_load_dword %0, %2, 0x0\n\ts_load_dword %1, %2, 0x40" : "=&s"(ka0), "=&s"(ka1) : "s"(kp));
  SCHED();
  const int tid = threadIdx.x, l = tid & 63;
  const int w = __builtin_amdgcn_readfirstlane(tid >> 6);
  const int bid = blockIdx.x, xcd = bid & 7, idx = bid >> 3;
  const int dg = ((idx & 7) + 2 * (idx >> 3)) & 7;
  const bool is_conv = dg >= 3;
  const int slot = 3 * (idx >> 3) + dg;
  const int cidx = 5 * (idx >> 3) + dg - 3;
  const int quad = xcd * 20 + cidx;
  const unsigned l16 = (unsigned)l * 16u;
  const int blk = (quad / 5) * 8 + w, sl = quad % 5;
  unsigned a0g = (unsigned)(l >> 4) * 1280u + (unsigned)(((l & 15) ^ (4 * (l >> 5))) * 16), a0x = a0g ^ 128u, a0h = a0g + (l < 16 ? 1280u : 0u);
  unsigned a0s = (unsigned)blk * 10240u + (unsigned)sl * 256u, a0p = blk ? a0s - 5120u : a0s, a0l = L_A0 + w * 6144;
  asm volatile("" : "+v"(a0g), "+v"(a0x), "+v"(a0h));
  asm volatile("" : "+s"(a0s), "+s"(a0p), "+s"(a0l));
  SCHED();
  asm volatile("" : "+s"(kp));
  Params p = {kp->q, kp->k, kp->v, kp->shift_k, kp->ssm_k, kp->ssm_D, kp->w_in, kp->w_sp, kp->b_sp, kp->w_out, kp->cvec, kp->w_mod, kp->out, kp->ws};
  asm volatile("s_waitcnt lgkmcnt(0)" : "+s"(p.q), "+s"(p.k), "+s"(p.v), "+s"(p.shift_k), "+s"(p.ssm_k), "+s"(p.ssm_D), "+s"(p.w_in), "+s"(p.w_sp),
               "+s"(p.b_sp), "+s"(p.w_out), "+s"(p.cvec), "+s"(p.w_mod), "+s"(p.out), "+s"(p.ws) : "s"(ka0), "s"(ka1));
  char* const ws = p.ws;
  STAMP(0);
  unsigned cpf = 0;
  if (!is_conv) {
    unsigned long long pc;
    unsigned len, len2;
    asm volatile("s_getpc_b64 %0\n.Lmega_here:\n\ts_mov_b32 %1, .Lmega_end-.Lmega_here\n\ts_mov_b32 %2, .Lmega_aux_end-.Lmega_here" : "=s"(pc), "=s"(len), "=s"(len2));
    len = len > len2 ? len : len2;
    unsigned o = (unsigned)((slot * 8 + w) * 1024 + (l & 7) * 128);
    o = o + 16u <= len ? o : 0u;
    cpf = *(const GAS unsigned*)((pc & ~15ull) + o);
  }
  const rsrc_t rs = __builtin_amdgcn_make_buffer_rsrc(ws, 0, WS_END, 0x00020000);
  const unsigned epv = ld4(rs, WS_EPOCH);
  SCHED();
  u32x2 dd2;
  {
    int cq = dg;
    asm volatile("" : "+s"(cq));
    if (cq >= 3) {
      const rsrc_t rk = __builtin_amdgcn_make_buffer_rsrc((void*)p.k, 0, S_ * D_ * 2, 0x00020000);
      const rsrc_t rv = __builtin_amdgcn_make_buffer_rsrc((void*)p.v, 0, S_ * D_ * 2, 0x00020000);
      const rsrc_t rh = __builtin_amdgcn_make_buffer_rsrc((void*)p.shift_k, 0, D_ * 8, 0x00020000);
      char* const la = lds + a0l;
      __builtin_amdgcn_raw_ptr_buffer_load_lds(rh, (LAS void*)(la + 5120), 16, l16, (unsigned)sl * 1024u, 0, 0);
      __builtin_amdgcn_raw_ptr_buffer_load_lds(rk, (LAS void*)(la + 1024), 16, a0x, a0s, 0, 0);
      __builtin_amdgcn_raw_ptr_buffer_load_lds(rk, (LAS void*)(la + 2048), 16, a0g, a0s + 5120u, 0, 0);
      __builtin_amdgcn_raw_ptr_buffer_load_lds(rv, (LAS void*)(la + 3072), 16, a0g, a0s, 0, 0);
      __builtin_amdgcn_raw_ptr_buffer_load_lds(rv, (LAS void*)(la + 4096), 16, a0x, a0s + 5120u, 0, 0);
      __builtin_amdgcn_raw_ptr_buffer_load_lds(rk, (LAS void*)(la), 16, a0h, a0p, 0, 0);
      SCHED();
      const rsrc_t rf = __builtin_amdgcn_make_buffer_rsrc((void*)p.ssm_k, 0, S_ * D_ * 2, 0x00020000);
      dd2 = *(const CAS u32x2*)((const CAS char*)p.ssm_D + quad * 8);
      STAMP(32);
      {
        const u32x4 z = {0u, 0u, 0u, 0u};
        if (tid < 128) *(u32x4*)(lds + (tid >> 5) * CH_BYTES + (tid & 31) * 16) = z;
        else if (tid < 152) {
          const int i = (tid - 128) >> 2;
          *(u32x4*)(lds + (tid & 3) * CH_BYTES + U_BYTES + (i < 4 ? i * G_COPY : (i & 1 ? 4128 : 16))) = z;
        }
        const LAS char* a0r = (const LAS char*)(lds + L_A0) + w * 6144 + (l & 3) * 512;
#define A0R(k) (a0r + ((((l >> 2) ^ (4 * ((l + (k)) & 3))) * 16)))
        unsigned go = WS_KVT + (unsigned)(quad / 5) * 81920u + (unsigned)sl * 16384u + (unsigned)(l >> 2) * 1024u + (unsigned)w * 64u + (unsigned)(l & 3) * 16u;
        const LAS char* r0 = A0R(0), * r1 = A0R(1), * r2 = A0R(2);
        const LAS char* rsh = (const LAS char*)(lds + L_A0) + w * 6144 + 5120 + (l >> 2) * 64;
        asm volatile("" : "+v"(go), "+v"(r0), "+v"(r1), "+v"(r2), "+v"(rsh));
        SCHED();
#pragma unroll
        for (int j = 0; j < 2; ++j)
          __builtin_amdgcn_raw_ptr_buffer_load_lds(rf, (LAS void*)(lds + (w >> 1) * CH_BYTES + U_BYTES + 32 + (2 * (w & 1) + j) * 1024), 16, l16,
                                                   (unsigned)quad * 16384u + (unsigned)(2 * w + j) * 1024u, 0, 0);
        STAMP(34);
        SCHED();
        VMW(7);
        f32x2 wt[4][4];
#pragma unroll
        for (int e = 0; e < 4; ++e) {
          const u32x4 sh = *(const volatile LAS u32x4*)(rsh + 16 * e);
          wt[e][0] = (f32x2){bflo(sh[0]), bflo(sh[2])}; wt[e][1] = (f32x2){bfhi(sh[0]), bfhi(sh[2])};
          wt[e][2] = (f32x2){bflo(sh[1]), bflo(sh[3])}; wt[e][3] = (f32x2){bfhi(sh[1]), bfhi(sh[3])};
        }
#pragma unroll
        for (int e = 0; e < 4; ++e) asm volatile("" : "+v"(wt[e][0]), "+v"(wt[e][1]), "+v"(wt[e][2]), "+v"(wt[e][3]));
        asm volatile("" : "+s"(dd2));
        SCHED();
        VMW(2);
        STAMP(35);
        u32x4 ka[5], va[2];
        ka[0] = *(const volatile LAS u32x4*)(r0 + 256);
        ka[1] = *(const volatile LAS u32x4*)(r1 + 512);
        ka[2] = *(const volatile LAS u32x4*)(r1 + 768);
        ka[3] = *(const volatile LAS u32x4*)(r2 + 1024);
        ka[4] = *(const volatile LAS u32x4*)(r2 + 1280);
        va[0] = *(const volatile LAS u32x4*)(r0 + 3072);
        va[1] = *(const volatile LAS u32x4*)(r0 + 3328);
        SCHED();
        if (blk == 0) {
          if ((l & 3) < 2) ka[0] = z;
          if ((l & 3) == 0) { ka[1] = z; ka[2] = z; }
        }
        u32x4 ko;
#pragma unroll
        for (int e = 0; e < 4; ++e) {
          const f32x2 w0 = wt[e][0], w1 = wt[e][1], w2 = wt[e][2], w3 = wt[e][3];
          const f32x2 km3 = bf2(ka[0][e]), km2 = bf2(ka[1][e]), km1 = bf2(ka[2][e]), k0 = bf2(ka[3][e]), k1 = bf2(ka[4][e]);
          const f32x2 s0 = __builtin_elementwise_fma(k0, w0, __builtin_elementwise_fma(km1, w1, __builtin_elementwise_fma(km2, w2, km3 * w3)));
          const f32x2 s1 = __builtin_elementwise_fma(k1, w0, __builtin_elementwise_fma(k0, w1, __builtin_elementwise_fma(km1, w2, km2 * w3)));
          const f32x2 o0 = s0 * bf2(va[0][e]), o1 = s1 * bf2(va[1][e]);
          ko[2 * (e & 1)] = pack2(o1[0], o0[0]); ko[2 * (e & 1) + 1] = pack2(o1[1], o0[1]);
          if (e & 1) st16(rs, go + (unsigned)(e >> 1) * 512u, ko);
          SCHED();
        }
      }
    }
  }
  const int R = (xcd & 3) * 8 + (idx & 7);
  const int c = (xcd >> 2) * 4 + (idx >> 3);
  const int n16 = l & 15, h4 = l >> 4;
  const rsrc_t rq = __builtin_amdgcn_make_buffer_rsrc((void*)p.q, 0, S_ * D_ * 2, 0x00020000);
  const rsrc_t rsp = __builtin_amdgcn_make_buffer_rsrc((void*)p.w_sp, 0, S_ * S_ * 2, 0x00020000);
  const int wj = w < 7 ? w : 0;
  u32x4 wsp[4][8];
  const int wk = w ^ (dg == 5 || dg == 6 ? 4 : 0);
  const unsigned wso = (unsigned)(R * 64) * 4096u + (unsigned)wk * 512u;
  const unsigned wdo = (unsigned)(h4 * 8192 + ((n16 ^ h4) * 16));
#define WVO(q) (wdo ^ (unsigned)(((4 - (q)) & 3) * 64))
#define WSO(g, q) (wso + (unsigned)((g) >> 2) * 256u + (unsigned)(32 * (((g) >> 1) & 1) + ((g) & 1) + 8 * (q)) * 4096u)
  u32x4 raw[NRAW];
#define LDW(i) raw[i] = __builtin_amdgcn_raw_buffer_load_b128(rsp, WVO((i) & 3), WSO((i) >> 2, (i) & 3), 0)
  const int trow = R * 64 + 32 * ((w >> 1) & 1) + 2 * n16 + (w & 1);
  unsigned bs_raw;
  unsigned tag;
  unsigned fw2 = OOB;
  unsigned e_fw1;
  const unsigned fw1o = WS_FW1 + (unsigned)(((8 + c) * 8 + wj) * 4 + (l & 3)) * 128u;

  if (__builtin_expect(is_conv, 1)) {
    STAMP(1);
    DRAIN();
    tag = __builtin_amdgcn_readfirstlane(epv) * 2654435761u + 0x9E3779B9u;
    BAR();
    st4(rs, tid == 0 ? WS_FK + (unsigned)quad * 128u : OOB, tag);
    unsigned atv, ato;
    const unsigned long long atb = (unsigned long long)(w < 5 ? p.w_in : p.w_out);
    {
      const int i = w < 5 ? (cidx * 5 + w) * 20 + (l < 19 ? l : 19) : (cidx * 3 + w - 5) * 19 + (l < 18 ? l : 18);
      const int r = i / 3;
      ato = w < 5 ? (unsigned)((r < 639 ? r : 639) * (F_ * 2) + xcd * CB * 2 + (i - 3 * r) * 108) : (unsigned)(xcd * CB * D_ * 2 + (i < 1079 ? i : 1079) * 128);
      asm volatile("" : "+v"(ato));
    }
    {
      u32x4 d[3];
      u32x2 n[3];
#pragma unroll
      for (int j = 0; j < 3; ++j) {
        char* G = j < 2 ? lds + ((tid >> 8) + 2 * j) * CH_BYTES + U_BYTES + ((tid & 255) + 2) * 16 : lds + (tid & 3) * CH_BYTES + U_BYTES + 16;
        d[j] = *(const u32x4*)G;
        n[j] = *(const u32x2*)(G + 16);
      }
#pragma unroll
      for (int j = 0; j < 3; ++j) {
        char* G = j < 2 ? lds + ((tid >> 8) + 2 * j) * CH_BYTES + U_BYTES + ((tid & 255) + 2) * 16 : lds + (tid & 3) * CH_BYTES + U_BYTES + 16;
        const unsigned a0 = __builtin_amdgcn_alignbit(d[j][1], d[j][0], 16), a1 = __builtin_amdgcn_alignbit(d[j][2], d[j][1], 16),
                       a2 = __builtin_amdgcn_alignbit(d[j][3], d[j][2], 16), a3 = __builtin_amdgcn_alignbit(n[j][0], d[j][3], 16),
                       a4 = __builtin_amdgcn_alignbit(n[j][1], n[j][0], 16);
        if (j < 2 || tid < 4) {
          *(u32x4*)(G + G_COPY) = (u32x4){a0, a1, a2, a3};
          *(u32x4*)(G + 2 * G_COPY) = (u32x4){d[j][1], d[j][2], d[j][3], n[j][0]};
          *(u32x4*)(G + 3 * G_COPY) = (u32x4){a1, a2, a3, a4};
        }
      }
    }
    const int ch = w & 3, hf = w >> 2;
    const LAS char *ap, *bp;
    {
      const char* U = lds + ch * CH_BYTES;
      const char* G = U + U_BYTES;
      const int a = n16, jj = h4 >> 1, e2 = h4 & 1;
      const int sg = (a + 1) & 3;
      bp = (const LAS char*)U + 512 + 32 * (a - jj) + 16 * e2 - 256 * hf - 192;
      ap = (const LAS char*)G + sg * G_COPY + 2 * (16 * jj + a - 8 * e2 + 9 - sg) + 256 * hf;
      asm volatile("" : "+v"(ap), "+v"(bp));
    }
    STAMP(36);
    {
      wait_flag<1>(rs, WS_FK + (unsigned)(5 * (4 * w + (l & 3)) + (quad >> 5)) * 128u, tag);
      STAMP(29);
      const unsigned gp = WS_KVT + (unsigned)(4 * w + (l >> 5)) * 81920u + (unsigned)quad * 512u + (unsigned)(l & 31) * 16u;
      const u32x4 g0 = ld16(rs, gp), g1 = ld16(rs, gp + 163840u);
      SCHED();
      char* Uw = lds + 512 + (128 * w + (l & ~3) + 3 - (l & 3)) * 4;
#pragma unroll
      for (int i = 0; i < 4; ++i) *(unsigned*)(Uw + i * CH_BYTES) = g0[i];
#pragma unroll
      for (int i = 0; i < 4; ++i) *(unsigned*)(Uw + 256 + i * CH_BYTES) = g1[i];
    }
    STAMP(28);
    BAR();
    STAMP(2);
    const int te = 2 * tid;
    u32x4 qa[2];
    u32x4 sv[2];
    u32x2 mg;
    const unsigned xo = (unsigned)(tid >> 4) * WX1P + (unsigned)quad * 256u + (unsigned)(tid & 15) * 16u;
    const unsigned mgo = WS_MOD + (unsigned)((l < 12 ? (l >> 2) * D_ + (l & 3) : 0) + quad * 4) * 8u;
    auto ld_sv = [&]() {
      sv[0] = ld16(rs, WS_ST1 + (unsigned)tid * 16u);
      sv[1] = ld16(rs, WS_ST1 + (unsigned)tid * 16u + 8192u);
    };
    auto ld_q = [&]() {
      qa[0] = ld16(rs, WS_QB + xo);
      qa[1] = ld16(rs, WS_QB + xo + 32u * WX1P);
    };
    f32x2 hh[2][2][2], base[2][2][2];
    auto prep = [&]() {
      const LAS char* ur = (const LAS char*)(lds + 512) + ((te & ~7) + 6 - (te & 7)) * 2;
      unsigned uu[2][4];
#pragma unroll
      for (int i = 0; i < 8; ++i) uu[i >> 2][i & 3] = *(const volatile LAS unsigned*)(ur + (i >> 2) * 2048 + (i & 3) * CH_BYTES);
      if (!__all(((sv[0][0] ^ tag) | (sv[0][2] ^ tag) | (sv[1][0] ^ tag) | (sv[1][2] ^ tag)) == 0u)) {
        _Pragma("unroll 1") for (unsigned spins = 0; spins < SPIN_CAP; ++spins) {
          ld_sv();
          if (__all(sv[0][0] == tag && sv[0][2] == tag && sv[1][0] == tag && sv[1][2] == tag)) break;
          __builtin_amdgcn_s_sleep(4);
        }
        asm volatile("" ::: "memory");
        ld_q();
        mg = ld8(rs, mgo);
        DRAIN();
      }
      if (!__all(mg[0] == tag)) {
        _Pragma("unroll 1") for (unsigned spins = 0; spins < SPIN_CAP; ++spins) {
          mg = ld8(rs, mgo);
          if (__all(mg[0] == tag)) break;
          __builtin_amdgcn_s_sleep(2);
        }
      }
      f32x2 cc2[2], ss2[2], dv2[2];
      float cch[4], sch[4];
#pragma unroll
      for (int i = 0; i < 4; ++i) {
        const float sa = __uint_as_float(__builtin_amdgcn_readlane(mg[1], i)), ca = __uint_as_float(__builtin_amdgcn_readlane(mg[1], 4 + i)),
                    ga = __uint_as_float(__builtin_amdgcn_readlane(mg[1], 8 + i));
        cch[i] = ga * rbf(1.0f + ca);
        sch[i] = ga * sa;
      }
      cc2[0] = (f32x2){cch[0], cch[1]}; cc2[1] = (f32x2){cch[2], cch[3]};
      ss2[0] = (f32x2){sch[0], sch[1]}; ss2[1] = (f32x2){sch[2], sch[3]};
      dv2[0] = bf2(dd2[0]); dv2[1] = bf2(dd2[1]);
#pragma unroll
      for (int H = 0; H < 2; ++H)
#pragma unroll
        for (int e = 0; e < 2; ++e) {
          const unsigned st = sv[H][1 + 2 * e];
          const float a = __builtin_amdgcn_rcpf(bfhi(st)), b = -bflo(st) * a;
#pragma unroll
          for (int i = 0; i < 2; ++i) {
            const f32x2 q2 = bf2(qa[H][2 * e + i]);
            const f32x2 u2 = e ? (f32x2){bflo(uu[H][2 * i]), bflo(uu[H][2 * i + 1])} : (f32x2){bfhi(uu[H][2 * i]), bfhi(uu[H][2 * i + 1])};
            const f32x2 n1 = __builtin_elementwise_fma(q2, (f32x2){a, a}, (f32x2){b, b});
            hh[H][e][i] = __builtin_elementwise_fma(n1, cc2[i], ss2[i]);
            base[H][e][i] = __builtin_elementwise_fma(hh[H][e][i], u2 * dv2[i], q2);
            asm volatile("" : "+v"(hh[H][e][i]), "+v"(base[H][e][i]));
          }
        }
    };
    auto HF = [&]() { int x = hf; asm volatile("" : "+s"(x)); return x; };
    {
      const int a = n16;
      char* yp = lds + L_YP + (hf * 4 + ch) * 8192 + (h4 * 16 + ((a + 4 * h4) & 15)) * 16;
      f32x4 acc[8];
      u32x4 A[2][8], B[2][8];
      auto rd = [&](int k) {
        const int r = k / 24, j = k % 24;
        if (j >= 2 && j < 10) B[r & 1][9 - j] = *(const volatile LAS u32x4*)(bp + 512 * (9 - j) + 64 * (3 - r));
        else {
          const int pp = j < 2 ? 0 : (j - 8) >> 1;
          const u32x2 v = *(const volatile LAS u32x2*)(ap + 512 * pp + 64 * r + 8 * (j & 1));
          A[r & 1][pp][2 * (j & 1)] = v[0]; A[r & 1][pp][2 * (j & 1) + 1] = v[1];
        }
      };
#pragma unroll
      for (int k = 0; k < IC<conv_sched(CDIST).pre>::v; ++k) rd(k);
      SCHED();
      auto step = [&](auto ic) {
        constexpr int i = decltype(ic)::v, r = i / 36, pp = conv_sched(CDIST).pp[i], m = conv_sched(CDIST).m[i], k = conv_sched(CDIST).rd[i];
        if (i == 0) __builtin_amdgcn_s_setprio(3);
        if (i == 60) __builtin_amdgcn_s_setprio(2);
        if (i == 100) __builtin_amdgcn_s_setprio(1);
        if (i == 126) __builtin_amdgcn_s_setprio(0);
        acc[m + pp] = MFMA16(A[r & 1][pp], B[r & 1][m], i < 8 ? ZERO4 : acc[m + pp]);
        SCHED();
        if (k >= 0) rd(k);
        if (i == 0) atv = *(const GAS unsigned*)(atb + ato);
        if (i % 6 == 2 && i / 3 < NRAW) {
          if (!HF()) { LDW(i / 3); LDW(i / 3 + 1); }
        }
        if (i > Q_AT && (i - Q_AT) % 2 == 1 && i - Q_AT < NRAW) {
          if (HF()) { LDW(i - Q_AT - 1); LDW(i - Q_AT); }
        }
        if (i == SV_AT) { ld_sv(); bs_raw = ((const GAS u16*)p.b_sp)[trow]; }
        if (r == 3 && pp >= 1 && m == 7 - pp) *(f32x4*)(yp + (pp - 1) * 1024) = acc[pp - 1];
        SCHED();
      };
      static_for<0, Q_AT>(step);
      STAMP(3);
      DRAIN();
      asm volatile("" ::"v"(atv));
      ld_q();
      mg = ld8(rs, mgo);
      SCHED();
      STAMP(4);
      static_for<Q_AT, 144>(step);
      *(f32x4*)(yp + 7 * 1024) = acc[7];
      STAMP(5);
    }
    prep();
    e_fw1 = ld4(rs, fw1o);
    STAMP(6);
    const LAS char* yr = (const LAS char*)(lds + L_YP) + (te >> 8) * 1024 + (((te >> 2) & 3) * 16 + ((((te >> 4) & 15) + 4 * ((te >> 2) & 3)) & 15)) * 16 + (te & 3) * 4;
    asm volatile("" : "+v"(yr));
    BAR();
    STAMP(7);
    f32x2 y[2][8];
#pragma unroll
    for (int i = 0; i < 16; ++i) y[i >> 3][i & 7] = *(const volatile LAS f32x2*)(yr + (i >> 3) * 4096 + (i & 7) * 8192);
#pragma unroll
    for (int H = 0; H < 2; ++H) {
      u32x4 o;
#pragma unroll
      for (int e = 0; e < 2; ++e)
#pragma unroll
        for (int i = 0; i < 2; ++i) {
          const float x0 = __builtin_fmaf(hh[H][e][i][0], y[H][2 * i][e] + y[H][4 + 2 * i][e], base[H][e][i][0]);
          const float x1 = __builtin_fmaf(hh[H][e][i][1], y[H][2 * i + 1][e] + y[H][5 + 2 * i][e], base[H][e][i][1]);
          o[2 * e + i] = pack2(x0, x1);
        }
      st16(rs, WS_X1B + (unsigned)(32 * H) * WX1P + xo, o);
    }
    STAMP(8);
  } else {
    const rsrc_t rwi = __builtin_amdgcn_make_buffer_rsrc((void*)p.w_in, 0, D_ * F_ * 2, 0x00020000);
    const rsrc_t rwo = __builtin_amdgcn_make_buffer_rsrc((void*)p.w_out, 0, FH * D_ * 2, 0x00020000);
    STAMP(32);
    const int job = xcd + 8 * slot;
    const int T = job >= 50 && w < 3 && job - 50 + 46 * w < 128 ? job - 50 + 46 * w : -1;
    const bool unit = slot >= 8 || (slot >= 2 && slot < 6);
    const int ucb = slot >= 8 ? 8 + xcd : xcd, kg = (slot >= 8 ? slot : slot - 2) & 3;
    float* tab = (float*)(lds + L_TAB);
    float* red = (float*)(lds + L_RED);
    auto issue_unit = [&](int i) {
      const int g = i * 64 + l;
      __builtin_amdgcn_raw_ptr_buffer_load_lds(rwi, (LAS void*)(lds + L_J3 + i * 1024), 16, (unsigned)((g / 14) * (F_ * 2) + (g % 14) * 16),
                                               (unsigned)(kg * 160 * F_ * 2 + ucb * CB * 2), 0, 0);
    };
    if (T >= 0) {
      char* qt = lds + L_QT + w * 20480;
      const unsigned so = (unsigned)(16 * T) * 1280u;
#pragma unroll
      for (int n = 0; n < 16; ++n)
        __builtin_amdgcn_raw_ptr_buffer_load_lds(rq, (LAS void*)(qt + n * 1024), 16, l16 ^ (unsigned)(n * 16), so + (unsigned)(n * 1280), 0, 0);
#pragma unroll
      for (int j = 0; j < 4; ++j)
        __builtin_amdgcn_raw_ptr_buffer_load_lds(rq, (LAS void*)(qt + 16384 + j * 1024), 16, (unsigned)(h4 * 1280 + ((n16 ^ (4 * j + h4)) * 16)), so + (unsigned)(4 * j * 1280 + 1024), 0, 0);
      tag = __builtin_amdgcn_readfirstlane(epv) * 2654435761u + 0x9E3779B9u;
      STAMP(34);
      DRAIN();
      STAMP(35);
      const bf2_t one = {(__bf16)1.0f, (__bf16)1.0f};
      float s1[2] = {0.f, 0.f}, s2[2] = {0.f, 0.f};
      const int n0 = 2 * (l & 7);
      u32x4 qv[20];
#pragma unroll
      for (int i = 0; i < 20; ++i)
#pragma unroll
        for (int e = 0; e < 2; ++e) {
          const int n = n0 + e, ck = 4 * (i & 15) + h4;
          const u32x2 v = *(const volatile LAS u32x2*)(qt + (i < 16 ? n * 1024 : 16384 + n * 256) + ((ck ^ n) * 16) + (l & 8));
          qv[i][2 * e] = v[0]; qv[i][2 * e + 1] = v[1];
        }
#pragma unroll
      for (int i = 0; i < 20; ++i) {
        st16(rs, WS_QB + (unsigned)(T >> 1) * WX1P + (unsigned)i * 2048u + (unsigned)((l >> 3) * 256 + (T & 1) * 128 + (l & 7) * 16), qv[i]);
#pragma unroll
        for (int e = 0; e < 4; ++e) {
          const unsigned x = qv[i][e];
          const bf2_t v = __builtin_bit_cast(bf2_t, x);
          s1[e >> 1] = __builtin_amdgcn_fdot2_f32_bf16(v, one, s1[e >> 1], false);
          s2[e >> 1] = __builtin_amdgcn_fdot2_f32_bf16(v, v, s2[e >> 1], false);
        }
        SCHED();
      }
      u32x4 o = {tag, 0u, tag, 0u};
#pragma unroll
      for (int e = 0; e < 2; ++e) {
        const float t1 = sum_rows(s1[e] + dpp_f<0x128>(s1[e])), t2 = sum_rows(s2[e] + dpp_f<0x128>(s2[e]));
        const float mean = t1 * (1.0f / D_);
        const float var = __builtin_fmaxf(t2 * (1.0f / D_) - mean * mean, 0.f);
        o[2 * e + 1] = pack2(mean, __builtin_amdgcn_sqrtf(rbf(rbf(var) + 1e-5f)));
      }
      STAMP(1);
      DRAIN();
      if (l < 8) st16(rs, WS_ST1 + (unsigned)T * 128u + l16, o);
      STAMP(3);
    }
    if (job < 60) {
      const int grp = job < 10 ? 40 + job : (job < 50 ? job - 10 : job);
      if (job >= 50 && T < 0) __builtin_amdgcn_s_sleep(LATE_SLEEP);
      if (job >= 10 && T < 0) __builtin_amdgcn_s_sleep(AUX_SLEEP);
      const unsigned cv = *(const GAS unsigned*)((const GAS u16*)p.cvec + 80 * w + 2 * (l < 40 ? l : 0));
      u32x4 pre[10];
#pragma unroll
      for (int pp = 0; pp < 10; ++pp) pre[pp] = *(const GAS u32x4*)((const GAS u16*)p.w_mod + (size_t)(80 * w + pp * 8 + (l >> 3)) * 3840 + grp * 64 + (l & 7) * 8);
      SCHED();
      STAMP(34);
      tag = __builtin_amdgcn_readfirstlane(epv) * 2654435761u + 0x9E3779B9u;
      if (l < 40) {
        const float c0 = bflo(cv), c1 = bfhi(cv);
        *(f32x2*)(tab + w * 80 + 2 * l) = (f32x2){rbf(c0 * __builtin_amdgcn_rcpf(1.0f + __expf(-c0))), rbf(c1 * __builtin_amdgcn_rcpf(1.0f + __expf(-c1)))};
      }
      asm volatile("s_waitcnt lgkmcnt(0)" ::: "memory");
      f32x2 acc[4];
#pragma unroll
      for (int e = 0; e < 4; ++e) acc[e] = (f32x2){0.f, 0.f};
#pragma unroll
      for (int pp = 0; pp < 10; ++pp) {
        const float sv = tab[w * 80 + pp * 8 + (l >> 3)];
#pragma unroll
        for (int e = 0; e < 4; ++e) acc[e] = __builtin_elementwise_fma((f32x2){sv, sv}, bf2(pre[pp][e]), acc[e]);
      }
      STAMP(35);
      asm volatile("" : "+v"(acc[0]), "+v"(acc[1]), "+v"(acc[2]), "+v"(acc[3]));
#pragma unroll
      for (int e = 0; e < 8; ++e) { float a = acc[e >> 1][e & 1]; a += dpp_f<0x128>(a); acc[e >> 1][e & 1] = sum_rows(a); }
      if (l < 8) {
        *(f32x4*)(red + w * 64 + l * 8) = (f32x4){acc[0][0], acc[0][1], acc[1][0], acc[1][1]};
        *(f32x4*)(red + w * 64 + l * 8 + 4) = (f32x4){acc[2][0], acc[2][1], acc[3][0], acc[3][1]};
      }
      if (w == 7) __builtin_amdgcn_s_setprio(2);
      BAR();
      if (w == 7) {
        float t = 0.f;
#pragma unroll
        for (int r = 0; r < 8; ++r) t += red[r * 64 + l];
        st8(rs, WS_MOD + (unsigned)(grp * 64 + l) * 8u, (u32x2){tag, __float_as_uint(rbf(t))});
        __builtin_amdgcn_s_setprio(0);
      } else if (unit) {
#pragma unroll
        for (int k = 0; k < 5; ++k) issue_unit(w * 5 + k);
      }
    } else if (unit && w >= 3 && w < 7) {
      __builtin_amdgcn_s_sleep(AUX_SLEEP);
#pragma unroll
      for (int k = 0; k < 9; ++k) issue_unit((w - 3) * 9 + k < 35 ? (w - 3) * 9 + k : 34);
    }
    tag = __builtin_amdgcn_readfirstlane(epv) * 2654435761u + 0x9E3779B9u;
    if (unit) {
      auto mod_tab = [&](int which, float add, float* dst) {
        const unsigned o = WS_MOD + (unsigned)(which * D_ + kg * 160) * 8u;
        u32x4 g0, g1;
        _Pragma("unroll 1") for (unsigned spins = 0; spins < SPIN_CAP; ++spins) {
          g0 = ld16(rs, o + l16);
          g1 = ld16(rs, o + 1024u + (l16 & 255u));
          if (__all(g0[0] == tag && g0[2] == tag && g1[0] == tag && g1[2] == tag)) break;
          __builtin_amdgcn_s_sleep(2);
        }
        *(f32x2*)(dst + 2 * l) = (f32x2){rbf(add + __uint_as_float(g0[1])), rbf(add + __uint_as_float(g0[3]))};
        if (l < 16) *(f32x2*)(dst + 128 + 2 * l) = (f32x2){rbf(add + __uint_as_float(g1[1])), rbf(add + __uint_as_float(g1[3]))};
      };
      if (w == 7) {
        STAMP(1);
        if (slot >= 8) __builtin_amdgcn_s_sleep(64);
        mod_tab(4, 1.0f, tab);
        BAR();
        STAMP(2);
        mod_tab(3, 0.0f, tab + 160);
        BAR();
      } else {
        DRAIN();
        STAMP(36);
        BAR();
        STAMP(2);
        const char* bp = lds + L_J3 + (4 * h4 + (n16 >> 2)) * 224 + (n16 & 3) * 8 + w * 32;
        const float* tp = tab + 4 * h4;
        u32x4 f3[5];
        float cs = 0.f, b1 = 0.f;
        const bool pad = w == 6 && n16 >= 12;
        const unsigned padv = (n16 == 12 && ucb >= 8) ? 0x3f803f80u : 0u;
#pragma unroll
        for (int k = 0; k < 5; ++k) f3[k] = trfrag(bp + k * 7168, 16 * 224);
#pragma unroll
        for (int k = 0; k < 5; ++k) {
          const f32x4 ca = *(const f32x4*)(tp + k * 32), cb4 = *(const f32x4*)(tp + k * 32 + 16);
          u32x4 o;
#pragma unroll
          for (int e = 0; e < 4; ++e) {
            const float c0 = e < 2 ? ca[2 * e] : cb4[2 * e - 4], c1 = e < 2 ? ca[2 * e + 1] : cb4[2 * e - 3];
            const float p0 = bflo(f3[k][e]) * c0, p1 = bfhi(f3[k][e]) * c1;
            cs += p0 + p1;
            o[e] = pad ? padv : pack2(p0, p1);
          }
          st16(rs, WS_WINF + (unsigned)(ucb * 7 + w) * 20480u + (unsigned)(kg * 5 + k) * 1024u + l16, o);
        }
        cs = sum_rows(cs);
        DRAIN();
        if (l == 0) st4(rs, WS_FW1 + (unsigned)((ucb * 8 + w) * 4 + kg) * 128u, tag);
        STAMP(5);
        BAR();
#pragma unroll
        for (int k = 0; k < 5; ++k) {
          const f32x4 sa = *(const f32x4*)(tp + 160 + k * 32), sb4 = *(const f32x4*)(tp + 160 + k * 32 + 16);
#pragma unroll
          for (int e = 0; e < 4; ++e) {
            const float s0 = e < 2 ? sa[2 * e] : sb4[2 * e - 4], s1 = e < 2 ? sa[2 * e + 1] : sb4[2 * e - 3];
            b1 = __builtin_fmaf(s0, bflo(f3[k][e]), __builtin_fmaf(s1, bfhi(f3[k][e]), b1));
          }
        }
        b1 = sum_rows(b1);
        if (pad) { cs = padv ? 160.0f : 0.0f; b1 = 0.0f; }
        if (l < 16) st16(rs, WS_CS + (unsigned)((ucb * 4 + kg) * 112 + w * 16 + l) * 16u, (u32x4){tag, __float_as_uint(cs), tag, __float_as_uint(b1)});
      }
    } else {
      if (job >= 60 && T < 0) __builtin_amdgcn_s_sleep(100);
      if (slot < 2 && w >= 2 && w < 6) {
        const int c5 = slot * 4 + w - 2, c2 = xcd;
        char* st = lds + L_J5 + (w - 2) * 18432;
#pragma unroll
        for (int r = 0; r < 18; ++r) {
          const int g = r * 64 + l;
          __builtin_amdgcn_raw_ptr_buffer_load_lds(rwo, (LAS void*)(st + r * 1024), 16, (unsigned)((g / 10) * (D_ * 2) + (g % 10) * 16),
                                                   (unsigned)(xcd * CB * D_ * 2 + c5 * 160), 0, 0);
        }
        DRAIN();
        const char* bp = st + (4 * h4 + (n16 >> 2)) * 160 + (n16 & 3) * 8;
        const unsigned base = WS_WOUTF + (unsigned)c5 * WBLK;
#pragma unroll
        for (int dj = 0; dj < 5; ++dj) {
          st16(rs, base + (unsigned)c2 * 10240u + (unsigned)dj * 1024u + l16, trfrag(bp + dj * 32, 32 * 160));
          st16(rs, base + (unsigned)c2 * 10240u + (unsigned)(5 + dj) * 1024u + l16, trfrag(bp + 16 * 160 + dj * 32, 32 * 160));
          st16(rs, base + 81920u + (unsigned)c2 * 5120u + (unsigned)dj * 1024u + l16, trfrag(bp + 64 * 160 + dj * 32, 16 * 160));
          st8(rs, base + 122880u + (unsigned)((c2 >> 1) * 5 + dj) * 1024u + (unsigned)(c2 & 1) * 512u + (unsigned)l * 8u, trd(bp + 96 * 160 + dj * 32));
        }
        fw2 = WS_FW2 + (unsigned)(c5 * 8 + xcd) * 128u;
      }
    }
    STAMP(4);
#pragma unroll
    for (int i = 0; i < NRAW; ++i) LDW(i);
    bs_raw = ((const GAS u16*)p.b_sp)[trow];
    e_fw1 = ~tag;
    asm volatile(".Lmega_aux_end:");
    asm volatile("" ::"v"(cpf));
    STAMP(8);
  }

  LAS char* xa = (LAS char*)(lds + L_XI) + l * 16;
  LAS char* xb = xa + 65536;
  asm volatile("" : "+v"(xb));
#define LDA(ks, rt) (*(const volatile LAS u32x4*)((ks) < 16 ? xa + ((ks) * 4 + (rt)) * 1024 : xb + (((ks) - 16) * 4 + (rt)) * 1024))
  auto lda1 = [&](int ks, u32x4* A) {
#pragma unroll
    for (int rt = 0; rt < 4; ++rt) A[rt] = LDA(ks, rt);
  };
  const LAS char* tb4 = (const LAS char*)(lds + L_MISC) + h4 * 16;
  const LAS char* tbn = (const LAS char*)(lds + L_MISC) + n16 * 4;
  asm volatile("" : "+v"(tb4), "+v"(tbn));
  u32x4 xr[5][2];
  auto ld_x1 = [&](int m0, int m1) {
#pragma unroll
    for (int m = m0; m < m1; ++m) {
      if (m == 0) __builtin_amdgcn_s_setprio(2);
      if (m == 1) __builtin_amdgcn_s_setprio(1);
      if (m == 3) __builtin_amdgcn_s_setprio(0);
#pragma unroll
      for (int e = 0; e < 2; ++e)
        xr[m][e] = ld16(rs, l16, WS_X1B + (unsigned)(R * 2 + (w & 1)) * WX1P + (unsigned)(4 * m + (w >> 1)) * 2048u + (unsigned)e * 1024u);
      SCHED();
    }
  };
  LAS char* xw = xa + w * 2048;
  auto put_x1 = [&](int m) {
    LAS char* d = m < 4 ? xw + m * 16384 : xb + w * 2048;
#pragma unroll
    for (int e = 0; e < 2; ++e) {
      *(volatile LAS u32x2*)(d + 8 * e) = (u32x2){xr[m][e][0], xr[m][e][1]};
      *(volatile LAS u32x2*)(d + 1024 + 8 * e) = (u32x2){xr[m][e][2], xr[m][e][3]};
    }
  };
  auto poison_u = [&]() {
#pragma unroll
    for (int k = 0; k < 2; ++k) {
      const unsigned o = (unsigned)(wj * 2 + k) * 1024u + l16;
      const unsigned rt = o / 3584u, r = o - rt * 3584u;
      st16(rs, WS_UF + ((unsigned)(R * 4) + rt) * 8u * 3584u + (unsigned)c * 3584u + r, (u32x4){~0u, ~0u, ~0u, ~0u});
    }
  };
  const unsigned wob = WS_WOUTF + (unsigned)c * WBLK;
  const LAS char* wrd = (const LAS char*)(lds + L_WS) + w * 8192 + n16 * 256 + ((h4 ^ ((4 - (n16 >> 2)) & 3)) * 64);
#define DMA_W(g, q) __builtin_amdgcn_raw_ptr_buffer_load_lds(rsp, (LAS void*)(lds + L_WS + w * 8192 + (((g) + 1) & 1) * 4096 + (q) * 1024), 16, WVO(q), WSO(g, q), 0, 0)
#define RD_W(g) do { _Pragma("unroll") for (int i_ = 0; i_ < 4; ++i_) \
                      wsp[(g) & 3][((g) >> 2) * 4 + i_] = *(const volatile LAS u32x4*)(wrd + (((g) + 1) & 1) * 4096 + ((i_ ^ (n16 & 3)) * 16)); } while (0)
  auto park = [&]() {
    LAS char* d = (LAS char*)(lds + L_WS) + w * 8192 + l * 16;
#pragma unroll
    for (int g = 0; g < NRAW / 4; ++g) {
#pragma unroll
      for (int q = 0; q < 4; ++q) *(volatile LAS u32x4*)(d + ((g + 1) & 1) * 4096 + q * 1024) = raw[4 * g + q];
      if (g < 2) RD_W(g);
    }
    asm volatile("s_waitcnt lgkmcnt(0)" ::: "memory");
  };
#define DMA_AB(k) dma1k(rs, lds + L_AB + ((k) * 8 + w) * 1024, l16, wob + (unsigned)((k) * 8 + w) * 1024u)
  const int rto = w & 3, cls = w >> 2;
  u32x2 xp[3];
  constexpr int W = G2W;
  const unsigned zb = WS_Z2F + (unsigned)c * 458752u + (unsigned)wk * 8192u;
  const unsigned fzo = WS_FZ + (unsigned)(c * 32 + wk * 4 + (l & 3)) * 128u;
  u32x4 fr[W];
#define LDZ(i) fr[(i) % W] = ld16(rs, l16, zb + (unsigned)((i) >> 3) * 65536u + (unsigned)((i) & 7) * 1024u)
  u32x4 cr[5];
#define LDCR(k) cr[k] = ld16(rs, l16, wob + 81920u + (unsigned)(w * 5 + (k)) * 1024u)
  auto rd_xp = [&]() {
#pragma unroll
    for (int j = 0; j < 3; ++j) {
      const int q4 = 5 * c + (cls ? (3 + (j < 2 ? j : 1)) : j);
      xp[j] = *(const volatile LAS u32x2*)(xa + ((q4 >> 1) * 4 + rto) * 1024 + (q4 & 1) * 8);
    }
  };
  if (w < 7) {
    u32x4 B2[20], B1[20];
    const unsigned b2o = WS_WINF + (unsigned)((8 + c) * 7 + w) * 20480u, b1o = WS_WINF + (unsigned)(c * 7 + w) * 20480u;
#define LDB2(ks) B2[ks] = ld16(rs, l16, b2o + (unsigned)(ks) * 1024u)
#define LDB1(ks) B1[ks] = ld16(rs, l16, b1o + (unsigned)(ks) * 1024u)
    const bool early = __all(e_fw1 == tag);
    asm volatile("" ::: "memory");
    {
      const unsigned le = early ? l16 : OOB;
#pragma unroll
      for (int ks = 0; ks < NB2A; ++ks) B2[ks] = ld16(rs, le, b2o + (unsigned)ks * 1024u);
      SCHED();
      if (is_conv) VMW(NB2A);
    }
    STAMP(33);
    BAR();
    STAMP(37);
    if (!early) {
      wait_flag<4>(rs, fw1o, tag);
      DRAIN();
      st4(rs, l == 0 ? fw2 : OOB, tag);
#pragma unroll
      for (int ks = 0; ks < NB2A; ++ks) LDB2(ks);
    }
#pragma unroll
    for (int ks = NB2A; ks < 20; ++ks) {
      LDB2(ks);
      if (ks == 12) { SCHED(); park(); SCHED(); }
      if (ks == 13) { SCHED(); __builtin_amdgcn_s_barrier(); SCHED(); }
    }
    STAMP(9);
    BAR();
    STAMP(10);
    ld_x1(0, 3);
    SCHED();
#pragma unroll
    for (int m = 0; m < 3; ++m) put_x1(m);
    ld_x1(3, 5);
    poison_u();
    SCHED();
    if (w == 6) __builtin_amdgcn_s_setprio(2);
    BAR();
    STAMP(11);
    f32x4 acc[4];
    u32x4 A[2][4];
    lda1(0, A[0]);
#pragma unroll
    for (int ks = 0; ks < 20; ++ks) {
      if (ks == 6) put_x1(3);
      if (ks == 8) put_x1(4);
      if (ks == 11) BAR();
      if (ks >= 4) LDB1(ks - 4);
      if (ks >= 16) LDB1(ks);
      if (ks == 12) RD_W(2);
      SCHED();
#pragma unroll
      for (int rt = 0; rt < 4; ++rt) {
        if (ks < 19) A[(ks + 1) & 1][rt] = LDA(ks + 1, rt);
        SCHED();
        acc[rt] = MFMA16(A[ks & 1][rt], B2[ks], ks ? acc[rt] : ZERO4);
        SCHED();
      }
    }
    STAMP(12);
    if (w == 6) {
      if (n16 == 12) {
#pragma unroll
        for (int rt = 0; rt < 4; ++rt) *(f32x4*)(lds + L_S1 + (rt * 16 + 4 * h4) * 4) = acc[rt];
      }
      asm volatile("" ::: "memory");
      *(volatile LAS unsigned*)(lds + L_S1 + 256) = tag;
      __builtin_amdgcn_s_setprio(0);
    }
    VMW(20);
    BAR();
    STAMP(13);
    f32x2 zz[8];
    f32x4 cs1a, cs1b;
    {
      f32x4 rs4[4], nm4[4];
#pragma unroll
      for (int rt = 0; rt < 4; ++rt) {
        rs4[rt] = *(const LAS f32x4*)(tb4 + (L_T2 - L_MISC) + rt * 64);
        nm4[rt] = *(const LAS f32x4*)(tb4 + (L_T2 - L_MISC) + 256 + rt * 64);
      }
      const f32x2 cs2 = *(const f32x2*)(lds + L_CS1 + (112 + 16 * w + n16) * 8);
      const f32x2 csv = {cs2[0], cs2[0]}, b1v = {cs2[1], cs2[1]};
      float v1[16], v2[16];
#pragma unroll
      for (int i = 0; i < 8; ++i) {
        const int rt = i >> 1;
        const f32x2 a2 = i & 1 ? HI2(acc[rt]) : LO2(acc[rt]), n2 = i & 1 ? HI2(nm4[rt]) : LO2(nm4[rt]), r2 = i & 1 ? HI2(rs4[rt]) : LO2(rs4[rt]);
        zz[i] = gelu2(__builtin_elementwise_fma(__builtin_elementwise_fma(n2, csv, a2), r2, b1v));
        asm volatile("" : "+v"(zz[i]));
        DMA_W(3 + (i >> 2), i & 3);
        const f32x2 sq = zz[i] * zz[i];
        v1[2 * i] = zz[i][0]; v1[2 * i + 1] = zz[i][1]; v2[2 * i] = sq[0]; v2[2 * i + 1] = sq[1];
      }
      const float t1 = tr_sum16(v1, l);
      const float t2 = tr_sum16(v2, l);
      *(f32x2*)(lds + L_Z1S + (w * 64 + (n16 >> 2) * 16 + 4 * h4 + (n16 & 3)) * 8) = (f32x2){t1, t2};
      lda1(0, A[0]);
      cs1a = *(const f32x4*)(lds + L_CS1 + (16 * w + 4 * h4) * 8);
      cs1b = *(const f32x4*)(lds + L_CS1 + (16 * w + 4 * h4) * 8 + 16);
    }
    BAR();
    STAMP(14);
#pragma unroll
    for (int ks = 0; ks < 20; ++ks) {
      if (ks == Z1SPLIT) {
        STAMP(15);
        BAR();
        STAMP(16);
        f32x4 ra[4], rb[4];
#pragma unroll
        for (int rt = 0; rt < 4; ++rt) {
          ra[rt] = *(const LAS f32x4*)(tb4 + (L_ST2 - L_MISC) + rt * 64);
          rb[rt] = *(const LAS f32x4*)(tb4 + (L_ST2 - L_MISC) + 256 + rt * 64);
        }
#pragma unroll
        for (int i = 0; i < 8; ++i) {
          zz[i] = __builtin_elementwise_fma(zz[i], i & 1 ? HI2(ra[i >> 1]) : LO2(ra[i >> 1]), i & 1 ? HI2(rb[i >> 1]) : LO2(rb[i >> 1]));
          asm volatile("" : "+v"(zz[i]));
        }
        const u32x4 o0 = {pack2(zz[0][0], zz[2][0]), pack2(zz[0][1], zz[2][1]), pack2(zz[1][0], zz[3][0]), pack2(zz[1][1], zz[3][1])};
        const u32x4 o1 = {pack2(zz[4][0], zz[6][0]), pack2(zz[4][1], zz[6][1]), pack2(zz[5][0], zz[7][0]), pack2(zz[5][1], zz[7][1])};
        const unsigned zo = WS_Z2F + (unsigned)((c * 7 + w) * 64 + 4 * (R >> 1) + h4) * 1024u + (unsigned)(R & 1) * 512u + (unsigned)n16 * 16u;
        st16(rs, zo, o0);
        st16(rs, zo + 256u, o1);
        SCHED();
      }
      if (ks == 0) { VMW(4); RD_W(3); }
      if (ks == 2) { VMW(0); RD_W(4); }
      if (ks == 8) { VMW(4 + 2); RD_W(5); }
      if (ks == 11) rd_xp();
      if (ks == 13) { VMW(4 + 5 + 2); RD_W(6); }
      if (ks >= 13 && ks < 19) DMA_AB(ks - 13);
      if (ks >= 2 && ks < 6) { DMA_W(5 + ((ks - 2) >> 1), 2 * (ks & 1)); DMA_W(5 + ((ks - 2) >> 1), 2 * (ks & 1) + 1); }
      if (ks >= 6 && ks < 9) LDCR(ks - 6);
      if (ks == 12) { LDCR(3); LDCR(4); }
      if (ks == 10 || ks == 11) { DMA_W(7, 2 * (ks & 1)); DMA_W(7, 2 * (ks & 1) + 1); }
      SCHED();
#pragma unroll
      for (int rt = 0; rt < 4; ++rt) {
        if (ks < 19) A[(ks + 1) & 1][rt] = LDA(ks + 1, rt);
        SCHED();
        acc[rt] = MFMA16(B1[ks], A[ks & 1][rt], ks ? acc[rt] : ZERO4);
        SCHED();
      }
    }
    STAMP(18);
    VMW(2 + 2 + 6);
    RD_W(7);
    const f32x2 c01 = {cs1a[0], cs1a[2]}, b01 = {cs1a[1], cs1a[3]}, c23 = {cs1b[0], cs1b[2]}, b23 = {cs1b[1], cs1b[3]};
    u32x2 g1[4];
    auto z1g = [&](int rt) {
      const float r1 = *(const LAS float*)(tbn + (L_T2 - L_MISC) + rt * 64), n1 = *(const LAS float*)(tbn + (L_T2 - L_MISC) + 256 + rt * 64);
      const f32x2 r2 = {r1, r1}, n2 = {n1, n1};
      const f32x2 a = gelu2(__builtin_elementwise_fma(__builtin_elementwise_fma(n2, c01, LO2(acc[rt])), r2, b01));
      const f32x2 b = gelu2(__builtin_elementwise_fma(__builtin_elementwise_fma(n2, c23, HI2(acc[rt])), r2, b23));
      g1[rt] = (u32x2){pack2(a[0], a[1]), pack2(b[0], b[1])};
      asm volatile("" : "+v"(g1[rt]));
    };
    SCHED();
    VMW(2 + 6);
    BAR();
    STAMP(17);
#pragma unroll
    for (int rt = 0; rt < 4; ++rt) {
      if (rt < 2) { DMA_AB(6 + 2 * rt); DMA_AB(7 + 2 * rt); }
      z1g(rt);
      *(u32x2*)(lds + L_Z1S + ((w * 4 + rt) * 64 + l) * 8) = g1[rt];
    }
  } else {
    __builtin_amdgcn_s_setprio(3);
    DRAIN();
    BAR();
    if (is_conv && l == 0) st4(rs, WS_FX + (unsigned)quad * 128u, tag);
    STAMP(9);
    *(volatile LAS unsigned*)(lds + L_S1 + 256) = ~tag;
    u32x4 gv, cv[2][2][4];
    const unsigned gvo = WS_MOD + (unsigned)(5 * D_ + 80 * c) * 8u + (l < 40 ? l16 : 0u);
    auto ld_consts = [&]() {
      gv = ld16(rs, gvo);
#pragma unroll
      for (int i = 0; i < 16; ++i) cv[i >> 3][(i >> 2) & 1][i & 3] = ld16(rs, l < 56 ? l16 : 0u, WS_CS + (unsigned)(((c + 8 * (i >> 3)) * 4 + (i & 3)) * 112 + 56 * ((i >> 2) & 1)) * 16u);
    };
    auto ok_consts = [&]() {
      unsigned x = (gv[0] ^ tag) | (gv[2] ^ tag);
#pragma unroll
      for (int i = 0; i < 16; ++i) x |= (cv[i >> 3][(i >> 2) & 1][i & 3][0] ^ tag) | (cv[i >> 3][(i >> 2) & 1][i & 3][2] ^ tag);
      asm volatile("" : "+v"(x));
      return __all(x == 0u);
    };
    auto touch_w = [&](int k) {
      cpf ^= __builtin_amdgcn_raw_buffer_load_b32(rsp, (unsigned)(h4 * 4096 + (n16 >> 1) * 512 + (n16 & 1) * 128),
                                                  (unsigned)(R * 64 + (c & 3) * 16 + k * 4) * 4096u + 256u, 0);
    };
    park();
    __builtin_amdgcn_s_barrier();
    ld_consts();
    STAMP(31);
    _Pragma("unroll 1") for (unsigned spins = 0; spins < SPIN_CAP; ++spins) {
      const unsigned f0 = ld4(rs, WS_FX + (unsigned)l * 128u), f1 = ld4(rs, WS_FX + 8192u + (unsigned)l * 128u),
                     f2 = ld4(rs, l < 32 ? WS_FX + 16384u + (unsigned)l * 128u : WS_FW1 + (unsigned)(c * 32 + (l < 60 ? l - 32 : 0)) * 128u);
      if (__all(f0 == tag && f1 == tag && f2 == tag)) break;
      __builtin_amdgcn_s_sleep(POLL_SLEEP);
    }
    BAR();
    STAMP(10);
    ld_x1(0, 5);
    __builtin_amdgcn_s_setprio(3);
    if (!ok_consts()) {
      _Pragma("unroll 1") for (unsigned spins = 0; spins < SPIN_CAP; ++spins) {
        ld_consts();
        if (ok_consts()) break;
        __builtin_amdgcn_s_sleep(8);
      }
    }
    if (l < 40) *(f32x2*)(lds + L_GB + l * 8) = (f32x2){__uint_as_float(gv[1]), __uint_as_float(gv[3])};
    if (l < 56) {
#pragma unroll
      for (int i = 0; i < 4; ++i) {
        const u32x4* q4 = cv[i >> 1][i & 1];
        *(f32x2*)(lds + L_CS1 + (56 * i + l) * 8) =
            (f32x2){(__uint_as_float(q4[0][1]) + __uint_as_float(q4[1][1])) + (__uint_as_float(q4[2][1]) + __uint_as_float(q4[3][1])),
                    (__uint_as_float(q4[0][3]) + __uint_as_float(q4[1][3])) + (__uint_as_float(q4[2][3]) + __uint_as_float(q4[3][3]))};
      }
    }
    SCHED();
#pragma unroll
    for (int m = 0; m < 3; ++m) put_x1(m);
    BAR();
    f32x4 acc[4];
    u32x4 A[2][4];
    lda1(0, A[0]);
#pragma unroll
    for (int ks = 0; ks < 20; ++ks) {
      if (ks == 6) put_x1(3);
      if (ks == 8) put_x1(4);
      if (ks == 11) BAR();
      if (ks == 4) RD_W(2);
      if (ks >= 6 && ks < 14) DMA_W(3 + ((ks - 6) >> 2), (ks - 6) & 3);
      if (ks >= 1 && ks < 5) touch_w(ks - 1);
      if (ks < 19) lda1(ks + 1, A[(ks + 1) & 1]);
      SCHED();
#pragma unroll
      for (int rt = 0; rt < 4; ++rt) acc[rt] = MFMA16(A[ks & 1][rt], A[ks & 1][rt], ks ? acc[rt] : ZERO4);
      SCHED();
    }
    {
      if ((n16 >> 2) == h4) {
#pragma unroll
        for (int rt = 0; rt < 4; ++rt) {
          const float lo = (l & 1) ? acc[rt][1] : acc[rt][0], hi = (l & 1) ? acc[rt][3] : acc[rt][2];
          *(volatile LAS float*)(tbn + (L_T2 - L_MISC) + rt * 64) = (l & 2) ? hi : lo;
        }
      }
      const float s2 = *(volatile LAS float*)(lds + L_T2 + l * 4) * (1.0f / D_);
      float s1;
      _Pragma("unroll 1") for (unsigned spins = 0; spins < SPIN_CAP * 16; ++spins) {
        const unsigned f = *(volatile LAS unsigned*)(lds + L_S1 + 256);
        s1 = *(volatile LAS float*)(lds + L_S1 + l * 4);
        if (f == tag) break;
      }
      const float nm = s1 * (-1.0f / D_);
      *(float*)(lds + L_T2 + l * 4) = __builtin_amdgcn_rsqf(__builtin_fmaxf(s2 - nm * nm, 0.f) + 1e-5f);
      *(float*)(lds + L_T2 + 256 + l * 4) = nm;
    }
    BAR();
    __builtin_amdgcn_s_setprio(0);
    asm volatile("" ::"v"(cpf));
    DRAIN();
    RD_W(3); RD_W(4);
    rd_xp();
    asm volatile("s_waitcnt lgkmcnt(0)" ::: "memory");
#pragma unroll
    for (int i = 0; i < 8; ++i) DMA_W(5 + (i >> 2), i & 3);
    const unsigned e_fw2 = ld4(rs, WS_FW2 + (unsigned)(c * 8 + (l & 7)) * 128u);
    if (!__all(e_fw2 == tag)) wait_flag<2>(rs, WS_FW2 + (unsigned)(c * 8 + (l & 7)) * 128u, tag);
    __builtin_amdgcn_s_setprio(3);
    BAR();
    {
      f32x2 t = {0.f, 0.f};
#pragma unroll
      for (int k = 0; k < 7; ++k) t += *(const f32x2*)(lds + L_Z1S + (k * 64 + l) * 8);
      st16(rs, WS_PART + (unsigned)(R * 8 + c) * 1024u + l16, (u32x4){tag, __float_as_uint(t[0]), tag, __float_as_uint(t[1])});
    }
    STAMP(14);
    {
      u32x4 v[8];
      const unsigned po = WS_PART + (unsigned)R * 8192u + l16;
      __builtin_amdgcn_s_sleep(16);
      _Pragma("unroll 1") for (unsigned spins = 0; spins < SPIN_CAP; ++spins) {
#pragma unroll
        for (int k = 0; k < 8; ++k) v[k] = ld16(rs, po + (unsigned)k * 1024u);
        unsigned x = 0;
#pragma unroll
        for (int k = 0; k < 8; ++k) x |= (v[k][0] ^ tag) | (v[k][2] ^ tag);
        asm volatile("" : "+v"(x));
        if (__all(x == 0u)) break;
        __builtin_amdgcn_s_sleep(1);
      }
      float t1 = 0.f, t2 = 0.f;
#pragma unroll
      for (int k = 0; k < 8; ++k) { t1 += __uint_as_float(v[k][1]); t2 += __uint_as_float(v[k][3]); }
      const float mean = t1 * (1.0f / FH);
      const float var = __builtin_fmaxf(t2 * (1.0f / FH) - mean * mean, 0.f);
      const float rstd = __builtin_amdgcn_rsqf(var + 1e-5f);
      *(float*)(lds + L_ST2 + l * 4) = rstd;
      *(float*)(lds + L_ST2 + 256 + l * 4) = -mean * rstd;
    }
    STAMP(15);
    BAR();
    RD_W(5); RD_W(6);
    asm volatile("s_waitcnt lgkmcnt(0)" ::: "memory");
#pragma unroll
    for (int q = 0; q < 4; ++q) DMA_W(7, q);
    DRAIN();
    RD_W(7);
    BAR();
    if (l == 0) st4(rs, WS_FZ + (unsigned)(c * 32 + R) * 128u, tag);
    STAMP(17);
    __builtin_amdgcn_s_setprio(0);
#pragma unroll
    for (int k = 0; k < 10; ++k) DMA_AB(k);
#pragma unroll
    for (int k = 0; k < 5; ++k) LDCR(k);
  }
  const rsrc_t rout = __builtin_amdgcn_make_buffer_rsrc((void*)p.out, 0, S_ * D_ * 2, 0x00020000);
  STAMP(19);
  u32x2 z1o[4];
  wait_flag<POLL_SLEEP>(rs, fzo, tag);
#pragma unroll
  for (int i = 0; i < W; ++i) { LDZ(i); SCHED(); }
  STAMP(20);

  const unsigned ub = WS_UF + (unsigned)((R * 4 + rto) * 8 + c) * 3584u;
  const float bs = bflo(bs_raw);
  const unsigned ur = WS_UF + (unsigned)((R * 4 + rto) * 8 + cls) * 3584u;
  u32x4 uf[4][2];
  f32x4 acc[2][4];
  f32x4* const bufw = (f32x4*)(lds + L_BUF) + w * 256 + l;
#define BUFW(slot, rt) bufw[(slot) * 2048 + (rt) * 64]
  {
    const f32x4* const rb = (const f32x4*)(lds + L_BUF) + cls * 2048 + rto * 64 + l;
    f32x4 rdv[4], s;
    u32x4 uo;
#pragma unroll
    for (int j = 0; j < 7; ++j) {
#pragma unroll
      for (int ks = 0; ks < 8; ++ks) {
        const int i = 8 * j + ks;
#pragma unroll
        for (int rt = 0; rt < 4; ++rt) acc[j & 1][rt] = MFMA16(fr[i % W], wsp[rt][ks], ks ? acc[j & 1][rt] : ZERO4);
        if (i == 0) STAMP(21);
        if (i == 24) STAMP(22);
        SCHED();
        if (i + W < 56) LDZ(i + W);
        if (j > 0) {
          if (ks == 0 && (j & 1)) BAR();
          if (j == 1 && ks == 0) {
#pragma unroll
            for (int k = 0; k < 4; ++k) z1o[k] = *(const u32x2*)(lds + L_Z1S + (((k < 3 ? 2 * k + cls : 6) * 4 + rto) * 64 + l) * 8);
          }
          if (i >= (56 - G2W) && i < 53) {
#pragma unroll
            for (int k = 0; k < 8; ++k)
              if ((56 - G2W) + k * (53 - (56 - G2W)) / 8 == i) uf[k >> 1][k & 1] = ld16(rs, l16, ur + (unsigned)(k >> 1) * 7168u + (unsigned)(k & 1) * 1024u);
            SCHED();
          }
          const bool fast = j == 6;
          if (!fast && ks < 4) BUFW((j - 1) & 1, ks) = acc[(j - 1) & 1][ks];
          if (fast && ks < 2) { BUFW(1, 2 * ks) = acc[1][2 * ks]; BUFW(1, 2 * ks + 1) = acc[1][2 * ks + 1]; }
          if (!(j & 1)) {
            const int k0 = fast ? 2 : 5;
            if (ks == k0) {
              BAR();
#pragma unroll
              for (int k = 0; k < 4; ++k) rdv[k] = rb[k * 256];
            }
            if (ks == k0 + 1) {
              s = (rdv[0] + rdv[1]) + (rdv[2] + rdv[3]);
#pragma unroll
              for (int k = 0; k < 4; ++k) rdv[k] = rb[(4 + k) * 256];
            }
            if (ks == k0 + 2) {
              s += (rdv[0] + rdv[1]) + (rdv[2] + rdv[3]);
              const u32x2 z = z1o[(j >> 1) - 1];
              const int e = j == 4 ? 2 : 0;
              uo[e] = pack2(bflo(z[0]) * (s[0] + bs), bfhi(z[0]) * (s[1] + bs));
              uo[e + 1] = pack2(bflo(z[1]) * (s[2] + bs), bfhi(z[1]) * (s[3] + bs));
              if (j == 4) st16(rs, ub + (unsigned)cls * 1024u + l16, uo);
              if (j == 6) st16(rs, l < 32 ? ub + 2048u + (unsigned)cls * 512u + l16 : OOB, half_swap((u32x4){uo[0], uo[1], uo[0], uo[1]}));
            }
            if (j == 6 && ks == 4) BAR();
            if (j == 6 && ks >= 4 && ks < 7) {
#pragma unroll
              for (int k = 0; k < 5; ++k)
                if (k / 2 == ks - 4) *(u32x4*)(lds + (w < 6 ? L_BUF + 32768 + w * 5120 : L_Z1S + (w - 6) * 5120) + k * 1024 + l16) = cr[k];
            }
          }
        }
        SCHED();
      }
    }
  }
  STAMP(23);

  {
    u32x4 uc[4], uh[4];
    const unsigned uho = (unsigned)(l >> 5) * 3584u + (unsigned)(l & 31) * 16u;
#define LD_C(cc) uc[cc] = ld16(rs, l16, ur + (unsigned)(cc) * 7168u + 2048u)
#define LD_H(cc) uh[cc] = ld16(rs, uho, WS_UF + (unsigned)((R * 4 + rto) * 8) * 3584u + (unsigned)(cc) * 7168u + 3072u)
    auto mx = [](unsigned a, unsigned b) { return __builtin_elementwise_max(a, b); };
    auto ok4 = [&](const u32x4* v, int n) {
      unsigned m = 0;
#pragma unroll
      for (int i = 0; i < 8; ++i)
        if (i < n) m = mx(m, mx(mx(v[i][0], v[i][1]), mx(v[i][2], v[i][3])));
      return __all(m != ~0u);
    };
#pragma unroll
    for (int rt = 0; rt < 4; ++rt) BUFW(0, rt) = acc[0][rt];
    BAR();
    f32x4 a3[5];
    u32x4 af[2][5];
    const LAS char* wb[2] = {(const LAS char*)(lds + L_AB) + cls * (10240 + 3072) + l16, (const LAS char*)(lds + L_AB) + cls * (10240 - 2048) + l16};
    const LAS char* wb2[2] = {wb[0] + 40960, wb[1] + 40960};
    asm volatile("" : "+v"(wb[0]), "+v"(wb2[0]), "+v"(wb[1]), "+v"(wb2[1]));
    u32x4 ho = {0u, 0u, 0u, 0u};
    if (w < 4) {
      __builtin_amdgcn_s_setprio(2);
      const f32x4* const rb = (const f32x4*)(lds + L_BUF) + rto * 64 + l;
      f32x4 r8[8];
#pragma unroll
      for (int k = 0; k < 8; ++k) r8[k] = rb[k * 256];
      const f32x4 s = ((r8[0] + r8[1]) + (r8[2] + r8[3])) + ((r8[4] + r8[5]) + (r8[6] + r8[7]));
      const u32x2 z = z1o[3];
      const unsigned o0 = pack2(bflo(z[0]) * (s[0] + bs), bfhi(z[0]) * (s[1] + bs)), o1 = pack2(bflo(z[1]) * (s[2] + bs), bfhi(z[1]) * (s[3] + bs));
      ho = half_swap((u32x4){o0, o1, o0, o1});
    }
    st16(rs, tid < 256 && l < 32 ? ub + 3072u + l16 : OOB, ho);
    SCHED();
    __builtin_amdgcn_s_setprio(0);
#pragma unroll
    for (int dj = 0; dj < 5; ++dj) af[0][dj] = *(const LAS u32x4*)(wb[dj >= 2] + dj * 1024);
    const bool ab_ok = ok4(&uf[0][0], 8);
    STAMP(24);
    if (!ab_ok) {
      _Pragma("unroll 1") for (unsigned spins = 0; spins < SPIN_CAP; ++spins) {
#pragma unroll
        for (int i = 0; i < 8; ++i) uf[i >> 1][i & 1] = ld16(rs, l16, ur + (unsigned)(i >> 1) * 7168u + (unsigned)(i & 1) * 1024u);
        if (ok4(&uf[0][0], 8)) break;
        __builtin_amdgcn_s_sleep(1);
      }
    }
    STAMP(25);
    const LAS char* cb0[2] = {(const LAS char*)(lds + L_BUF + 32768) + cls * (5120 + 3072) + l16, (const LAS char*)(lds + L_BUF + 32768) + cls * (5120 - 2048) + l16};
    u32x4 ah[4][3];
    const LAS char* hb = (const LAS char*)(lds + L_BUF) + cls * 12288 + l * 8;
    f32x4 gb[3], xo3[3], xf[3];
    LAS char *otw, *xw3;
    const LAS char *ota, *otb, *xr3;
    unsigned oa_o, ob_o;
#pragma unroll
    for (int st = 0; st < 12; ++st) {
      if (st < 2) { LD_C(2 * st); LD_C(2 * st + 1); }
      else if (st < 7 && w < 4) dma1k(rs, lds + L_BUF + ((st - 2) * 4 + w) * 1024, l16, wob + 122880u + (unsigned)(w * 5 + st - 2) * 1024u);
      if (st == 2) {
        LAS char* ot = (LAS char*)(lds + L_AB + 32768) + w * 2048;
        otw = ot + n16 * 112 + h4 * 8;
        ota = ot + (l >> 2) * 112 + (l & 3) * 16;
        otb = ot + ((l >> 1) & 15) * 112 + 64 + (l & 1) * 16;
        asm volatile("" : "+v"(otw), "+v"(ota), "+v"(otb));
      }
      if (st == 3) {
        oa_o = (unsigned)(l >> 2) * 2560u + (unsigned)(l & 3) * 16u;
        ob_o = l < 32 ? (unsigned)(l >> 1) * 2560u + 64u + (unsigned)(l & 1) * 16u : OOB;
        xw3 = (LAS char*)(lds + L_AB) + w * 3072 + l16;
        xr3 = (const LAS char*)(lds + L_AB) + (w ^ 4) * 3072 + cls * 1024 + l16;
        asm volatile("" : "+v"(oa_o), "+v"(ob_o), "+v"(xw3), "+v"(xr3));
      }
      if (st >= 4 && st < 7) {
        const int j = st - 4;
        gb[j] = *(const f32x4*)(lds + L_GB + (48 * cls + 16 * (cls && j == 2 ? 1 : j) + 4 * h4) * 4);
        xf[j] = (f32x4){bflo(xp[j][0]), bfhi(xp[j][0]), bflo(xp[j][1]), bfhi(xp[j][1])};
        asm volatile("" : "+v"(xf[j]));
      }
      if (st == 8) {
        STAMP(26);
        if (!ok4(uc, 4)) {
          _Pragma("unroll 1") for (unsigned spins = 0; spins < SPIN_CAP; ++spins) {
            LD_C(0); LD_C(1); LD_C(2); LD_C(3);
            if (ok4(uc, 4)) break;
            __builtin_amdgcn_s_sleep(1);
          }
        }
        LD_H(0); LD_H(1); LD_H(2); LD_H(3);
#pragma unroll
        for (int cc = 0; cc < 4; ++cc) uc[cc] = half_swap(uc[cc]);
        VMW(4);
        __builtin_amdgcn_s_barrier();
        asm volatile("" ::: "memory");
        STAMP(27);
      }
      SCHED();
#pragma unroll
      for (int dj = 0; dj < 5; ++dj) {
        const int sn = st + 1;
        if (sn < 8) af[sn & 1][dj] = *(const LAS u32x4*)((sn < 4 ? wb : wb2)[dj >= 2] + ((sn >> 1) & 1) * 20480 + ((sn & 1) * 5 + dj) * 1024);
        else if (sn < 12) af[sn & 1][dj] = *(const LAS u32x4*)(cb0[dj >= 2] + (sn < 11 ? (sn - 8) * 10240 : L_Z1S - L_BUF - 32768) + dj * 1024);
        if (st >= 8) {
#pragma unroll
          for (int k = 0; k < 2; ++k) {
            const int h = st < 11 ? (k ? 99 : (st - 8) * 5 + dj) : 15 + 2 * dj + k;
            if (h < 24) {
              const u32x2 v = *(const volatile LAS u32x2*)(hb + (((h >> 1) % 3) * 4 + (h >> 1) / 3) * 1024 + (h & 1) * 512);
              ah[(h >> 1) / 3][(h >> 1) % 3][2 * (h & 1)] = v[0]; ah[(h >> 1) / 3][(h >> 1) % 3][2 * (h & 1) + 1] = v[1];
            }
          }
        }
        SCHED();
        a3[dj] = MFMA16(af[st & 1][dj], st < 8 ? uf[(st >> 1) & 3][st & 1] : uc[st & 3], st ? a3[dj] : ZERO4);
        SCHED();
      }
    }
#pragma unroll
    for (int j = 0; j < 3; ++j) *(volatile LAS f32x4*)(xw3 + j * 1024) = a3[2 + j];
    BAR();
#pragma unroll
    for (int j = 0; j < 3; ++j) xo3[j] = *(const volatile LAS f32x4*)(xr3 + j * 1024);
    SCHED();
    if (!ok4(uh, 4)) {
      _Pragma("unroll 1") for (unsigned spins = 0; spins < SPIN_CAP; ++spins) {
        LD_H(0); LD_H(1); LD_H(2); LD_H(3);
        if (ok4(uh, 4)) break;
        __builtin_amdgcn_s_sleep(1);
      }
    }
#pragma unroll
    for (int cc = 0; cc < 4; ++cc) {
      const u32x4 b = half_swap(uh[cc]);
#pragma unroll
      for (int j = 0; j < 3; ++j) a3[j] = MFMA16(ah[cc][j], b, a3[j]);
      if (cc >= 1) {
        const int j = cc - 1;
        xo3[j] = __builtin_elementwise_fma(gb[j], xo3[j], xf[j]);
        asm volatile("" : "+v"(xo3[j]));
      }
      SCHED();
    }
#pragma unroll
    for (int j = 0; j < 3; ++j) {
      const f32x4 r = __builtin_elementwise_fma(gb[j], a3[j], xo3[j]);
      *(volatile LAS u32x2*)(otw + j * 32) = (u32x2){pack2(r[0], r[1]), pack2(r[2], r[3])};
    }
    const u32x4 oa = *(const volatile LAS u32x4*)(ota);
    const u32x4 ob = *(const volatile LAS u32x4*)(otb);
    const unsigned obase = (unsigned)((R * 64 + 32 * ((w >> 1) & 1) + (w & 1)) * D_ + 80 * c + 48 * cls) * 2u;
    __builtin_amdgcn_raw_buffer_store_b128(oa, rout, oa_o, obase, 0);
    if (cls == 0) __builtin_amdgcn_raw_buffer_store_b128(ob, rout, ob_o, obase, 0);
    if (bid == 0 && tid == 0) st4(rs, WS_EPOCH, tag);
  }
  STAMP(30);
  asm volatile(".Lmega_end:");
}

extern "C" void kernel_launch(void* const* d_in, const int* in_sizes, int n_in, void* d_out, int out_size,
                              void* d_ws, size_t ws_size, hipStream_t stream) {
  Params p;
  p.q = (const u16*)d_in[0];
  p.k = (const u16*)d_in[1];
  p.v = (const u16*)d_in[2];
  p.shift_k = (const u16*)d_in[3];
  p.ssm_k = (const u16*)d_in[4];
  p.ssm_D = (const u16*)d_in[5];
  p.w_in = (const u16*)d_in[7];
  p.w_sp = (const u16*)d_in[8];
  p.b_sp = (const u16*)d_in[9];
  p.w_out = (const u16*)d_in[10];
  p.cvec = (const u16*)d_in[11];
  p.w_mod = (const u16*)d_in[12];
  p.out = (u16*)d_out;
  p.ws = (char*)d_ws;
  mega<<<256, 512, 0, stream>>>(p);
}
```

```cpp
#define XST 35
#define XCONV 1
#include <hip/hip_runtime.h>
#include <stdint.h>

typedef unsigned short u16;
typedef __attribute__((ext_vector_type(8))) short bf16x8;
typedef __attribute__((ext_vector_type(4))) float f32x4;
typedef __attribute__((ext_vector_type(2))) float f32x2;
typedef __attribute__((ext_vector_type(4))) unsigned int u32x4;
typedef __attribute__((ext_vector_type(2))) unsigned int u32x2;
#define LAS __attribute__((address_space(3)))
#define CAS __attribute__((address_space(4)))
#define GAS __attribute__((address_space(1)))
typedef __amdgpu_buffer_rsrc_t rsrc_t;
typedef __attribute__((ext_vector_type(4))) short s16x4;

constexpr int S_ = 2048, D_ = 640, F_ = 1728, FH = 864, CB = 108;

#define X1PAD 256
constexpr unsigned WS_EPOCH = 0;
constexpr unsigned WS_FX = 4096;
constexpr unsigned WS_FZ = WS_FX + 24576;
constexpr unsigned WS_FW2 = WS_FZ + 32768;
constexpr unsigned WS_FW1 = WS_FW2 + 8192;
constexpr unsigned WS_CS = WS_FW1 + 65536;
constexpr unsigned WS_PART = WS_CS + 131072;
constexpr unsigned WS_MOD = WS_PART + 262144;
constexpr unsigned WS_ST1 = WS_MOD + 32768;
constexpr unsigned WS_X1B = WS_ST1 + 16384;
constexpr unsigned WX1P = 40960 + X1PAD;
constexpr unsigned WBLK = 143360;
constexpr unsigned WS_QB = WS_X1B + 2686976;
constexpr unsigned WS_WINF = WS_QB + 2686976;
constexpr unsigned WS_WOUTF = WS_WINF + 16 * WBLK;
constexpr unsigned WS_Z2F = WS_WOUTF + 8 * WBLK;
constexpr unsigned WS_UF = WS_Z2F + 8 * 64 * 7168;
constexpr unsigned WS_KVT = WS_UF + 32 * 4 * 8 * 3584;
constexpr unsigned WS_FK = WS_KVT + 32 * 81920;
constexpr unsigned WS_STAMP = WS_FK + 20480;
constexpr unsigned WS_END = WS_STAMP + 256 * 40 * 8;

constexpr int U_BYTES = (256 + S_) * 2;
constexpr int G_COPY = 4160;
constexpr int CH_BYTES = U_BYTES + 4 * G_COPY;
constexpr int L_YP = 4 * CH_BYTES;
constexpr int L_A0 = 98304;
constexpr int L_RED = 0;
constexpr int L_TAB = 2048;
constexpr int L_J3 = 8192;
constexpr int L_QT = 49152;
constexpr int L_J5 = L_QT;
constexpr int L_XI = 0;
constexpr int L_BUF = 81920;
constexpr int L_AB = 0;
constexpr int L_MISC = 147456;
constexpr int L_Z1S = L_MISC;
constexpr int L_S1 = L_MISC + 14336;
constexpr int L_T2 = L_S1 + 512;
constexpr int L_ST2 = L_T2 + 512;
constexpr int L_GB = L_ST2 + 512;
constexpr int L_CS1 = L_Z1S + 4096;
constexpr int L_WS = 81920;
constexpr int LDS_TOTAL = 163840;

constexpr int POLL_SLEEP = 1;
constexpr int AUX_SLEEP = 36, LATE_SLEEP = 96;
constexpr int CDIST = 4;
constexpr int NRAW = 12;
constexpr int SV_AT = 108, Q_AT = 130;
constexpr int NB2A = 6;
constexpr int G2W = 7;
constexpr int Z1SPLIT = 12;
constexpr int SC1 = 16;
constexpr unsigned SPIN_CAP = 1u << 16;
constexpr unsigned OOB = 0x80000000u;

__device__ __forceinline__ float bflo(unsigned x) { return __uint_as_float(x << 16); }
__device__ __forceinline__ float bfhi(unsigned x) { return __uint_as_float(x & 0xffff0000u); }
__device__ __forceinline__ f32x2 bf2(unsigned x) { return (f32x2){bflo(x), bfhi(x)}; }
__device__ __forceinline__ unsigned f2bf(float x) {
  __bf16 b = (__bf16)x;
  return (unsigned)__builtin_bit_cast(unsigned short, b);
}
__device__ __forceinline__ float rbf(float x) { return bflo(f2bf(x)); }
typedef __attribute__((ext_vector_type(2))) __bf16 bf2_t;
__device__ __forceinline__ unsigned pack2(float lo, float hi) {
  f32x2 v = {lo, hi};
  return __builtin_bit_cast(unsigned, __builtin_convertvector(v, bf2_t));
}
template <int CTRL>
__device__ __forceinline__ float dpp_f(float v) {
  return __int_as_float(__builtin_amdgcn_update_dpp(0, __float_as_int(v), CTRL, 0xf, 0xf, true));
}
__device__ __forceinline__ float sum_rows(float v) {
  auto a = __builtin_amdgcn_permlane16_swap(__float_as_uint(v), __float_as_uint(v), false, false);
  v = __uint_as_float(a[0]) + __uint_as_float(a[1]);
  auto b = __builtin_amdgcn_permlane32_swap(__float_as_uint(v), __float_as_uint(v), false, false);
  return __uint_as_float(b[0]) + __uint_as_float(b[1]);
}
__device__ __forceinline__ f32x2 gelu2(f32x2 x) {
  const float c0 = -2.0f * 0.7978845608028654f * 1.4426950408889634f;
  const float c1 = c0 * 0.044715f;
  const f32x2 t = x * __builtin_elementwise_fma(x * x, (f32x2){c1, c1}, (f32x2){c0, c0});
  const f32x2 d = (f32x2){__builtin_amdgcn_exp2f(t[0]), __builtin_amdgcn_exp2f(t[1])} + (f32x2){1.0f, 1.0f};
  return x * (f32x2){__builtin_amdgcn_rcpf(d[0]), __builtin_amdgcn_rcpf(d[1])};
}
#define LO2(v) __builtin_shufflevector(v, v, 0, 1)
#define HI2(v) __builtin_shufflevector(v, v, 2, 3)
__device__ __forceinline__ float tr_sum16(const float* v, int l) {
  float x[8], y[4];
  asm volatile(
      "s_nop 1\n\t"
      "v_add_f32_dpp %0, %8, %8 row_ror:8 row_mask:0xf bank_mask:0x3\n\t"
      "v_add_f32_dpp %1, %9, %9 row_ror:8 row_mask:0xf bank_mask:0x3\n\t"
      "v_add_f32_dpp %2, %10, %10 row_ror:8 row_mask:0xf bank_mask:0x3\n\t"
      "v_add_f32_dpp %3, %11, %11 row_ror:8 row_mask:0xf bank_mask:0x3\n\t"
      "v_add_f32_dpp %4, %12, %12 row_ror:8 row_mask:0xf bank_mask:0x3\n\t"
      "v_add_f32_dpp %5, %13, %13 row_ror:8 row_mask:0xf bank_mask:0x3\n\t"
      "v_add_f32_dpp %6, %14, %14 row_ror:8 row_mask:0xf bank_mask:0x3\n\t"
      "v_add_f32_dpp %7, %15, %15 row_ror:8 row_mask:0xf bank_mask:0x3\n\t"
      "v_add_f32_dpp %0, %16, %16 row_ror:8 row_mask:0xf bank_mask:0xc\n\t"
      "v_add_f32_dpp %1, %17, %17 row_ror:8 row_mask:0xf bank_mask:0xc\n\t"
      "v_add_f32_dpp %2, %18, %18 row_ror:8 row_mask:0xf bank_mask:0xc\n\t"
      "v_add_f32_dpp %3, %19, %19 row_ror:8 row_mask:0xf bank_mask:0xc\n\t"
      "v_add_f32_dpp %4, %20, %20 row_ror:8 row_mask:0xf bank_mask:0xc\n\t"
      "v_add_f32_dpp %5, %21, %21 row_ror:8 row_mask:0xf bank_mask:0xc\n\t"
      "v_add_f32_dpp %6, %22, %22 row_ror:8 row_mask:0xf bank_mask:0xc\n\t"
      "v_add_f32_dpp %7, %23, %23 row_ror:8 row_mask:0xf bank_mask:0xc"
      : "=&v"(x[0]), "=&v"(x[1]), "=&v"(x[2]), "=&v"(x[3]), "=&v"(x[4]), "=&v"(x[5]), "=&v"(x[6]), "=&v"(x[7])
      : "v"(v[0]), "v"(v[1]), "v"(v[2]), "v"(v[3]), "v"(v[4]), "v"(v[5]), "v"(v[6]), "v"(v[7]), "v"(v[8]), "v"(v[9]), "v"(v[10]),
        "v"(v[11]), "v"(v[12]), "v"(v[13]), "v"(v[14]), "v"(v[15]));
  asm volatile(
      "s_nop 1\n\t"
      "v_add_f32_dpp %0, %4, %4 row_shl:4 row_mask:0xf bank_mask:0x5\n\t"
      "v_add_f32_dpp %1, %5, %5 row_shl:4 row_mask:0xf bank_mask:0x5\n\t"
      "v_add_f32_dpp %2, %6, %6 row_shl:4 row_mask:0xf bank_mask:0x5\n\t"
      "v_add_f32_dpp %3, %7, %7 row_shl:4 row_mask:0xf bank_mask:0x5\n\t"
      "v_add_f32_dpp %0, %8, %8 row_shr:4 row_mask:0xf bank_mask:0xa\n\t"
      "v_add_f32_dpp %1, %9, %9 row_shr:4 row_mask:0xf bank_mask:0xa\n\t"
      "v_add_f32_dpp %2, %10, %10 row_shr:4 row_mask:0xf bank_mask:0xa\n\t"
      "v_add_f32_dpp %3, %11, %11 row_shr:4 row_mask:0xf bank_mask:0xa\n\t"
      "s_nop 1"
      : "=&v"(y[0]), "=&v"(y[1]), "=&v"(y[2]), "=&v"(y[3])
      : "v"(x[0]), "v"(x[1]), "v"(x[2]), "v"(x[3]), "v"(x[4]), "v"(x[5]), "v"(x[6]), "v"(x[7]));
  const bool b1 = l & 2, b0 = l & 1;
  const float z0 = (b1 ? y[2] : y[0]) + dpp_f<0x4E>(b1 ? y[0] : y[2]);
  const float z1 = (b1 ? y[3] : y[1]) + dpp_f<0x4E>(b1 ? y[1] : y[3]);
  return (b0 ? z1 : z0) + dpp_f<0xB1>(b0 ? z0 : z1);
}
__device__ __forceinline__ u32x4 half_swap(u32x4 v) {
  const auto a = __builtin_amdgcn_permlane32_swap(v[0], v[2], false, false);
  const auto b = __builtin_amdgcn_permlane32_swap(v[1], v[3], false, false);
  return (u32x4){a[0], b[0], a[1], b[1]};
}
#define MFMA16(a, b, c) __builtin_amdgcn_mfma_f32_16x16x32_bf16(__builtin_bit_cast(bf16x8, a), __builtin_bit_cast(bf16x8, b), c, 0, 0, 0)
__device__ __forceinline__ u32x2 trd(const char* p) {
  return __builtin_bit_cast(u32x2, __builtin_amdgcn_ds_read_tr16_b64_v4i16((LAS s16x4*)p));
}
__device__ __forceinline__ u32x4 trfrag(const char* p, int second) {
  u32x2 a = trd(p), b = trd(p + second);
  return (u32x4){a[0], a[1], b[0], b[1]};
}
#define BAR() asm volatile("s_waitcnt lgkmcnt(0)\n\ts_barrier" ::: "memory")
#define DRAIN() do { __builtin_amdgcn_s_waitcnt(0x0F70); asm volatile("" ::: "memory"); } while (0)
#define VMW(n) do { __builtin_amdgcn_s_waitcnt(0x0F70 | ((n) & 15) | (((n) >> 4) << 14)); asm volatile("" ::: "memory"); } while (0)
#define SCHED() __builtin_amdgcn_sched_barrier(0)
#define ZERO4 ((f32x4){0.f, 0.f, 0.f, 0.f})

__device__ __forceinline__ u32x4 ld16(rsrc_t r, unsigned off, unsigned so = 0) { return __builtin_amdgcn_raw_buffer_load_b128(r, off, so, SC1); }
__device__ __forceinline__ u32x2 ld8(rsrc_t r, unsigned off, unsigned so = 0) { return __builtin_amdgcn_raw_buffer_load_b64(r, off, so, SC1); }
__device__ __forceinline__ unsigned ld4(rsrc_t r, unsigned off, unsigned so = 0) { return __builtin_amdgcn_raw_buffer_load_b32(r, off, so, SC1); }
__device__ __forceinline__ void st16(rsrc_t r, unsigned off, u32x4 v) { __builtin_amdgcn_raw_buffer_store_b128(v, r, off, 0, SC1); }
__device__ __forceinline__ void st8(rsrc_t r, unsigned off, u32x2 v) { __builtin_amdgcn_raw_buffer_store_b64(v, r, off, 0, SC1); }
__device__ __forceinline__ void st4(rsrc_t r, unsigned off, unsigned v) { __builtin_amdgcn_raw_buffer_store_b32(v, r, off, 0, SC1); }
__device__ __forceinline__ void dma1k(rsrc_t r, char* lds_dst, unsigned l16, unsigned soff) {
  __builtin_amdgcn_raw_ptr_buffer_load_lds(r, (LAS void*)lds_dst, 16, l16, soff, 0, SC1);
}

#define STAMP(i) do { if ((i) == XST) tx = (unsigned)__builtin_amdgcn_s_memrealtime(); } while (0)

template <int SLEEP>
__device__ __forceinline__ void wait_flag(rsrc_t r, unsigned off, unsigned tag) {
  _Pragma("unroll 1") for (unsigned spins = 0; spins < SPIN_CAP; ++spins) {
    if (__all(ld4(r, off) == tag)) break;
    __builtin_amdgcn_s_sleep(SLEEP);
  }
  asm volatile("" ::: "memory");
}

struct ConvSched {
  signed char pp[144], m[144], rd[144];
  int pre;
};
constexpr ConvSched conv_sched(int dist) {
  ConvSched s = {};
  int need[96] = {}, i = 0;
  for (int r = 0; r < 4; ++r)
    for (int pp = 0; pp < 8; ++pp) {
      const int j = 24 * r + (pp ? 8 + 2 * pp : 0);
      need[j] = need[j + 1] = i;
      for (int m = 7 - pp; m >= 0; --m) {
        if (pp == 0) need[24 * r + 9 - m] = i;
        s.pp[i] = pp; s.m[i] = m; s.rd[i] = -1;
        ++i;
      }
    }
  int next = 144;
  for (int j = 95; j >= 0; --j) {
    next = need[j] - dist < next - 1 ? need[j] - dist : next - 1;
    if (next >= 0) s.rd[next] = j;
    else if (s.pre == 0) s.pre = j + 1;
  }
  return s;
}

template <int I> struct IC { static constexpr int v = I; };
template <int I0, int I1, class F>
__device__ __forceinline__ void static_for(F&& f) {
  if constexpr (I0 < I1) { f(IC<I0>{}); static_for<I0 + 1, I1>(f); }
}

struct Params {
  const u16 *q, *k, *v, *shift_k, *ssm_k, *ssm_D, *w_in, *w_sp, *b_sp, *w_out, *cvec, *w_mod;
  u16* out;
  char* ws;
};

__global__ __launch_bounds__(512, 2) void mega(Params) {
  __shared__ __attribute__((aligned(16))) char lds[LDS_TOTAL];
  const CAS Params* kp = (const CAS Params*)__builtin_amdgcn_kernarg_segment_ptr();
  unsigned ka0, ka1;
  asm volatile("s_load_dword %0, %2, 0x0\n\ts_load_dword %1, %2, 0x40" : "=&s"(ka0), "=&s"(ka1) : "s"(kp));
  SCHED();
  const unsigned long long tstart = __builtin_amdgcn_s_memrealtime();
  unsigned tx = (unsigned)tstart;
  const int tid = threadIdx.x, l = tid & 63;
  const int w = __builtin_amdgcn_readfirstlane(tid >> 6);
  const int bid = blockIdx.x, xcd = bid & 7, idx = bid >> 3;
  const int dg = ((idx & 7) + 2 * (idx >> 3)) & 7;
  const bool is_conv = dg >= 3;
  const int slot = 3 * (idx >> 3) + dg;
  const int cidx = 5 * (idx >> 3) + dg - 3;
  const int quad = xcd * 20 + cidx;
  const unsigned l16 = (unsigned)l * 16u;
  const int blk = (quad / 5) * 8 + w, sl = quad % 5;
  unsigned a0g = (unsigned)(l >> 4) * 1280u + (unsigned)(((l & 15) ^ (4 * (l >> 5))) * 16), a0x = a0g ^ 128u, a0h = a0g + (l < 16 ? 1280u : 0u);
  unsigned a0s = (unsigned)blk * 10240u + (unsigned)sl * 256u, a0p = blk ? a0s - 5120u : a0s, a0l = L_A0 + w * 6144;
  asm volatile("" : "+v"(a0g), "+v"(a0x), "+v"(a0h));
  asm volatile("" : "+s"(a0s), "+s"(a0p), "+s"(a0l));
  SCHED();
  asm volatile("" : "+s"(kp));
  Params p = {kp->q, kp->k, kp->v, kp->shift_k, kp->ssm_k, kp->ssm_D, kp->w_in, kp->w_sp, kp->b_sp, kp->w_out, kp->cvec, kp->w_mod, kp->out, kp->ws};
  asm volatile("s_waitcnt lgkmcnt(0)" : "+s"(p.q), "+s"(p.k), "+s"(p.v), "+s"(p.shift_k), "+s"(p.ssm_k), "+s"(p.ssm_D), "+s"(p.w_in), "+s"(p.w_sp),
               "+s"(p.b_sp), "+s"(p.w_out), "+s"(p.cvec), "+s"(p.w_mod), "+s"(p.out), "+s"(p.ws) : "s"(ka0), "s"(ka1));
  char* const ws = p.ws;
  STAMP(0);
  unsigned cpf = 0;
  if (!is_conv) {
    unsigned long long pc;
    unsigned len, len2;
    asm volatile("s_getpc_b64 %0\n.Lmega_here:\n\ts_mov_b32 %1, .Lmega_end-.Lmega_here\n\ts_mov_b32 %2, .Lmega_aux_end-.Lmega_here" : "=s"(pc), "=s"(len), "=s"(len2));
    len = len > len2 ? len : len2;
    unsigned o = (unsigned)((slot * 8 + w) * 1024 + (l & 7) * 128);
    o = o + 16u <= len ? o : 0u;
    cpf = *(const GAS unsigned*)((pc & ~15ull) + o);
  }
  const rsrc_t rs = __builtin_amdgcn_make_buffer_rsrc(ws, 0, WS_END, 0x00020000);
  const unsigned epv = ld4(rs, WS_EPOCH);
  SCHED();
  u32x2 dd2;
  {
    int cq = dg;
    asm volatile("" : "+s"(cq));
    if (cq >= 3) {
      const rsrc_t rk = __builtin_amdgcn_make_buffer_rsrc((void*)p.k, 0, S_ * D_ * 2, 0x00020000);
      const rsrc_t rv = __builtin_amdgcn_make_buffer_rsrc((void*)p.v, 0, S_ * D_ * 2, 0x00020000);
      const rsrc_t rh = __builtin_amdgcn_make_buffer_rsrc((void*)p.shift_k, 0, D_ * 8, 0x00020000);
      char* const la = lds + a0l;
      __builtin_amdgcn_raw_ptr_buffer_load_lds(rh, (LAS void*)(la + 5120), 16, l16, (unsigned)sl * 1024u, 0, 0);
      __builtin_amdgcn_raw_ptr_buffer_load_lds(rk, (LAS void*)(la + 1024), 16, a0x, a0s, 0, 0);
      __builtin_amdgcn_raw_ptr_buffer_load_lds(rk, (LAS void*)(la + 2048), 16, a0g, a0s + 5120u, 0, 0);
      __builtin_amdgcn_raw_ptr_buffer_load_lds(rv, (LAS void*)(la + 3072), 16, a0g, a0s, 0, 0);
      __builtin_amdgcn_raw_ptr_buffer_load_lds(rv, (LAS void*)(la + 4096), 16, a0x, a0s + 5120u, 0, 0);
      __builtin_amdgcn_raw_ptr_buffer_load_lds(rk, (LAS void*)(la), 16, a0h, a0p, 0, 0);
      SCHED();
      const rsrc_t rf = __builtin_amdgcn_make_buffer_rsrc((void*)p.ssm_k, 0, S_ * D_ * 2, 0x00020000);
      dd2 = *(const CAS u32x2*)((const CAS char*)p.ssm_D + quad * 8);
      STAMP(32);
      {
        const u32x4 z = {0u, 0u, 0u, 0u};
        if (tid < 128) *(u32x4*)(lds + (tid >> 5) * CH_BYTES + (tid & 31) * 16) = z;
        else if (tid < 152) {
          const int i = (tid - 128) >> 2;
          *(u32x4*)(lds + (tid & 3) * CH_BYTES + U_BYTES + (i < 4 ? i * G_COPY : (i & 1 ? 4128 : 16))) = z;
        }
        const LAS char* a0r = (const LAS char*)(lds + L_A0) + w * 6144 + (l & 3) * 512;
#define A0R(k) (a0r + ((((l >> 2) ^ (4 * ((l + (k)) & 3))) * 16)))
        unsigned go = WS_KVT + (unsigned)(quad / 5) * 81920u + (unsigned)sl * 16384u + (unsigned)(l >> 2) * 1024u + (unsigned)w * 64u + (unsigned)(l & 3) * 16u;
        const LAS char* r0 = A0R(0), * r1 = A0R(1), * r2 = A0R(2);
        const LAS char* rsh = (const LAS char*)(lds + L_A0) + w * 6144 + 5120 + (l >> 2) * 64;
        asm volatile("" : "+v"(go), "+v"(r0), "+v"(r1), "+v"(r2), "+v"(rsh));
        SCHED();
#pragma unroll
        for (int j = 0; j < 2; ++j)
          __builtin_amdgcn_raw_ptr_buffer_load_lds(rf, (LAS void*)(lds + (w >> 1) * CH_BYTES + U_BYTES + 32 + (2 * (w & 1) + j) * 1024), 16, l16,
                                                   (unsigned)quad * 16384u + (unsigned)(2 * w + j) * 1024u, 0, 0);
        STAMP(34);
        SCHED();
        VMW(7);
        f32x2 wt[4][4];
#pragma unroll
        for (int e = 0; e < 4; ++e) {
          const u32x4 sh = *(const volatile LAS u32x4*)(rsh + 16 * e);
          wt[e][0] = (f32x2){bflo(sh[0]), bflo(sh[2])}; wt[e][1] = (f32x2){bfhi(sh[0]), bfhi(sh[2])};
          wt[e][2] = (f32x2){bflo(sh[1]), bflo(sh[3])}; wt[e][3] = (f32x2){bfhi(sh[1]), bfhi(sh[3])};
        }
#pragma unroll
        for (int e = 0; e < 4; ++e) asm volatile("" : "+v"(wt[e][0]), "+v"(wt[e][1]), "+v"(wt[e][2]), "+v"(wt[e][3]));
        asm volatile("" : "+s"(dd2));
        SCHED();
        VMW(2);
        STAMP(35);
        u32x4 ka[5], va[2];
        ka[0] = *(const volatile LAS u32x4*)(r0 + 256);
        ka[1] = *(const volatile LAS u32x4*)(r1 + 512);
        ka[2] = *(const volatile LAS u32x4*)(r1 + 768);
        ka[3] = *(const volatile LAS u32x4*)(r2 + 1024);
        ka[4] = *(const volatile LAS u32x4*)(r2 + 1280);
        va[0] = *(const volatile LAS u32x4*)(r0 + 3072);
        va[1] = *(const volatile LAS u32x4*)(r0 + 3328);
        SCHED();
        if (blk == 0) {
          if ((l & 3) < 2) ka[0] = z;
          if ((l & 3) == 0) { ka[1] = z; ka[2] = z; }
        }
        u32x4 ko;
#pragma unroll
        for (int e = 0; e < 4; ++e) {
          const f32x2 w0 = wt[e][0], w1 = wt[e][1], w2 = wt[e][2], w3 = wt[e][3];
          const f32x2 km3 = bf2(ka[0][e]), km2 = bf2(ka[1][e]), km1 = bf2(ka[2][e]), k0 = bf2(ka[3][e]), k1 = bf2(ka[4][e]);
          const f32x2 s0 = __builtin_elementwise_fma(k0, w0, __builtin_elementwise_fma(km1, w1, __builtin_elementwise_fma(km2, w2, km3 * w3)));
          const f32x2 s1 = __builtin_elementwise_fma(k1, w0, __builtin_elementwise_fma(k0, w1, __builtin_elementwise_fma(km1, w2, km2 * w3)));
          const f32x2 o0 = s0 * bf2(va[0][e]), o1 = s1 * bf2(va[1][e]);
          ko[2 * (e & 1)] = pack2(o1[0], o0[0]); ko[2 * (e & 1) + 1] = pack2(o1[1], o0[1]);
          if (e & 1) st16(rs, go + (unsigned)(e >> 1) * 512u, ko);
          SCHED();
        }
      }
    }
  }
  const int R = (xcd & 3) * 8 + (idx & 7);
  const int c = (xcd >> 2) * 4 + (idx >> 3);
  const int n16 = l & 15, h4 = l >> 4;
  const rsrc_t rq = __builtin_amdgcn_make_buffer_rsrc((void*)p.q, 0, S_ * D_ * 2, 0x00020000);
  const rsrc_t rsp = __builtin_amdgcn_make_buffer_rsrc((void*)p.w_sp, 0, S_ * S_ * 2, 0x00020000);
  const int wj = w < 7 ? w : 0;
  u32x4 wsp[4][8];
  const int wk = w ^ (dg == 5 || dg == 6 ? 4 : 0);
  const unsigned wso = (unsigned)(R * 64) * 4096u + (unsigned)wk * 512u;
  const unsigned wdo = (unsigned)(h4 * 8192 + ((n16 ^ h4) * 16));
#define WVO(q) (wdo ^ (unsigned)(((4 - (q)) & 3) * 64))
#define WSO(g, q) (wso + (unsigned)((g) >> 2) * 256u + (unsigned)(32 * (((g) >> 1) & 1) + ((g) & 1) + 8 * (q)) * 4096u)
  u32x4 raw[NRAW];
#define LDW(i) raw[i] = __builtin_amdgcn_raw_buffer_load_b128(rsp, WVO((i) & 3), WSO((i) >> 2, (i) & 3), 0)
  const int trow = R * 64 + 32 * ((w >> 1) & 1) + 2 * n16 + (w & 1);
  unsigned bs_raw;
  unsigned tag;
  unsigned fw2 = OOB;
  unsigned e_fw1;
  const unsigned fw1o = WS_FW1 + (unsigned)(((8 + c) * 8 + wj) * 4 + (l & 3)) * 128u;

  if (__builtin_expect(is_conv, 1)) {
    STAMP(1);
    DRAIN();
    tag = __builtin_amdgcn_readfirstlane(epv) * 2654435761u + 0x9E3779B9u;
    BAR();
    st4(rs, tid == 0 ? WS_FK + (unsigned)quad * 128u : OOB, tag);
    unsigned atv, ato;
    const unsigned long long atb = (unsigned long long)(w < 5 ? p.w_in : p.w_out);
    {
      const int i = w < 5 ? (cidx * 5 + w) * 20 + (l < 19 ? l : 19) : (cidx * 3 + w - 5) * 19 + (l < 18 ? l : 18);
      const int r = i / 3;
      ato = w < 5 ? (unsigned)((r < 639 ? r : 639) * (F_ * 2) + xcd * CB * 2 + (i - 3 * r) * 108) : (unsigned)(xcd * CB * D_ * 2 + (i < 1079 ? i : 1079) * 128);
      asm volatile("" : "+v"(ato));
    }
    {
      u32x4 d[3];
      u32x2 n[3];
#pragma unroll
      for (int j = 0; j < 3; ++j) {
        char* G = j < 2 ? lds + ((tid >> 8) + 2 * j) * CH_BYTES + U_BYTES + ((tid & 255) + 2) * 16 : lds + (tid & 3) * CH_BYTES + U_BYTES + 16;
        d[j] = *(const u32x4*)G;
        n[j] = *(const u32x2*)(G + 16);
      }
#pragma unroll
      for (int j = 0; j < 3; ++j) {
        char* G = j < 2 ? lds + ((tid >> 8) + 2 * j) * CH_BYTES + U_BYTES + ((tid & 255) + 2) * 16 : lds + (tid & 3) * CH_BYTES + U_BYTES + 16;
        const unsigned a0 = __builtin_amdgcn_alignbit(d[j][1], d[j][0], 16), a1 = __builtin_amdgcn_alignbit(d[j][2], d[j][1], 16),
                       a2 = __builtin_amdgcn_alignbit(d[j][3], d[j][2], 16), a3 = __builtin_amdgcn_alignbit(n[j][0], d[j][3], 16),
                       a4 = __builtin_amdgcn_alignbit(n[j][1], n[j][0], 16);
        if (j < 2 || tid < 4) {
          *(u32x4*)(G + G_COPY) = (u32x4){a0, a1, a2, a3};
          *(u32x4*)(G + 2 * G_COPY) = (u32x4){d[j][1], d[j][2], d[j][3], n[j][0]};
          *(u32x4*)(G + 3 * G_COPY) = (u32x4){a1, a2, a3, a4};
        }
      }
    }
    const int ch = w & 3, hf = w >> 2;
    const LAS char *ap, *bp;
    {
      const char* U = lds + ch * CH_BYTES;
      const char* G = U + U_BYTES;
      const int a = n16, jj = h4 >> 1, e2 = h4 & 1;
      const int sg = (a + 1) & 3;
      bp = (const LAS char*)U + 512 + 32 * (a - jj) + 16 * e2 - 256 * hf - 192;
      ap = (const LAS char*)G + sg * G_COPY + 2 * (16 * jj + a - 8 * e2 + 9 - sg) + 256 * hf;
      asm volatile("" : "+v"(ap), "+v"(bp));
    }
    STAMP(36);
    {
      wait_flag<1>(rs, WS_FK + (unsigned)(5 * (4 * w + (l & 3)) + (quad >> 5)) * 128u, tag);
      STAMP(29);
      const unsigned gp = WS_KVT + (unsigned)(4 * w + (l >> 5)) * 81920u + (unsigned)quad * 512u + (unsigned)(l & 31) * 16u;
      const u32x4 g0 = ld16(rs, gp), g1 = ld16(rs, gp + 163840u);
      SCHED();
      char* Uw = lds + 512 + (128 * w + (l & ~3) + 3 - (l & 3)) * 4;
#pragma unroll
      for (int i = 0; i < 4; ++i) *(unsigned*)(Uw + i * CH_BYTES) = g0[i];
#pragma unroll
      for (int i = 0; i < 4; ++i) *(unsigned*)(Uw + 256 + i * CH_BYTES) = g1[i];
    }
    STAMP(28);
    BAR();
    STAMP(2);
    const int te = 2 * tid;
    u32x4 qa[2];
    u32x4 sv[2];
    u32x2 mg;
    const unsigned xo = (unsigned)(tid >> 4) * WX1P + (unsigned)quad * 256u + (unsigned)(tid & 15) * 16u;
    const unsigned mgo = WS_MOD + (unsigned)((l < 12 ? (l >> 2) * D_ + (l & 3) : 0) + quad * 4) * 8u;
    auto ld_sv = [&]() {
      sv[0] = ld16(rs, WS_ST1 + (unsigned)tid * 16u);
      sv[1] = ld16(rs, WS_ST1 + (unsigned)tid * 16u + 8192u);
    };
    auto ld_q = [&]() {
      qa[0] = ld16(rs, WS_QB + xo);
      qa[1] = ld16(rs, WS_QB + xo + 32u * WX1P);
    };
    f32x2 hh[2][2][2], base[2][2][2];
    auto prep = [&]() {
      const LAS char* ur = (const LAS char*)(lds + 512) + ((te & ~7) + 6 - (te & 7)) * 2;
      unsigned uu[2][4];
#pragma unroll
      for (int i = 0; i < 8; ++i) uu[i >> 2][i & 3] = *(const volatile LAS unsigned*)(ur + (i >> 2) * 2048 + (i & 3) * CH_BYTES);
      if (!__all(((sv[0][0] ^ tag) | (sv[0][2] ^ tag) | (sv[1][0] ^ tag) | (sv[1][2] ^ tag)) == 0u)) {
        _Pragma("unroll 1") for (unsigned spins = 0; spins < SPIN_CAP; ++spins) {
          ld_sv();
          if (__all(sv[0][0] == tag && sv[0][2] == tag && sv[1][0] == tag && sv[1][2] == tag)) break;
          __builtin_amdgcn_s_sleep(4);
        }
        asm volatile("" ::: "memory");
        ld_q();
        mg = ld8(rs, mgo);
        DRAIN();
      }
      if (!__all(mg[0] == tag)) {
        _Pragma("unroll 1") for (unsigned spins = 0; spins < SPIN_CAP; ++spins) {
          mg = ld8(rs, mgo);
          if (__all(mg[0] == tag)) break;
          __builtin_amdgcn_s_sleep(2);
        }
      }
      f32x2 cc2[2], ss2[2], dv2[2];
      float cch[4], sch[4];
#pragma unroll
      for (int i = 0; i < 4; ++i) {
        const float sa = __uint_as_float(__builtin_amdgcn_readlane(mg[1], i)), ca = __uint_as_float(__builtin_amdgcn_readlane(mg[1], 4 + i)),
                    ga = __uint_as_float(__builtin_amdgcn_readlane(mg[1], 8 + i));
        cch[i] = ga * rbf(1.0f + ca);
        sch[i] = ga * sa;
      }
      cc2[0] = (f32x2){cch[0], cch[1]}; cc2[1] = (f32x2){cch[2], cch[3]};
      ss2[0] = (f32x2){sch[0], sch[1]}; ss2[1] = (f32x2){sch[2], sch[3]};
      dv2[0] = bf2(dd2[0]); dv2[1] = bf2(dd2[1]);
#pragma unroll
      for (int H = 0; H < 2; ++H)
#pragma unroll
        for (int e = 0; e < 2; ++e) {
          const unsigned st = sv[H][1 + 2 * e];
          const float a = __builtin_amdgcn_rcpf(bfhi(st)), b = -bflo(st) * a;
#pragma unroll
          for (int i = 0; i < 2; ++i) {
            const f32x2 q2 = bf2(qa[H][2 * e + i]);
            const f32x2 u2 = e ? (f32x2){bflo(uu[H][2 * i]), bflo(uu[H][2 * i + 1])} : (f32x2){bfhi(uu[H][2 * i]), bfhi(uu[H][2 * i + 1])};
            const f32x2 n1 = __builtin_elementwise_fma(q2, (f32x2){a, a}, (f32x2){b, b});
            hh[H][e][i] = __builtin_elementwise_fma(n1, cc2[i], ss2[i]);
            base[H][e][i] = __builtin_elementwise_fma(hh[H][e][i], u2 * dv2[i], q2);
            asm volatile("" : "+v"(hh[H][e][i]), "+v"(base[H][e][i]));
          }
        }
    };
    auto HF = [&]() { int x = hf; asm volatile("" : "+s"(x)); return x; };
    {
      const int a = n16;
      char* yp = lds + L_YP + (hf * 4 + ch) * 8192 + (h4 * 16 + ((a + 4 * h4) & 15)) * 16;
      f32x4 acc[8];
      u32x4 A[2][8], B[2][8];
      auto rd = [&](int k) {
        const int r = k / 24, j = k % 24;
        if (j >= 2 && j < 10) B[r & 1][9 - j] = *(const volatile LAS u32x4*)(bp + 512 * (9 - j) + 64 * (3 - r));
        else {
          const int pp = j < 2 ? 0 : (j - 8) >> 1;
          const u32x2 v = *(const volatile LAS u32x2*)(ap + 512 * pp + 64 * r + 8 * (j & 1));
          A[r & 1][pp][2 * (j & 1)] = v[0]; A[r & 1][pp][2 * (j & 1) + 1] = v[1];
        }
      };
#pragma unroll
      for (int k = 0; k < IC<conv_sched(CDIST).pre>::v; ++k) rd(k);
      SCHED();
      auto step = [&](auto ic) {
        constexpr int i = decltype(ic)::v, r = i / 36, pp = conv_sched(CDIST).pp[i], m = conv_sched(CDIST).m[i], k = conv_sched(CDIST).rd[i];
        if (i == 0) __builtin_amdgcn_s_setprio(3);
        if (i == 60) __builtin_amdgcn_s_setprio(2);
        if (i == 100) __builtin_amdgcn_s_setprio(1);
        if (i == 126) __builtin_amdgcn_s_setprio(0);
        acc[m + pp] = MFMA16(A[r & 1][pp], B[r & 1][m], i < 8 ? ZERO4 : acc[m + pp]);
        SCHED();
        if (k >= 0) rd(k);
        if (i == 0) atv = *(const GAS unsigned*)(atb + ato);
        if (i % 6 == 2 && i / 3 < NRAW) {
          if (!HF()) { LDW(i / 3); LDW(i / 3 + 1); }
        }
        if (i > Q_AT && (i - Q_AT) % 2 == 1 && i - Q_AT < NRAW) {
          if (HF()) { LDW(i - Q_AT - 1); LDW(i - Q_AT); }
        }
        if (i == SV_AT) { ld_sv(); bs_raw = ((const GAS u16*)p.b_sp)[trow]; }
        if (r == 3 && pp >= 1 && m == 7 - pp) *(f32x4*)(yp + (pp - 1) * 1024) = acc[pp - 1];
        SCHED();
      };
      static_for<0, Q_AT>(step);
      STAMP(3);
      DRAIN();
      asm volatile("" ::"v"(atv));
      ld_q();
      mg = ld8(rs, mgo);
      SCHED();
      STAMP(4);
      static_for<Q_AT, 144>(step);
      *(f32x4*)(yp + 7 * 1024) = acc[7];
      STAMP(5);
    }
    prep();
    e_fw1 = ld4(rs, fw1o);
    STAMP(6);
    const LAS char* yr = (const LAS char*)(lds + L_YP) + (te >> 8) * 1024 + (((te >> 2) & 3) * 16 + ((((te >> 4) & 15) + 4 * ((te >> 2) & 3)) & 15)) * 16 + (te & 3) * 4;
    asm volatile("" : "+v"(yr));
    BAR();
    STAMP(7);
    f32x2 y[2][8];
#pragma unroll
    for (int i = 0; i < 16; ++i) y[i >> 3][i & 7] = *(const volatile LAS f32x2*)(yr + (i >> 3) * 4096 + (i & 7) * 8192);
#pragma unroll
    for (int H = 0; H < 2; ++H) {
      u32x4 o;
#pragma unroll
      for (int e = 0; e < 2; ++e)
#pragma unroll
        for (int i = 0; i < 2; ++i) {
          const float x0 = __builtin_fmaf(hh[H][e][i][0], y[H][2 * i][e] + y[H][4 + 2 * i][e], base[H][e][i][0]);
          const float x1 = __builtin_fmaf(hh[H][e][i][1], y[H][2 * i + 1][e] + y[H][5 + 2 * i][e], base[H][e][i][1]);
          o[2 * e + i] = pack2(x0, x1);
        }
      st16(rs, WS_X1B + (unsigned)(32 * H) * WX1P + xo, o);
    }
    STAMP(8);
  } else {
    const rsrc_t rwi = __builtin_amdgcn_make_buffer_rsrc((void*)p.w_in, 0, D_ * F_ * 2, 0x00020000);
    const rsrc_t rwo = __builtin_amdgcn_make_buffer_rsrc((void*)p.w_out, 0, FH * D_ * 2, 0x00020000);
    STAMP(32);
    const int job = xcd + 8 * slot;
    const int T = job >= 50 && w < 3 && job - 50 + 46 * w < 128 ? job - 50 + 46 * w : -1;
    const bool unit = slot >= 8 || (slot >= 2 && slot < 6);
    const int ucb = slot >= 8 ? 8 + xcd : xcd, kg = (slot >= 8 ? slot : slot - 2) & 3;
    float* tab = (float*)(lds + L_TAB);
    float* red = (float*)(lds + L_RED);
    auto issue_unit = [&](int i) {
      const int g = i * 64 + l;
      __builtin_amdgcn_raw_ptr_buffer_load_lds(rwi, (LAS void*)(lds + L_J3 + i * 1024), 16, (unsigned)((g / 14) * (F_ * 2) + (g % 14) * 16),
                                               (unsigned)(kg * 160 * F_ * 2 + ucb * CB * 2), 0, 0);
    };
    if (T >= 0) {
      char* qt = lds + L_QT + w * 20480;
      const unsigned so = (unsigned)(16 * T) * 1280u;
#pragma unroll
      for (int n = 0; n < 16; ++n)
        __builtin_amdgcn_raw_ptr_buffer_load_lds(rq, (LAS void*)(qt + n * 1024), 16, l16 ^ (unsigned)(n * 16), so + (unsigned)(n * 1280), 0, 0);
#pragma unroll
      for (int j = 0; j < 4; ++j)
        __builtin_amdgcn_raw_ptr_buffer_load_lds(rq, (LAS void*)(qt + 16384 + j * 1024), 16, (unsigned)(h4 * 1280 + ((n16 ^ (4 * j + h4)) * 16)), so + (unsigned)(4 * j * 1280 + 1024), 0, 0);
      tag = __builtin_amdgcn_readfirstlane(epv) * 2654435761u + 0x9E3779B9u;
      STAMP(34);
      DRAIN();
      STAMP(35);
      const bf2_t one = {(__bf16)1.0f, (__bf16)1.0f};
      float s1[2] = {0.f, 0.f}, s2[2] = {0.f, 0.f};
      const int n0 = 2 * (l & 7);
      u32x4 qv[20];
#pragma unroll
      for (int i = 0; i < 20; ++i)
#pragma unroll
        for (int e = 0; e < 2; ++e) {
          const int n = n0 + e, ck = 4 * (i & 15) + h4;
          const u32x2 v = *(const volatile LAS u32x2*)(qt + (i < 16 ? n * 1024 : 16384 + n * 256) + ((ck ^ n) * 16) + (l & 8));
          qv[i][2 * e] = v[0]; qv[i][2 * e + 1] = v[1];
        }
#pragma unroll
      for (int i = 0; i < 20; ++i) {
        st16(rs, WS_QB + (unsigned)(T >> 1) * WX1P + (unsigned)i * 2048u + (unsigned)((l >> 3) * 256 + (T & 1) * 128 + (l & 7) * 16), qv[i]);
#pragma unroll
        for (int e = 0; e < 4; ++e) {
          const unsigned x = qv[i][e];
          const bf2_t v = __builtin_bit_cast(bf2_t, x);
          s1[e >> 1] = __builtin_amdgcn_fdot2_f32_bf16(v, one, s1[e >> 1], false);
          s2[e >> 1] = __builtin_amdgcn_fdot2_f32_bf16(v, v, s2[e >> 1], false);
        }
        SCHED();
      }
      u32x4 o = {tag, 0u, tag, 0u};
#pragma unroll
      for (int e = 0; e < 2; ++e) {
        const float t1 = sum_rows(s1[e] + dpp_f<0x128>(s1[e])), t2 = sum_rows(s2[e] + dpp_f<0x128>(s2[e]));
        const float mean = t1 * (1.0f / D_);
        const float var = __builtin_fmaxf(t2 * (1.0f / D_) - mean * mean, 0.f);
        o[2 * e + 1] = pack2(mean, __builtin_amdgcn_sqrtf(rbf(rbf(var) + 1e-5f)));
      }
      STAMP(1);
      DRAIN();
      if (l < 8) st16(rs, WS_ST1 + (unsigned)T * 128u + l16, o);
      STAMP(3);
    }
    if (job < 60) {
      const int grp = job < 10 ? 40 + job : (job < 50 ? job - 10 : job);
      if (job >= 50 && T < 0) __builtin_amdgcn_s_sleep(LATE_SLEEP);
      if (job >= 10 && T < 0) __builtin_amdgcn_s_sleep(AUX_SLEEP);
      const unsigned cv = *(const GAS unsigned*)((const GAS u16*)p.cvec + 80 * w + 2 * (l < 40 ? l : 0));
      u32x4 pre[10];
#pragma unroll
      for (int pp = 0; pp < 10; ++pp) pre[pp] = *(const GAS u32x4*)((const GAS u16*)p.w_mod + (size_t)(80 * w + pp * 8 + (l >> 3)) * 3840 + grp * 64 + (l & 7) * 8);
      SCHED();
      STAMP(34);
      tag = __builtin_amdgcn_readfirstlane(epv) * 2654435761u + 0x9E3779B9u;
      if (l < 40) {
        const float c0 = bflo(cv), c1 = bfhi(cv);
        *(f32x2*)(tab + w * 80 + 2 * l) = (f32x2){rbf(c0 * __builtin_amdgcn_rcpf(1.0f + __expf(-c0))), rbf(c1 * __builtin_amdgcn_rcpf(1.0f + __expf(-c1)))};
      }
      asm volatile("s_waitcnt lgkmcnt(0)" ::: "memory");
      f32x2 acc[4];
#pragma unroll
      for (int e = 0; e < 4; ++e) acc[e] = (f32x2){0.f, 0.f};
#pragma unroll
      for (int pp = 0; pp < 10; ++pp) {
        const float sv = tab[w * 80 + pp * 8 + (l >> 3)];
#pragma unroll
        for (int e = 0; e < 4; ++e) acc[e] = __builtin_elementwise_fma((f32x2){sv, sv}, bf2(pre[pp][e]), acc[e]);
      }
      STAMP(35);
      asm volatile("" : "+v"(acc[0]), "+v"(acc[1]), "+v"(acc[2]), "+v"(acc[3]));
#pragma unroll
      for (int e = 0; e < 8; ++e) { float a = acc[e >> 1][e & 1]; a += dpp_f<0x128>(a); acc[e >> 1][e & 1] = sum_rows(a); }
      if (l < 8) {
        *(f32x4*)(red + w * 64 + l * 8) = (f32x4){acc[0][0], acc[0][1], acc[1][0], acc[1][1]};
        *(f32x4*)(red + w * 64 + l * 8 + 4) = (f32x4){acc[2][0], acc[2][1], acc[3][0], acc[3][1]};
      }
      if (w == 7) __builtin_amdgcn_s_setprio(2);
      BAR();
      if (w == 7) {
        float t = 0.f;
#pragma unroll
        for (int r = 0; r < 8; ++r) t += red[r * 64 + l];
        st8(rs, WS_MOD + (unsigned)(grp * 64 + l) * 8u, (u32x2){tag, __float_as_uint(rbf(t))});
        __builtin_amdgcn_s_setprio(0);
      } else if (unit) {
#pragma unroll
        for (int k = 0; k < 5; ++k) issue_unit(w * 5 + k);
      }
    } else if (unit && w >= 3 && w < 7) {
      __builtin_amdgcn_s_sleep(AUX_SLEEP);
#pragma unroll
      for (int k = 0; k < 9; ++k) issue_unit((w - 3) * 9 + k < 35 ? (w - 3) * 9 + k : 34);
    }
    tag = __builtin_amdgcn_readfirstlane(epv) * 2654435761u + 0x9E3779B9u;
    if (unit) {
      auto mod_tab = [&](int which, float add, float* dst) {
        const unsigned o = WS_MOD + (unsigned)(which * D_ + kg * 160) * 8u;
        u32x4 g0, g1;
        _Pragma("unroll 1") for (unsigned spins = 0; spins < SPIN_CAP; ++spins) {
          g0 = ld16(rs, o + l16);
          g1 = ld16(rs, o + 1024u + (l16 & 255u));
          if (__all(g0[0] == tag && g0[2] == tag && g1[0] == tag && g1[2] == tag)) break;
          __builtin_amdgcn_s_sleep(2);
        }
        *(f32x2*)(dst + 2 * l) = (f32x2){rbf(add + __uint_as_float(g0[1])), rbf(add + __uint_as_float(g0[3]))};
        if (l < 16) *(f32x2*)(dst + 128 + 2 * l) = (f32x2){rbf(add + __uint_as_float(g1[1])), rbf(add + __uint_as_float(g1[3]))};
      };
      if (w == 7) {
        STAMP(1);
        if (slot >= 8) __builtin_amdgcn_s_sleep(64);
        mod_tab(4, 1.0f, tab);
        BAR();
        STAMP(2);
        mod_tab(3, 0.0f, tab + 160);
        BAR();
      } else {
        DRAIN();
        STAMP(36);
        BAR();
        STAMP(2);
        const char* bp = lds + L_J3 + (4 * h4 + (n16 >> 2)) * 224 + (n16 & 3) * 8 + w * 32;
        const float* tp = tab + 4 * h4;
        u32x4 f3[5];
        float cs = 0.f, b1 = 0.f;
        const bool pad = w == 6 && n16 >= 12;
        const unsigned padv = (n16 == 12 && ucb >= 8) ? 0x3f803f80u : 0u;
#pragma unroll
        for (int k = 0; k < 5; ++k) f3[k] = trfrag(bp + k * 7168, 16 * 224);
#pragma unroll
        for (int k = 0; k < 5; ++k) {
          const f32x4 ca = *(const f32x4*)(tp + k * 32), cb4 = *(const f32x4*)(tp + k * 32 + 16);
          u32x4 o;
#pragma unroll
          for (int e = 0; e < 4; ++e) {
            const float c0 = e < 2 ? ca[2 * e] : cb4[2 * e - 4], c1 = e < 2 ? ca[2 * e + 1] : cb4[2 * e - 3];
            const float p0 = bflo(f3[k][e]) * c0, p1 = bfhi(f3[k][e]) * c1;
            cs += p0 + p1;
            o[e] = pad ? padv : pack2(p0, p1);
          }
          st16(rs, WS_WINF + (unsigned)(ucb * 7 + w) * 20480u + (unsigned)(kg * 5 + k) * 1024u + l16, o);
        }
        cs = sum_rows(cs);
        DRAIN();
        if (l == 0) st4(rs, WS_FW1 + (unsigned)((ucb * 8 + w) * 4 + kg) * 128u, tag);
        STAMP(5);
        BAR();
#pragma unroll
        for (int k = 0; k < 5; ++k) {
          const f32x4 sa = *(const f32x4*)(tp + 160 + k * 32), sb4 = *(const f32x4*)(tp + 160 + k * 32 + 16);
#pragma unroll
          for (int e = 0; e < 4; ++e) {
            const float s0 = e < 2 ? sa[2 * e] : sb4[2 * e - 4], s1 = e < 2 ? sa[2 * e + 1] : sb4[2 * e - 3];
            b1 = __builtin_fmaf(s0, bflo(f3[k][e]), __builtin_fmaf(s1, bfhi(f3[k][e]), b1));
          }
        }
        b1 = sum_rows(b1);
        if (pad) { cs = padv ? 160.0f : 0.0f; b1 = 0.0f; }
        if (l < 16) st16(rs, WS_CS + (unsigned)((ucb * 4 + kg) * 112 + w * 16 + l) * 16u, (u32x4){tag, __float_as_uint(cs), tag, __float_as_uint(b1)});
      }
    } else {
      if (job >= 60 && T < 0) __builtin_amdgcn_s_sleep(100);
      if (slot < 2 && w >= 2 && w < 6) {
        const int c5 = slot * 4 + w - 2, c2 = xcd;
        char* st = lds + L_J5 + (w - 2) * 18432;
#pragma unroll
        for (int r = 0; r < 18; ++r) {
          const int g = r * 64 + l;
          __builtin_amdgcn_raw_ptr_buffer_load_lds(rwo, (LAS void*)(st + r * 1024), 16, (unsigned)((g / 10) * (D_ * 2) + (g % 10) * 16),
                                                   (unsigned)(xcd * CB * D_ * 2 + c5 * 160), 0, 0);
        }
        DRAIN();
        const char* bp = st + (4 * h4 + (n16 >> 2)) * 160 + (n16 & 3) * 8;
        const unsigned base = WS_WOUTF + (unsigned)c5 * WBLK;
#pragma unroll
        for (int dj = 0; dj < 5; ++dj) {
          st16(rs, base + (unsigned)c2 * 10240u + (unsigned)dj * 1024u + l16, trfrag(bp + dj * 32, 32 * 160));
          st16(rs, base + (unsigned)c2 * 10240u + (unsigned)(5 + dj) * 1024u + l16, trfrag(bp + 16 * 160 + dj * 32, 32 * 160));
          st16(rs, base + 81920u + (unsigned)c2 * 5120u + (unsigned)dj * 1024u + l16, trfrag(bp + 64 * 160 + dj * 32, 16 * 160));
          st8(rs, base + 122880u + (unsigned)((c2 >> 1) * 5 + dj) * 1024u + (unsigned)(c2 & 1) * 512u + (unsigned)l * 8u, trd(bp + 96 * 160 + dj * 32));
        }
        fw2 = WS_FW2 + (unsigned)(c5 * 8 + xcd) * 128u;
      }
    }
    STAMP(4);
#pragma unroll
    for (int i = 0; i < NRAW; ++i) LDW(i);
    bs_raw = ((const GAS u16*)p.b_sp)[trow];
    e_fw1 = ~tag;
    asm volatile(".Lmega_aux_end:");
    asm volatile("" ::"v"(cpf));
    STAMP(8);
  }

  LAS char* xa = (LAS char*)(lds + L_XI) + l * 16;
  LAS char* xb = xa + 65536;
  asm volatile("" : "+v"(xb));
#define LDA(ks, rt) (*(const volatile LAS u32x4*)((ks) < 16 ? xa + ((ks) * 4 + (rt)) * 1024 : xb + (((ks) - 16) * 4 + (rt)) * 1024))
  auto lda1 = [&](int ks, u32x4* A) {
#pragma unroll
    for (int rt = 0; rt < 4; ++rt) A[rt] = LDA(ks, rt);
  };
  const LAS char* tb4 = (const LAS char*)(lds + L_MISC) + h4 * 16;
  const LAS char* tbn = (const LAS char*)(lds + L_MISC) + n16 * 4;
  asm volatile("" : "+v"(tb4), "+v"(tbn));
  u32x4 xr[5][2];
  auto ld_x1 = [&](int m0, int m1) {
#pragma unroll
    for (int m = m0; m < m1; ++m) {
      if (m == 0) __builtin_amdgcn_s_setprio(2);
      if (m == 1) __builtin_amdgcn_s_setprio(1);
      if (m == 3) __builtin_amdgcn_s_setprio(0);
#pragma unroll
      for (int e = 0; e < 2; ++e)
        xr[m][e] = ld16(rs, l16, WS_X1B + (unsigned)(R * 2 + (w & 1)) * WX1P + (unsigned)(4 * m + (w >> 1)) * 2048u + (unsigned)e * 1024u);
      SCHED();
    }
  };
  LAS char* xw = xa + w * 2048;
  auto put_x1 = [&](int m) {
    LAS char* d = m < 4 ? xw + m * 16384 : xb + w * 2048;
#pragma unroll
    for (int e = 0; e < 2; ++e) {
      *(volatile LAS u32x2*)(d + 8 * e) = (u32x2){xr[m][e][0], xr[m][e][1]};
      *(volatile LAS u32x2*)(d + 1024 + 8 * e) = (u32x2){xr[m][e][2], xr[m][e][3]};
    }
  };
  auto poison_u = [&]() {
#pragma unroll
    for (int k = 0; k < 2; ++k) {
      const unsigned o = (unsigned)(wj * 2 + k) * 1024u + l16;
      const unsigned rt = o / 3584u, r = o - rt * 3584u;
      st16(rs, WS_UF + ((unsigned)(R * 4) + rt) * 8u * 3584u + (unsigned)c * 3584u + r, (u32x4){~0u, ~0u, ~0u, ~0u});
    }
  };
  const unsigned wob = WS_WOUTF + (unsigned)c * WBLK;
  const LAS char* wrd = (const LAS char*)(lds + L_WS) + w * 8192 + n16 * 256 + ((h4 ^ ((4 - (n16 >> 2)) & 3)) * 64);
#define DMA_W(g, q) __builtin_amdgcn_raw_ptr_buffer_load_lds(rsp, (LAS void*)(lds + L_WS + w * 8192 + (((g) + 1) & 1) * 4096 + (q) * 1024), 16, WVO(q), WSO(g, q), 0, 0)
#define RD_W(g) do { _Pragma("unroll") for (int i_ = 0; i_ < 4; ++i_) \
                      wsp[(g) & 3][((g) >> 2) * 4 + i_] = *(const volatile LAS u32x4*)(wrd + (((g) + 1) & 1) * 4096 + ((i_ ^ (n16 & 3)) * 16)); } while (0)
  auto park = [&]() {
    LAS char* d = (LAS char*)(lds + L_WS) + w * 8192 + l * 16;
#pragma unroll
    for (int g = 0; g < NRAW / 4; ++g) {
#pragma unroll
      for (int q = 0; q < 4; ++q) *(volatile LAS u32x4*)(d + ((g + 1) & 1) * 4096 + q * 1024) = raw[4 * g + q];
      if (g < 2) RD_W(g);
    }
    asm volatile("s_waitcnt lgkmcnt(0)" ::: "memory");
  };
#define DMA_AB(k) dma1k(rs, lds + L_AB + ((k) * 8 + w) * 1024, l16, wob + (unsigned)((k) * 8 + w) * 1024u)
  const int rto = w & 3, cls = w >> 2;
  u32x2 xp[3];
  constexpr int W = G2W;
  const unsigned zb = WS_Z2F + (unsigned)c * 458752u + (unsigned)wk * 8192u;
  const unsigned fzo = WS_FZ + (unsigned)(c * 32 + wk * 4 + (l & 3)) * 128u;
  u32x4 fr[W];
#define LDZ(i) fr[(i) % W] = ld16(rs, l16, zb + (unsigned)((i) >> 3) * 65536u + (unsigned)((i) & 7) * 1024u)
  u32x4 cr[5];
#define LDCR(k) cr[k] = ld16(rs, l16, wob + 81920u + (unsigned)(w * 5 + (k)) * 1024u)
  auto rd_xp = [&]() {
#pragma unroll
    for (int j = 0; j < 3; ++j) {
      const int q4 = 5 * c + (cls ? (3 + (j < 2 ? j : 1)) : j);
      xp[j] = *(const volatile LAS u32x2*)(xa + ((q4 >> 1) * 4 + rto) * 1024 + (q4 & 1) * 8);
    }
  };
  if (w < 7) {
    u32x4 B2[20], B1[20];
    const unsigned b2o = WS_WINF + (unsigned)((8 + c) * 7 + w) * 20480u, b1o = WS_WINF + (unsigned)(c * 7 + w) * 20480u;
#define LDB2(ks) B2[ks] = ld16(rs, l16, b2o + (unsigned)(ks) * 1024u)
#define LDB1(ks) B1[ks] = ld16(rs, l16, b1o + (unsigned)(ks) * 1024u)
    const bool early = __all(e_fw1 == tag);
    asm volatile("" ::: "memory");
    {
      const unsigned le = early ? l16 : OOB;
#pragma unroll
      for (int ks = 0; ks < NB2A; ++ks) B2[ks] = ld16(rs, le, b2o + (unsigned)ks * 1024u);
      SCHED();
      if (is_conv) VMW(NB2A);
    }
    STAMP(33);
    BAR();
    STAMP(37);
    if (!early) {
      wait_flag<4>(rs, fw1o, tag);
      DRAIN();
      st4(rs, l == 0 ? fw2 : OOB, tag);
#pragma unroll
      for (int ks = 0; ks < NB2A; ++ks) LDB2(ks);
    }
#pragma unroll
    for (int ks = NB2A; ks < 20; ++ks) {
      LDB2(ks);
      if (ks == 12) { SCHED(); park(); SCHED(); }
      if (ks == 13) { SCHED(); __builtin_amdgcn_s_barrier(); SCHED(); }
    }
    STAMP(9);
    BAR();
    STAMP(10);
    ld_x1(0, 3);
    SCHED();
#pragma unroll
    for (int m = 0; m < 3; ++m) put_x1(m);
    ld_x1(3, 5);
    poison_u();
    SCHED();
    if (w == 6) __builtin_amdgcn_s_setprio(2);
    BAR();
    STAMP(11);
    f32x4 acc[4];
    u32x4 A[2][4];
    lda1(0, A[0]);
#pragma unroll
    for (int ks = 0; ks < 20; ++ks) {
      if (ks == 6) put_x1(3);
      if (ks == 8) put_x1(4);
      if (ks == 11) BAR();
      if (ks >= 4) LDB1(ks - 4);
      if (ks >= 16) LDB1(ks);
      if (ks == 12) RD_W(2);
      SCHED();
#pragma unroll
      for (int rt = 0; rt < 4; ++rt) {
        if (ks < 19) A[(ks + 1) & 1][rt] = LDA(ks + 1, rt);
        SCHED();
        acc[rt] = MFMA16(A[ks & 1][rt], B2[ks], ks ? acc[rt] : ZERO4);
        SCHED();
      }
    }
    STAMP(12);
    if (w == 6) {
      if (n16 == 12) {
#pragma unroll
        for (int rt = 0; rt < 4; ++rt) *(f32x4*)(lds + L_S1 + (rt * 16 + 4 * h4) * 4) = acc[rt];
      }
      asm volatile("" ::: "memory");
      *(volatile LAS unsigned*)(lds + L_S1 + 256) = tag;
      __builtin_amdgcn_s_setprio(0);
    }
    VMW(20);
    BAR();
    STAMP(13);
    f32x2 zz[8];
    f32x4 cs1a, cs1b;
    {
      f32x4 rs4[4], nm4[4];
#pragma unroll
      for (int rt = 0; rt < 4; ++rt) {
        rs4[rt] = *(const LAS f32x4*)(tb4 + (L_T2 - L_MISC) + rt * 64);
        nm4[rt] = *(const LAS f32x4*)(tb4 + (L_T2 - L_MISC) + 256 + rt * 64);
      }
      const f32x2 cs2 = *(const f32x2*)(lds + L_CS1 + (112 + 16 * w + n16) * 8);
      const f32x2 csv = {cs2[0], cs2[0]}, b1v = {cs2[1], cs2[1]};
      float v1[16], v2[16];
#pragma unroll
      for (int i = 0; i < 8; ++i) {
        const int rt = i >> 1;
        const f32x2 a2 = i & 1 ? HI2(acc[rt]) : LO2(acc[rt]), n2 = i & 1 ? HI2(nm4[rt]) : LO2(nm4[rt]), r2 = i & 1 ? HI2(rs4[rt]) : LO2(rs4[rt]);
        zz[i] = gelu2(__builtin_elementwise_fma(__builtin_elementwise_fma(n2, csv, a2), r2, b1v));
        asm volatile("" : "+v"(zz[i]));
        DMA_W(3 + (i >> 2), i & 3);
        const f32x2 sq = zz[i] * zz[i];
        v1[2 * i] = zz[i][0]; v1[2 * i + 1] = zz[i][1]; v2[2 * i] = sq[0]; v2[2 * i + 1] = sq[1];
      }
      const float t1 = tr_sum16(v1, l);
      const float t2 = tr_sum16(v2, l);
      *(f32x2*)(lds + L_Z1S + (w * 64 + (n16 >> 2) * 16 + 4 * h4 + (n16 & 3)) * 8) = (f32x2){t1, t2};
      lda1(0, A[0]);
      cs1a = *(const f32x4*)(lds + L_CS1 + (16 * w + 4 * h4) * 8);
      cs1b = *(const f32x4*)(lds + L_CS1 + (16 * w + 4 * h4) * 8 + 16);
    }
    BAR();
    STAMP(14);
#pragma unroll
    for (int ks = 0; ks < 20; ++ks) {
      if (ks == Z1SPLIT) {
        STAMP(15);
        BAR();
        STAMP(16);
        f32x4 ra[4], rb[4];
#pragma unroll
        for (int rt = 0; rt < 4; ++rt) {
          ra[rt] = *(const LAS f32x4*)(tb4 + (L_ST2 - L_MISC) + rt * 64);
          rb[rt] = *(const LAS f32x4*)(tb4 + (L_ST2 - L_MISC) + 256 + rt * 64);
        }
#pragma unroll
        for (int i = 0; i < 8; ++i) {
          zz[i] = __builtin_elementwise_fma(zz[i], i & 1 ? HI2(ra[i >> 1]) : LO2(ra[i >> 1]), i & 1 ? HI2(rb[i >> 1]) : LO2(rb[i >> 1]));
          asm volatile("" : "+v"(zz[i]));
        }
        const u32x4 o0 = {pack2(zz[0][0], zz[2][0]), pack2(zz[0][1], zz[2][1]), pack2(zz[1][0], zz[3][0]), pack2(zz[1][1], zz[3][1])};
        const u32x4 o1 = {pack2(zz[4][0], zz[6][0]), pack2(zz[4][1], zz[6][1]), pack2(zz[5][0], zz[7][0]), pack2(zz[5][1], zz[7][1])};
        const unsigned zo = WS_Z2F + (unsigned)((c * 7 + w) * 64 + 4 * (R >> 1) + h4) * 1024u + (unsigned)(R & 1) * 512u + (unsigned)n16 * 16u;
        st16(rs, zo, o0);
        st16(rs, zo + 256u, o1);
        SCHED();
      }
      if (ks == 0) { VMW(4); RD_W(3); }
      if (ks == 2) { VMW(0); RD_W(4); }
      if (ks == 8) { VMW(4 + 2); RD_W(5); }
      if (ks == 11) rd_xp();
      if (ks == 13) { VMW(4 + 5 + 2); RD_W(6); }
      if (ks >= 13 && ks < 19) DMA_AB(ks - 13);
      if (ks >= 2 && ks < 6) { DMA_W(5 + ((ks - 2) >> 1), 2 * (ks & 1)); DMA_W(5 + ((ks - 2) >> 1), 2 * (ks & 1) + 1); }
      if (ks >= 6 && ks < 9) LDCR(ks - 6);
      if (ks == 12) { LDCR(3); LDCR(4); }
      if (ks == 10 || ks == 11) { DMA_W(7, 2 * (ks & 1)); DMA_W(7, 2 * (ks & 1) + 1); }
      SCHED();
#pragma unroll
      for (int rt = 0; rt < 4; ++rt) {
        if (ks < 19) A[(ks + 1) & 1][rt] = LDA(ks + 1, rt);
        SCHED();
        acc[rt] = MFMA16(B1[ks], A[ks & 1][rt], ks ? acc[rt] : ZERO4);
        SCHED();
      }
    }
    STAMP(18);
    VMW(2 + 2 + 6);
    RD_W(7);
    const f32x2 c01 = {cs1a[0], cs1a[2]}, b01 = {cs1a[1], cs1a[3]}, c23 = {cs1b[0], cs1b[2]}, b23 = {cs1b[1], cs1b[3]};
    u32x2 g1[4];
    auto z1g = [&](int rt) {
      const float r1 = *(const LAS float*)(tbn + (L_T2 - L_MISC) + rt * 64), n1 = *(const LAS float*)(tbn + (L_T2 - L_MISC) + 256 + rt * 64);
      const f32x2 r2 = {r1, r1}, n2 = {n1, n1};
      const f32x2 a = gelu2(__builtin_elementwise_fma(__builtin_elementwise_fma(n2, c01, LO2(acc[rt])), r2, b01));
      const f32x2 b = gelu2(__builtin_elementwise_fma(__builtin_elementwise_fma(n2, c23, HI2(acc[rt])), r2, b23));
      g1[rt] = (u32x2){pack2(a[0], a[1]), pack2(b[0], b[1])};
      asm volatile("" : "+v"(g1[rt]));
    };
    SCHED();
    VMW(2 + 6);
    BAR();
    STAMP(17);
#pragma unroll
    for (int rt = 0; rt < 4; ++rt) {
      if (rt < 2) { DMA_AB(6 + 2 * rt); DMA_AB(7 + 2 * rt); }
      z1g(rt);
      *(u32x2*)(lds + L_Z1S + ((w * 4 + rt) * 64 + l) * 8) = g1[rt];
    }
  } else {
    __builtin_amdgcn_s_setprio(3);
    DRAIN();
    BAR();
    if (is_conv && l == 0) st4(rs, WS_FX + (unsigned)quad * 128u, tag);
    STAMP(9);
    *(volatile LAS unsigned*)(lds + L_S1 + 256) = ~tag;
    u32x4 gv, cv[2][2][4];
    const unsigned gvo = WS_MOD + (unsigned)(5 * D_ + 80 * c) * 8u + (l < 40 ? l16 : 0u);
    auto ld_consts = [&]() {
      gv = ld16(rs, gvo);
#pragma unroll
      for (int i = 0; i < 16; ++i) cv[i >> 3][(i >> 2) & 1][i & 3] = ld16(rs, l < 56 ? l16 : 0u, WS_CS + (unsigned)(((c + 8 * (i >> 3)) * 4 + (i & 3)) * 112 + 56 * ((i >> 2) & 1)) * 16u);
    };
    auto ok_consts = [&]() {
      unsigned x = (gv[0] ^ tag) | (gv[2] ^ tag);
#pragma unroll
      for (int i = 0; i < 16; ++i) x |= (cv[i >> 3][(i >> 2) & 1][i & 3][0] ^ tag) | (cv[i >> 3][(i >> 2) & 1][i & 3][2] ^ tag);
      asm volatile("" : "+v"(x));
      return __all(x == 0u);
    };
    auto touch_w = [&](int k) {
      cpf ^= __builtin_amdgcn_raw_buffer_load_b32(rsp, (unsigned)(h4 * 4096 + (n16 >> 1) * 512 + (n16 & 1) * 128),
                                                  (unsigned)(R * 64 + (c & 3) * 16 + k * 4) * 4096u + 256u, 0);
    };
    park();
    __builtin_amdgcn_s_barrier();
    ld_consts();
    STAMP(31);
    _Pragma("unroll 1") for (unsigned spins = 0; spins < SPIN_CAP; ++spins) {
      const unsigned f0 = ld4(rs, WS_FX + (unsigned)l * 128u), f1 = ld4(rs, WS_FX + 8192u + (unsigned)l * 128u),
                     f2 = ld4(rs, l < 32 ? WS_FX + 16384u + (unsigned)l * 128u : WS_FW1 + (unsigned)(c * 32 + (l < 60 ? l - 32 : 0)) * 128u);
      if (__all(f0 == tag && f1 == tag && f2 == tag)) break;
      __builtin_amdgcn_s_sleep(POLL_SLEEP);
    }
    BAR();
    STAMP(10);
    ld_x1(0, 5);
    __builtin_amdgcn_s_setprio(3);
    if (!ok_consts()) {
      _Pragma("unroll 1") for (unsigned spins = 0; spins < SPIN_CAP; ++spins) {
        ld_consts();
        if (ok_consts()) break;
        __builtin_amdgcn_s_sleep(8);
      }
    }
    if (l < 40) *(f32x2*)(lds + L_GB + l * 8) = (f32x2){__uint_as_float(gv[1]), __uint_as_float(gv[3])};
    if (l < 56) {
#pragma unroll
      for (int i = 0; i < 4; ++i) {
        const u32x4* q4 = cv[i >> 1][i & 1];
        *(f32x2*)(lds + L_CS1 + (56 * i + l) * 8) =
            (f32x2){(__uint_as_float(q4[0][1]) + __uint_as_float(q4[1][1])) + (__uint_as_float(q4[2][1]) + __uint_as_float(q4[3][1])),
                    (__uint_as_float(q4[0][3]) + __uint_as_float(q4[1][3])) + (__uint_as_float(q4[2][3]) + __uint_as_float(q4[3][3]))};
      }
    }
    SCHED();
#pragma unroll
    for (int m = 0; m < 3; ++m) put_x1(m);
    BAR();
    f32x4 acc[4];
    u32x4 A[2][4];
    lda1(0, A[0]);
#pragma unroll
    for (int ks = 0; ks < 20; ++ks) {
      if (ks == 6) put_x1(3);
      if (ks == 8) put_x1(4);
      if (ks == 11) BAR();
      if (ks == 4) RD_W(2);
      if (ks >= 6 && ks < 14) DMA_W(3 + ((ks - 6) >> 2), (ks - 6) & 3);
      if (ks >= 1 && ks < 5) touch_w(ks - 1);
      if (ks < 19) lda1(ks + 1, A[(ks + 1) & 1]);
      SCHED();
#pragma unroll
      for (int rt = 0; rt < 4; ++rt) acc[rt] = MFMA16(A[ks & 1][rt], A[ks & 1][rt], ks ? acc[rt] : ZERO4);
      SCHED();
    }
    {
      if ((n16 >> 2) == h4) {
#pragma unroll
        for (int rt = 0; rt < 4; ++rt) {
          const float lo = (l & 1) ? acc[rt][1] : acc[rt][0], hi = (l & 1) ? acc[rt][3] : acc[rt][2];
          *(volatile LAS float*)(tbn + (L_T2 - L_MISC) + rt * 64) = (l & 2) ? hi : lo;
        }
      }
      const float s2 = *(volatile LAS float*)(lds + L_T2 + l * 4) * (1.0f / D_);
      float s1;
      _Pragma("unroll 1") for (unsigned spins = 0; spins < SPIN_CAP * 16; ++spins) {
        const unsigned f = *(volatile LAS unsigned*)(lds + L_S1 + 256);
        s1 = *(volatile LAS float*)(lds + L_S1 + l * 4);
        if (f == tag) break;
      }
      const float nm = s1 * (-1.0f / D_);
      *(float*)(lds + L_T2 + l * 4) = __builtin_amdgcn_rsqf(__builtin_fmaxf(s2 - nm * nm, 0.f) + 1e-5f);
      *(float*)(lds + L_T2 + 256 + l * 4) = nm;
    }
    BAR();
    __builtin_amdgcn_s_setprio(0);
    asm volatile("" ::"v"(cpf));
    DRAIN();
    RD_W(3); RD_W(4);
    rd_xp();
    asm volatile("s_waitcnt lgkmcnt(0)" ::: "memory");
#pragma unroll
    for (int i = 0; i < 8; ++i) DMA_W(5 + (i >> 2), i & 3);
    const unsigned e_fw2 = ld4(rs, WS_FW2 + (unsigned)(c * 8 + (l & 7)) * 128u);
    if (!__all(e_fw2 == tag)) wait_flag<2>(rs, WS_FW2 + (unsigned)(c * 8 + (l & 7)) * 128u, tag);
    __builtin_amdgcn_s_setprio(3);
    BAR();
    {
      f32x2 t = {0.f, 0.f};
#pragma unroll
      for (int k = 0; k < 7; ++k) t += *(const f32x2*)(lds + L_Z1S + (k * 64 + l) * 8);
      st16(rs, WS_PART + (unsigned)(R * 8 + c) * 1024u + l16, (u32x4){tag, __float_as_uint(t[0]), tag, __float_as_uint(t[1])});
    }
    STAMP(14);
    {
      u32x4 v[8];
      const unsigned po = WS_PART + (unsigned)R * 8192u + l16;
      __builtin_amdgcn_s_sleep(16);
      _Pragma("unroll 1") for (unsigned spins = 0; spins < SPIN_CAP; ++spins) {
#pragma unroll
        for (int k = 0; k < 8; ++k) v[k] = ld16(rs, po + (unsigned)k * 1024u);
        unsigned x = 0;
#pragma unroll
        for (int k = 0; k < 8; ++k) x |= (v[k][0] ^ tag) | (v[k][2] ^ tag);
        asm volatile("" : "+v"(x));
        if (__all(x == 0u)) break;
        __builtin_amdgcn_s_sleep(1);
      }
      float t1 = 0.f, t2 = 0.f;
#pragma unroll
      for (int k = 0; k < 8; ++k) { t1 += __uint_as_float(v[k][1]); t2 += __uint_as_float(v[k][3]); }
      const float mean = t1 * (1.0f / FH);
      const float var = __builtin_fmaxf(t2 * (1.0f / FH) - mean * mean, 0.f);
      const float rstd = __builtin_amdgcn_rsqf(var + 1e-5f);
      *(float*)(lds + L_ST2 + l * 4) = rstd;
      *(float*)(lds + L_ST2 + 256 + l * 4) = -mean * rstd;
    }
    STAMP(15);
    BAR();
    RD_W(5); RD_W(6);
    asm volatile("s_waitcnt lgkmcnt(0)" ::: "memory");
#pragma unroll
    for (int q = 0; q < 4; ++q) DMA_W(7, q);
    DRAIN();
    RD_W(7);
    BAR();
    if (l == 0) st4(rs, WS_FZ + (unsigned)(c * 32 + R) * 128u, tag);
    STAMP(17);
    __builtin_amdgcn_s_setprio(0);
#pragma unroll
    for (int k = 0; k < 10; ++k) DMA_AB(k);
#pragma unroll
    for (int k = 0; k < 5; ++k) LDCR(k);
  }
  const rsrc_t rout = __builtin_amdgcn_make_buffer_rsrc((void*)p.out, 0, S_ * D_ * 2, 0x00020000);
  STAMP(19);
  u32x2 z1o[4];
  wait_flag<POLL_SLEEP>(rs, fzo, tag);
#pragma unroll
  for (int i = 0; i < W; ++i) { LDZ(i); SCHED(); }
  STAMP(20);

  const unsigned ub = WS_UF + (unsigned)((R * 4 + rto) * 8 + c) * 3584u;
  const float bs = bflo(bs_raw);
  const unsigned ur = WS_UF + (unsigned)((R * 4 + rto) * 8 + cls) * 3584u;
  u32x4 uf[4][2];
  f32x4 acc[2][4];
  f32x4* const bufw = (f32x4*)(lds + L_BUF) + w * 256 + l;
#define BUFW(slot, rt) bufw[(slot) * 2048 + (rt) * 64]
  {
    const f32x4* const rb = (const f32x4*)(lds + L_BUF) + cls * 2048 + rto * 64 + l;
    f32x4 rdv[4], s;
    u32x4 uo;
#pragma unroll
    for (int j = 0; j < 7; ++j) {
#pragma unroll
      for (int ks = 0; ks < 8; ++ks) {
        const int i = 8 * j + ks;
#pragma unroll
        for (int rt = 0; rt < 4; ++rt) acc[j & 1][rt] = MFMA16(fr[i % W], wsp[rt][ks], ks ? acc[j & 1][rt] : ZERO4);
        if (i == 0) STAMP(21);
        if (i == 24) STAMP(22);
        SCHED();
        if (i + W < 56) LDZ(i + W);
        if (j > 0) {
          if (ks == 0 && (j & 1)) BAR();
          if (j == 1 && ks == 0) {
#pragma unroll
            for (int k = 0; k < 4; ++k) z1o[k] = *(const u32x2*)(lds + L_Z1S + (((k < 3 ? 2 * k + cls : 6) * 4 + rto) * 64 + l) * 8);
          }
          if (i >= (56 - G2W) && i < 53) {
#pragma unroll
            for (int k = 0; k < 8; ++k)
              if ((56 - G2W) + k * (53 - (56 - G2W)) / 8 == i) uf[k >> 1][k & 1] = ld16(rs, l16, ur + (unsigned)(k >> 1) * 7168u + (unsigned)(k & 1) * 1024u);
            SCHED();
          }
          const bool fast = j == 6;
          if (!fast && ks < 4) BUFW((j - 1) & 1, ks) = acc[(j - 1) & 1][ks];
          if (fast && ks < 2) { BUFW(1, 2 * ks) = acc[1][2 * ks]; BUFW(1, 2 * ks + 1) = acc[1][2 * ks + 1]; }
          if (!(j & 1)) {
            const int k0 = fast ? 2 : 5;
            if (ks == k0) {
              BAR();
#pragma unroll
              for (int k = 0; k < 4; ++k) rdv[k] = rb[k * 256];
            }
            if (ks == k0 + 1) {
              s = (rdv[0] + rdv[1]) + (rdv[2] + rdv[3]);
#pragma unroll
              for (int k = 0; k < 4; ++k) rdv[k] = rb[(4 + k) * 256];
            }
            if (ks == k0 + 2) {
              s += (rdv[0] + rdv[1]) + (rdv[2] + rdv[3]);
              const u32x2 z = z1o[(j >> 1) - 1];
              const int e = j == 4 ? 2 : 0;
              uo[e] = pack2(bflo(z[0]) * (s[0] + bs), bfhi(z[0]) * (s[1] + bs));
              uo[e + 1] = pack2(bflo(z[1]) * (s[2] + bs), bfhi(z[1]) * (s[3] + bs));
              if (j == 4) st16(rs, ub + (unsigned)cls * 1024u + l16, uo);
              if (j == 6) st16(rs, l < 32 ? ub + 2048u + (unsigned)cls * 512u + l16 : OOB, half_swap((u32x4){uo[0], uo[1], uo[0], uo[1]}));
            }
            if (j == 6 && ks == 4) BAR();
            if (j == 6 && ks >= 4 && ks < 7) {
#pragma unroll
              for (int k = 0; k < 5; ++k)
                if (k / 2 == ks - 4) *(u32x4*)(lds + (w < 6 ? L_BUF + 32768 + w * 5120 : L_Z1S + (w - 6) * 5120) + k * 1024 + l16) = cr[k];
            }
          }
        }
        SCHED();
      }
    }
  }
  STAMP(23);

  {
    u32x4 uc[4], uh[4];
    const unsigned uho = (unsigned)(l >> 5) * 3584u + (unsigned)(l & 31) * 16u;
#define LD_C(cc) uc[cc] = ld16(rs, l16, ur + (unsigned)(cc) * 7168u + 2048u)
#define LD_H(cc) uh[cc] = ld16(rs, uho, WS_UF + (unsigned)((R * 4 + rto) * 8) * 3584u + (unsigned)(cc) * 7168u + 3072u)
    auto mx = [](unsigned a, unsigned b) { return __builtin_elementwise_max(a, b); };
    auto ok4 = [&](const u32x4* v, int n) {
      unsigned m = 0;
#pragma unroll
      for (int i = 0; i < 8; ++i)
        if (i < n) m = mx(m, mx(mx(v[i][0], v[i][1]), mx(v[i][2], v[i][3])));
      return __all(m != ~0u);
    };
#pragma unroll
    for (int rt = 0; rt < 4; ++rt) BUFW(0, rt) = acc[0][rt];
    BAR();
    f32x4 a3[5];
    u32x4 af[2][5];
    const LAS char* wb[2] = {(const LAS char*)(lds + L_AB) + cls * (10240 + 3072) + l16, (const LAS char*)(lds + L_AB) + cls * (10240 - 2048) + l16};
    const LAS char* wb2[2] = {wb[0] + 40960, wb[1] + 40960};
    asm volatile("" : "+v"(wb[0]), "+v"(wb2[0]), "+v"(wb[1]), "+v"(wb2[1]));
    u32x4 ho = {0u, 0u, 0u, 0u};
    if (w < 4) {
      __builtin_amdgcn_s_setprio(2);
      const f32x4* const rb = (const f32x4*)(lds + L_BUF) + rto * 64 + l;
      f32x4 r8[8];
#pragma unroll
      for (int k = 0; k < 8; ++k) r8[k] = rb[k * 256];
      const f32x4 s = ((r8[0] + r8[1]) + (r8[2] + r8[3])) + ((r8[4] + r8[5]) + (r8[6] + r8[7]));
      const u32x2 z = z1o[3];
      const unsigned o0 = pack2(bflo(z[0]) * (s[0] + bs), bfhi(z[0]) * (s[1] + bs)), o1 = pack2(bflo(z[1]) * (s[2] + bs), bfhi(z[1]) * (s[3] + bs));
      ho = half_swap((u32x4){o0, o1, o0, o1});
    }
    st16(rs, tid < 256 && l < 32 ? ub + 3072u + l16 : OOB, ho);
    SCHED();
    __builtin_amdgcn_s_setprio(0);
#pragma unroll
    for (int dj = 0; dj < 5; ++dj) af[0][dj] = *(const LAS u32x4*)(wb[dj >= 2] + dj * 1024);
    const bool ab_ok = ok4(&uf[0][0], 8);
    STAMP(24);
    if (!ab_ok) {
      _Pragma("unroll 1") for (unsigned spins = 0; spins < SPIN_CAP; ++spins) {
#pragma unroll
        for (int i = 0; i < 8; ++i) uf[i >> 1][i & 1] = ld16(rs, l16, ur + (unsigned)(i >> 1) * 7168u + (unsigned)(i & 1) * 1024u);
        if (ok4(&uf[0][0], 8)) break;
        __builtin_amdgcn_s_sleep(1);
      }
    }
    STAMP(25);
    const LAS char* cb0[2] = {(const LAS char*)(lds + L_BUF + 32768) + cls * (5120 + 3072) + l16, (const LAS char*)(lds + L_BUF + 32768) + cls * (5120 - 2048) + l16};
    u32x4 ah[4][3];
    const LAS char* hb = (const LAS char*)(lds + L_BUF) + cls * 12288 + l * 8;
    f32x4 gb[3], xo3[3], xf[3];
    LAS char *otw, *xw3;
    const LAS char *ota, *otb, *xr3;
    unsigned oa_o, ob_o;
#pragma unroll
    for (int st = 0; st < 12; ++st) {
      if (st < 2) { LD_C(2 * st); LD_C(2 * st + 1); }
      else if (st < 7 && w < 4) dma1k(rs, lds + L_BUF + ((st - 2) * 4 + w) * 1024, l16, wob + 122880u + (unsigned)(w * 5 + st - 2) * 1024u);
      if (st == 2) {
        LAS char* ot = (LAS char*)(lds + L_AB + 32768) + w * 2048;
        otw = ot + n16 * 112 + h4 * 8;
        ota = ot + (l >> 2) * 112 + (l & 3) * 16;
        otb = ot + ((l >> 1) & 15) * 112 + 64 + (l & 1) * 16;
        asm volatile("" : "+v"(otw), "+v"(ota), "+v"(otb));
      }
      if (st == 3) {
        oa_o = (unsigned)(l >> 2) * 2560u + (unsigned)(l & 3) * 16u;
        ob_o = l < 32 ? (unsigned)(l >> 1) * 2560u + 64u + (unsigned)(l & 1) * 16u : OOB;
        xw3 = (LAS char*)(lds + L_AB) + w * 3072 + l16;
        xr3 = (const LAS char*)(lds + L_AB) + (w ^ 4) * 3072 + cls * 1024 + l16;
        asm volatile("" : "+v"(oa_o), "+v"(ob_o), "+v"(xw3), "+v"(xr3));
      }
      if (st >= 4 && st < 7) {
        const int j = st - 4;
        gb[j] = *(const f32x4*)(lds + L_GB + (48 * cls + 16 * (cls && j == 2 ? 1 : j) + 4 * h4) * 4);
        xf[j] = (f32x4){bflo(xp[j][0]), bfhi(xp[j][0]), bflo(xp[j][1]), bfhi(xp[j][1])};
        asm volatile("" : "+v"(xf[j]));
      }
      if (st == 8) {
        STAMP(26);
        if (!ok4(uc, 4)) {
          _Pragma("unroll 1") for (unsigned spins = 0; spins < SPIN_CAP; ++spins) {
            LD_C(0); LD_C(1); LD_C(2); LD_C(3);
            if (ok4(uc, 4)) break;
            __builtin_amdgcn_s_sleep(1);
          }
        }
        LD_H(0); LD_H(1); LD_H(2); LD_H(3);
#pragma unroll
        for (int cc = 0; cc < 4; ++cc) uc[cc] = half_swap(uc[cc]);
        VMW(4);
        __builtin_amdgcn_s_barrier();
        asm volatile("" ::: "memory");
        STAMP(27);
      }
      SCHED();
#pragma unroll
      for (int dj = 0; dj < 5; ++dj) {
        const int sn = st + 1;
        if (sn < 8) af[sn & 1][dj] = *(const LAS u32x4*)((sn < 4 ? wb : wb2)[dj >= 2] + ((sn >> 1) & 1) * 20480 + ((sn & 1) * 5 + dj) * 1024);
        else if (sn < 12) af[sn & 1][dj] = *(const LAS u32x4*)(cb0[dj >= 2] + (sn < 11 ? (sn - 8) * 10240 : L_Z1S - L_BUF - 32768) + dj * 1024);
        if (st >= 8) {
#pragma unroll
          for (int k = 0; k < 2; ++k) {
            const int h = st < 11 ? (k ? 99 : (st - 8) * 5 + dj) : 15 + 2 * dj + k;
            if (h < 24) {
              const u32x2 v = *(const volatile LAS u32x2*)(hb + (((h >> 1) % 3) * 4 + (h >> 1) / 3) * 1024 + (h & 1) * 512);
              ah[(h >> 1) / 3][(h >> 1) % 3][2 * (h & 1)] = v[0]; ah[(h >> 1) / 3][(h >> 1) % 3][2 * (h & 1) + 1] = v[1];
            }
          }
        }
        SCHED();
        a3[dj] = MFMA16(af[st & 1][dj], st < 8 ? uf[(st >> 1) & 3][st & 1] : uc[st & 3], st ? a3[dj] : ZERO4);
        SCHED();
      }
    }
#pragma unroll
    for (int j = 0; j < 3; ++j) *(volatile LAS f32x4*)(xw3 + j * 1024) = a3[2 + j];
    BAR();
#pragma unroll
    for (int j = 0; j < 3; ++j) xo3[j] = *(const volatile LAS f32x4*)(xr3 + j * 1024);
    SCHED();
    if (!ok4(uh, 4)) {
      _Pragma("unroll 1") for (unsigned spins = 0; spins < SPIN_CAP; ++spins) {
        LD_H(0); LD_H(1); LD_H(2); LD_H(3);
        if (ok4(uh, 4)) break;
        __builtin_amdgcn_s_sleep(1);
      }
    }
#pragma unroll
    for (int cc = 0; cc < 4; ++cc) {
      const u32x4 b = half_swap(uh[cc]);
#pragma unroll
      for (int j = 0; j < 3; ++j) a3[j] = MFMA16(ah[cc][j], b, a3[j]);
      if (cc >= 1) {
        const int j = cc - 1;
        xo3[j] = __builtin_elementwise_fma(gb[j], xo3[j], xf[j]);
        asm volatile("" : "+v"(xo3[j]));
      }
      SCHED();
    }
#pragma unroll
    for (int j = 0; j < 3; ++j) {
      const f32x4 r = __builtin_elementwise_fma(gb[j], a3[j], xo3[j]);
      *(volatile LAS u32x2*)(otw + j * 32) = (u32x2){pack2(r[0], r[1]), pack2(r[2], r[3])};
    }
    const u32x4 oa = *(const volatile LAS u32x4*)(ota);
    const u32x4 ob = *(const volatile LAS u32x4*)(otb);
    const unsigned obase = (unsigned)((R * 64 + 32 * ((w >> 1) & 1) + (w & 1)) * D_ + 80 * c + 48 * cls) * 2u;
    __builtin_amdgcn_raw_buffer_store_b128(oa, rout, oa_o, obase, 0);
    if (cls == 0) __builtin_amdgcn_raw_buffer_store_b128(ob, rout, ob_o, obase, 0);
    if (bid == 0 && tid == 0) st4(rs, WS_EPOCH, tag);
  }
  STAMP(30);
  if (XCONV && !is_conv) tx = (unsigned)tstart;
  while (__builtin_amdgcn_s_memrealtime() - tstart < 5000 + 4 * (tx - (unsigned)tstart)) __builtin_amdgcn_s_sleep(1);
  asm volatile(".Lmega_end:");
}

extern "C" void kernel_launch(void* const* d_in, const int* in_sizes, int n_in, void* d_out, int out_size,
                              void* d_ws, size_t ws_size, hipStream_t stream) {
  Params p;
  p.q = (const u16*)d_in[0];
  p.k = (const u16*)d_in[1];
  p.v = (const u16*)d_in[2];
  p.shift_k = (const u16*)d_in[3];
  p.ssm_k = (const u16*)d_in[4];
  p.ssm_D = (const u16*)d_in[5];
  p.w_in = (const u16*)d_in[7];
  p.w_sp = (const u16*)d_in[8];
  p.b_sp = (const u16*)d_in[9];
  p.w_out = (const u16*)d_in[10];
  p.cvec = (const u16*)d_in[11];
  p.w_mod = (const u16*)d_in[12];
  p.out = (u16*)d_out;
  p.ws = (char*)d_ws;
  mega<<<256, 512, 0, stream>>>(p);
}
```

```cpp
#define XA 2
#define XB 5
#define XCYC 0
#include <hip/hip_runtime.h>
#include <stdint.h>

typedef unsigned short u16;
typedef __attribute__((ext_vector_type(8))) short bf16x8;
typedef __attribute__((ext_vector_type(4))) float f32x4;
typedef __attribute__((ext_vector_type(2))) float f32x2;
typedef __attribute__((ext_vector_type(4))) unsigned int u32x4;
typedef __attribute__((ext_vector_type(2))) unsigned int u32x2;
#define LAS __attribute__((address_space(3)))
#define CAS __attribute__((address_space(4)))
#define GAS __attribute__((address_space(1)))
typedef __amdgpu_buffer_rsrc_t rsrc_t;
typedef __attribute__((ext_vector_type(4))) short s16x4;

constexpr int S_ = 2048, D_ = 640, F_ = 1728, FH = 864, CB = 108;

#define X1PAD 256
constexpr unsigned WS_EPOCH = 0;
constexpr unsigned WS_FX = 4096;
constexpr unsigned WS_FZ = WS_FX + 24576;
constexpr unsigned WS_FW2 = WS_FZ + 32768;
constexpr unsigned WS_FW1 = WS_FW2 + 8192;
constexpr unsigned WS_CS = WS_FW1 + 65536;
constexpr unsigned WS_PART = WS_CS + 131072;
constexpr unsigned WS_MOD = WS_PART + 262144;
constexpr unsigned WS_ST1 = WS_MOD + 32768;
constexpr unsigned WS_X1B = WS_ST1 + 16384;
constexpr unsigned WX1P = 40960 + X1PAD;
constexpr unsigned WBLK = 143360;
constexpr unsigned WS_QB = WS_X1B + 2686976;
constexpr unsigned WS_WINF = WS_QB + 2686976;
constexpr unsigned WS_WOUTF = WS_WINF + 16 * WBLK;
constexpr unsigned WS_Z2F = WS_WOUTF + 8 * WBLK;
constexpr unsigned WS_UF = WS_Z2F + 8 * 64 * 7168;
constexpr unsigned WS_KVT = WS_UF + 32 * 4 * 8 * 3584;
constexpr unsigned WS_FK = WS_KVT + 32 * 81920;
constexpr unsigned WS_STAMP = WS_FK + 20480;
constexpr unsigned WS_END = WS_STAMP + 256 * 40 * 8;

constexpr int U_BYTES = (256 + S_) * 2;
constexpr int G_COPY = 4160;
constexpr int CH_BYTES = U_BYTES + 4 * G_COPY;
constexpr int L_YP = 4 * CH_BYTES;
constexpr int L_A0 = 98304;
constexpr int L_RED = 0;
constexpr int L_TAB = 2048;
constexpr int L_J3 = 8192;
constexpr int L_QT = 49152;
constexpr int L_J5 = L_QT;
constexpr int L_XI = 0;
constexpr int L_BUF = 81920;
constexpr int L_AB = 0;
constexpr int L_MISC = 147456;
constexpr int L_Z1S = L_MISC;
constexpr int L_S1 = L_MISC + 14336;
constexpr int L_T2 = L_S1 + 512;
constexpr int L_ST2 = L_T2 + 512;
constexpr int L_GB = L_ST2 + 512;
constexpr int L_CS1 = L_Z1S + 4096;
constexpr int L_WS = 81920;
constexpr int LDS_TOTAL = 163840;

constexpr int POLL_SLEEP = 1;
constexpr int AUX_SLEEP = 36, LATE_SLEEP = 96;
constexpr int CDIST = 4;
constexpr int NRAW = 12;
constexpr int SV_AT = 108, Q_AT = 130;
constexpr int NB2A = 6;
constexpr int G2W = 7;
constexpr int Z1SPLIT = 12;
constexpr int SC1 = 16;
constexpr unsigned SPIN_CAP = 1u << 16;
constexpr unsigned OOB = 0x80000000u;

__device__ __forceinline__ float bflo(unsigned x) { return __uint_as_float(x << 16); }
__device__ __forceinline__ float bfhi(unsigned x) { return __uint_as_float(x & 0xffff0000u); }
__device__ __forceinline__ f32x2 bf2(unsigned x) { return (f32x2){bflo(x), bfhi(x)}; }
__device__ __forceinline__ unsigned f2bf(float x) {
  __bf16 b = (__bf16)x;
  return (unsigned)__builtin_bit_cast(unsigned short, b);
}
__device__ __forceinline__ float rbf(float x) { return bflo(f2bf(x)); }
typedef __attribute__((ext_vector_type(2))) __bf16 bf2_t;
__device__ __forceinline__ unsigned pack2(float lo, float hi) {
  f32x2 v = {lo, hi};
  return __builtin_bit_cast(unsigned, __builtin_convertvector(v, bf2_t));
}
template <int CTRL>
__device__ __forceinline__ float dpp_f(float v) {
  return __int_as_float(__builtin_amdgcn_update_dpp(0, __float_as_int(v), CTRL, 0xf, 0xf, true));
}
__device__ __forceinline__ float sum_rows(float v) {
  auto a = __builtin_amdgcn_permlane16_swap(__float_as_uint(v), __float_as_uint(v), false, false);
  v = __uint_as_float(a[0]) + __uint_as_float(a[1]);
  auto b = __builtin_amdgcn_permlane32_swap(__float_as_uint(v), __float_as_uint(v), false, false);
  return __uint_as_float(b[0]) + __uint_as_float(b[1]);
}
__device__ __forceinline__ f32x2 gelu2(f32x2 x) {
  const float c0 = -2.0f * 0.7978845608028654f * 1.4426950408889634f;
  const float c1 = c0 * 0.044715f;
  const f32x2 t = x * __builtin_elementwise_fma(x * x, (f32x2){c1, c1}, (f32x2){c0, c0});
  const f32x2 d = (f32x2){__builtin_amdgcn_exp2f(t[0]), __builtin_amdgcn_exp2f(t[1])} + (f32x2){1.0f, 1.0f};
  return x * (f32x2){__builtin_amdgcn_rcpf(d[0]), __builtin_amdgcn_rcpf(d[1])};
}
#define LO2(v) __builtin_shufflevector(v, v, 0, 1)
#define HI2(v) __builtin_shufflevector(v, v, 2, 3)
__device__ __forceinline__ float tr_sum16(const float* v, int l) {
  float x[8], y[4];
  asm volatile(
      "s_nop 1\n\t"
      "v_add_f32_dpp %0, %8, %8 row_ror:8 row_mask:0xf bank_mask:0x3\n\t"
      "v_add_f32_dpp %1, %9, %9 row_ror:8 row_mask:0xf bank_mask:0x3\n\t"
      "v_add_f32_dpp %2, %10, %10 row_ror:8 row_mask:0xf bank_mask:0x3\n\t"
      "v_add_f32_dpp %3, %11, %11 row_ror:8 row_mask:0xf bank_mask:0x3\n\t"
      "v_add_f32_dpp %4, %12, %12 row_ror:8 row_mask:0xf bank_mask:0x3\n\t"
      "v_add_f32_dpp %5, %13, %13 row_ror:8 row_mask:0xf bank_mask:0x3\n\t"
      "v_add_f32_dpp %6, %14, %14 row_ror:8 row_mask:0xf bank_mask:0x3\n\t"
      "v_add_f32_dpp %7, %15, %15 row_ror:8 row_mask:0xf bank_mask:0x3\n\t"
      "v_add_f32_dpp %0, %16, %16 row_ror:8 row_mask:0xf bank_mask:0xc\n\t"
      "v_add_f32_dpp %1, %17, %17 row_ror:8 row_mask:0xf bank_mask:0xc\n\t"
      "v_add_f32_dpp %2, %18, %18 row_ror:8 row_mask:0xf bank_mask:0xc\n\t"
      "v_add_f32_dpp %3, %19, %19 row_ror:8 row_mask:0xf bank_mask:0xc\n\t"
      "v_add_f32_dpp %4, %20, %20 row_ror:8 row_mask:0xf bank_mask:0xc\n\t"
      "v_add_f32_dpp %5, %21, %21 row_ror:8 row_mask:0xf bank_mask:0xc\n\t"
      "v_add_f32_dpp %6, %22, %22 row_ror:8 row_mask:0xf bank_mask:0xc\n\t"
      "v_add_f32_dpp %7, %23, %23 row_ror:8 row_mask:0xf bank_mask:0xc"
      : "=&v"(x[0]), "=&v"(x[1]), "=&v"(x[2]), "=&v"(x[3]), "=&v"(x[4]), "=&v"(x[5]), "=&v"(x[6]), "=&v"(x[7])
      : "v"(v[0]), "v"(v[1]), "v"(v[2]), "v"(v[3]), "v"(v[4]), "v"(v[5]), "v"(v[6]), "v"(v[7]), "v"(v[8]), "v"(v[9]), "v"(v[10]),
        "v"(v[11]), "v"(v[12]), "v"(v[13]), "v"(v[14]), "v"(v[15]));
  asm volatile(
      "s_nop 1\n\t"
      "v_add_f32_dpp %0, %4, %4 row_shl:4 row_mask:0xf bank_mask:0x5\n\t"
      "v_add_f32_dpp %1, %5, %5 row_shl:4 row_mask:0xf bank_mask:0x5\n\t"
      "v_add_f32_dpp %2, %6, %6 row_shl:4 row_mask:0xf bank_mask:0x5\n\t"
      "v_add_f32_dpp %3, %7, %7 row_shl:4 row_mask:0xf bank_mask:0x5\n\t"
      "v_add_f32_dpp %0, %8, %8 row_shr:4 row_mask:0xf bank_mask:0xa\n\t"
      "v_add_f32_dpp %1, %9, %9 row_shr:4 row_mask:0xf bank_mask:0xa\n\t"
      "v_add_f32_dpp %2, %10, %10 row_shr:4 row_mask:0xf bank_mask:0xa\n\t"
      "v_add_f32_dpp %3, %11, %11 row_shr:4 row_mask:0xf bank_mask:0xa\n\t"
      "s_nop 1"
      : "=&v"(y[0]), "=&v"(y[1]), "=&v"(y[2]), "=&v"(y[3])
      : "v"(x[0]), "v"(x[1]), "v"(x[2]), "v"(x[3]), "v"(x[4]), "v"(x[5]), "v"(x[6]), "v"(x[7]));
  const bool b1 = l & 2, b0 = l & 1;
  const float z0 = (b1 ? y[2] : y[0]) + dpp_f<0x4E>(b1 ? y[0] : y[2]);
  const float z1 = (b1 ? y[3] : y[1]) + dpp_f<0x4E>(b1 ? y[1] : y[3]);
  return (b0 ? z1 : z0) + dpp_f<0xB1>(b0 ? z0 : z1);
}
__device__ __forceinline__ u32x4 half_swap(u32x4 v) {
  const auto a = __builtin_amdgcn_permlane32_swap(v[0], v[2], false, false);
  const auto b = __builtin_amdgcn_permlane32_swap(v[1], v[3], false, false);
  return (u32x4){a[0], b[0], a[1], b[1]};
}
#define MFMA16(a, b, c) __builtin_amdgcn_mfma_f32_16x16x32_bf16(__builtin_bit_cast(bf16x8, a), __builtin_bit_cast(bf16x8, b), c, 0, 0, 0)
__device__ __forceinline__ u32x2 trd(const char* p) {
  return __builtin_bit_cast(u32x2, __builtin_amdgcn_ds_read_tr16_b64_v4i16((LAS s16x4*)p));
}
__device__ __forceinline__ u32x4 trfrag(const char* p, int second) {
  u32x2 a = trd(p), b = trd(p + second);
  return (u32x4){a[0], a[1], b[0], b[1]};
}
#define BAR() asm volatile("s_waitcnt lgkmcnt(0)\n\ts_barrier" ::: "memory")
#define DRAIN() do { __builtin_amdgcn_s_waitcnt(0x0F70); asm volatile("" ::: "memory"); } while (0)
#define VMW(n) do { __builtin_amdgcn_s_waitcnt(0x0F70 | ((n) & 15) | (((n) >> 4) << 14)); asm volatile("" ::: "memory"); } while (0)
#define SCHED() __builtin_amdgcn_sched_barrier(0)
#define ZERO4 ((f32x4){0.f, 0.f, 0.f, 0.f})

__device__ __forceinline__ u32x4 ld16(rsrc_t r, unsigned off, unsigned so = 0) { return __builtin_amdgcn_raw_buffer_load_b128(r, off, so, SC1); }
__device__ __forceinline__ u32x2 ld8(rsrc_t r, unsigned off, unsigned so = 0) { return __builtin_amdgcn_raw_buffer_load_b64(r, off, so, SC1); }
__device__ __forceinline__ unsigned ld4(rsrc_t r, unsigned off, unsigned so = 0) { return __builtin_amdgcn_raw_buffer_load_b32(r, off, so, SC1); }
__device__ __forceinline__ void st16(rsrc_t r, unsigned off, u32x4 v) { __builtin_amdgcn_raw_buffer_store_b128(v, r, off, 0, SC1); }
__device__ __forceinline__ void st8(rsrc_t r, unsigned off, u32x2 v) { __builtin_amdgcn_raw_buffer_store_b64(v, r, off, 0, SC1); }
__device__ __forceinline__ void st4(rsrc_t r, unsigned off, unsigned v) { __builtin_amdgcn_raw_buffer_store_b32(v, r, off, 0, SC1); }
__device__ __forceinline__ void dma1k(rsrc_t r, char* lds_dst, unsigned l16, unsigned soff) {
  __builtin_amdgcn_raw_ptr_buffer_load_lds(r, (LAS void*)lds_dst, 16, l16, soff, 0, SC1);
}

#define XNOW() (XCYC ? (unsigned)__builtin_amdgcn_s_memtime() : (unsigned)__builtin_amdgcn_s_memrealtime())
#define STAMP(i) do { if ((i) == XA) ta = XNOW(); if ((i) == XB) tb = XNOW(); } while (0)

template <int SLEEP>
__device__ __forceinline__ void wait_flag(rsrc_t r, unsigned off, unsigned tag) {
  _Pragma("unroll 1") for (unsigned spins = 0; spins < SPIN_CAP; ++spins) {
    if (__all(ld4(r, off) == tag)) break;
    __builtin_amdgcn_s_sleep(SLEEP);
  }
  asm volatile("" ::: "memory");
}

struct ConvSched {
  signed char pp[144], m[144], rd[144];
  int pre;
};
constexpr ConvSched conv_sched(int dist) {
  ConvSched s = {};
  int need[96] = {}, i = 0;
  for (int r = 0; r < 4; ++r)
    for (int pp = 0; pp < 8; ++pp) {
      const int j = 24 * r + (pp ? 8 + 2 * pp : 0);
      need[j] = need[j + 1] = i;
      for (int m = 7 - pp; m >= 0; --m) {
        if (pp == 0) need[24 * r + 9 - m] = i;
        s.pp[i] = pp; s.m[i] = m; s.rd[i] = -1;
        ++i;
      }
    }
  int next = 144;
  for (int j = 95; j >= 0; --j) {
    next = need[j] - dist < next - 1 ? need[j] - dist : next - 1;
    if (next >= 0) s.rd[next] = j;
    else if (s.pre == 0) s.pre = j + 1;
  }
  return s;
}

template <int I> struct IC { static constexpr int v = I; };
template <int I0, int I1, class F>
__device__ __forceinline__ void static_for(F&& f) {
  if constexpr (I0 < I1) { f(IC<I0>{}); static_for<I0 + 1, I1>(f); }
}

struct Params {
  const u16 *q, *k, *v, *shift_k, *ssm_k, *ssm_D, *w_in, *w_sp, *b_sp, *w_out, *cvec, *w_mod;
  u16* out;
  char* ws;
};

__global__ __launch_bounds__(512, 2) void mega(Params) {
  __shared__ __attribute__((aligned(16))) char lds[LDS_TOTAL];
  const CAS Params* kp = (const CAS Params*)__builtin_amdgcn_kernarg_segment_ptr();
  unsigned ka0, ka1;
  asm volatile("s_load_dword %0, %2, 0x0\n\ts_load_dword %1, %2, 0x40" : "=&s"(ka0), "=&s"(ka1) : "s"(kp));
  SCHED();
  const unsigned long long tstart = __builtin_amdgcn_s_memrealtime();
  unsigned ta = 0, tb = 0;
  const int tid = threadIdx.x, l = tid & 63;
  const int w = __builtin_amdgcn_readfirstlane(tid >> 6);
  const int bid = blockIdx.x, xcd = bid & 7, idx = bid >> 3;
  const int dg = ((idx & 7) + 2 * (idx >> 3)) & 7;
  const bool is_conv = dg >= 3;
  const int slot = 3 * (idx >> 3) + dg;
  const int cidx = 5 * (idx >> 3) + dg - 3;
  const int quad = xcd * 20 + cidx;
  const unsigned l16 = (unsigned)l * 16u;
  const int blk = (quad / 5) * 8 + w, sl = quad % 5;
  unsigned a0g = (unsigned)(l >> 4) * 1280u + (unsigned)(((l & 15) ^ (4 * (l >> 5))) * 16), a0x = a0g ^ 128u, a0h = a0g + (l < 16 ? 1280u : 0u);
  unsigned a0s = (unsigned)blk * 10240u + (unsigned)sl * 256u, a0p = blk ? a0s - 5120u : a0s, a0l = L_A0 + w * 6144;
  asm volatile("" : "+v"(a0g), "+v"(a0x), "+v"(a0h));
  asm volatile("" : "+s"(a0s), "+s"(a0p), "+s"(a0l));
  SCHED();
  asm volatile("" : "+s"(kp));
  Params p = {kp->q, kp->k, kp->v, kp->shift_k, kp->ssm_k, kp->ssm_D, kp->w_in, kp->w_sp, kp->b_sp, kp->w_out, kp->cvec, kp->w_mod, kp->out, kp->ws};
  asm volatile("s_waitcnt lgkmcnt(0)" : "+s"(p.q), "+s"(p.k), "+s"(p.v), "+s"(p.shift_k), "+s"(p.ssm_k), "+s"(p.ssm_D), "+s"(p.w_in), "+s"(p.w_sp),
               "+s"(p.b_sp), "+s"(p.w_out), "+s"(p.cvec), "+s"(p.w_mod), "+s"(p.out), "+s"(p.ws) : "s"(ka0), "s"(ka1));
  char* const ws = p.ws;
  STAMP(0);
  unsigned cpf = 0;
  if (!is_conv) {
    unsigned long long pc;
    unsigned len, len2;
    asm volatile("s_getpc_b64 %0\n.Lmega_here:\n\ts_mov_b32 %1, .Lmega_end-.Lmega_here\n\ts_mov_b32 %2, .Lmega_aux_end-.Lmega_here" : "=s"(pc), "=s"(len), "=s"(len2));
    len = len > len2 ? len : len2;
    unsigned o = (unsigned)((slot * 8 + w) * 1024 + (l & 7) * 128);
    o = o + 16u <= len ? o : 0u;
    cpf = *(const GAS unsigned*)((pc & ~15ull) + o);
  }
  const rsrc_t rs = __builtin_amdgcn_make_buffer_rsrc(ws, 0, WS_END, 0x00020000);
  const unsigned epv = ld4(rs, WS_EPOCH);
  SCHED();
  u32x2 dd2;
  {
    int cq = dg;
    asm volatile("" : "+s"(cq));
    if (cq >= 3) {
      const rsrc_t rk = __builtin_amdgcn_make_buffer_rsrc((void*)p.k, 0, S_ * D_ * 2, 0x00020000);
      const rsrc_t rv = __builtin_amdgcn_make_buffer_rsrc((void*)p.v, 0, S_ * D_ * 2, 0x00020000);
      const rsrc_t rh = __builtin_amdgcn_make_buffer_rsrc((void*)p.shift_k, 0, D_ * 8, 0x00020000);
      char* const la = lds + a0l;
      __builtin_amdgcn_raw_ptr_buffer_load_lds(rh, (LAS void*)(la + 5120), 16, l16, (unsigned)sl * 1024u, 0, 0);
      __builtin_amdgcn_raw_ptr_buffer_load_lds(rk, (LAS void*)(la + 1024), 16, a0x, a0s, 0, 0);
      __builtin_amdgcn_raw_ptr_buffer_load_lds(rk, (LAS void*)(la + 2048), 16, a0g, a0s + 5120u, 0, 0);
      __builtin_amdgcn_raw_ptr_buffer_load_lds(rv, (LAS void*)(la + 3072), 16, a0g, a0s, 0, 0);
      __builtin_amdgcn_raw_ptr_buffer_load_lds(rv, (LAS void*)(la + 4096), 16, a0x, a0s + 5120u, 0, 0);
      __builtin_amdgcn_raw_ptr_buffer_load_lds(rk, (LAS void*)(la), 16, a0h, a0p, 0, 0);
      SCHED();
      const rsrc_t rf = __builtin_amdgcn_make_buffer_rsrc((void*)p.ssm_k, 0, S_ * D_ * 2, 0x00020000);
      dd2 = *(const CAS u32x2*)((const CAS char*)p.ssm_D + quad * 8);
      STAMP(32);
      {
        const u32x4 z = {0u, 0u, 0u, 0u};
        if (tid < 128) *(u32x4*)(lds + (tid >> 5) * CH_BYTES + (tid & 31) * 16) = z;
        else if (tid < 152) {
          const int i = (tid - 128) >> 2;
          *(u32x4*)(lds + (tid & 3) * CH_BYTES + U_BYTES + (i < 4 ? i * G_COPY : (i & 1 ? 4128 : 16))) = z;
        }
        const LAS char* a0r = (const LAS char*)(lds + L_A0) + w * 6144 + (l & 3) * 512;
#define A0R(k) (a0r + ((((l >> 2) ^ (4 * ((l + (k)) & 3))) * 16)))
        unsigned go = WS_KVT + (unsigned)(quad / 5) * 81920u + (unsigned)sl * 16384u + (unsigned)(l >> 2) * 1024u + (unsigned)w * 64u + (unsigned)(l & 3) * 16u;
        const LAS char* r0 = A0R(0), * r1 = A0R(1), * r2 = A0R(2);
        const LAS char* rsh = (const LAS char*)(lds + L_A0) + w * 6144 + 5120 + (l >> 2) * 64;
        asm volatile("" : "+v"(go), "+v"(r0), "+v"(r1), "+v"(r2), "+v"(rsh));
        SCHED();
#pragma unroll
        for (int j = 0; j < 2; ++j)
          __builtin_amdgcn_raw_ptr_buffer_load_lds(rf, (LAS void*)(lds + (w >> 1) * CH_BYTES + U_BYTES + 32 + (2 * (w & 1) + j) * 1024), 16, l16,
                                                   (unsigned)quad * 16384u + (unsigned)(2 * w + j) * 1024u, 0, 0);
        STAMP(34);
        SCHED();
        VMW(7);
        f32x2 wt[4][4];
#pragma unroll
        for (int e = 0; e < 4; ++e) {
          const u32x4 sh = *(const volatile LAS u32x4*)(rsh + 16 * e);
          wt[e][0] = (f32x2){bflo(sh[0]), bflo(sh[2])}; wt[e][1] = (f32x2){bfhi(sh[0]), bfhi(sh[2])};
          wt[e][2] = (f32x2){bflo(sh[1]), bflo(sh[3])}; wt[e][3] = (f32x2){bfhi(sh[1]), bfhi(sh[3])};
        }
#pragma unroll
        for (int e = 0; e < 4; ++e) asm volatile("" : "+v"(wt[e][0]), "+v"(wt[e][1]), "+v"(wt[e][2]), "+v"(wt[e][3]));
        asm volatile("" : "+s"(dd2));
        SCHED();
        VMW(2);
        STAMP(35);
        u32x4 ka[5], va[2];
        ka[0] = *(const volatile LAS u32x4*)(r0 + 256);
        ka[1] = *(const volatile LAS u32x4*)(r1 + 512);
        ka[2] = *(const volatile LAS u32x4*)(r1 + 768);
        ka[3] = *(const volatile LAS u32x4*)(r2 + 1024);
        ka[4] = *(const volatile LAS u32x4*)(r2 + 1280);
        va[0] = *(const volatile LAS u32x4*)(r0 + 3072);
        va[1] = *(const volatile LAS u32x4*)(r0 + 3328);
        SCHED();
        if (blk == 0) {
          if ((l & 3) < 2) ka[0] = z;
          if ((l & 3) == 0) { ka[1] = z; ka[2] = z; }
        }
        u32x4 ko;
#pragma unroll
        for (int e = 0; e < 4; ++e) {
          const f32x2 w0 = wt[e][0], w1 = wt[e][1], w2 = wt[e][2], w3 = wt[e][3];
          const f32x2 km3 = bf2(ka[0][e]), km2 = bf2(ka[1][e]), km1 = bf2(ka[2][e]), k0 = bf2(ka[3][e]), k1 = bf2(ka[4][e]);
          const f32x2 s0 = __builtin_elementwise_fma(k0, w0, __builtin_elementwise_fma(km1, w1, __builtin_elementwise_fma(km2, w2, km3 * w3)));
          const f32x2 s1 = __builtin_elementwise_fma(k1, w0, __builtin_elementwise_fma(k0, w1, __builtin_elementwise_fma(km1, w2, km2 * w3)));
          const f32x2 o0 = s0 * bf2(va[0][e]), o1 = s1 * bf2(va[1][e]);
          ko[2 * (e & 1)] = pack2(o1[0], o0[0]); ko[2 * (e & 1) + 1] = pack2(o1[1], o0[1]);
          if (e & 1) st16(rs, go + (unsigned)(e >> 1) * 512u, ko);
          SCHED();
        }
      }
    }
  }
  const int R = (xcd & 3) * 8 + (idx & 7);
  const int c = (xcd >> 2) * 4 + (idx >> 3);
  const int n16 = l & 15, h4 = l >> 4;
  const rsrc_t rq = __builtin_amdgcn_make_buffer_rsrc((void*)p.q, 0, S_ * D_ * 2, 0x00020000);
  const rsrc_t rsp = __builtin_amdgcn_make_buffer_rsrc((void*)p.w_sp, 0, S_ * S_ * 2, 0x00020000);
  const int wj = w < 7 ? w : 0;
  u32x4 wsp[4][8];
  const int wk = w ^ (dg == 5 || dg == 6 ? 4 : 0);
  const unsigned wso = (unsigned)(R * 64) * 4096u + (unsigned)wk * 512u;
  const unsigned wdo = (unsigned)(h4 * 8192 + ((n16 ^ h4) * 16));
#define WVO(q) (wdo ^ (unsigned)(((4 - (q)) & 3) * 64))
#define WSO(g, q) (wso + (unsigned)((g) >> 2) * 256u + (unsigned)(32 * (((g) >> 1) & 1) + ((g) & 1) + 8 * (q)) * 4096u)
  u32x4 raw[NRAW];
#define LDW(i) raw[i] = __builtin_amdgcn_raw_buffer_load_b128(rsp, WVO((i) & 3), WSO((i) >> 2, (i) & 3), 0)
  const int trow = R * 64 + 32 * ((w >> 1) & 1) + 2 * n16 + (w & 1);
  unsigned bs_raw;
  unsigned tag;
  unsigned fw2 = OOB;
  unsigned e_fw1;
  const unsigned fw1o = WS_FW1 + (unsigned)(((8 + c) * 8 + wj) * 4 + (l & 3)) * 128u;

  if (__builtin_expect(is_conv, 1)) {
    STAMP(1);
    DRAIN();
    tag = __builtin_amdgcn_readfirstlane(epv) * 2654435761u + 0x9E3779B9u;
    BAR();
    st4(rs, tid == 0 ? WS_FK + (unsigned)quad * 128u : OOB, tag);
    unsigned atv, ato;
    const unsigned long long atb = (unsigned long long)(w < 5 ? p.w_in : p.w_out);
    {
      const int i = w < 5 ? (cidx * 5 + w) * 20 + (l < 19 ? l : 19) : (cidx * 3 + w - 5) * 19 + (l < 18 ? l : 18);
      const int r = i / 3;
      ato = w < 5 ? (unsigned)((r < 639 ? r : 639) * (F_ * 2) + xcd * CB * 2 + (i - 3 * r) * 108) : (unsigned)(xcd * CB * D_ * 2 + (i < 1079 ? i : 1079) * 128);
      asm volatile("" : "+v"(ato));
    }
    {
      u32x4 d[3];
      u32x2 n[3];
#pragma unroll
      for (int j = 0; j < 3; ++j) {
        char* G = j < 2 ? lds + ((tid >> 8) + 2 * j) * CH_BYTES + U_BYTES + ((tid & 255) + 2) * 16 : lds + (tid & 3) * CH_BYTES + U_BYTES + 16;
        d[j] = *(const u32x4*)G;
        n[j] = *(const u32x2*)(G + 16);
      }
#pragma unroll
      for (int j = 0; j < 3; ++j) {
        char* G = j < 2 ? lds + ((tid >> 8) + 2 * j) * CH_BYTES + U_BYTES + ((tid & 255) + 2) * 16 : lds + (tid & 3) * CH_BYTES + U_BYTES + 16;
        const unsigned a0 = __builtin_amdgcn_alignbit(d[j][1], d[j][0], 16), a1 = __builtin_amdgcn_alignbit(d[j][2], d[j][1], 16),
                       a2 = __builtin_amdgcn_alignbit(d[j][3], d[j][2], 16), a3 = __builtin_amdgcn_alignbit(n[j][0], d[j][3], 16),
                       a4 = __builtin_amdgcn_alignbit(n[j][1], n[j][0], 16);
        if (j < 2 || tid < 4) {
          *(u32x4*)(G + G_COPY) = (u32x4){a0, a1, a2, a3};
          *(u32x4*)(G + 2 * G_COPY) = (u32x4){d[j][1], d[j][2], d[j][3], n[j][0]};
          *(u32x4*)(G + 3 * G_COPY) = (u32x4){a1, a2, a3, a4};
        }
      }
    }
    const int ch = w & 3, hf = w >> 2;
    const LAS char *ap, *bp;
    {
      const char* U = lds + ch * CH_BYTES;
      const char* G = U + U_BYTES;
      const int a = n16, jj = h4 >> 1, e2 = h4 & 1;
      const int sg = (a + 1) & 3;
      bp = (const LAS char*)U + 512 + 32 * (a - jj) + 16 * e2 - 256 * hf - 192;
      ap = (const LAS char*)G + sg * G_COPY + 2 * (16 * jj + a - 8 * e2 + 9 - sg) + 256 * hf;
      asm volatile("" : "+v"(ap), "+v"(bp));
    }
    STAMP(36);
    {
      wait_flag<1>(rs, WS_FK + (unsigned)(5 * (4 * w + (l & 3)) + (quad >> 5)) * 128u, tag);
      STAMP(29);
      const unsigned gp = WS_KVT + (unsigned)(4 * w + (l >> 5)) * 81920u + (unsigned)quad * 512u + (unsigned)(l & 31) * 16u;
      const u32x4 g0 = ld16(rs, gp), g1 = ld16(rs, gp + 163840u);
      SCHED();
      char* Uw = lds + 512 + (128 * w + (l & ~3) + 3 - (l & 3)) * 4;
#pragma unroll
      for (int i = 0; i < 4; ++i) *(unsigned*)(Uw + i * CH_BYTES) = g0[i];
#pragma unroll
      for (int i = 0; i < 4; ++i) *(unsigned*)(Uw + 256 + i * CH_BYTES) = g1[i];
    }
    STAMP(28);
    BAR();
    STAMP(2);
    const int te = 2 * tid;
    u32x4 qa[2];
    u32x4 sv[2];
    u32x2 mg;
    const unsigned xo = (unsigned)(tid >> 4) * WX1P + (unsigned)quad * 256u + (unsigned)(tid & 15) * 16u;
    const unsigned mgo = WS_MOD + (unsigned)((l < 12 ? (l >> 2) * D_ + (l & 3) : 0) + quad * 4) * 8u;
    auto ld_sv = [&]() {
      sv[0] = ld16(rs, WS_ST1 + (unsigned)tid * 16u);
      sv[1] = ld16(rs, WS_ST1 + (unsigned)tid * 16u + 8192u);
    };
    auto ld_q = [&]() {
      qa[0] = ld16(rs, WS_QB + xo);
      qa[1] = ld16(rs, WS_QB + xo + 32u * WX1P);
    };
    f32x2 hh[2][2][2], base[2][2][2];
    auto prep = [&]() {
      const LAS char* ur = (const LAS char*)(lds + 512) + ((te & ~7) + 6 - (te & 7)) * 2;
      unsigned uu[2][4];
#pragma unroll
      for (int i = 0; i < 8; ++i) uu[i >> 2][i & 3] = *(const volatile LAS unsigned*)(ur + (i >> 2) * 2048 + (i & 3) * CH_BYTES);
      if (!__all(((sv[0][0] ^ tag) | (sv[0][2] ^ tag) | (sv[1][0] ^ tag) | (sv[1][2] ^ tag)) == 0u)) {
        _Pragma("unroll 1") for (unsigned spins = 0; spins < SPIN_CAP; ++spins) {
          ld_sv();
          if (__all(sv[0][0] == tag && sv[0][2] == tag && sv[1][0] == tag && sv[1][2] == tag)) break;
          __builtin_amdgcn_s_sleep(4);
        }
        asm volatile("" ::: "memory");
        ld_q();
        mg = ld8(rs, mgo);
        DRAIN();
      }
      if (!__all(mg[0] == tag)) {
        _Pragma("unroll 1") for (unsigned spins = 0; spins < SPIN_CAP; ++spins) {
          mg = ld8(rs, mgo);
          if (__all(mg[0] == tag)) break;
          __builtin_amdgcn_s_sleep(2);
        }
      }
      f32x2 cc2[2], ss2[2], dv2[2];
      float cch[4], sch[4];
#pragma unroll
      for (int i = 0; i < 4; ++i) {
        const float sa = __uint_as_float(__builtin_amdgcn_readlane(mg[1], i)), ca = __uint_as_float(__builtin_amdgcn_readlane(mg[1], 4 + i)),
                    ga = __uint_as_float(__builtin_amdgcn_readlane(mg[1], 8 + i));
        cch[i] = ga * rbf(1.0f + ca);
        sch[i] = ga * sa;
      }
      cc2[0] = (f32x2){cch[0], cch[1]}; cc2[1] = (f32x2){cch[2], cch[3]};
      ss2[0] = (f32x2){sch[0], sch[1]}; ss2[1] = (f32x2){sch[2], sch[3]};
      dv2[0] = bf2(dd2[0]); dv2[1] = bf2(dd2[1]);
#pragma unroll
      for (int H = 0; H < 2; ++H)
#pragma unroll
        for (int e = 0; e < 2; ++e) {
          const unsigned st = sv[H][1 + 2 * e];
          const float a = __builtin_amdgcn_rcpf(bfhi(st)), b = -bflo(st) * a;
#pragma unroll
          for (int i = 0; i < 2; ++i) {
            const f32x2 q2 = bf2(qa[H][2 * e + i]);
            const f32x2 u2 = e ? (f32x2){bflo(uu[H][2 * i]), bflo(uu[H][2 * i + 1])} : (f32x2){bfhi(uu[H][2 * i]), bfhi(uu[H][2 * i + 1])};
            const f32x2 n1 = __builtin_elementwise_fma(q2, (f32x2){a, a}, (f32x2){b, b});
            hh[H][e][i] = __builtin_elementwise_fma(n1, cc2[i], ss2[i]);
            base[H][e][i] = __builtin_elementwise_fma(hh[H][e][i], u2 * dv2[i], q2);
            asm volatile("" : "+v"(hh[H][e][i]), "+v"(base[H][e][i]));
          }
        }
    };
    auto HF = [&]() { int x = hf; asm volatile("" : "+s"(x)); return x; };
    {
      const int a = n16;
      char* yp = lds + L_YP + (hf * 4 + ch) * 8192 + (h4 * 16 + ((a + 4 * h4) & 15)) * 16;
      f32x4 acc[8];
      u32x4 A[2][8], B[2][8];
      auto rd = [&](int k) {
        const int r = k / 24, j = k % 24;
        if (j >= 2 && j < 10) B[r & 1][9 - j] = *(const volatile LAS u32x4*)(bp + 512 * (9 - j) + 64 * (3 - r));
        else {
          const int pp = j < 2 ? 0 : (j - 8) >> 1;
          const u32x2 v = *(const volatile LAS u32x2*)(ap + 512 * pp + 64 * r + 8 * (j & 1));
          A[r & 1][pp][2 * (j & 1)] = v[0]; A[r & 1][pp][2 * (j & 1) + 1] = v[1];
        }
      };
#pragma unroll
      for (int k = 0; k < IC<conv_sched(CDIST).pre>::v; ++k) rd(k);
      SCHED();
      auto step = [&](auto ic) {
        constexpr int i = decltype(ic)::v, r = i / 36, pp = conv_sched(CDIST).pp[i], m = conv_sched(CDIST).m[i], k = conv_sched(CDIST).rd[i];
        if (i == 0) __builtin_amdgcn_s_setprio(3);
        if (i == 60) __builtin_amdgcn_s_setprio(2);
        if (i == 100) __builtin_amdgcn_s_setprio(1);
        if (i == 126) __builtin_amdgcn_s_setprio(0);
        acc[m + pp] = MFMA16(A[r & 1][pp], B[r & 1][m], i < 8 ? ZERO4 : acc[m + pp]);
        SCHED();
        if (k >= 0) rd(k);
        if (i == 0) atv = *(const GAS unsigned*)(atb + ato);
        if (i % 6 == 2 && i / 3 < NRAW) {
          if (!HF()) { LDW(i / 3); LDW(i / 3 + 1); }
        }
        if (i > Q_AT && (i - Q_AT) % 2 == 1 && i - Q_AT < NRAW) {
          if (HF()) { LDW(i - Q_AT - 1); LDW(i - Q_AT); }
        }
        if (i == SV_AT) { ld_sv(); bs_raw = ((const GAS u16*)p.b_sp)[trow]; }
        if (r == 3 && pp >= 1 && m == 7 - pp) *(f32x4*)(yp + (pp - 1) * 1024) = acc[pp - 1];
        SCHED();
      };
      static_for<0, Q_AT>(step);
      STAMP(3);
      DRAIN();
      asm volatile("" ::"v"(atv));
      ld_q();
      mg = ld8(rs, mgo);
      SCHED();
      STAMP(4);
      static_for<Q_AT, 144>(step);
      *(f32x4*)(yp + 7 * 1024) = acc[7];
      STAMP(5);
    }
    prep();
    e_fw1 = ld4(rs, fw1o);
    STAMP(6);
    const LAS char* yr = (const LAS char*)(lds + L_YP) + (te >> 8) * 1024 + (((te >> 2) & 3) * 16 + ((((te >> 4) & 15) + 4 * ((te >> 2) & 3)) & 15)) * 16 + (te & 3) * 4;
    asm volatile("" : "+v"(yr));
    BAR();
    STAMP(7);
    f32x2 y[2][8];
#pragma unroll
    for (int i = 0; i < 16; ++i) y[i >> 3][i & 7] = *(const volatile LAS f32x2*)(yr + (i >> 3) * 4096 + (i & 7) * 8192);
#pragma unroll
    for (int H = 0; H < 2; ++H) {
      u32x4 o;
#pragma unroll
      for (int e = 0; e < 2; ++e)
#pragma unroll
        for (int i = 0; i < 2; ++i) {
          const float x0 = __builtin_fmaf(hh[H][e][i][0], y[H][2 * i][e] + y[H][4 + 2 * i][e], base[H][e][i][0]);
          const float x1 = __builtin_fmaf(hh[H][e][i][1], y[H][2 * i + 1][e] + y[H][5 + 2 * i][e], base[H][e][i][1]);
          o[2 * e + i] = pack2(x0, x1);
        }
      st16(rs, WS_X1B + (unsigned)(32 * H) * WX1P + xo, o);
    }
    STAMP(8);
  } else {
    const rsrc_t rwi = __builtin_amdgcn_make_buffer_rsrc((void*)p.w_in, 0, D_ * F_ * 2, 0x00020000);
    const rsrc_t rwo = __builtin_amdgcn_make_buffer_rsrc((void*)p.w_out, 0, FH * D_ * 2, 0x00020000);
    STAMP(32);
    const int job = xcd + 8 * slot;
    const int T = job >= 50 && w < 3 && job - 50 + 46 * w < 128 ? job - 50 + 46 * w : -1;
    const bool unit = slot >= 8 || (slot >= 2 && slot < 6);
    const int ucb = slot >= 8 ? 8 + xcd : xcd, kg = (slot >= 8 ? slot : slot - 2) & 3;
    float* tab = (float*)(lds + L_TAB);
    float* red = (float*)(lds + L_RED);
    auto issue_unit = [&](int i) {
      const int g = i * 64 + l;
      __builtin_amdgcn_raw_ptr_buffer_load_lds(rwi, (LAS void*)(lds + L_J3 + i * 1024), 16, (unsigned)((g / 14) * (F_ * 2) + (g % 14) * 16),
                                               (unsigned)(kg * 160 * F_ * 2 + ucb * CB * 2), 0, 0);
    };
    if (T >= 0) {
      char* qt = lds + L_QT + w * 20480;
      const unsigned so = (unsigned)(16 * T) * 1280u;
#pragma unroll
      for (int n = 0; n < 16; ++n)
        __builtin_amdgcn_raw_ptr_buffer_load_lds(rq, (LAS void*)(qt + n * 1024), 16, l16 ^ (unsigned)(n * 16), so + (unsigned)(n * 1280), 0, 0);
#pragma unroll
      for (int j = 0; j < 4; ++j)
        __builtin_amdgcn_raw_ptr_buffer_load_lds(rq, (LAS void*)(qt + 16384 + j * 1024), 16, (unsigned)(h4 * 1280 + ((n16 ^ (4 * j + h4)) * 16)), so + (unsigned)(4 * j * 1280 + 1024), 0, 0);
      tag = __builtin_amdgcn_readfirstlane(epv) * 2654435761u + 0x9E3779B9u;
      STAMP(34);
      DRAIN();
      STAMP(35);
      const bf2_t one = {(__bf16)1.0f, (__bf16)1.0f};
      float s1[2] = {0.f, 0.f}, s2[2] = {0.f, 0.f};
      const int n0 = 2 * (l & 7);
      u32x4 qv[20];
#pragma unroll
      for (int i = 0; i < 20; ++i)
#pragma unroll
        for (int e = 0; e < 2; ++e) {
          const int n = n0 + e, ck = 4 * (i & 15) + h4;
          const u32x2 v = *(const volatile LAS u32x2*)(qt + (i < 16 ? n * 1024 : 16384 + n * 256) + ((ck ^ n) * 16) + (l & 8));
          qv[i][2 * e] = v[0]; qv[i][2 * e + 1] = v[1];
        }
#pragma unroll
      for (int i = 0; i < 20; ++i) {
        st16(rs, WS_QB + (unsigned)(T >> 1) * WX1P + (unsigned)i * 2048u + (unsigned)((l >> 3) * 256 + (T & 1) * 128 + (l & 7) * 16), qv[i]);
#pragma unroll
        for (int e = 0; e < 4; ++e) {
          const unsigned x = qv[i][e];
          const bf2_t v = __builtin_bit_cast(bf2_t, x);
          s1[e >> 1] = __builtin_amdgcn_fdot2_f32_bf16(v, one, s1[e >> 1], false);
          s2[e >> 1] = __builtin_amdgcn_fdot2_f32_bf16(v, v, s2[e >> 1], false);
        }
        SCHED();
      }
      u32x4 o = {tag, 0u, tag, 0u};
#pragma unroll
      for (int e = 0; e < 2; ++e) {
        const float t1 = sum_rows(s1[e] + dpp_f<0x128>(s1[e])), t2 = sum_rows(s2[e] + dpp_f<0x128>(s2[e]));
        const float mean = t1 * (1.0f / D_);
        const float var = __builtin_fmaxf(t2 * (1.0f / D_) - mean * mean, 0.f);
        o[2 * e + 1] = pack2(mean, __builtin_amdgcn_sqrtf(rbf(rbf(var) + 1e-5f)));
      }
      STAMP(1);
      DRAIN();
      if (l < 8) st16(rs, WS_ST1 + (unsigned)T * 128u + l16, o);
      STAMP(3);
    }
    if (job < 60) {
      const int grp = job < 10 ? 40 + job : (job < 50 ? job - 10 : job);
      if (job >= 50 && T < 0) __builtin_amdgcn_s_sleep(LATE_SLEEP);
      if (job >= 10 && T < 0) __builtin_amdgcn_s_sleep(AUX_SLEEP);
      const unsigned cv = *(const GAS unsigned*)((const GAS u16*)p.cvec + 80 * w + 2 * (l < 40 ? l : 0));
      u32x4 pre[10];
#pragma unroll
      for (int pp = 0; pp < 10; ++pp) pre[pp] = *(const GAS u32x4*)((const GAS u16*)p.w_mod + (size_t)(80 * w + pp * 8 + (l >> 3)) * 3840 + grp * 64 + (l & 7) * 8);
      SCHED();
      STAMP(34);
      tag = __builtin_amdgcn_readfirstlane(epv) * 2654435761u + 0x9E3779B9u;
      if (l < 40) {
        const float c0 = bflo(cv), c1 = bfhi(cv);
        *(f32x2*)(tab + w * 80 + 2 * l) = (f32x2){rbf(c0 * __builtin_amdgcn_rcpf(1.0f + __expf(-c0))), rbf(c1 * __builtin_amdgcn_rcpf(1.0f + __expf(-c1)))};
      }
      asm volatile("s_waitcnt lgkmcnt(0)" ::: "memory");
      f32x2 acc[4];
#pragma unroll
      for (int e = 0; e < 4; ++e) acc[e] = (f32x2){0.f, 0.f};
#pragma unroll
      for (int pp = 0; pp < 10; ++pp) {
        const float sv = tab[w * 80 + pp * 8 + (l >> 3)];
#pragma unroll
        for (int e = 0; e < 4; ++e) acc[e] = __builtin_elementwise_fma((f32x2){sv, sv}, bf2(pre[pp][e]), acc[e]);
      }
      STAMP(35);
      asm volatile("" : "+v"(acc[0]), "+v"(acc[1]), "+v"(acc[2]), "+v"(acc[3]));
#pragma unroll
      for (int e = 0; e < 8; ++e) { float a = acc[e >> 1][e & 1]; a += dpp_f<0x128>(a); acc[e >> 1][e & 1] = sum_rows(a); }
      if (l < 8) {
        *(f32x4*)(red + w * 64 + l * 8) = (f32x4){acc[0][0], acc[0][1], acc[1][0], acc[1][1]};
        *(f32x4*)(red + w * 64 + l * 8 + 4) = (f32x4){acc[2][0], acc[2][1], acc[3][0], acc[3][1]};
      }
      if (w == 7) __builtin_amdgcn_s_setprio(2);
      BAR();
      if (w == 7) {
        float t = 0.f;
#pragma unroll
        for (int r = 0; r < 8; ++r) t += red[r * 64 + l];
        st8(rs, WS_MOD + (unsigned)(grp * 64 + l) * 8u, (u32x2){tag, __float_as_uint(rbf(t))});
        __builtin_amdgcn_s_setprio(0);
      } else if (unit) {
#pragma unroll
        for (int k = 0; k < 5; ++k) issue_unit(w * 5 + k);
      }
    } else if (unit && w >= 3 && w < 7) {
      __builtin_amdgcn_s_sleep(AUX_SLEEP);
#pragma unroll
      for (int k = 0; k < 9; ++k) issue_unit((w - 3) * 9 + k < 35 ? (w - 3) * 9 + k : 34);
    }
    tag = __builtin_amdgcn_readfirstlane(epv) * 2654435761u + 0x9E3779B9u;
    if (unit) {
      auto mod_tab = [&](int which, float add, float* dst) {
        const unsigned o = WS_MOD + (unsigned)(which * D_ + kg * 160) * 8u;
        u32x4 g0, g1;
        _Pragma("unroll 1") for (unsigned spins = 0; spins < SPIN_CAP; ++spins) {
          g0 = ld16(rs, o + l16);
          g1 = ld16(rs, o + 1024u + (l16 & 255u));
          if (__all(g0[0] == tag && g0[2] == tag && g1[0] == tag && g1[2] == tag)) break;
          __builtin_amdgcn_s_sleep(2);
        }
        *(f32x2*)(dst + 2 * l) = (f32x2){rbf(add + __uint_as_float(g0[1])), rbf(add + __uint_as_float(g0[3]))};
        if (l < 16) *(f32x2*)(dst + 128 + 2 * l) = (f32x2){rbf(add + __uint_as_float(g1[1])), rbf(add + __uint_as_float(g1[3]))};
      };
      if (w == 7) {
        STAMP(1);
        if (slot >= 8) __builtin_amdgcn_s_sleep(64);
        mod_tab(4, 1.0f, tab);
        BAR();
        STAMP(2);
        mod_tab(3, 0.0f, tab + 160);
        BAR();
      } else {
        DRAIN();
        STAMP(36);
        BAR();
        STAMP(2);
        const char* bp = lds + L_J3 + (4 * h4 + (n16 >> 2)) * 224 + (n16 & 3) * 8 + w * 32;
        const float* tp = tab + 4 * h4;
        u32x4 f3[5];
        float cs = 0.f, b1 = 0.f;
        const bool pad = w == 6 && n16 >= 12;
        const unsigned padv = (n16 == 12 && ucb >= 8) ? 0x3f803f80u : 0u;
#pragma unroll
        for (int k = 0; k < 5; ++k) f3[k] = trfrag(bp + k * 7168, 16 * 224);
#pragma unroll
        for (int k = 0; k < 5; ++k) {
          const f32x4 ca = *(const f32x4*)(tp + k * 32), cb4 = *(const f32x4*)(tp + k * 32 + 16);
          u32x4 o;
#pragma unroll
          for (int e = 0; e < 4; ++e) {
            const float c0 = e < 2 ? ca[2 * e] : cb4[2 * e - 4], c1 = e < 2 ? ca[2 * e + 1] : cb4[2 * e - 3];
            const float p0 = bflo(f3[k][e]) * c0, p1 = bfhi(f3[k][e]) * c1;
            cs += p0 + p1;
            o[e] = pad ? padv : pack2(p0, p1);
          }
          st16(rs, WS_WINF + (unsigned)(ucb * 7 + w) * 20480u + (unsigned)(kg * 5 + k) * 1024u + l16, o);
        }
        cs = sum_rows(cs);
        DRAIN();
        if (l == 0) st4(rs, WS_FW1 + (unsigned)((ucb * 8 + w) * 4 + kg) * 128u, tag);
        STAMP(5);
        BAR();
#pragma unroll
        for (int k = 0; k < 5; ++k) {
          const f32x4 sa = *(const f32x4*)(tp + 160 + k * 32), sb4 = *(const f32x4*)(tp + 160 + k * 32 + 16);
#pragma unroll
          for (int e = 0; e < 4; ++e) {
            const float s0 = e < 2 ? sa[2 * e] : sb4[2 * e - 4], s1 = e < 2 ? sa[2 * e + 1] : sb4[2 * e - 3];
            b1 = __builtin_fmaf(s0, bflo(f3[k][e]), __builtin_fmaf(s1, bfhi(f3[k][e]), b1));
          }
        }
        b1 = sum_rows(b1);
        if (pad) { cs = padv ? 160.0f : 0.0f; b1 = 0.0f; }
        if (l < 16) st16(rs, WS_CS + (unsigned)((ucb * 4 + kg) * 112 + w * 16 + l) * 16u, (u32x4){tag, __float_as_uint(cs), tag, __float_as_uint(b1)});
      }
    } else {
      if (job >= 60 && T < 0) __builtin_amdgcn_s_sleep(100);
      if (slot < 2 && w >= 2 && w < 6) {
        const int c5 = slot * 4 + w - 2, c2 = xcd;
        char* st = lds + L_J5 + (w - 2) * 18432;
#pragma unroll
        for (int r = 0; r < 18; ++r) {
          const int g = r * 64 + l;
          __builtin_amdgcn_raw_ptr_buffer_load_lds(rwo, (LAS void*)(st + r * 1024), 16, (unsigned)((g / 10) * (D_ * 2) + (g % 10) * 16),
                                                   (unsigned)(xcd * CB * D_ * 2 + c5 * 160), 0, 0);
        }
        DRAIN();
        const char* bp = st + (4 * h4 + (n16 >> 2)) * 160 + (n16 & 3) * 8;
        const unsigned base = WS_WOUTF + (unsigned)c5 * WBLK;
#pragma unroll
        for (int dj = 0; dj < 5; ++dj) {
          st16(rs, base + (unsigned)c2 * 10240u + (unsigned)dj * 1024u + l16, trfrag(bp + dj * 32, 32 * 160));
          st16(rs, base + (unsigned)c2 * 10240u + (unsigned)(5 + dj) * 1024u + l16, trfrag(bp + 16 * 160 + dj * 32, 32 * 160));
          st16(rs, base + 81920u + (unsigned)c2 * 5120u + (unsigned)dj * 1024u + l16, trfrag(bp + 64 * 160 + dj * 32, 16 * 160));
          st8(rs, base + 122880u + (unsigned)((c2 >> 1) * 5 + dj) * 1024u + (unsigned)(c2 & 1) * 512u + (unsigned)l * 8u, trd(bp + 96 * 160 + dj * 32));
        }
        fw2 = WS_FW2 + (unsigned)(c5 * 8 + xcd) * 128u;
      }
    }
    STAMP(4);
#pragma unroll
    for (int i = 0; i < NRAW; ++i) LDW(i);
    bs_raw = ((const GAS u16*)p.b_sp)[trow];
    e_fw1 = ~tag;
    asm volatile(".Lmega_aux_end:");
    asm volatile("" ::"v"(cpf));
    STAMP(8);
  }

  LAS char* xa = (LAS char*)(lds + L_XI) + l * 16;
  LAS char* xb = xa + 65536;
  asm volatile("" : "+v"(xb));
#define LDA(ks, rt) (*(const volatile LAS u32x4*)((ks) < 16 ? xa + ((ks) * 4 + (rt)) * 1024 : xb + (((ks) - 16) * 4 + (rt)) * 1024))
  auto lda1 = [&](int ks, u32x4* A) {
#pragma unroll
    for (int rt = 0; rt < 4; ++rt) A[rt] = LDA(ks, rt);
  };
  const LAS char* tb4 = (const LAS char*)(lds + L_MISC) + h4 * 16;
  const LAS char* tbn = (const LAS char*)(lds + L_MISC) + n16 * 4;
  asm volatile("" : "+v"(tb4), "+v"(tbn));
  u32x4 xr[5][2];
  auto ld_x1 = [&](int m0, int m1) {
#pragma unroll
    for (int m = m0; m < m1; ++m) {
      if (m == 0) __builtin_amdgcn_s_setprio(2);
      if (m == 1) __builtin_amdgcn_s_setprio(1);
      if (m == 3) __builtin_amdgcn_s_setprio(0);
#pragma unroll
      for (int e = 0; e < 2; ++e)
        xr[m][e] = ld16(rs, l16, WS_X1B + (unsigned)(R * 2 + (w & 1)) * WX1P + (unsigned)(4 * m + (w >> 1)) * 2048u + (unsigned)e * 1024u);
      SCHED();
    }
  };
  LAS char* xw = xa + w * 2048;
  auto put_x1 = [&](int m) {
    LAS char* d = m < 4 ? xw + m * 16384 : xb + w * 2048;
#pragma unroll
    for (int e = 0; e < 2; ++e) {
      *(volatile LAS u32x2*)(d + 8 * e) = (u32x2){xr[m][e][0], xr[m][e][1]};
      *(volatile LAS u32x2*)(d + 1024 + 8 * e) = (u32x2){xr[m][e][2], xr[m][e][3]};
    }
  };
  auto poison_u = [&]() {
#pragma unroll
    for (int k = 0; k < 2; ++k) {
      const unsigned o = (unsigned)(wj * 2 + k) * 1024u + l16;
      const unsigned rt = o / 3584u, r = o - rt * 3584u;
      st16(rs, WS_UF + ((unsigned)(R * 4) + rt) * 8u * 3584u + (unsigned)c * 3584u + r, (u32x4){~0u, ~0u, ~0u, ~0u});
    }
  };
  const unsigned wob = WS_WOUTF + (unsigned)c * WBLK;
  const LAS char* wrd = (const LAS char*)(lds + L_WS) + w * 8192 + n16 * 256 + ((h4 ^ ((4 - (n16 >> 2)) & 3)) * 64);
#define DMA_W(g, q) __builtin_amdgcn_raw_ptr_buffer_load_lds(rsp, (LAS void*)(lds + L_WS + w * 8192 + (((g) + 1) & 1) * 4096 + (q) * 1024), 16, WVO(q), WSO(g, q), 0, 0)
#define RD_W(g) do { _Pragma("unroll") for (int i_ = 0; i_ < 4; ++i_) \
                      wsp[(g) & 3][((g) >> 2) * 4 + i_] = *(const volatile LAS u32x4*)(wrd + (((g) + 1) & 1) * 4096 + ((i_ ^ (n16 & 3)) * 16)); } while (0)
  auto park = [&]() {
    LAS char* d = (LAS char*)(lds + L_WS) + w * 8192 + l * 16;
#pragma unroll
    for (int g = 0; g < NRAW / 4; ++g) {
#pragma unroll
      for (int q = 0; q < 4; ++q) *(volatile LAS u32x4*)(d + ((g + 1) & 1) * 4096 + q * 1024) = raw[4 * g + q];
      if (g < 2) RD_W(g);
    }
    asm volatile("s_waitcnt lgkmcnt(0)" ::: "memory");
  };
#define DMA_AB(k) dma1k(rs, lds + L_AB + ((k) * 8 + w) * 1024, l16, wob + (unsigned)((k) * 8 + w) * 1024u)
  const int rto = w & 3, cls = w >> 2;
  u32x2 xp[3];
  constexpr int W = G2W;
  const unsigned zb = WS_Z2F + (unsigned)c * 458752u + (unsigned)wk * 8192u;
  const unsigned fzo = WS_FZ + (unsigned)(c * 32 + wk * 4 + (l & 3)) * 128u;
  u32x4 fr[W];
#define LDZ(i) fr[(i) % W] = ld16(rs, l16, zb + (unsigned)((i) >> 3) * 65536u + (unsigned)((i) & 7) * 1024u)
  u32x4 cr[5];
#define LDCR(k) cr[k] = ld16(rs, l16, wob + 81920u + (unsigned)(w * 5 + (k)) * 1024u)
  auto rd_xp = [&]() {
#pragma unroll
    for (int j = 0; j < 3; ++j) {
      const int q4 = 5 * c + (cls ? (3 + (j < 2 ? j : 1)) : j);
      xp[j] = *(const volatile LAS u32x2*)(xa + ((q4 >> 1) * 4 + rto) * 1024 + (q4 & 1) * 8);
    }
  };
  if (w < 7) {
    u32x4 B2[20], B1[20];
    const unsigned b2o = WS_WINF + (unsigned)((8 + c) * 7 + w) * 20480u, b1o = WS_WINF + (unsigned)(c * 7 + w) * 20480u;
#define LDB2(ks) B2[ks] = ld16(rs, l16, b2o + (unsigned)(ks) * 1024u)
#define LDB1(ks) B1[ks] = ld16(rs, l16, b1o + (unsigned)(ks) * 1024u)
    const bool early = __all(e_fw1 == tag);
    asm volatile("" ::: "memory");
    {
      const unsigned le = early ? l16 : OOB;
#pragma unroll
      for (int ks = 0; ks < NB2A; ++ks) B2[ks] = ld16(rs, le, b2o + (unsigned)ks * 1024u);
      SCHED();
      if (is_conv) VMW(NB2A);
    }
    STAMP(33);
    BAR();
    STAMP(37);
    if (!early) {
      wait_flag<4>(rs, fw1o, tag);
      DRAIN();
      st4(rs, l == 0 ? fw2 : OOB, tag);
#pragma unroll
      for (int ks = 0; ks < NB2A; ++ks) LDB2(ks);
    }
#pragma unroll
    for (int ks = NB2A; ks < 20; ++ks) {
      LDB2(ks);
      if (ks == 12) { SCHED(); park(); SCHED(); }
      if (ks == 13) { SCHED(); __builtin_amdgcn_s_barrier(); SCHED(); }
    }
    STAMP(9);
    BAR();
    STAMP(10);
    ld_x1(0, 3);
    SCHED();
#pragma unroll
    for (int m = 0; m < 3; ++m) put_x1(m);
    ld_x1(3, 5);
    poison_u();
    SCHED();
    if (w == 6) __builtin_amdgcn_s_setprio(2);
    BAR();
    STAMP(11);
    f32x4 acc[4];
    u32x4 A[2][4];
    lda1(0, A[0]);
#pragma unroll
    for (int ks = 0; ks < 20; ++ks) {
      if (ks == 6) put_x1(3);
      if (ks == 8) put_x1(4);
      if (ks == 11) BAR();
      if (ks >= 4) LDB1(ks - 4);
      if (ks >= 16) LDB1(ks);
      if (ks == 12) RD_W(2);
      SCHED();
#pragma unroll
      for (int rt = 0; rt < 4; ++rt) {
        if (ks < 19) A[(ks + 1) & 1][rt] = LDA(ks + 1, rt);
        SCHED();
        acc[rt] = MFMA16(A[ks & 1][rt], B2[ks], ks ? acc[rt] : ZERO4);
        SCHED();
      }
    }
    STAMP(12);
    if (w == 6) {
      if (n16 == 12) {
#pragma unroll
        for (int rt = 0; rt < 4; ++rt) *(f32x4*)(lds + L_S1 + (rt * 16 + 4 * h4) * 4) = acc[rt];
      }
      asm volatile("" ::: "memory");
      *(volatile LAS unsigned*)(lds + L_S1 + 256) = tag;
      __builtin_amdgcn_s_setprio(0);
    }
    VMW(20);
    BAR();
    STAMP(13);
    f32x2 zz[8];
    f32x4 cs1a, cs1b;
    {
      f32x4 rs4[4], nm4[4];
#pragma unroll
      for (int rt = 0; rt < 4; ++rt) {
        rs4[rt] = *(const LAS f32x4*)(tb4 + (L_T2 - L_MISC) + rt * 64);
        nm4[rt] = *(const LAS f32x4*)(tb4 + (L_T2 - L_MISC) + 256 + rt * 64);
      }
      const f32x2 cs2 = *(const f32x2*)(lds + L_CS1 + (112 + 16 * w + n16) * 8);
      const f32x2 csv = {cs2[0], cs2[0]}, b1v = {cs2[1], cs2[1]};
      float v1[16], v2[16];
#pragma unroll
      for (int i = 0; i < 8; ++i) {
        const int rt = i >> 1;
        const f32x2 a2 = i & 1 ? HI2(acc[rt]) : LO2(acc[rt]), n2 = i & 1 ? HI2(nm4[rt]) : LO2(nm4[rt]), r2 = i & 1 ? HI2(rs4[rt]) : LO2(rs4[rt]);
        zz[i] = gelu2(__builtin_elementwise_fma(__builtin_elementwise_fma(n2, csv, a2), r2, b1v));
        asm volatile("" : "+v"(zz[i]));
        DMA_W(3 + (i >> 2), i & 3);
        const f32x2 sq = zz[i] * zz[i];
        v1[2 * i] = zz[i][0]; v1[2 * i + 1] = zz[i][1]; v2[2 * i] = sq[0]; v2[2 * i + 1] = sq[1];
      }
      const float t1 = tr_sum16(v1, l);
      const float t2 = tr_sum16(v2, l);
      *(f32x2*)(lds + L_Z1S + (w * 64 + (n16 >> 2) * 16 + 4 * h4 + (n16 & 3)) * 8) = (f32x2){t1, t2};
      lda1(0, A[0]);
      cs1a = *(const f32x4*)(lds + L_CS1 + (16 * w + 4 * h4) * 8);
      cs1b = *(const f32x4*)(lds + L_CS1 + (16 * w + 4 * h4) * 8 + 16);
    }
    BAR();
    STAMP(14);
#pragma unroll
    for (int ks = 0; ks < 20; ++ks) {
      if (ks == Z1SPLIT) {
        STAMP(15);
        BAR();
        STAMP(16);
        f32x4 ra[4], rb[4];
#pragma unroll
        for (int rt = 0; rt < 4; ++rt) {
          ra[rt] = *(const LAS f32x4*)(tb4 + (L_ST2 - L_MISC) + rt * 64);
          rb[rt] = *(const LAS f32x4*)(tb4 + (L_ST2 - L_MISC) + 256 + rt * 64);
        }
#pragma unroll
        for (int i = 0; i < 8; ++i) {
          zz[i] = __builtin_elementwise_fma(zz[i], i & 1 ? HI2(ra[i >> 1]) : LO2(ra[i >> 1]), i & 1 ? HI2(rb[i >> 1]) : LO2(rb[i >> 1]));
          asm volatile("" : "+v"(zz[i]));
        }
        const u32x4 o0 = {pack2(zz[0][0], zz[2][0]), pack2(zz[0][1], zz[2][1]), pack2(zz[1][0], zz[3][0]), pack2(zz[1][1], zz[3][1])};
        const u32x4 o1 = {pack2(zz[4][0], zz[6][0]), pack2(zz[4][1], zz[6][1]), pack2(zz[5][0], zz[7][0]), pack2(zz[5][1], zz[7][1])};
        const unsigned zo = WS_Z2F + (unsigned)((c * 7 + w) * 64 + 4 * (R >> 1) + h4) * 1024u + (unsigned)(R & 1) * 512u + (unsigned)n16 * 16u;
        st16(rs, zo, o0);
        st16(rs, zo + 256u, o1);
        SCHED();
      }
      if (ks == 0) { VMW(4); RD_W(3); }
      if (ks == 2) { VMW(0); RD_W(4); }
      if (ks == 8) { VMW(4 + 2); RD_W(5); }
      if (ks == 11) rd_xp();
      if (ks == 13) { VMW(4 + 5 + 2); RD_W(6); }
      if (ks >= 13 && ks < 19) DMA_AB(ks - 13);
      if (ks >= 2 && ks < 6) { DMA_W(5 + ((ks - 2) >> 1), 2 * (ks & 1)); DMA_W(5 + ((ks - 2) >> 1), 2 * (ks & 1) + 1); }
      if (ks >= 6 && ks < 9) LDCR(ks - 6);
      if (ks == 12) { LDCR(3); LDCR(4); }
      if (ks == 10 || ks == 11) { DMA_W(7, 2 * (ks & 1)); DMA_W(7, 2 * (ks & 1) + 1); }
      SCHED();
#pragma unroll
      for (int rt = 0; rt < 4; ++rt) {
        if (ks < 19) A[(ks + 1) & 1][rt] = LDA(ks + 1, rt);
        SCHED();
        acc[rt] = MFMA16(B1[ks], A[ks & 1][rt], ks ? acc[rt] : ZERO4);
        SCHED();
      }
    }
    STAMP(18);
    VMW(2 + 2 + 6);
    RD_W(7);
    const f32x2 c01 = {cs1a[0], cs1a[2]}, b01 = {cs1a[1], cs1a[3]}, c23 = {cs1b[0], cs1b[2]}, b23 = {cs1b[1], cs1b[3]};
    u32x2 g1[4];
    auto z1g = [&](int rt) {
      const float r1 = *(const LAS float*)(tbn + (L_T2 - L_MISC) + rt * 64), n1 = *(const LAS float*)(tbn + (L_T2 - L_MISC) + 256 + rt * 64);
      const f32x2 r2 = {r1, r1}, n2 = {n1, n1};
      const f32x2 a = gelu2(__builtin_elementwise_fma(__builtin_elementwise_fma(n2, c01, LO2(acc[rt])), r2, b01));
      const f32x2 b = gelu2(__builtin_elementwise_fma(__builtin_elementwise_fma(n2, c23, HI2(acc[rt])), r2, b23));
      g1[rt] = (u32x2){pack2(a[0], a[1]), pack2(b[0], b[1])};
      asm volatile("" : "+v"(g1[rt]));
    };
    SCHED();
    VMW(2 + 6);
    BAR();
    STAMP(17);
#pragma unroll
    for (int rt = 0; rt < 4; ++rt) {
      if (rt < 2) { DMA_AB(6 + 2 * rt); DMA_AB(7 + 2 * rt); }
      z1g(rt);
      *(u32x2*)(lds + L_Z1S + ((w * 4 + rt) * 64 + l) * 8) = g1[rt];
    }
  } else {
    __builtin_amdgcn_s_setprio(3);
    DRAIN();
    BAR();
    if (is_conv && l == 0) st4(rs, WS_FX + (unsigned)quad * 128u, tag);
    STAMP(9);
    *(volatile LAS unsigned*)(lds + L_S1 + 256) = ~tag;
    u32x4 gv, cv[2][2][4];
    const unsigned gvo = WS_MOD + (unsigned)(5 * D_ + 80 * c) * 8u + (l < 40 ? l16 : 0u);
    auto ld_consts = [&]() {
      gv = ld16(rs, gvo);
#pragma unroll
      for (int i = 0; i < 16; ++i) cv[i >> 3][(i >> 2) & 1][i & 3] = ld16(rs, l < 56 ? l16 : 0u, WS_CS + (unsigned)(((c + 8 * (i >> 3)) * 4 + (i & 3)) * 112 + 56 * ((i >> 2) & 1)) * 16u);
    };
    auto ok_consts = [&]() {
      unsigned x = (gv[0] ^ tag) | (gv[2] ^ tag);
#pragma unroll
      for (int i = 0; i < 16; ++i) x |= (cv[i >> 3][(i >> 2) & 1][i & 3][0] ^ tag) | (cv[i >> 3][(i >> 2) & 1][i & 3][2] ^ tag);
      asm volatile("" : "+v"(x));
      return __all(x == 0u);
    };
    auto touch_w = [&](int k) {
      cpf ^= __builtin_amdgcn_raw_buffer_load_b32(rsp, (unsigned)(h4 * 4096 + (n16 >> 1) * 512 + (n16 & 1) * 128),
                                                  (unsigned)(R * 64 + (c & 3) * 16 + k * 4) * 4096u + 256u, 0);
    };
    park();
    __builtin_amdgcn_s_barrier();
    ld_consts();
    STAMP(31);
    _Pragma("unroll 1") for (unsigned spins = 0; spins < SPIN_CAP; ++spins) {
      const unsigned f0 = ld4(rs, WS_FX + (unsigned)l * 128u), f1 = ld4(rs, WS_FX + 8192u + (unsigned)l * 128u),
                     f2 = ld4(rs, l < 32 ? WS_FX + 16384u + (unsigned)l * 128u : WS_FW1 + (unsigned)(c * 32 + (l < 60 ? l - 32 : 0)) * 128u);
      if (__all(f0 == tag && f1 == tag && f2 == tag)) break;
      __builtin_amdgcn_s_sleep(POLL_SLEEP);
    }
    BAR();
    STAMP(10);
    ld_x1(0, 5);
    __builtin_amdgcn_s_setprio(3);
    if (!ok_consts()) {
      _Pragma("unroll 1") for (unsigned spins = 0; spins < SPIN_CAP; ++spins) {
        ld_consts();
        if (ok_consts()) break;
        __builtin_amdgcn_s_sleep(8);
      }
    }
    if (l < 40) *(f32x2*)(lds + L_GB + l * 8) = (f32x2){__uint_as_float(gv[1]), __uint_as_float(gv[3])};
    if (l < 56) {
#pragma unroll
      for (int i = 0; i < 4; ++i) {
        const u32x4* q4 = cv[i >> 1][i & 1];
        *(f32x2*)(lds + L_CS1 + (56 * i + l) * 8) =
            (f32x2){(__uint_as_float(q4[0][1]) + __uint_as_float(q4[1][1])) + (__uint_as_float(q4[2][1]) + __uint_as_float(q4[3][1])),
                    (__uint_as_float(q4[0][3]) + __uint_as_float(q4[1][3])) + (__uint_as_float(q4[2][3]) + __uint_as_float(q4[3][3]))};
      }
    }
    SCHED();
#pragma unroll
    for (int m = 0; m < 3; ++m) put_x1(m);
    BAR();
    f32x4 acc[4];
    u32x4 A[2][4];
    lda1(0, A[0]);
#pragma unroll
    for (int ks = 0; ks < 20; ++ks) {
      if (ks == 6) put_x1(3);
      if (ks == 8) put_x1(4);
      if (ks == 11) BAR();
      if (ks == 4) RD_W(2);
      if (ks >= 6 && ks < 14) DMA_W(3 + ((ks - 6) >> 2), (ks - 6) & 3);
      if (ks >= 1 && ks < 5) touch_w(ks - 1);
      if (ks < 19) lda1(ks + 1, A[(ks + 1) & 1]);
      SCHED();
#pragma unroll
      for (int rt = 0; rt < 4; ++rt) acc[rt] = MFMA16(A[ks & 1][rt], A[ks & 1][rt], ks ? acc[rt] : ZERO4);
      SCHED();
    }
    {
      if ((n16 >> 2) == h4) {
#pragma unroll
        for (int rt = 0; rt < 4; ++rt) {
          const float lo = (l & 1) ? acc[rt][1] : acc[rt][0], hi = (l & 1) ? acc[rt][3] : acc[rt][2];
          *(volatile LAS float*)(tbn + (L_T2 - L_MISC) + rt * 64) = (l & 2) ? hi : lo;
        }
      }
      const float s2 = *(volatile LAS float*)(lds + L_T2 + l * 4) * (1.0f / D_);
      float s1;
      _Pragma("unroll 1") for (unsigned spins = 0; spins < SPIN_CAP * 16; ++spins) {
        const unsigned f = *(volatile LAS unsigned*)(lds + L_S1 + 256);
        s1 = *(volatile LAS float*)(lds + L_S1 + l * 4);
        if (f == tag) break;
      }
      const float nm = s1 * (-1.0f / D_);
      *(float*)(lds + L_T2 + l * 4) = __builtin_amdgcn_rsqf(__builtin_fmaxf(s2 - nm * nm, 0.f) + 1e-5f);
      *(float*)(lds + L_T2 + 256 + l * 4) = nm;
    }
    BAR();
    __builtin_amdgcn_s_setprio(0);
    asm volatile("" ::"v"(cpf));
    DRAIN();
    RD_W(3); RD_W(4);
    rd_xp();
    asm volatile("s_waitcnt lgkmcnt(0)" ::: "memory");
#pragma unroll
    for (int i = 0; i < 8; ++i) DMA_W(5 + (i >> 2), i & 3);
    const unsigned e_fw2 = ld4(rs, WS_FW2 + (unsigned)(c * 8 + (l & 7)) * 128u);
    if (!__all(e_fw2 == tag)) wait_flag<2>(rs, WS_FW2 + (unsigned)(c * 8 + (l & 7)) * 128u, tag);
    __builtin_amdgcn_s_setprio(3);
    BAR();
    {
      f32x2 t = {0.f, 0.f};
#pragma unroll
      for (int k = 0; k < 7; ++k) t += *(const f32x2*)(lds + L_Z1S + (k * 64 + l) * 8);
      st16(rs, WS_PART + (unsigned)(R * 8 + c) * 1024u + l16, (u32x4){tag, __float_as_uint(t[0]), tag, __float_as_uint(t[1])});
    }
    STAMP(14);
    {
      u32x4 v[8];
      const unsigned po = WS_PART + (unsigned)R * 8192u + l16;
      __builtin_amdgcn_s_sleep(16);
      _Pragma("unroll 1") for (unsigned spins = 0; spins < SPIN_CAP; ++spins) {
#pragma unroll
        for (int k = 0; k < 8; ++k) v[k] = ld16(rs, po + (unsigned)k * 1024u);
        unsigned x = 0;
#pragma unroll
        for (int k = 0; k < 8; ++k) x |= (v[k][0] ^ tag) | (v[k][2] ^ tag);
        asm volatile("" : "+v"(x));
        if (__all(x == 0u)) break;
        __builtin_amdgcn_s_sleep(1);
      }
      float t1 = 0.f, t2 = 0.f;
#pragma unroll
      for (int k = 0; k < 8; ++k) { t1 += __uint_as_float(v[k][1]); t2 += __uint_as_float(v[k][3]); }
      const float mean = t1 * (1.0f / FH);
      const float var = __builtin_fmaxf(t2 * (1.0f / FH) - mean * mean, 0.f);
      const float rstd = __builtin_amdgcn_rsqf(var + 1e-5f);
      *(float*)(lds + L_ST2 + l * 4) = rstd;
      *(float*)(lds + L_ST2 + 256 + l * 4) = -mean * rstd;
    }
    STAMP(15);
    BAR();
    RD_W(5); RD_W(6);
    asm volatile("s_waitcnt lgkmcnt(0)" ::: "memory");
#pragma unroll
    for (int q = 0; q < 4; ++q) DMA_W(7, q);
    DRAIN();
    RD_W(7);
    BAR();
    if (l == 0) st4(rs, WS_FZ + (unsigned)(c * 32 + R) * 128u, tag);
    STAMP(17);
    __builtin_amdgcn_s_setprio(0);
#pragma unroll
    for (int k = 0; k < 10; ++k) DMA_AB(k);
#pragma unroll
    for (int k = 0; k < 5; ++k) LDCR(k);
  }
  const rsrc_t rout = __builtin_amdgcn_make_buffer_rsrc((void*)p.out, 0, S_ * D_ * 2, 0x00020000);
  STAMP(19);
  u32x2 z1o[4];
  wait_flag<POLL_SLEEP>(rs, fzo, tag);
#pragma unroll
  for (int i = 0; i < W; ++i) { LDZ(i); SCHED(); }
  STAMP(20);

  const unsigned ub = WS_UF + (unsigned)((R * 4 + rto) * 8 + c) * 3584u;
  const float bs = bflo(bs_raw);
  const unsigned ur = WS_UF + (unsigned)((R * 4 + rto) * 8 + cls) * 3584u;
  u32x4 uf[4][2];
  f32x4 acc[2][4];
  f32x4* const bufw = (f32x4*)(lds + L_BUF) + w * 256 + l;
#define BUFW(slot, rt) bufw[(slot) * 2048 + (rt) * 64]
  {
    const f32x4* const rb = (const f32x4*)(lds + L_BUF) + cls * 2048 + rto * 64 + l;
    f32x4 rdv[4], s;
    u32x4 uo;
#pragma unroll
    for (int j = 0; j < 7; ++j) {
#pragma unroll
      for (int ks = 0; ks < 8; ++ks) {
        const int i = 8 * j + ks;
#pragma unroll
        for (int rt = 0; rt < 4; ++rt) acc[j & 1][rt] = MFMA16(fr[i % W], wsp[rt][ks], ks ? acc[j & 1][rt] : ZERO4);
        if (i == 0) STAMP(21);
        if (i == 24) STAMP(22);
        SCHED();
        if (i + W < 56) LDZ(i + W);
        if (j > 0) {
          if (ks == 0 && (j & 1)) BAR();
          if (j == 1 && ks == 0) {
#pragma unroll
            for (int k = 0; k < 4; ++k) z1o[k] = *(const u32x2*)(lds + L_Z1S + (((k < 3 ? 2 * k + cls : 6) * 4 + rto) * 64 + l) * 8);
          }
          if (i >= (56 - G2W) && i < 53) {
#pragma unroll
            for (int k = 0; k < 8; ++k)
              if ((56 - G2W) + k * (53 - (56 - G2W)) / 8 == i) uf[k >> 1][k & 1] = ld16(rs, l16, ur + (unsigned)(k >> 1) * 7168u + (unsigned)(k & 1) * 1024u);
            SCHED();
          }
          const bool fast = j == 6;
          if (!fast && ks < 4) BUFW((j - 1) & 1, ks) = acc[(j - 1) & 1][ks];
          if (fast && ks < 2) { BUFW(1, 2 * ks) = acc[1][2 * ks]; BUFW(1, 2 * ks + 1) = acc[1][2 * ks + 1]; }
          if (!(j & 1)) {
            const int k0 = fast ? 2 : 5;
            if (ks == k0) {
              BAR();
#pragma unroll
              for (int k = 0; k < 4; ++k) rdv[k] = rb[k * 256];
            }
            if (ks == k0 + 1) {
              s = (rdv[0] + rdv[1]) + (rdv[2] + rdv[3]);
#pragma unroll
              for (int k = 0; k < 4; ++k) rdv[k] = rb[(4 + k) * 256];
            }
            if (ks == k0 + 2) {
              s += (rdv[0] + rdv[1]) + (rdv[2] + rdv[3]);
              const u32x2 z = z1o[(j >> 1) - 1];
              const int e = j == 4 ? 2 : 0;
              uo[e] = pack2(bflo(z[0]) * (s[0] + bs), bfhi(z[0]) * (s[1] + bs));
              uo[e + 1] = pack2(bflo(z[1]) * (s[2] + bs), bfhi(z[1]) * (s[3] + bs));
              if (j == 4) st16(rs, ub + (unsigned)cls * 1024u + l16, uo);
              if (j == 6) st16(rs, l < 32 ? ub + 2048u + (unsigned)cls * 512u + l16 : OOB, half_swap((u32x4){uo[0], uo[1], uo[0], uo[1]}));
            }
            if (j == 6 && ks == 4) BAR();
            if (j == 6 && ks >= 4 && ks < 7) {
#pragma unroll
              for (int k = 0; k < 5; ++k)
                if (k / 2 == ks - 4) *(u32x4*)(lds + (w < 6 ? L_BUF + 32768 + w * 5120 : L_Z1S + (w - 6) * 5120) + k * 1024 + l16) = cr[k];
            }
          }
        }
        SCHED();
      }
    }
  }
  STAMP(23);

  {
    u32x4 uc[4], uh[4];
    const unsigned uho = (unsigned)(l >> 5) * 3584u + (unsigned)(l & 31) * 16u;
#define LD_C(cc) uc[cc] = ld16(rs, l16, ur + (unsigned)(cc) * 7168u + 2048u)
#define LD_H(cc) uh[cc] = ld16(rs, uho, WS_UF + (unsigned)((R * 4 + rto) * 8) * 3584u + (unsigned)(cc) * 7168u + 3072u)
    auto mx = [](unsigned a, unsigned b) { return __builtin_elementwise_max(a, b); };
    auto ok4 = [&](const u32x4* v, int n) {
      unsigned m = 0;
#pragma unroll
      for (int i = 0; i < 8; ++i)
        if (i < n) m = mx(m, mx(mx(v[i][0], v[i][1]), mx(v[i][2], v[i][3])));
      return __all(m != ~0u);
    };
#pragma unroll
    for (int rt = 0; rt < 4; ++rt) BUFW(0, rt) = acc[0][rt];
    BAR();
    f32x4 a3[5];
    u32x4 af[2][5];
    const LAS char* wb[2] = {(const LAS char*)(lds + L_AB) + cls * (10240 + 3072) + l16, (const LAS char*)(lds + L_AB) + cls * (10240 - 2048) + l16};
    const LAS char* wb2[2] = {wb[0] + 40960, wb[1] + 40960};
    asm volatile("" : "+v"(wb[0]), "+v"(wb2[0]), "+v"(wb[1]), "+v"(wb2[1]));
    u32x4 ho = {0u, 0u, 0u, 0u};
    if (w < 4) {
      __builtin_amdgcn_s_setprio(2);
      const f32x4* const rb = (const f32x4*)(lds + L_BUF) + rto * 64 + l;
      f32x4 r8[8];
#pragma unroll
      for (int k = 0; k < 8; ++k) r8[k] = rb[k * 256];
      const f32x4 s = ((r8[0] + r8[1]) + (r8[2] + r8[3])) + ((r8[4] + r8[5]) + (r8[6] + r8[7]));
      const u32x2 z = z1o[3];
      const unsigned o0 = pack2(bflo(z[0]) * (s[0] + bs), bfhi(z[0]) * (s[1] + bs)), o1 = pack2(bflo(z[1]) * (s[2] + bs), bfhi(z[1]) * (s[3] + bs));
      ho = half_swap((u32x4){o0, o1, o0, o1});
    }
    st16(rs, tid < 256 && l < 32 ? ub + 3072u + l16 : OOB, ho);
    SCHED();
    __builtin_amdgcn_s_setprio(0);
#pragma unroll
    for (int dj = 0; dj < 5; ++dj) af[0][dj] = *(const LAS u32x4*)(wb[dj >= 2] + dj * 1024);
    const bool ab_ok = ok4(&uf[0][0], 8);
    STAMP(24);
    if (!ab_ok) {
      _Pragma("unroll 1") for (unsigned spins = 0; spins < SPIN_CAP; ++spins) {
#pragma unroll
        for (int i = 0; i < 8; ++i) uf[i >> 1][i & 1] = ld16(rs, l16, ur + (unsigned)(i >> 1) * 7168u + (unsigned)(i & 1) * 1024u);
        if (ok4(&uf[0][0], 8)) break;
        __builtin_amdgcn_s_sleep(1);
      }
    }
    STAMP(25);
    const LAS char* cb0[2] = {(const LAS char*)(lds + L_BUF + 32768) + cls * (5120 + 3072) + l16, (const LAS char*)(lds + L_BUF + 32768) + cls * (5120 - 2048) + l16};
    u32x4 ah[4][3];
    const LAS char* hb = (const LAS char*)(lds + L_BUF) + cls * 12288 + l * 8;
    f32x4 gb[3], xo3[3], xf[3];
    LAS char *otw, *xw3;
    const LAS char *ota, *otb, *xr3;
    unsigned oa_o, ob_o;
#pragma unroll
    for (int st = 0; st < 12; ++st) {
      if (st < 2) { LD_C(2 * st); LD_C(2 * st + 1); }
      else if (st < 7 && w < 4) dma1k(rs, lds + L_BUF + ((st - 2) * 4 + w) * 1024, l16, wob + 122880u + (unsigned)(w * 5 + st - 2) * 1024u);
      if (st == 2) {
        LAS char* ot = (LAS char*)(lds + L_AB + 32768) + w * 2048;
        otw = ot + n16 * 112 + h4 * 8;
        ota = ot + (l >> 2) * 112 + (l & 3) * 16;
        otb = ot + ((l >> 1) & 15) * 112 + 64 + (l & 1) * 16;
        asm volatile("" : "+v"(otw), "+v"(ota), "+v"(otb));
      }
      if (st == 3) {
        oa_o = (unsigned)(l >> 2) * 2560u + (unsigned)(l & 3) * 16u;
        ob_o = l < 32 ? (unsigned)(l >> 1) * 2560u + 64u + (unsigned)(l & 1) * 16u : OOB;
        xw3 = (LAS char*)(lds + L_AB) + w * 3072 + l16;
        xr3 = (const LAS char*)(lds + L_AB) + (w ^ 4) * 3072 + cls * 1024 + l16;
        asm volatile("" : "+v"(oa_o), "+v"(ob_o), "+v"(xw3), "+v"(xr3));
      }
      if (st >= 4 && st < 7) {
        const int j = st - 4;
        gb[j] = *(const f32x4*)(lds + L_GB + (48 * cls + 16 * (cls && j == 2 ? 1 : j) + 4 * h4) * 4);
        xf[j] = (f32x4){bflo(xp[j][0]), bfhi(xp[j][0]), bflo(xp[j][1]), bfhi(xp[j][1])};
        asm volatile("" : "+v"(xf[j]));
      }
      if (st == 8) {
        STAMP(26);
        if (!ok4(uc, 4)) {
          _Pragma("unroll 1") for (unsigned spins = 0; spins < SPIN_CAP; ++spins) {
            LD_C(0); LD_C(1); LD_C(2); LD_C(3);
            if (ok4(uc, 4)) break;
            __builtin_amdgcn_s_sleep(1);
          }
        }
        LD_H(0); LD_H(1); LD_H(2); LD_H(3);
#pragma unroll
        for (int cc = 0; cc < 4; ++cc) uc[cc] = half_swap(uc[cc]);
        VMW(4);
        __builtin_amdgcn_s_barrier();
        asm volatile("" ::: "memory");
        STAMP(27);
      }
      SCHED();
#pragma unroll
      for (int dj = 0; dj < 5; ++dj) {
        const int sn = st + 1;
        if (sn < 8) af[sn & 1][dj] = *(const LAS u32x4*)((sn < 4 ? wb : wb2)[dj >= 2] + ((sn >> 1) & 1) * 20480 + ((sn & 1) * 5 + dj) * 1024);
        else if (sn < 12) af[sn & 1][dj] = *(const LAS u32x4*)(cb0[dj >= 2] + (sn < 11 ? (sn - 8) * 10240 : L_Z1S - L_BUF - 32768) + dj * 1024);
        if (st >= 8) {
#pragma unroll
          for (int k = 0; k < 2; ++k) {
            const int h = st < 11 ? (k ? 99 : (st - 8) * 5 + dj) : 15 + 2 * dj + k;
            if (h < 24) {
              const u32x2 v = *(const volatile LAS u32x2*)(hb + (((h >> 1) % 3) * 4 + (h >> 1) / 3) * 1024 + (h & 1) * 512);
              ah[(h >> 1) / 3][(h >> 1) % 3][2 * (h & 1)] = v[0]; ah[(h >> 1) / 3][(h >> 1) % 3][2 * (h & 1) + 1] = v[1];
            }
          }
        }
        SCHED();
        a3[dj] = MFMA16(af[st & 1][dj], st < 8 ? uf[(st >> 1) & 3][st & 1] : uc[st & 3], st ? a3[dj] : ZERO4);
        SCHED();
      }
    }
#pragma unroll
    for (int j = 0; j < 3; ++j) *(volatile LAS f32x4*)(xw3 + j * 1024) = a3[2 + j];
    BAR();
#pragma unroll
    for (int j = 0; j < 3; ++j) xo3[j] = *(const volatile LAS f32x4*)(xr3 + j * 1024);
    SCHED();
    if (!ok4(uh, 4)) {
      _Pragma("unroll 1") for (unsigned spins = 0; spins < SPIN_CAP; ++spins) {
        LD_H(0); LD_H(1); LD_H(2); LD_H(3);
        if (ok4(uh, 4)) break;
        __builtin_amdgcn_s_sleep(1);
      }
    }
#pragma unroll
    for (int cc = 0; cc < 4; ++cc) {
      const u32x4 b = half_swap(uh[cc]);
#pragma unroll
      for (int j = 0; j < 3; ++j) a3[j] = MFMA16(ah[cc][j], b, a3[j]);
      if (cc >= 1) {
        const int j = cc - 1;
        xo3[j] = __builtin_elementwise_fma(gb[j], xo3[j], xf[j]);
        asm volatile("" : "+v"(xo3[j]));
      }
      SCHED();
    }
#pragma unroll
    for (int j = 0; j < 3; ++j) {
      const f32x4 r = __builtin_elementwise_fma(gb[j], a3[j], xo3[j]);
      *(volatile LAS u32x2*)(otw + j * 32) = (u32x2){pack2(r[0], r[1]), pack2(r[2], r[3])};
    }
    const u32x4 oa = *(const volatile LAS u32x4*)(ota);
    const u32x4 ob = *(const volatile LAS u32x4*)(otb);
    const unsigned obase = (unsigned)((R * 64 + 32 * ((w >> 1) & 1) + (w & 1)) * D_ + 80 * c + 48 * cls) * 2u;
    __builtin_amdgcn_raw_buffer_store_b128(oa, rout, oa_o, obase, 0);
    if (cls == 0) __builtin_amdgcn_raw_buffer_store_b128(ob, rout, ob_o, obase, 0);
    if (bid == 0 && tid == 0) st4(rs, WS_EPOCH, tag);
  }
  STAMP(30);
  if (bid != 43 || w != 1) tb = ta;
  while (__builtin_amdgcn_s_memrealtime() - tstart < 5000 + (XCYC ? (tb - ta) / 4 : 4 * (tb - ta))) __builtin_amdgcn_s_sleep(1);
  asm volatile(".Lmega_end:");
}

extern "C" void kernel_launch(void* const* d_in, const int* in_sizes, int n_in, void* d_out, int out_size,
                              void* d_ws, size_t ws_size, hipStream_t stream) {
  Params p;
  p.q = (const u16*)d_in[0];
  p.k = (const u16*)d_in[1];
  p.v = (const u16*)d_in[2];
  p.shift_k = (const u16*)d_in[3];
  p.ssm_k = (const u16*)d_in[4];
  p.ssm_D = (const u16*)d_in[5];
  p.w_in = (const u16*)d_in[7];
  p.w_sp = (const u16*)d_in[8];
  p.b_sp = (const u16*)d_in[9];
  p.w_out = (const u16*)d_in[10];
  p.cvec = (const u16*)d_in[11];
  p.w_mod = (const u16*)d_in[12];
  p.out = (u16*)d_out;
  p.ws = (char*)d_ws;
  mega<<<256, 512, 0, stream>>>(p);
}
```

```cpp
#include <hip/hip_runtime.h>
#include <stdint.h>

typedef unsigned short u16;
typedef __attribute__((ext_vector_type(8))) short bf16x8;
typedef __attribute__((ext_vector_type(4))) float f32x4;
typedef __attribute__((ext_vector_type(2))) float f32x2;
typedef __attribute__((ext_vector_type(4))) unsigned int u32x4;
typedef __attribute__((ext_vector_type(2))) unsigned int u32x2;
#define LAS __attribute__((address_space(3)))
#define CAS __attribute__((address_space(4)))
#define GAS __attribute__((address_space(1)))
typedef __amdgpu_buffer_rsrc_t rsrc_t;
typedef __attribute__((ext_vector_type(4))) short s16x4;

constexpr int S_ = 2048, D_ = 640, F_ = 1728, FH = 864, CB = 108;

#define X1PAD 256
constexpr unsigned WS_EPOCH = 0;
constexpr unsigned WS_FX = 4096;
constexpr unsigned WS_FZ = WS_FX + 24576;
constexpr unsigned WS_FW2 = WS_FZ + 32768;
constexpr unsigned WS_FW1 = WS_FW2 + 8192;
constexpr unsigned WS_CS = WS_FW1 + 65536;
constexpr unsigned WS_PART = WS_CS + 131072;
constexpr unsigned WS_MOD = WS_PART + 262144;
constexpr unsigned WS_ST1 = WS_MOD + 32768;
constexpr unsigned WS_X1B = WS_ST1 + 16384;
constexpr unsigned WX1P = 40960 + X1PAD;
constexpr unsigned WBLK = 143360;
constexpr unsigned WS_QB = WS_X1B + 2686976;
constexpr unsigned WS_WINF = WS_QB + 2686976;
constexpr unsigned WS_WOUTF = WS_WINF + 16 * WBLK;
constexpr unsigned WS_Z2F = WS_WOUTF + 8 * WBLK;
constexpr unsigned WS_UF = WS_Z2F + 8 * 64 * 7168;
constexpr unsigned WS_KVT = WS_UF + 32 * 4 * 8 * 3584;
constexpr unsigned WS_FK = WS_KVT + 32 * 81920;
constexpr unsigned WS_STAMP = WS_FK + 20480;
constexpr unsigned WS_END = WS_STAMP + 256 * 40 * 8;

constexpr int U_BYTES = (256 + S_) * 2;
constexpr int G_COPY = 4160;
constexpr int CH_BYTES = U_BYTES + 4 * G_COPY;
constexpr int L_YP = 4 * CH_BYTES;
constexpr int L_A0 = 98304;
constexpr int L_RED = 0;
constexpr int L_TAB = 2048;
constexpr int L_J3 = 8192;
constexpr int L_QT = 49152;
constexpr int L_J5 = L_QT;
constexpr int L_XI = 0;
constexpr int L_BUF = 81920;
constexpr int L_AB = 0;
constexpr int L_MISC = 147456;
constexpr int L_Z1S = L_MISC;
constexpr int L_S1 = L_MISC + 14336;
constexpr int L_T2 = L_S1 + 512;
constexpr int L_ST2 = L_T2 + 512;
constexpr int L_GB = L_ST2 + 512;
constexpr int L_CS1 = L_Z1S + 4096;
constexpr int L_WS = 81920;
constexpr int LDS_TOTAL = 163840;

constexpr int POLL_SLEEP = 1;
constexpr int AUX_SLEEP = 36, LATE_SLEEP = 96;
constexpr int CDIST = 4;
constexpr int NRAW = 12;
constexpr int SV_AT = 108, Q_AT = 130;
constexpr int NB2A = 6;
constexpr int G2W = 7;
constexpr int Z1SPLIT = 12;
constexpr int SC1 = 16;
constexpr unsigned SPIN_CAP = 1u << 16;
constexpr unsigned OOB = 0x80000000u;

__device__ __forceinline__ float bflo(unsigned x) { return __uint_as_float(x << 16); }
__device__ __forceinline__ float bfhi(unsigned x) { return __uint_as_float(x & 0xffff0000u); }
__device__ __forceinline__ f32x2 bf2(unsigned x) { return (f32x2){bflo(x), bfhi(x)}; }
__device__ __forceinline__ unsigned f2bf(float x) {
  __bf16 b = (__bf16)x;
  return (unsigned)__builtin_bit_cast(unsigned short, b);
}
__device__ __forceinline__ float rbf(float x) { return bflo(f2bf(x)); }
typedef __attribute__((ext_vector_type(2))) __bf16 bf2_t;
__device__ __forceinline__ unsigned pack2(float lo, float hi) {
  f32x2 v = {lo, hi};
  return __builtin_bit_cast(unsigned, __builtin_convertvector(v, bf2_t));
}
template <int CTRL>
__device__ __forceinline__ float dpp_f(float v) {
  return __int_as_float(__builtin_amdgcn_update_dpp(0, __float_as_int(v), CTRL, 0xf, 0xf, true));
}
__device__ __forceinline__ float sum_rows(float v) {
  auto a = __builtin_amdgcn_permlane16_swap(__float_as_uint(v), __float_as_uint(v), false, false);
  v = __uint_as_float(a[0]) + __uint_as_float(a[1]);
  auto b = __builtin_amdgcn_permlane32_swap(__float_as_uint(v), __float_as_uint(v), false, false);
  return __uint_as_float(b[0]) + __uint_as_float(b[1]);
}
__device__ __forceinline__ f32x2 gelu2(f32x2 x) {
  const float c0 = -2.0f * 0.7978845608028654f * 1.4426950408889634f;
  const float c1 = c0 * 0.044715f;
  const f32x2 t = x * __builtin_elementwise_fma(x * x, (f32x2){c1, c1}, (f32x2){c0, c0});
  const f32x2 d = (f32x2){__builtin_amdgcn_exp2f(t[0]), __builtin_amdgcn_exp2f(t[1])} + (f32x2){1.0f, 1.0f};
  return x * (f32x2){__builtin_amdgcn_rcpf(d[0]), __builtin_amdgcn_rcpf(d[1])};
}
#define LO2(v) __builtin_shufflevector(v, v, 0, 1)
#define HI2(v) __builtin_shufflevector(v, v, 2, 3)
__device__ __forceinline__ float tr_sum16(const float* v, int l) {
  float x[8], y[4];
  asm volatile(
      "s_nop 1\n\t"
      "v_add_f32_dpp %0, %8, %8 row_ror:8 row_mask:0xf bank_mask:0x3\n\t"
      "v_add_f32_dpp %1, %9, %9 row_ror:8 row_mask:0xf bank_mask:0x3\n\t"
      "v_add_f32_dpp %2, %10, %10 row_ror:8 row_mask:0xf bank_mask:0x3\n\t"
      "v_add_f32_dpp %3, %11, %11 row_ror:8 row_mask:0xf bank_mask:0x3\n\t"
      "v_add_f32_dpp %4, %12, %12 row_ror:8 row_mask:0xf bank_mask:0x3\n\t"
      "v_add_f32_dpp %5, %13, %13 row_ror:8 row_mask:0xf bank_mask:0x3\n\t"
      "v_add_f32_dpp %6, %14, %14 row_ror:8 row_mask:0xf bank_mask:0x3\n\t"
      "v_add_f32_dpp %7, %15, %15 row_ror:8 row_mask:0xf bank_mask:0x3\n\t"
      "v_add_f32_dpp %0, %16, %16 row_ror:8 row_mask:0xf bank_mask:0xc\n\t"
      "v_add_f32_dpp %1, %17, %17 row_ror:8 row_mask:0xf bank_mask:0xc\n\t"
      "v_add_f32_dpp %2, %18, %18 row_ror:8 row_mask:0xf bank_mask:0xc\n\t"
      "v_add_f32_dpp %3, %19, %19 row_ror:8 row_mask:0xf bank_mask:0xc\n\t"
      "v_add_f32_dpp %4, %20, %20 row_ror:8 row_mask:0xf bank_mask:0xc\n\t"
      "v_add_f32_dpp %5, %21, %21 row_ror:8 row_mask:0xf bank_mask:0xc\n\t"
      "v_add_f32_dpp %6, %22, %22 row_ror:8 row_mask:0xf bank_mask:0xc\n\t"
      "v_add_f32_dpp %7, %23, %23 row_ror:8 row_mask:0xf bank_mask:0xc"
      : "=&v"(x[0]), "=&v"(x[1]), "=&v"(x[2]), "=&v"(x[3]), "=&v"(x[4]), "=&v"(x[5]), "=&v"(x[6]), "=&v"(x[7])
      : "v"(v[0]), "v"(v[1]), "v"(v[2]), "v"(v[3]), "v"(v[4]), "v"(v[5]), "v"(v[6]), "v"(v[7]), "v"(v[8]), "v"(v[9]), "v"(v[10]),
        "v"(v[11]), "v"(v[12]), "v"(v[13]), "v"(v[14]), "v"(v[15]));
  asm volatile(
      "s_nop 1\n\t"
      "v_add_f32_dpp %0, %4, %4 row_shl:4 row_mask:0xf bank_mask:0x5\n\t"
      "v_add_f32_dpp %1, %5, %5 row_shl:4 row_mask:0xf bank_mask:0x5\n\t"
      "v_add_f32_dpp %2, %6, %6 row_shl:4 row_mask:0xf bank_mask:0x5\n\t"
      "v_add_f32_dpp %3, %7, %7 row_shl:4 row_mask:0xf bank_mask:0x5\n\t"
      "v_add_f32_dpp %0, %8, %8 row_shr:4 row_mask:0xf bank_mask:0xa\n\t"
      "v_add_f32_dpp %1, %9, %9 row_shr:4 row_mask:0xf bank_mask:0xa\n\t"
      "v_add_f32_dpp %2, %10, %10 row_shr:4 row_mask:0xf bank_mask:0xa\n\t"
      "v_add_f32_dpp %3, %11, %11 row_shr:4 row_mask:0xf bank_mask:0xa\n\t"
      "s_nop 1"
      : "=&v"(y[0]), "=&v"(y[1]), "=&v"(y[2]), "=&v"(y[3])
      : "v"(x[0]), "v"(x[1]), "v"(x[2]), "v"(x[3]), "v"(x[4]), "v"(x[5]), "v"(x[6]), "v"(x[7]));
  const bool b1 = l & 2, b0 = l & 1;
  const float z0 = (b1 ? y[2] : y[0]) + dpp_f<0x4E>(b1 ? y[0] : y[2]);
  const float z1 = (b1 ? y[3] : y[1]) + dpp_f<0x4E>(b1 ? y[1] : y[3]);
  return (b0 ? z1 : z0) + dpp_f<0xB1>(b0 ? z0 : z1);
}
__device__ __forceinline__ u32x4 half_swap(u32x4 v) {
  const auto a = __builtin_amdgcn_permlane32_swap(v[0], v[2], false, false);
  const auto b = __builtin_amdgcn_permlane32_swap(v[1], v[3], false, false);
  return (u32x4){a[0], b[0], a[1], b[1]};
}
#define MFMA16(a, b, c) __builtin_amdgcn_mfma_f32_16x16x32_bf16(__builtin_bit_cast(bf16x8, a), __builtin_bit_cast(bf16x8, b), c, 0, 0, 0)
__device__ __forceinline__ u32x2 trd(const char* p) {
  return __builtin_bit_cast(u32x2, __builtin_amdgcn_ds_read_tr16_b64_v4i16((LAS s16x4*)p));
}
__device__ __forceinline__ u32x4 trfrag(const char* p, int second) {
  u32x2 a = trd(p), b = trd(p + second);
  return (u32x4){a[0], a[1], b[0], b[1]};
}
#define BAR() asm volatile("s_waitcnt lgkmcnt(0)\n\ts_barrier" ::: "memory")
#define DRAIN() do { __builtin_amdgcn_s_waitcnt(0x0F70); asm volatile("" ::: "memory"); } while (0)
#define VMW(n) do { __builtin_amdgcn_s_waitcnt(0x0F70 | ((n) & 15) | (((n) >> 4) << 14)); asm volatile("" ::: "memory"); } while (0)
#define SCHED() __builtin_amdgcn_sched_barrier(0)
#define ZERO4 ((f32x4){0.f, 0.f, 0.f, 0.f})

__device__ __forceinline__ u32x4 ld16(rsrc_t r, unsigned off, unsigned so = 0) { return __builtin_amdgcn_raw_buffer_load_b128(r, off, so, SC1); }
__device__ __forceinline__ u32x2 ld8(rsrc_t r, unsigned off, unsigned so = 0) { return __builtin_amdgcn_raw_buffer_load_b64(r, off, so, SC1); }
__device__ __forceinline__ unsigned ld4(rsrc_t r, unsigned off, unsigned so = 0) { return __builtin_amdgcn_raw_buffer_load_b32(r, off, so, SC1); }
__device__ __forceinline__ void st16(rsrc_t r, unsigned off, u32x4 v) { __builtin_amdgcn_raw_buffer_store_b128(v, r, off, 0, SC1); }
__device__ __forceinline__ void st8(rsrc_t r, unsigned off, u32x2 v) { __builtin_amdgcn_raw_buffer_store_b64(v, r, off, 0, SC1); }
__device__ __forceinline__ void st4(rsrc_t r, unsigned off, unsigned v) { __builtin_amdgcn_raw_buffer_store_b32(v, r, off, 0, SC1); }
__device__ __forceinline__ void dma1k(rsrc_t r, char* lds_dst, unsigned l16, unsigned soff) {
  __builtin_amdgcn_raw_ptr_buffer_load_lds(r, (LAS void*)lds_dst, 16, l16, soff, 0, SC1);
}

#define STAMP(i)

template <int SLEEP>
__device__ __forceinline__ void wait_flag(rsrc_t r, unsigned off, unsigned tag) {
  _Pragma("unroll 1") for (unsigned spins = 0; spins < SPIN_CAP; ++spins) {
    if (__all(ld4(r, off) == tag)) break;
    __builtin_amdgcn_s_sleep(SLEEP);
  }
  asm volatile("" ::: "memory");
}

struct ConvSched {
  signed char pp[144], m[144], rd[144];
  int pre;
};
constexpr ConvSched conv_sched(int dist) {
  ConvSched s = {};
  int need[96] = {}, i = 0;
  for (int r = 0; r < 4; ++r)
    for (int pp = 0; pp < 8; ++pp) {
      const int j = 24 * r + (pp ? 8 + 2 * pp : 0);
      need[j] = need[j + 1] = i;
      for (int m = 7 - pp; m >= 0; --m) {
        if (pp == 0) need[24 * r + 9 - m] = i;
        s.pp[i] = pp; s.m[i] = m; s.rd[i] = -1;
        ++i;
      }
    }
  int next = 144;
  for (int j = 95; j >= 0; --j) {
    next = need[j] - dist < next - 1 ? need[j] - dist : next - 1;
    if (next >= 0) s.rd[next] = j;
    else if (s.pre == 0) s.pre = j + 1;
  }
  return s;
}

template <int I> struct IC { static constexpr int v = I; };
template <int I0, int I1, class F>
__device__ __forceinline__ void static_for(F&& f) {
  if constexpr (I0 < I1) { f(IC<I0>{}); static_for<I0 + 1, I1>(f); }
}

struct Params {
  const u16 *q, *k, *v, *shift_k, *ssm_k, *ssm_D, *w_in, *w_sp, *b_sp, *w_out, *cvec, *w_mod;
  u16* out;
  char* ws;
};

__global__ __launch_bounds__(512, 2) void mega(Params) {
  __shared__ __attribute__((aligned(16))) char lds[LDS_TOTAL];
  const CAS Params* kp = (const CAS Params*)__builtin_amdgcn_kernarg_segment_ptr();
  unsigned ka0, ka1;
  asm volatile("s_load_dword %0, %2, 0x0\n\ts_load_dword %1, %2, 0x40" : "=&s"(ka0), "=&s"(ka1) : "s"(kp));
  SCHED();
  const int tid = threadIdx.x, l = tid & 63;
  const int w = __builtin_amdgcn_readfirstlane(tid >> 6);
  const int bid = blockIdx.x, xcd = bid & 7, idx = bid >> 3;
  const int dg = ((idx & 7) + 2 * (idx >> 3)) & 7;
  const bool is_conv = dg >= 3;
  const int slot = 3 * (idx >> 3) + dg;
  const int cidx = 5 * (idx >> 3) + dg - 3;
  const int quad = xcd * 20 + cidx;
  const unsigned l16 = (unsigned)l * 16u;
  const int blk = (quad / 5) * 8 + w, sl = quad % 5;
  unsigned a0g = (unsigned)(l >> 4) * 1280u + (unsigned)(((l & 15) ^ (4 * (l >> 5))) * 16), a0x = a0g ^ 128u, a0h = a0g + (l < 16 ? 1280u : 0u);
  unsigned a0s = (unsigned)blk * 10240u + (unsigned)sl * 256u, a0p = blk ? a0s - 5120u : a0s, a0l = L_A0 + w * 6144;
  asm volatile("" : "+v"(a0g), "+v"(a0x), "+v"(a0h));
  asm volatile("" : "+s"(a0s), "+s"(a0p), "+s"(a0l));
  SCHED();
  asm volatile("" : "+s"(kp));
  Params p = {kp->q, kp->k, kp->v, kp->shift_k, kp->ssm_k, kp->ssm_D, kp->w_in, kp->w_sp, kp->b_sp, kp->w_out, kp->cvec, kp->w_mod, kp->out, kp->ws};
  asm volatile("s_waitcnt lgkmcnt(0)" : "+s"(p.q), "+s"(p.k), "+s"(p.v), "+s"(p.shift_k), "+s"(p.ssm_k), "+s"(p.ssm_D), "+s"(p.w_in), "+s"(p.w_sp),
               "+s"(p.b_sp), "+s"(p.w_out), "+s"(p.cvec), "+s"(p.w_mod), "+s"(p.out), "+s"(p.ws) : "s"(ka0), "s"(ka1));
  char* const ws = p.ws;
  STAMP(0);
  unsigned cpf = 0;
  if (!is_conv) {
    unsigned long long pc;
    unsigned len, len2;
    asm volatile("s_getpc_b64 %0\n.Lmega_here:\n\ts_mov_b32 %1, .Lmega_end-.Lmega_here\n\ts_mov_b32 %2, .Lmega_aux_end-.Lmega_here" : "=s"(pc), "=s"(len), "=s"(len2));
    len = len > len2 ? len : len2;
    unsigned o = (unsigned)((slot * 8 + w) * 1024 + (l & 7) * 128);
    o = o + 16u <= len ? o : 0u;
    cpf = *(const GAS unsigned*)((pc & ~15ull) + o);
  }
  const rsrc_t rs = __builtin_amdgcn_make_buffer_rsrc(ws, 0, WS_END, 0x00020000);
  const unsigned epv = ld4(rs, WS_EPOCH);
  SCHED();
  u32x2 dd2;
  {
    int cq = dg;
    asm volatile("" : "+s"(cq));
    if (cq >= 3) {
      const rsrc_t rk = __builtin_amdgcn_make_buffer_rsrc((void*)p.k, 0, S_ * D_ * 2, 0x00020000);
      const rsrc_t rv = __builtin_amdgcn_make_buffer_rsrc((void*)p.v, 0, S_ * D_ * 2, 0x00020000);
      const rsrc_t rh = __builtin_amdgcn_make_buffer_rsrc((void*)p.shift_k, 0, D_ * 8, 0x00020000);
      char* const la = lds + a0l;
      __builtin_amdgcn_raw_ptr_buffer_load_lds(rh, (LAS void*)(la + 5120), 16, l16, (unsigned)sl * 1024u, 0, 0);
      __builtin_amdgcn_raw_ptr_buffer_load_lds(rk, (LAS void*)(la + 1024), 16, a0x, a0s, 0, 0);
      __builtin_amdgcn_raw_ptr_buffer_load_lds(rk, (LAS void*)(la + 2048), 16, a0g, a0s + 5120u, 0, 0);
      __builtin_amdgcn_raw_ptr_buffer_load_lds(rv, (LAS void*)(la + 3072), 16, a0g, a0s, 0, 0);
      __builtin_amdgcn_raw_ptr_buffer_load_lds(rv, (LAS void*)(la + 4096), 16, a0x, a0s + 5120u, 0, 0);
      __builtin_amdgcn_raw_ptr_buffer_load_lds(rk, (LAS void*)(la), 16, a0h, a0p, 0, 0);
      SCHED();
      const rsrc_t rf = __builtin_amdgcn_make_buffer_rsrc((void*)p.ssm_k, 0, S_ * D_ * 2, 0x00020000);
      dd2 = *(const CAS u32x2*)((const CAS char*)p.ssm_D + quad * 8);
      STAMP(32);
      {
        const u32x4 z = {0u, 0u, 0u, 0u};
        if (tid < 128) *(u32x4*)(lds + (tid >> 5) * CH_BYTES + (tid & 31) * 16) = z;
        else if (tid < 152) {
          const int i = (tid - 128) >> 2;
          *(u32x4*)(lds + (tid & 3) * CH_BYTES + U_BYTES + (i < 4 ? i * G_COPY : (i & 1 ? 4128 : 16))) = z;
        }
        const LAS char* a0r = (const LAS char*)(lds + L_A0) + w * 6144 + (l & 3) * 512;
#define A0R(k) (a0r + ((((l >> 2) ^ (4 * ((l + (k)) & 3))) * 16)))
        unsigned go = WS_KVT + (unsigned)(quad / 5) * 81920u + (unsigned)sl * 16384u + (unsigned)(l >> 2) * 1024u + (unsigned)w * 64u + (unsigned)(l & 3) * 16u;
        const LAS char* r0 = A0R(0), * r1 = A0R(1), * r2 = A0R(2);
        const LAS char* rsh = (const LAS char*)(lds + L_A0) + w * 6144 + 5120 + (l >> 2) * 64;
        asm volatile("" : "+v"(go), "+v"(r0), "+v"(r1), "+v"(r2), "+v"(rsh));
        SCHED();
#pragma unroll
        for (int j = 0; j < 2; ++j)
          __builtin_amdgcn_raw_ptr_buffer_load_lds(rf, (LAS void*)(lds + (w >> 1) * CH_BYTES + U_BYTES + 32 + (2 * (w & 1) + j) * 1024), 16, l16,
                                                   (unsigned)quad * 16384u + (unsigned)(2 * w + j) * 1024u, 0, 0);
        STAMP(34);
        SCHED();
        VMW(7);
        f32x2 wt[4][4];
#pragma unroll
        for (int e = 0; e < 4; ++e) {
          const u32x4 sh = *(const volatile LAS u32x4*)(rsh + 16 * e);
          wt[e][0] = (f32x2){bflo(sh[0]), bflo(sh[2])}; wt[e][1] = (f32x2){bfhi(sh[0]), bfhi(sh[2])};
          wt[e][2] = (f32x2){bflo(sh[1]), bflo(sh[3])}; wt[e][3] = (f32x2){bfhi(sh[1]), bfhi(sh[3])};
        }
#pragma unroll
        for (int e = 0; e < 4; ++e) asm volatile("" : "+v"(wt[e][0]), "+v"(wt[e][1]), "+v"(wt[e][2]), "+v"(wt[e][3]));
        asm volatile("" : "+s"(dd2));
        SCHED();
        VMW(2);
        STAMP(35);
        u32x4 ka[5], va[2];
        ka[0] = *(const volatile LAS u32x4*)(r0 + 256);
        ka[1] = *(const volatile LAS u32x4*)(r1 + 512);
        ka[2] = *(const volatile LAS u32x4*)(r1 + 768);
        ka[3] = *(const volatile LAS u32x4*)(r2 + 1024);
        ka[4] = *(const volatile LAS u32x4*)(r2 + 1280);
        va[0] = *(const volatile LAS u32x4*)(r0 + 3072);
        va[1] = *(const volatile LAS u32x4*)(r0 + 3328);
        SCHED();
        if (blk == 0) {
          if ((l & 3) < 2) ka[0] = z;
          if ((l & 3) == 0) { ka[1] = z; ka[2] = z; }
        }
        u32x4 ko;
#pragma unroll
        for (int e = 0; e < 4; ++e) {
          const f32x2 w0 = wt[e][0], w1 = wt[e][1], w2 = wt[e][2], w3 = wt[e][3];
          const f32x2 km3 = bf2(ka[0][e]), km2 = bf2(ka[1][e]), km1 = bf2(ka[2][e]), k0 = bf2(ka[3][e]), k1 = bf2(ka[4][e]);
          const f32x2 s0 = __builtin_elementwise_fma(k0, w0, __builtin_elementwise_fma(km1, w1, __builtin_elementwise_fma(km2, w2, km3 * w3)));
          const f32x2 s1 = __builtin_elementwise_fma(k1, w0, __builtin_elementwise_fma(k0, w1, __builtin_elementwise_fma(km1, w2, km2 * w3)));
          const f32x2 o0 = s0 * bf2(va[0][e]), o1 = s1 * bf2(va[1][e]);
          ko[2 * (e & 1)] = pack2(o1[0], o0[0]); ko[2 * (e & 1) + 1] = pack2(o1[1], o0[1]);
          if (e & 1) st16(rs, go + (unsigned)(e >> 1) * 512u, ko);
          SCHED();
        }
      }
    }
  }
  const int R = (xcd & 3) * 8 + (idx & 7);
  const int c = (xcd >> 2) * 4 + (idx >> 3);
  const int n16 = l & 15, h4 = l >> 4;
  const rsrc_t rq = __builtin_amdgcn_make_buffer_rsrc((void*)p.q, 0, S_ * D_ * 2, 0x00020000);
  const rsrc_t rsp = __builtin_amdgcn_make_buffer_rsrc((void*)p.w_sp, 0, S_ * S_ * 2, 0x00020000);
  const int wj = w < 7 ? w : 0;
  u32x4 wsp[4][8];
  const int wk = w ^ (dg == 5 || dg == 6 ? 4 : 0);
  const unsigned wso = (unsigned)(R * 64) * 4096u + (unsigned)wk * 512u;
  const unsigned wdo = (unsigned)(h4 * 8192 + ((n16 ^ h4) * 16));
#define WVO(q) (wdo ^ (unsigned)(((4 - (q)) & 3) * 64))
#define WSO(g, q) (wso + (unsigned)((g) >> 2) * 256u + (unsigned)(32 * (((g) >> 1) & 1) + ((g) & 1) + 8 * (q)) * 4096u)
  u32x4 raw[NRAW];
#define LDW(i) raw[i] = __builtin_amdgcn_raw_buffer_load_b128(rsp, WVO((i) & 3), WSO((i) >> 2, (i) & 3), 0)
  const int trow = R * 64 + 32 * ((w >> 1) & 1) + 2 * n16 + (w & 1);
  unsigned bs_raw;
  unsigned tag;
  unsigned fw2 = OOB;
  unsigned e_fw1;
  const unsigned fw1o = WS_FW1 + (unsigned)(((8 + c) * 8 + wj) * 4 + (l & 3)) * 128u;

  if (__builtin_expect(is_conv, 1)) {
    STAMP(1);
    DRAIN();
    tag = __builtin_amdgcn_readfirstlane(epv) * 2654435761u + 0x9E3779B9u;
    BAR();
    st4(rs, tid == 0 ? WS_FK + (unsigned)quad * 128u : OOB, tag);
    unsigned atv, ato;
    const unsigned long long atb = (unsigned long long)(w < 5 ? p.w_in : p.w_out);
    {
      const int i = w < 5 ? (cidx * 5 + w) * 20 + (l < 19 ? l : 19) : (cidx * 3 + w - 5) * 19 + (l < 18 ? l : 18);
      const int r = i / 3;
      ato = w < 5 ? (unsigned)((r < 639 ? r : 639) * (F_ * 2) + xcd * CB * 2 + (i - 3 * r) * 108) : (unsigned)(xcd * CB * D_ * 2 + (i < 1079 ? i : 1079) * 128);
      asm volatile("" : "+v"(ato));
    }
    {
      u32x4 d[3];
      u32x2 n[3];
#pragma unroll
      for (int j = 0; j < 3; ++j) {
        char* G = j < 2 ? lds + ((tid >> 8) + 2 * j) * CH_BYTES + U_BYTES + ((tid & 255) + 2) * 16 : lds + (tid & 3) * CH_BYTES + U_BYTES + 16;
        d[j] = *(const u32x4*)G;
        n[j] = *(const u32x2*)(G + 16);
      }
#pragma unroll
      for (int j = 0; j < 3; ++j) {
        char* G = j < 2 ? lds + ((tid >> 8) + 2 * j) * CH_BYTES + U_BYTES + ((tid & 255) + 2) * 16 : lds + (tid & 3) * CH_BYTES + U_BYTES + 16;
        const unsigned a0 = __builtin_amdgcn_alignbit(d[j][1], d[j][0], 16), a1 = __builtin_amdgcn_alignbit(d[j][2], d[j][1], 16),
                       a2 = __builtin_amdgcn_alignbit(d[j][3], d[j][2], 16), a3 = __builtin_amdgcn_alignbit(n[j][0], d[j][3], 16),
                       a4 = __builtin_amdgcn_alignbit(n[j][1], n[j][0], 16);
        if (j < 2 || tid < 4) {
          *(u32x4*)(G + G_COPY) = (u32x4){a0, a1, a2, a3};
          *(u32x4*)(G + 2 * G_COPY) = (u32x4){d[j][1], d[j][2], d[j][3], n[j][0]};
          *(u32x4*)(G + 3 * G_COPY) = (u32x4){a1, a2, a3, a4};
        }
      }
    }
    const int ch = w & 3, hf = w >> 2;
    const LAS char *ap, *bp;
    {
      const char* U = lds + ch * CH_BYTES;
      const char* G = U + U_BYTES;
      const int a = n16, jj = h4 >> 1, e2 = h4 & 1;
      const int sg = (a + 1) & 3;
      bp = (const LAS char*)U + 512 + 32 * (a - jj) + 16 * e2 - 256 * hf - 192;
      ap = (const LAS char*)G + sg * G_COPY + 2 * (16 * jj + a - 8 * e2 + 9 - sg) + 256 * hf;
      asm volatile("" : "+v"(ap), "+v"(bp));
    }
    STAMP(36);
    {
      wait_flag<1>(rs, WS_FK + (unsigned)(5 * (4 * w + (l & 3)) + (quad >> 5)) * 128u, tag);
      STAMP(29);
      const unsigned gp = WS_KVT + (unsigned)(4 * w + (l >> 5)) * 81920u + (unsigned)quad * 512u + (unsigned)(l & 31) * 16u;
      const u32x4 g0 = ld16(rs, gp), g1 = ld16(rs, gp + 163840u);
      SCHED();
      char* Uw = lds + 512 + (128 * w + (l & ~3) + 3 - (l & 3)) * 4;
#pragma unroll
      for (int i = 0; i < 4; ++i) *(unsigned*)(Uw + i * CH_BYTES) = g0[i];
#pragma unroll
      for (int i = 0; i < 4; ++i) *(unsigned*)(Uw + 256 + i * CH_BYTES) = g1[i];
    }
    STAMP(28);
    BAR();
    STAMP(2);
    const int te = 2 * tid;
    u32x4 qa[2];
    u32x4 sv[2];
    u32x2 mg;
    const unsigned xo = (unsigned)(tid >> 4) * WX1P + (unsigned)quad * 256u + (unsigned)(tid & 15) * 16u;
    const unsigned mgo = WS_MOD + (unsigned)((l < 12 ? (l >> 2) * D_ + (l & 3) : 0) + quad * 4) * 8u;
    auto ld_sv = [&]() {
      sv[0] = ld16(rs, WS_ST1 + (unsigned)tid * 16u);
      sv[1] = ld16(rs, WS_ST1 + (unsigned)tid * 16u + 8192u);
    };
    auto ld_q = [&]() {
      qa[0] = ld16(rs, WS_QB + xo);
      qa[1] = ld16(rs, WS_QB + xo + 32u * WX1P);
    };
    f32x2 hh[2][2][2], base[2][2][2];
    auto prep = [&]() {
      const LAS char* ur = (const LAS char*)(lds + 512) + ((te & ~7) + 6 - (te & 7)) * 2;
      unsigned uu[2][4];
#pragma unroll
      for (int i = 0; i < 8; ++i) uu[i >> 2][i & 3] = *(const volatile LAS unsigned*)(ur + (i >> 2) * 2048 + (i & 3) * CH_BYTES);
      if (!__all(((sv[0][0] ^ tag) | (sv[0][2] ^ tag) | (sv[1][0] ^ tag) | (sv[1][2] ^ tag)) == 0u)) {
        _Pragma("unroll 1") for (unsigned spins = 0; spins < SPIN_CAP; ++spins) {
          ld_sv();
          if (__all(sv[0][0] == tag && sv[0][2] == tag && sv[1][0] == tag && sv[1][2] == tag)) break;
          __builtin_amdgcn_s_sleep(4);
        }
        asm volatile("" ::: "memory");
        ld_q();
        mg = ld8(rs, mgo);
        DRAIN();
      }
      if (!__all(mg[0] == tag)) {
        _Pragma("unroll 1") for (unsigned spins = 0; spins < SPIN_CAP; ++spins) {
          mg = ld8(rs, mgo);
          if (__all(mg[0] == tag)) break;
          __builtin_amdgcn_s_sleep(2);
        }
      }
      f32x2 cc2[2], ss2[2], dv2[2];
      float cch[4], sch[4];
#pragma unroll
      for (int i = 0; i < 4; ++i) {
        const float sa = __uint_as_float(__builtin_amdgcn_readlane(mg[1], i)), ca = __uint_as_float(__builtin_amdgcn_readlane(mg[1], 4 + i)),
                    ga = __uint_as_float(__builtin_amdgcn_readlane(mg[1], 8 + i));
        cch[i] = ga * rbf(1.0f + ca);
        sch[i] = ga * sa;
      }
      cc2[0] = (f32x2){cch[0], cch[1]}; cc2[1] = (f32x2){cch[2], cch[3]};
      ss2[0] = (f32x2){sch[0], sch[1]}; ss2[1] = (f32x2){sch[2], sch[3]};
      dv2[0] = bf2(dd2[0]); dv2[1] = bf2(dd2[1]);
#pragma unroll
      for (int H = 0; H < 2; ++H)
#pragma unroll
        for (int e = 0; e < 2; ++e) {
          const unsigned st = sv[H][1 + 2 * e];
          const float a = __builtin_amdgcn_rcpf(bfhi(st)), b = -bflo(st) * a;
#pragma unroll
          for (int i = 0; i < 2; ++i) {
            const f32x2 q2 = bf2(qa[H][2 * e + i]);
            const f32x2 u2 = e ? (f32x2){bflo(uu[H][2 * i]), bflo(uu[H][2 * i + 1])} : (f32x2){bfhi(uu[H][2 * i]), bfhi(uu[H][2 * i + 1])};
            const f32x2 n1 = __builtin_elementwise_fma(q2, (f32x2){a, a}, (f32x2){b, b});
            hh[H][e][i] = __builtin_elementwise_fma(n1, cc2[i], ss2[i]);
            base[H][e][i] = __builtin_elementwise_fma(hh[H][e][i], u2 * dv2[i], q2);
            asm volatile("" : "+v"(hh[H][e][i]), "+v"(base[H][e][i]));
          }
        }
    };
    auto HF = [&]() { int x = hf; asm volatile("" : "+s"(x)); return x; };
    {
      const int a = n16;
      char* yp = lds + L_YP + (hf * 4 + ch) * 8192 + (h4 * 16 + ((a + 4 * h4) & 15)) * 16;
      f32x4 acc[8];
      u32x4 A[2][8], B[2][8];
      auto rd = [&](int k) {
        const int r = k / 24, j = k % 24;
        if (j >= 2 && j < 10) B[r & 1][9 - j] = *(const volatile LAS u32x4*)(bp + 512 * (9 - j) + 64 * (3 - r));
        else {
          const int pp = j < 2 ? 0 : (j - 8) >> 1;
          const u32x2 v = *(const volatile LAS u32x2*)(ap + 512 * pp + 64 * r + 8 * (j & 1));
          A[r & 1][pp][2 * (j & 1)] = v[0]; A[r & 1][pp][2 * (j & 1) + 1] = v[1];
        }
      };
#pragma unroll
      for (int k = 0; k < IC<conv_sched(CDIST).pre>::v; ++k) rd(k);
      SCHED();
      auto step = [&](auto ic) {
        constexpr int i = decltype(ic)::v, r = i / 36, pp = conv_sched(CDIST).pp[i], m = conv_sched(CDIST).m[i], k = conv_sched(CDIST).rd[i];
        if (i == 0) __builtin_amdgcn_s_setprio(3);
        if (i == 60) __builtin_amdgcn_s_setprio(2);
        if (i == 100) __builtin_amdgcn_s_setprio(1);
        if (i == 126) __builtin_amdgcn_s_setprio(0);
        acc[m + pp] = MFMA16(A[r & 1][pp], B[r & 1][m], i < 8 ? ZERO4 : acc[m + pp]);
        SCHED();
        if (k >= 0) rd(k);
        if (i == 0) atv = *(const GAS unsigned*)(atb + ato);
        if (i % 6 == 2 && i / 3 < NRAW) {
          if (!HF()) { LDW(i / 3); LDW(i / 3 + 1); }
        }
        if (i > Q_AT && (i - Q_AT) % 2 == 1 && i - Q_AT < NRAW) {
          if (HF()) { LDW(i - Q_AT - 1); LDW(i - Q_AT); }
        }
        if (i == SV_AT) { ld_sv(); bs_raw = ((const GAS u16*)p.b_sp)[trow]; }
        if (r == 3 && pp >= 1 && m == 7 - pp) *(f32x4*)(yp + (pp - 1) * 1024) = acc[pp - 1];
        SCHED();
      };
      static_for<0, Q_AT>(step);
      STAMP(3);
      DRAIN();
      asm volatile("" ::"v"(atv));
      ld_q();
      mg = ld8(rs, mgo);
      SCHED();
      STAMP(4);
      static_for<Q_AT, 144>(step);
      *(f32x4*)(yp + 7 * 1024) = acc[7];
      STAMP(5);
    }
    prep();
    e_fw1 = ld4(rs, fw1o);
    STAMP(6);
    const LAS char* yr = (const LAS char*)(lds + L_YP) + (te >> 8) * 1024 + (((te >> 2) & 3) * 16 + ((((te >> 4) & 15) + 4 * ((te >> 2) & 3)) & 15)) * 16 + (te & 3) * 4;
    asm volatile("" : "+v"(yr));
    BAR();
    STAMP(7);
    f32x2 y[2][8];
#pragma unroll
    for (int i = 0; i < 16; ++i) y[i >> 3][i & 7] = *(const volatile LAS f32x2*)(yr + (i >> 3) * 4096 + (i & 7) * 8192);
#pragma unroll
    for (int H = 0; H < 2; ++H) {
      u32x4 o;
#pragma unroll
      for (int e = 0; e < 2; ++e)
#pragma unroll
        for (int i = 0; i < 2; ++i) {
          const float x0 = __builtin_fmaf(hh[H][e][i][0], y[H][2 * i][e] + y[H][4 + 2 * i][e], base[H][e][i][0]);
          const float x1 = __builtin_fmaf(hh[H][e][i][1], y[H][2 * i + 1][e] + y[H][5 + 2 * i][e], base[H][e][i][1]);
          o[2 * e + i] = pack2(x0, x1);
        }
      st16(rs, WS_X1B + (unsigned)(32 * H) * WX1P + xo, o);
    }
    STAMP(8);
  } else {
    const rsrc_t rwi = __builtin_amdgcn_make_buffer_rsrc((void*)p.w_in, 0, D_ * F_ * 2, 0x00020000);
    const rsrc_t rwo = __builtin_amdgcn_make_buffer_rsrc((void*)p.w_out, 0, FH * D_ * 2, 0x00020000);
    STAMP(32);
    const int job = xcd + 8 * slot;
    const int T = job >= 50 && w < 3 && job - 50 + 46 * w < 128 ? job - 50 + 46 * w : -1;
    const bool unit = slot >= 8 || (slot >= 2 && slot < 6);
    const int ucb = slot >= 8 ? 8 + xcd : xcd, kg = (slot >= 8 ? slot : slot - 2) & 3;
    float* tab = (float*)(lds + L_TAB);
    float* red = (float*)(lds + L_RED);
    auto issue_unit = [&](int i) {
      const int g = i * 64 + l;
      __builtin_amdgcn_raw_ptr_buffer_load_lds(rwi, (LAS void*)(lds + L_J3 + i * 1024), 16, (unsigned)((g / 14) * (F_ * 2) + (g % 14) * 16),
                                               (unsigned)(kg * 160 * F_ * 2 + ucb * CB * 2), 0, 0);
    };
    if (T >= 0) {
      char* qt = lds + L_QT + w * 20480;
      const unsigned so = (unsigned)(16 * T) * 1280u;
#pragma unroll
      for (int n = 0; n < 16; ++n)
        __builtin_amdgcn_raw_ptr_buffer_load_lds(rq, (LAS void*)(qt + n * 1024), 16, l16 ^ (unsigned)(n * 16), so + (unsigned)(n * 1280), 0, 0);
#pragma unroll
      for (int j = 0; j < 4; ++j)
        __builtin_amdgcn_raw_ptr_buffer_load_lds(rq, (LAS void*)(qt + 16384 + j * 1024), 16, (unsigned)(h4 * 1280 + ((n16 ^ (4 * j + h4)) * 16)), so + (unsigned)(4 * j * 1280 + 1024), 0, 0);
      tag = __builtin_amdgcn_readfirstlane(epv) * 2654435761u + 0x9E3779B9u;
      STAMP(34);
      DRAIN();
      STAMP(35);
      const bf2_t one = {(__bf16)1.0f, (__bf16)1.0f};
      float s1[2] = {0.f, 0.f}, s2[2] = {0.f, 0.f};
      const int n0 = 2 * (l & 7);
      u32x4 qv[20];
#pragma unroll
      for (int i = 0; i < 20; ++i)
#pragma unroll
        for (int e = 0; e < 2; ++e) {
          const int n = n0 + e, ck = 4 * (i & 15) + h4;
          const u32x2 v = *(const volatile LAS u32x2*)(qt + (i < 16 ? n * 1024 : 16384 + n * 256) + ((ck ^ n) * 16) + (l & 8));
          qv[i][2 * e] = v[0]; qv[i][2 * e + 1] = v[1];
        }
#pragma unroll
      for (int i = 0; i < 20; ++i) {
        st16(rs, WS_QB + (unsigned)(T >> 1) * WX1P + (unsigned)i * 2048u + (unsigned)((l >> 3) * 256 + (T & 1) * 128 + (l & 7) * 16), qv[i]);
#pragma unroll
        for (int e = 0; e < 4; ++e) {
          const unsigned x = qv[i][e];
          const bf2_t v = __builtin_bit_cast(bf2_t, x);
          s1[e >> 1] = __builtin_amdgcn_fdot2_f32_bf16(v, one, s1[e >> 1], false);
          s2[e >> 1] = __builtin_amdgcn_fdot2_f32_bf16(v, v, s2[e >> 1], false);
        }
        SCHED();
      }
      u32x4 o = {tag, 0u, tag, 0u};
#pragma unroll
      for (int e = 0; e < 2; ++e) {
        const float t1 = sum_rows(s1[e] + dpp_f<0x128>(s1[e])), t2 = sum_rows(s2[e] + dpp_f<0x128>(s2[e]));
        const float mean = t1 * (1.0f / D_);
        const float var = __builtin_fmaxf(t2 * (1.0f / D_) - mean * mean, 0.f);
        o[2 * e + 1] = pack2(mean, __builtin_amdgcn_sqrtf(rbf(rbf(var) + 1e-5f)));
      }
      STAMP(1);
      DRAIN();
      if (l < 8) st16(rs, WS_ST1 + (unsigned)T * 128u + l16, o);
      STAMP(3);
    }
    if (job < 60) {
      const int grp = job < 10 ? 40 + job : (job < 50 ? job - 10 : job);
      if (job >= 50 && T < 0) __builtin_amdgcn_s_sleep(LATE_SLEEP);
      if (job >= 10 && T < 0) __builtin_amdgcn_s_sleep(AUX_SLEEP);
      const unsigned cv = *(const GAS unsigned*)((const GAS u16*)p.cvec + 80 * w + 2 * (l < 40 ? l : 0));
      u32x4 pre[10];
#pragma unroll
      for (int pp = 0; pp < 10; ++pp) pre[pp] = *(const GAS u32x4*)((const GAS u16*)p.w_mod + (size_t)(80 * w + pp * 8 + (l >> 3)) * 3840 + grp * 64 + (l & 7) * 8);
      SCHED();
      STAMP(34);
      tag = __builtin_amdgcn_readfirstlane(epv) * 2654435761u + 0x9E3779B9u;
      if (l < 40) {
        const float c0 = bflo(cv), c1 = bfhi(cv);
        *(f32x2*)(tab + w * 80 + 2 * l) = (f32x2){rbf(c0 * __builtin_amdgcn_rcpf(1.0f + __expf(-c0))), rbf(c1 * __builtin_amdgcn_rcpf(1.0f + __expf(-c1)))};
      }
      asm volatile("s_waitcnt lgkmcnt(0)" ::: "memory");
      f32x2 acc[4];
#pragma unroll
      for (int e = 0; e < 4; ++e) acc[e] = (f32x2){0.f, 0.f};
#pragma unroll
      for (int pp = 0; pp < 10; ++pp) {
        const float sv = tab[w * 80 + pp * 8 + (l >> 3)];
#pragma unroll
        for (int e = 0; e < 4; ++e) acc[e] = __builtin_elementwise_fma((f32x2){sv, sv}, bf2(pre[pp][e]), acc[e]);
      }
      STAMP(35);
      asm volatile("" : "+v"(acc[0]), "+v"(acc[1]), "+v"(acc[2]), "+v"(acc[3]));
#pragma unroll
      for (int e = 0; e < 8; ++e) { float a = acc[e >> 1][e & 1]; a += dpp_f<0x128>(a); acc[e >> 1][e & 1] = sum_rows(a); }
      if (l < 8) {
        *(f32x4*)(red + w * 64 + l * 8) = (f32x4){acc[0][0], acc[0][1], acc[1][0], acc[1][1]};
        *(f32x4*)(red + w * 64 + l * 8 + 4) = (f32x4){acc[2][0], acc[2][1], acc[3][0], acc[3][1]};
      }
      if (w == 7) __builtin_amdgcn_s_setprio(2);
      BAR();
      if (w == 7) {
        float t = 0.f;
#pragma unroll
        for (int r = 0; r < 8; ++r) t += red[r * 64 + l];
        st8(rs, WS_MOD + (unsigned)(grp * 64 + l) * 8u, (u32x2){tag, __float_as_uint(rbf(t))});
        __builtin_amdgcn_s_setprio(0);
      } else if (unit) {
#pragma unroll
        for (int k = 0; k < 5; ++k) issue_unit(w * 5 + k);
      }
    } else if (unit && w >= 3 && w < 7) {
      __builtin_amdgcn_s_sleep(AUX_SLEEP);
#pragma unroll
      for (int k = 0; k < 9; ++k) issue_unit((w - 3) * 9 + k < 35 ? (w - 3) * 9 + k : 34);
    }
    tag = __builtin_amdgcn_readfirstlane(epv) * 2654435761u + 0x9E3779B9u;
    if (unit) {
      auto mod_tab = [&](int which, float add, float* dst) {
        const unsigned o = WS_MOD + (unsigned)(which * D_ + kg * 160) * 8u;
        u32x4 g0, g1;
        _Pragma("unroll 1") for (unsigned spins = 0; spins < SPIN_CAP; ++spins) {
          g0 = ld16(rs, o + l16);
          g1 = ld16(rs, o + 1024u + (l16 & 255u));
          if (__all(g0[0] == tag && g0[2] == tag && g1[0] == tag && g1[2] == tag)) break;
          __builtin_amdgcn_s_sleep(2);
        }
        *(f32x2*)(dst + 2 * l) = (f32x2){rbf(add + __uint_as_float(g0[1])), rbf(add + __uint_as_float(g0[3]))};
        if (l < 16) *(f32x2*)(dst + 128 + 2 * l) = (f32x2){rbf(add + __uint_as_float(g1[1])), rbf(add + __uint_as_float(g1[3]))};
      };
      if (w == 7) {
        STAMP(1);
        if (slot >= 8) __builtin_amdgcn_s_sleep(64);
        mod_tab(4, 1.0f, tab);
        BAR();
        STAMP(2);
        mod_tab(3, 0.0f, tab + 160);
        BAR();
      } else {
        DRAIN();
        STAMP(36);
        BAR();
        STAMP(2);
        const char* bp = lds + L_J3 + (4 * h4 + (n16 >> 2)) * 224 + (n16 & 3) * 8 + w * 32;
        const float* tp = tab + 4 * h4;
        u32x4 f3[5];
        float cs = 0.f, b1 = 0.f;
        const bool pad = w == 6 && n16 >= 12;
        const unsigned padv = (n16 == 12 && ucb >= 8) ? 0x3f803f80u : 0u;
#pragma unroll
        for (int k = 0; k < 5; ++k) f3[k] = trfrag(bp + k * 7168, 16 * 224);
#pragma unroll
        for (int k = 0; k < 5; ++k) {
          const f32x4 ca = *(const f32x4*)(tp + k * 32), cb4 = *(const f32x4*)(tp + k * 32 + 16);
          u32x4 o;
#pragma unroll
          for (int e = 0; e < 4; ++e) {
            const float c0 = e < 2 ? ca[2 * e] : cb4[2 * e - 4], c1 = e < 2 ? ca[2 * e + 1] : cb4[2 * e - 3];
            const float p0 = bflo(f3[k][e]) * c0, p1 = bfhi(f3[k][e]) * c1;
            cs += p0 + p1;
            o[e] = pad ? padv : pack2(p0, p1);
          }
          st16(rs, WS_WINF + (unsigned)(ucb * 7 + w) * 20480u + (unsigned)(kg * 5 + k) * 1024u + l16, o);
        }
        cs = sum_rows(cs);
        DRAIN();
        if (l == 0) st4(rs, WS_FW1 + (unsigned)((ucb * 8 + w) * 4 + kg) * 128u, tag);
        STAMP(5);
        BAR();
#pragma unroll
        for (int k = 0; k < 5; ++k) {
          const f32x4 sa = *(const f32x4*)(tp + 160 + k * 32), sb4 = *(const f32x4*)(tp + 160 + k * 32 + 16);
#pragma unroll
          for (int e = 0; e < 4; ++e) {
            const float s0 = e < 2 ? sa[2 * e] : sb4[2 * e - 4], s1 = e < 2 ? sa[2 * e + 1] : sb4[2 * e - 3];
            b1 = __builtin_fmaf(s0, bflo(f3[k][e]), __builtin_fmaf(s1, bfhi(f3[k][e]), b1));
          }
        }
        b1 = sum_rows(b1);
        if (pad) { cs = padv ? 160.0f : 0.0f; b1 = 0.0f; }
        if (l < 16) st16(rs, WS_CS + (unsigned)((ucb * 4 + kg) * 112 + w * 16 + l) * 16u, (u32x4){tag, __float_as_uint(cs), tag, __float_as_uint(b1)});
      }
    } else {
      if (job >= 60 && T < 0) __builtin_amdgcn_s_sleep(100);
      if (slot < 2 && w >= 2 && w < 6) {
        const int c5 = slot * 4 + w - 2, c2 = xcd;
        char* st = lds + L_J5 + (w - 2) * 18432;
#pragma unroll
        for (int r = 0; r < 18; ++r) {
          const int g = r * 64 + l;
          __builtin_amdgcn_raw_ptr_buffer_load_lds(rwo, (LAS void*)(st + r * 1024), 16, (unsigned)((g / 10) * (D_ * 2) + (g % 10) * 16),
                                                   (unsigned)(xcd * CB * D_ * 2 + c5 * 160), 0, 0);
        }
        DRAIN();
        const char* bp = st + (4 * h4 + (n16 >> 2)) * 160 + (n16 & 3) * 8;
        const unsigned base = WS_WOUTF + (unsigned)c5 * WBLK;
#pragma unroll
        for (int dj = 0; dj < 5; ++dj) {
          st16(rs, base + (unsigned)c2 * 10240u + (unsigned)dj * 1024u + l16, trfrag(bp + dj * 32, 32 * 160));
          st16(rs, base + (unsigned)c2 * 10240u + (unsigned)(5 + dj) * 1024u + l16, trfrag(bp + 16 * 160 + dj * 32, 32 * 160));
          st16(rs, base + 81920u + (unsigned)c2 * 5120u + (unsigned)dj * 1024u + l16, trfrag(bp + 64 * 160 + dj * 32, 16 * 160));
          st8(rs, base + 122880u + (unsigned)((c2 >> 1) * 5 + dj) * 1024u + (unsigned)(c2 & 1) * 512u + (unsigned)l * 8u, trd(bp + 96 * 160 + dj * 32));
        }
        fw2 = WS_FW2 + (unsigned)(c5 * 8 + xcd) * 128u;
      }
    }
    STAMP(4);
#pragma unroll
    for (int i = 0; i < NRAW; ++i) LDW(i);
    bs_raw = ((const GAS u16*)p.b_sp)[trow];
    e_fw1 = ~tag;
    asm volatile(".Lmega_aux_end:");
    asm volatile("" ::"v"(cpf));
    STAMP(8);
  }

  LAS char* xa = (LAS char*)(lds + L_XI) + l * 16;
  LAS char* xb = xa + 65536;
  asm volatile("" : "+v"(xb));
#define LDA(ks, rt) (*(const volatile LAS u32x4*)((ks) < 16 ? xa + ((ks) * 4 + (rt)) * 1024 : xb + (((ks) - 16) * 4 + (rt)) * 1024))
  auto lda1 = [&](int ks, u32x4* A) {
#pragma unroll
    for (int rt = 0; rt < 4; ++rt) A[rt] = LDA(ks, rt);
  };
  const LAS char* tb4 = (const LAS char*)(lds + L_MISC) + h4 * 16;
  const LAS char* tbn = (const LAS char*)(lds + L_MISC) + n16 * 4;
  asm volatile("" : "+v"(tb4), "+v"(tbn));
  u32x4 xr[5][2];
  auto ld_x1 = [&](int m0, int m1) {
#pragma unroll
    for (int m = m0; m < m1; ++m) {
      if (m == 0) __builtin_amdgcn_s_setprio(2);
      if (m == 1) __builtin_amdgcn_s_setprio(1);
      if (m == 3) __builtin_amdgcn_s_setprio(0);
#pragma unroll
      for (int e = 0; e < 2; ++e)
        xr[m][e] = ld16(rs, l16, WS_X1B + (unsigned)(R * 2 + (w & 1)) * WX1P + (unsigned)(4 * m + (w >> 1)) * 2048u + (unsigned)e * 1024u);
      SCHED();
    }
  };
  LAS char* xw = xa + w * 2048;
  auto put_x1 = [&](int m) {
    LAS char* d = m < 4 ? xw + m * 16384 : xb + w * 2048;
#pragma unroll
    for (int e = 0; e < 2; ++e) {
      *(volatile LAS u32x2*)(d + 8 * e) = (u32x2){xr[m][e][0], xr[m][e][1]};
      *(volatile LAS u32x2*)(d + 1024 + 8 * e) = (u32x2){xr[m][e][2], xr[m][e][3]};
    }
  };
  auto poison_u = [&]() {
#pragma unroll
    for (int k = 0; k < 2; ++k) {
      const unsigned o = (unsigned)(wj * 2 + k) * 1024u + l16;
      const unsigned rt = o / 3584u, r = o - rt * 3584u;
      st16(rs, WS_UF + ((unsigned)(R * 4) + rt) * 8u * 3584u + (unsigned)c * 3584u + r, (u32x4){~0u, ~0u, ~0u, ~0u});
    }
  };
  const unsigned wob = WS_WOUTF + (unsigned)c * WBLK;
  const LAS char* wrd = (const LAS char*)(lds + L_WS) + w * 8192 + n16 * 256 + ((h4 ^ ((4 - (n16 >> 2)) & 3)) * 64);
#define DMA_W(g, q) __builtin_amdgcn_raw_ptr_buffer_load_lds(rsp, (LAS void*)(lds + L_WS + w * 8192 + (((g) + 1) & 1) * 4096 + (q) * 1024), 16, WVO(q), WSO(g, q), 0, 0)
#define RD_W(g) do { _Pragma("unroll") for (int i_ = 0; i_ < 4; ++i_) \
                      wsp[(g) & 3][((g) >> 2) * 4 + i_] = *(const volatile LAS u32x4*)(wrd + (((g) + 1) & 1) * 4096 + ((i_ ^ (n16 & 3)) * 16)); } while (0)
  auto park = [&]() {
    LAS char* d = (LAS char*)(lds + L_WS) + w * 8192 + l * 16;
#pragma unroll
    for (int g = 0; g < NRAW / 4; ++g) {
#pragma unroll
      for (int q = 0; q < 4; ++q) *(volatile LAS u32x4*)(d + ((g + 1) & 1) * 4096 + q * 1024) = raw[4 * g + q];
      if (g < 2) RD_W(g);
    }
    asm volatile("s_waitcnt lgkmcnt(0)" ::: "memory");
  };
#define DMA_AB(k) dma1k(rs, lds + L_AB + ((k) * 8 + w) * 1024, l16, wob + (unsigned)((k) * 8 + w) * 1024u)
  const int rto = w & 3, cls = w >> 2;
  u32x2 xp[3];
  constexpr int W = G2W;
  const unsigned zb = WS_Z2F + (unsigned)c * 458752u + (unsigned)wk * 8192u;
  const unsigned fzo = WS_FZ + (unsigned)(c * 32 + wk * 4 + (l & 3)) * 128u;
  u32x4 fr[W];
#define LDZ(i) fr[(i) % W] = ld16(rs, l16, zb + (unsigned)((i) >> 3) * 65536u + (unsigned)((i) & 7) * 1024u)
  u32x4 cr[5];
#define LDCR(k) cr[k] = ld16(rs, l16, wob + 81920u + (unsigned)(w * 5 + (k)) * 1024u)
  auto rd_xp = [&]() {
#pragma unroll
    for (int j = 0; j < 3; ++j) {
      const int q4 = 5 * c + (cls ? (3 + (j < 2 ? j : 1)) : j);
      xp[j] = *(const volatile LAS u32x2*)(xa + ((q4 >> 1) * 4 + rto) * 1024 + (q4 & 1) * 8);
    }
  };
  if (w < 7) {
    u32x4 B2[20], B1[20];
    const unsigned b2o = WS_WINF + (unsigned)((8 + c) * 7 + w) * 20480u, b1o = WS_WINF + (unsigned)(c * 7 + w) * 20480u;
#define LDB2(ks) B2[ks] = ld16(rs, l16, b2o + (unsigned)(ks) * 1024u)
#define LDB1(ks) B1[ks] = ld16(rs, l16, b1o + (unsigned)(ks) * 1024u)
    const bool early = __all(e_fw1 == tag);
    asm volatile("" ::: "memory");
    {
      const unsigned le = early ? l16 : OOB;
#pragma unroll
      for (int ks = 0; ks < NB2A; ++ks) B2[ks] = ld16(rs, le, b2o + (unsigned)ks * 1024u);
      SCHED();
      if (is_conv) VMW(NB2A);
    }
    STAMP(33);
    BAR();
    STAMP(37);
    if (!early) {
      wait_flag<4>(rs, fw1o, tag);
      DRAIN();
      st4(rs, l == 0 ? fw2 : OOB, tag);
#pragma unroll
      for (int ks = 0; ks < NB2A; ++ks) LDB2(ks);
    }
#pragma unroll
    for (int ks = NB2A; ks < 20; ++ks) {
      LDB2(ks);
      if (ks == 12) { SCHED(); park(); SCHED(); }
      if (ks == 13) { SCHED(); __builtin_amdgcn_s_barrier(); SCHED(); }
    }
    STAMP(9);
    BAR();
    STAMP(10);
    ld_x1(0, 3);
    SCHED();
#pragma unroll
    for (int m = 0; m < 3; ++m) put_x1(m);
    ld_x1(3, 5);
    poison_u();
    SCHED();
    if (w == 6) __builtin_amdgcn_s_setprio(2);
    BAR();
    STAMP(11);
    f32x4 acc[4];
    u32x4 A[2][4];
    lda1(0, A[0]);
#pragma unroll
    for (int ks = 0; ks < 20; ++ks) {
      if (ks == 6) put_x1(3);
      if (ks == 8) put_x1(4);
      if (ks == 11) BAR();
      if (ks >= 4) LDB1(ks - 4);
      if (ks >= 16) LDB1(ks);
      if (ks == 12) RD_W(2);
      SCHED();
#pragma unroll
      for (int rt = 0; rt < 4; ++rt) {
        if (ks < 19) A[(ks + 1) & 1][rt] = LDA(ks + 1, rt);
        SCHED();
        acc[rt] = MFMA16(A[ks & 1][rt], B2[ks], ks ? acc[rt] : ZERO4);
        SCHED();
      }
    }
    STAMP(12);
    if (w == 6) {
      if (n16 == 12) {
#pragma unroll
        for (int rt = 0; rt < 4; ++rt) *(f32x4*)(lds + L_S1 + (rt * 16 + 4 * h4) * 4) = acc[rt];
      }
      asm volatile("" ::: "memory");
      *(volatile LAS unsigned*)(lds + L_S1 + 256) = tag;
      __builtin_amdgcn_s_setprio(0);
    }
    VMW(20);
    BAR();
    STAMP(13);
    f32x2 zz[8];
    f32x4 cs1a, cs1b;
    {
      f32x4 rs4[4], nm4[4];
#pragma unroll
      for (int rt = 0; rt < 4; ++rt) {
        rs4[rt] = *(const LAS f32x4*)(tb4 + (L_T2 - L_MISC) + rt * 64);
        nm4[rt] = *(const LAS f32x4*)(tb4 + (L_T2 - L_MISC) + 256 + rt * 64);
      }
      const f32x2 cs2 = *(const f32x2*)(lds + L_CS1 + (112 + 16 * w + n16) * 8);
      const f32x2 csv = {cs2[0], cs2[0]}, b1v = {cs2[1], cs2[1]};
      float v1[16], v2[16];
#pragma unroll
      for (int i = 0; i < 8; ++i) {
        const int rt = i >> 1;
        const f32x2 a2 = i & 1 ? HI2(acc[rt]) : LO2(acc[rt]), n2 = i & 1 ? HI2(nm4[rt]) : LO2(nm4[rt]), r2 = i & 1 ? HI2(rs4[rt]) : LO2(rs4[rt]);
        zz[i] = gelu2(__builtin_elementwise_fma(__builtin_elementwise_fma(n2, csv, a2), r2, b1v));
        asm volatile("" : "+v"(zz[i]));
        DMA_W(3 + (i >> 2), i & 3);
        const f32x2 sq = zz[i] * zz[i];
        v1[2 * i] = zz[i][0]; v1[2 * i + 1] = zz[i][1]; v2[2 * i] = sq[0]; v2[2 * i + 1] = sq[1];
      }
      const float t1 = tr_sum16(v1, l);
      const float t2 = tr_sum16(v2, l);
      *(f32x2*)(lds + L_Z1S + (w * 64 + (n16 >> 2) * 16 + 4 * h4 + (n16 & 3)) * 8) = (f32x2){t1, t2};
      lda1(0, A[0]);
      cs1a = *(const f32x4*)(lds + L_CS1 + (16 * w + 4 * h4) * 8);
      cs1b = *(const f32x4*)(lds + L_CS1 + (16 * w + 4 * h4) * 8 + 16);
    }
    BAR();
    STAMP(14);
#pragma unroll
    for (int ks = 0; ks < 20; ++ks) {
      if (ks == Z1SPLIT) {
        STAMP(15);
        BAR();
        STAMP(16);
        f32x4 ra[4], rb[4];
#pragma unroll
        for (int rt = 0; rt < 4; ++rt) {
          ra[rt] = *(const LAS f32x4*)(tb4 + (L_ST2 - L_MISC) + rt * 64);
          rb[rt] = *(const LAS f32x4*)(tb4 + (L_ST2 - L_MISC) + 256 + rt * 64);
        }
#pragma unroll
        for (int i = 0; i < 8; ++i) {
          zz[i] = __builtin_elementwise_fma(zz[i], i & 1 ? HI2(ra[i >> 1]) : LO2(ra[i >> 1]), i & 1 ? HI2(rb[i >> 1]) : LO2(rb[i >> 1]));
          asm volatile("" : "+v"(zz[i]));
        }
        const u32x4 o0 = {pack2(zz[0][0], zz[2][0]), pack2(zz[0][1], zz[2][1]), pack2(zz[1][0], zz[3][0]), pack2(zz[1][1], zz[3][1])};
        const u32x4 o1 = {pack2(zz[4][0], zz[6][0]), pack2(zz[4][1], zz[6][1]), pack2(zz[5][0], zz[7][0]), pack2(zz[5][1], zz[7][1])};
        const unsigned zo = WS_Z2F + (unsigned)((c * 7 + w) * 64 + 4 * (R >> 1) + h4) * 1024u + (unsigned)(R & 1) * 512u + (unsigned)n16 * 16u;
        st16(rs, zo, o0);
        st16(rs, zo + 256u, o1);
        SCHED();
      }
      if (ks == 0) { VMW(4); RD_W(3); }
      if (ks == 2) { VMW(0); RD_W(4); }
      if (ks == 8) { VMW(4 + 2); RD_W(5); }
      if (ks == 11) rd_xp();
      if (ks == 13) { VMW(4 + 5 + 2); RD_W(6); }
      if (ks >= 13 && ks < 19) DMA_AB(ks - 13);
      if (ks >= 2 && ks < 6) { DMA_W(5 + ((ks - 2) >> 1), 2 * (ks & 1)); DMA_W(5 + ((ks - 2) >> 1), 2 * (ks & 1) + 1); }
      if (ks >= 6 && ks < 9) LDCR(ks - 6);
      if (ks == 12) { LDCR(3); LDCR(4); }
      if (ks == 10 || ks == 11) { DMA_W(7, 2 * (ks & 1)); DMA_W(7, 2 * (ks & 1) + 1); }
      SCHED();
#pragma unroll
      for (int rt = 0; rt < 4; ++rt) {
        if (ks < 19) A[(ks + 1) & 1][rt] = LDA(ks + 1, rt);
        SCHED();
        acc[rt] = MFMA16(B1[ks], A[ks & 1][rt], ks ? acc[rt] : ZERO4);
        SCHED();
      }
    }
    STAMP(18);
    VMW(2 + 2 + 6);
    RD_W(7);
    const f32x2 c01 = {cs1a[0], cs1a[2]}, b01 = {cs1a[1], cs1a[3]}, c23 = {cs1b[0], cs1b[2]}, b23 = {cs1b[1], cs1b[3]};
    u32x2 g1[4];
    auto z1g = [&](int rt) {
      const float r1 = *(const LAS float*)(tbn + (L_T2 - L_MISC) + rt * 64), n1 = *(const LAS float*)(tbn + (L_T2 - L_MISC) + 256 + rt * 64);
      const f32x2 r2 = {r1, r1}, n2 = {n1, n1};
      const f32x2 a = gelu2(__builtin_elementwise_fma(__builtin_elementwise_fma(n2, c01, LO2(acc[rt])), r2, b01));
      const f32x2 b = gelu2(__builtin_elementwise_fma(__builtin_elementwise_fma(n2, c23, HI2(acc[rt])), r2, b23));
      g1[rt] = (u32x2){pack2(a[0], a[1]), pack2(b[0], b[1])};
      asm volatile("" : "+v"(g1[rt]));
    };
    SCHED();
    VMW(2 + 6);
    BAR();
    STAMP(17);
#pragma unroll
    for (int rt = 0; rt < 4; ++rt) {
      if (rt < 2) { DMA_AB(6 + 2 * rt); DMA_AB(7 + 2 * rt); }
      z1g(rt);
      *(u32x2*)(lds + L_Z1S + ((w * 4 + rt) * 64 + l) * 8) = g1[rt];
    }
  } else {
    __builtin_amdgcn_s_setprio(3);
    DRAIN();
    BAR();
    if (is_conv && l == 0) st4(rs, WS_FX + (unsigned)quad * 128u, tag);
    STAMP(9);
    *(volatile LAS unsigned*)(lds + L_S1 + 256) = ~tag;
    u32x4 gv, cv[2][2][4];
    const unsigned gvo = WS_MOD + (unsigned)(5 * D_ + 80 * c) * 8u + (l < 40 ? l16 : 0u);
    auto ld_consts = [&]() {
      gv = ld16(rs, gvo);
#pragma unroll
      for (int i = 0; i < 16; ++i) cv[i >> 3][(i >> 2) & 1][i & 3] = ld16(rs, l < 56 ? l16 : 0u, WS_CS + (unsigned)(((c + 8 * (i >> 3)) * 4 + (i & 3)) * 112 + 56 * ((i >> 2) & 1)) * 16u);
    };
    auto ok_consts = [&]() {
      unsigned x = (gv[0] ^ tag) | (gv[2] ^ tag);
#pragma unroll
      for (int i = 0; i < 16; ++i) x |= (cv[i >> 3][(i >> 2) & 1][i & 3][0] ^ tag) | (cv[i >> 3][(i >> 2) & 1][i & 3][2] ^ tag);
      asm volatile("" : "+v"(x));
      return __all(x == 0u);
    };
    auto touch_w = [&](int k) {
      cpf ^= __builtin_amdgcn_raw_buffer_load_b32(rsp, (unsigned)(h4 * 4096 + (n16 >> 1) * 512 + (n16 & 1) * 128),
                                                  (unsigned)(R * 64 + (c & 3) * 16 + k * 4) * 4096u + 256u, 0);
    };
    park();
    __builtin_amdgcn_s_barrier();
    ld_consts();
    STAMP(31);
    _Pragma("unroll 1") for (unsigned spins = 0; spins < SPIN_CAP; ++spins) {
      const unsigned f0 = ld4(rs, WS_FX + (unsigned)l * 128u), f1 = ld4(rs, WS_FX + 8192u + (unsigned)l * 128u),
                     f2 = ld4(rs, l < 32 ? WS_FX + 16384u + (unsigned)l * 128u : WS_FW1 + (unsigned)(c * 32 + (l < 60 ? l - 32 : 0)) * 128u);
      if (__all(f0 == tag && f1 == tag && f2 == tag)) break;
      __builtin_amdgcn_s_sleep(POLL_SLEEP);
    }
    BAR();
    STAMP(10);
    ld_x1(0, 5);
    __builtin_amdgcn_s_setprio(3);
    if (!ok_consts()) {
      _Pragma("unroll 1") for (unsigned spins = 0; spins < SPIN_CAP; ++spins) {
        ld_consts();
        if (ok_consts()) break;
        __builtin_amdgcn_s_sleep(8);
      }
    }
    if (l < 40) *(f32x2*)(lds + L_GB + l * 8) = (f32x2){__uint_as_float(gv[1]), __uint_as_float(gv[3])};
    if (l < 56) {
#pragma unroll
      for (int i = 0; i < 4; ++i) {
        const u32x4* q4 = cv[i >> 1][i & 1];
        *(f32x2*)(lds + L_CS1 + (56 * i + l) * 8) =
            (f32x2){(__uint_as_float(q4[0][1]) + __uint_as_float(q4[1][1])) + (__uint_as_float(q4[2][1]) + __uint_as_float(q4[3][1])),
                    (__uint_as_float(q4[0][3]) + __uint_as_float(q4[1][3])) + (__uint_as_float(q4[2][3]) + __uint_as_float(q4[3][3]))};
      }
    }
    SCHED();
#pragma unroll
    for (int m = 0; m < 3; ++m) put_x1(m);
    BAR();
    f32x4 acc[4];
    u32x4 A[2][4];
    lda1(0, A[0]);
#pragma unroll
    for (int ks = 0; ks < 20; ++ks) {
      if (ks == 6) put_x1(3);
      if (ks == 8) put_x1(4);
      if (ks == 11) BAR();
      if (ks == 4) RD_W(2);
      if (ks >= 6 && ks < 14) DMA_W(3 + ((ks - 6) >> 2), (ks - 6) & 3);
      if (ks >= 1 && ks < 5) touch_w(ks - 1);
      if (ks < 19) lda1(ks + 1, A[(ks + 1) & 1]);
      SCHED();
#pragma unroll
      for (int rt = 0; rt < 4; ++rt) acc[rt] = MFMA16(A[ks & 1][rt], A[ks & 1][rt], ks ? acc[rt] : ZERO4);
      SCHED();
    }
    {
      if ((n16 >> 2) == h4) {
#pragma unroll
        for (int rt = 0; rt < 4; ++rt) {
          const float lo = (l & 1) ? acc[rt][1] : acc[rt][0], hi = (l & 1) ? acc[rt][3] : acc[rt][2];
          *(volatile LAS float*)(tbn + (L_T2 - L_MISC) + rt * 64) = (l & 2) ? hi : lo;
        }
      }
      const float s2 = *(volatile LAS float*)(lds + L_T2 + l * 4) * (1.0f / D_);
      float s1;
      _Pragma("unroll 1") for (unsigned spins = 0; spins < SPIN_CAP * 16; ++spins) {
        const unsigned f = *(volatile LAS unsigned*)(lds + L_S1 + 256);
        s1 = *(volatile LAS float*)(lds + L_S1 + l * 4);
        if (f == tag) break;
      }
      const float nm = s1 * (-1.0f / D_);
      *(float*)(lds + L_T2 + l * 4) = __builtin_amdgcn_rsqf(__builtin_fmaxf(s2 - nm * nm, 0.f) + 1e-5f);
      *(float*)(lds + L_T2 + 256 + l * 4) = nm;
    }
    BAR();
    __builtin_amdgcn_s_setprio(0);
    asm volatile("" ::"v"(cpf));
    DRAIN();
    RD_W(3); RD_W(4);
    rd_xp();
    asm volatile("s_waitcnt lgkmcnt(0)" ::: "memory");
#pragma unroll
    for (int i = 0; i < 8; ++i) DMA_W(5 + (i >> 2), i & 3);
    const unsigned e_fw2 = ld4(rs, WS_FW2 + (unsigned)(c * 8 + (l & 7)) * 128u);
    if (!__all(e_fw2 == tag)) wait_flag<2>(rs, WS_FW2 + (unsigned)(c * 8 + (l & 7)) * 128u, tag);
    __builtin_amdgcn_s_setprio(3);
    BAR();
    {
      f32x2 t = {0.f, 0.f};
#pragma unroll
      for (int k = 0; k < 7; ++k) t += *(const f32x2*)(lds + L_Z1S + (k * 64 + l) * 8);
      st16(rs, WS_PART + (unsigned)(R * 8 + c) * 1024u + l16, (u32x4){tag, __float_as_uint(t[0]), tag, __float_as_uint(t[1])});
    }
    STAMP(14);
    {
      u32x4 v[8];
      const unsigned po = WS_PART + (unsigned)R * 8192u + l16;
      __builtin_amdgcn_s_sleep(16);
      _Pragma("unroll 1") for (unsigned spins = 0; spins < SPIN_CAP; ++spins) {
#pragma unroll
        for (int k = 0; k < 8; ++k) v[k] = ld16(rs, po + (unsigned)k * 1024u);
        unsigned x = 0;
#pragma unroll
        for (int k = 0; k < 8; ++k) x |= (v[k][0] ^ tag) | (v[k][2] ^ tag);
        asm volatile("" : "+v"(x));
        if (__all(x == 0u)) break;
        __builtin_amdgcn_s_sleep(1);
      }
      float t1 = 0.f, t2 = 0.f;
#pragma unroll
      for (int k = 0; k < 8; ++k) { t1 += __uint_as_float(v[k][1]); t2 += __uint_as_float(v[k][3]); }
      const float mean = t1 * (1.0f / FH);
      const float var = __builtin_fmaxf(t2 * (1.0f / FH) - mean * mean, 0.f);
      const float rstd = __builtin_amdgcn_rsqf(var + 1e-5f);
      *(float*)(lds + L_ST2 + l * 4) = rstd;
      *(float*)(lds + L_ST2 + 256 + l * 4) = -mean * rstd;
    }
    STAMP(15);
    BAR();
    RD_W(5); RD_W(6);
    asm volatile("s_waitcnt lgkmcnt(0)" ::: "memory");
#pragma unroll
    for (int q = 0; q < 4; ++q) DMA_W(7, q);
    DRAIN();
    RD_W(7);
    BAR();
    if (l == 0) st4(rs, WS_FZ + (unsigned)(c * 32 + R) * 128u, tag);
    STAMP(17);
    __builtin_amdgcn_s_setprio(0);
#pragma unroll
    for (int k = 0; k < 10; ++k) DMA_AB(k);
#pragma unroll
    for (int k = 0; k < 5; ++k) LDCR(k);
  }
  const rsrc_t rout = __builtin_amdgcn_make_buffer_rsrc((void*)p.out, 0, S_ * D_ * 2, 0x00020000);
  STAMP(19);
  u32x2 z1o[4];
  wait_flag<POLL_SLEEP>(rs, fzo, tag);
#pragma unroll
  for (int i = 0; i < W; ++i) { LDZ(i); SCHED(); }
  STAMP(20);

  const unsigned ub = WS_UF + (unsigned)((R * 4 + rto) * 8 + c) * 3584u;
  const float bs = bflo(bs_raw);
  const unsigned ur = WS_UF + (unsigned)((R * 4 + rto) * 8 + cls) * 3584u;
  u32x4 uf[4][2];
  f32x4 acc[2][4];
  f32x4* const bufw = (f32x4*)(lds + L_BUF) + w * 256 + l;
#define BUFW(slot, rt) bufw[(slot) * 2048 + (rt) * 64]
  {
    const f32x4* const rb = (const f32x4*)(lds + L_BUF) + cls * 2048 + rto * 64 + l;
    f32x4 rdv[4], s;
    u32x4 uo;
#pragma unroll
    for (int j = 0; j < 7; ++j) {
#pragma unroll
      for (int ks = 0; ks < 8; ++ks) {
        const int i = 8 * j + ks;
#pragma unroll
        for (int rt = 0; rt < 4; ++rt) acc[j & 1][rt] = MFMA16(fr[i % W], wsp[rt][ks], ks ? acc[j & 1][rt] : ZERO4);
        if (i == 0) STAMP(21);
        if (i == 24) STAMP(22);
        SCHED();
        if (i + W < 56) LDZ(i + W);
        if (j > 0) {
          if (ks == 0 && (j & 1)) BAR();
          if (j == 1 && ks == 0) {
#pragma unroll
            for (int k = 0; k < 4; ++k) z1o[k] = *(const u32x2*)(lds + L_Z1S + (((k < 3 ? 2 * k + cls : 6) * 4 + rto) * 64 + l) * 8);
          }
          if (i >= (56 - G2W) && i < 53) {
#pragma unroll
            for (int k = 0; k < 8; ++k)
              if ((56 - G2W) + k * (53 - (56 - G2W)) / 8 == i) uf[k >> 1][k & 1] = ld16(rs, l16, ur + (unsigned)(k >> 1) * 7168u + (unsigned)(k & 1) * 1024u);
            SCHED();
          }
          const bool fast = j == 6;
          if (!fast && ks < 4) BUFW((j - 1) & 1, ks) = acc[(j - 1) & 1][ks];
          if (fast && ks < 2) { BUFW(1, 2 * ks) = acc[1][2 * ks]; BUFW(1, 2 * ks + 1) = acc[1][2 * ks + 1]; }
          if (!(j & 1)) {
            const int k0 = fast ? 2 : 5;
            if (ks == k0) {
              BAR();
#pragma unroll
              for (int k = 0; k < 4; ++k) rdv[k] = rb[k * 256];
            }
            if (ks == k0 + 1) {
              s = (rdv[0] + rdv[1]) + (rdv[2] + rdv[3]);
#pragma unroll
              for (int k = 0; k < 4; ++k) rdv[k] = rb[(4 + k) * 256];
            }
            if (ks == k0 + 2) {
              s += (rdv[0] + rdv[1]) + (rdv[2] + rdv[3]);
              const u32x2 z = z1o[(j >> 1) - 1];
              const int e = j == 4 ? 2 : 0;
              uo[e] = pack2(bflo(z[0]) * (s[0] + bs), bfhi(z[0]) * (s[1] + bs));
              uo[e + 1] = pack2(bflo(z[1]) * (s[2] + bs), bfhi(z[1]) * (s[3] + bs));
              if (j == 4) st16(rs, ub + (unsigned)cls * 1024u + l16, uo);
              if (j == 6) st16(rs, l < 32 ? ub + 2048u + (unsigned)cls * 512u + l16 : OOB, half_swap((u32x4){uo[0], uo[1], uo[0], uo[1]}));
            }
            if (j == 6 && ks == 4) BAR();
            if (j == 6 && ks >= 4 && ks < 7) {
#pragma unroll
              for (int k = 0; k < 5; ++k)
                if (k / 2 == ks - 4) *(u32x4*)(lds + (w < 6 ? L_BUF + 32768 + w * 5120 : L_Z1S + (w - 6) * 5120) + k * 1024 + l16) = cr[k];
            }
          }
        }
        SCHED();
      }
    }
  }
  STAMP(23);

  {
    u32x4 uc[4], uh[4];
    const unsigned uho = (unsigned)(l >> 5) * 3584u + (unsigned)(l & 31) * 16u;
#define LD_C(cc) uc[cc] = ld16(rs, l16, ur + (unsigned)(cc) * 7168u + 2048u)
#define LD_H(cc) uh[cc] = ld16(rs, uho, WS_UF + (unsigned)((R * 4 + rto) * 8) * 3584u + (unsigned)(cc) * 7168u + 3072u)
    auto mx = [](unsigned a, unsigned b) { return __builtin_elementwise_max(a, b); };
    auto ok4 = [&](const u32x4* v, int n) {
      unsigned m = 0;
#pragma unroll
      for (int i = 0; i < 8; ++i)
        if (i < n) m = mx(m, mx(mx(v[i][0], v[i][1]), mx(v[i][2], v[i][3])));
      return __all(m != ~0u);
    };
#pragma unroll
    for (int rt = 0; rt < 4; ++rt) BUFW(0, rt) = acc[0][rt];
    BAR();
    f32x4 a3[5];
    u32x4 af[2][5];
    const LAS char* wb[2] = {(const LAS char*)(lds + L_AB) + cls * (10240 + 3072) + l16, (const LAS char*)(lds + L_AB) + cls * (10240 - 2048) + l16};
    const LAS char* wb2[2] = {wb[0] + 40960, wb[1] + 40960};
    asm volatile("" : "+v"(wb[0]), "+v"(wb2[0]), "+v"(wb[1]), "+v"(wb2[1]));
    u32x4 ho = {tag, tag, tag, tag};
    if (w < 4) {
      __builtin_amdgcn_s_setprio(2);
      const f32x4* const rb = (const f32x4*)(lds + L_BUF) + rto * 64 + l;
      f32x4 r8[8];
#pragma unroll
      for (int k = 0; k < 8; ++k) r8[k] = rb[k * 256];
      const f32x4 s = ((r8[0] + r8[1]) + (r8[2] + r8[3])) + ((r8[4] + r8[5]) + (r8[6] + r8[7]));
      const u32x2 z = z1o[3];
      const unsigned o0 = pack2(bflo(z[0]) * (s[0] + bs), bfhi(z[0]) * (s[1] + bs)), o1 = pack2(bflo(z[1]) * (s[2] + bs), bfhi(z[1]) * (s[3] + bs));
      ho = half_swap((u32x4){o0, o1, o0, o1});
    }
    st16(rs, tid < 256 ? (l < 32 ? ub + 3072u + l16 : OOB) : (bid == 0 && tid == 256 ? WS_EPOCH : OOB), ho);
    SCHED();
    __builtin_amdgcn_s_setprio(0);
#pragma unroll
    for (int dj = 0; dj < 5; ++dj) af[0][dj] = *(const LAS u32x4*)(wb[dj >= 2] + dj * 1024);
    const bool ab_ok = ok4(&uf[0][0], 8);
    STAMP(24);
    if (!ab_ok) {
      _Pragma("unroll 1") for (unsigned spins = 0; spins < SPIN_CAP; ++spins) {
#pragma unroll
        for (int i = 0; i < 8; ++i) uf[i >> 1][i & 1] = ld16(rs, l16, ur + (unsigned)(i >> 1) * 7168u + (unsigned)(i & 1) * 1024u);
        if (ok4(&uf[0][0], 8)) break;
        __builtin_amdgcn_s_sleep(1);
      }
    }
    STAMP(25);
    const LAS char* cb0[2] = {(const LAS char*)(lds + L_BUF + 32768) + cls * (5120 + 3072) + l16, (const LAS char*)(lds + L_BUF + 32768) + cls * (5120 - 2048) + l16};
    u32x4 ah[4][3];
    const LAS char* hb = (const LAS char*)(lds + L_BUF) + cls * 12288 + l * 8;
    f32x4 gb[3], xo3[3], xf[3];
    LAS char *otw, *xw3;
    const LAS char *ota, *otb, *xr3;
    unsigned oa_o, ob_o;
#pragma unroll
    for (int st = 0; st < 12; ++st) {
      if (st < 2) { LD_C(2 * st); LD_C(2 * st + 1); }
      else if (st < 7 && w < 4) dma1k(rs, lds + L_BUF + ((st - 2) * 4 + w) * 1024, l16, wob + 122880u + (unsigned)(w * 5 + st - 2) * 1024u);
      if (st == 2) {
        LAS char* ot = (LAS char*)(lds + L_AB + 32768) + w * 2048;
        otw = ot + n16 * 112 + h4 * 8;
        ota = ot + (l >> 2) * 112 + (l & 3) * 16;
        otb = ot + ((l >> 1) & 15) * 112 + 64 + (l & 1) * 16;
        asm volatile("" : "+v"(otw), "+v"(ota), "+v"(otb));
      }
      if (st == 3) {
        oa_o = (unsigned)(l >> 2) * 2560u + (unsigned)(l & 3) * 16u;
        ob_o = l < 32 ? (unsigned)(l >> 1) * 2560u + 64u + (unsigned)(l & 1) * 16u : OOB;
        xw3 = (LAS char*)(lds + L_AB) + w * 3072 + l16;
        xr3 = (const LAS char*)(lds + L_AB) + (w ^ 4) * 3072 + cls * 1024 + l16;
        asm volatile("" : "+v"(oa_o), "+v"(ob_o), "+v"(xw3), "+v"(xr3));
      }
      if (st >= 4 && st < 7) {
        const int j = st - 4;
        gb[j] = *(const f32x4*)(lds + L_GB + (48 * cls + 16 * (cls && j == 2 ? 1 : j) + 4 * h4) * 4);
        xf[j] = (f32x4){bflo(xp[j][0]), bfhi(xp[j][0]), bflo(xp[j][1]), bfhi(xp[j][1])};
        asm volatile("" : "+v"(xf[j]));
      }
      if (st == 8) {
        STAMP(26);
        if (!ok4(uc, 4)) {
          _Pragma("unroll 1") for (unsigned spins = 0; spins < SPIN_CAP; ++spins) {
            LD_C(0); LD_C(1); LD_C(2); LD_C(3);
            if (ok4(uc, 4)) break;
            __builtin_amdgcn_s_sleep(1);
          }
        }
        LD_H(0); LD_H(1); LD_H(2); LD_H(3);
#pragma unroll
        for (int cc = 0; cc < 4; ++cc) uc[cc] = half_swap(uc[cc]);
        VMW(4);
        __builtin_amdgcn_s_barrier();
        asm volatile("" ::: "memory");
        STAMP(27);
      }
      SCHED();
#pragma unroll
      for (int dj = 0; dj < 5; ++dj) {
        const int sn = st + 1;
        if (sn < 8) af[sn & 1][dj] = *(const LAS u32x4*)((sn < 4 ? wb : wb2)[dj >= 2] + ((sn >> 1) & 1) * 20480 + ((sn & 1) * 5 + dj) * 1024);
        else if (sn < 12) af[sn & 1][dj] = *(const LAS u32x4*)(cb0[dj >= 2] + (sn < 11 ? (sn - 8) * 10240 : L_Z1S - L_BUF - 32768) + dj * 1024);
        if (st >= 8) {
#pragma unroll
          for (int k = 0; k < 2; ++k) {
            const int h = st < 11 ? (k ? 99 : (st - 8) * 5 + dj) : 15 + 2 * dj + k;
            if (h < 24) {
              const u32x2 v = *(const volatile LAS u32x2*)(hb + (((h >> 1) % 3) * 4 + (h >> 1) / 3) * 1024 + (h & 1) * 512);
              ah[(h >> 1) / 3][(h >> 1) % 3][2 * (h & 1)] = v[0]; ah[(h >> 1) / 3][(h >> 1) % 3][2 * (h & 1) + 1] = v[1];
            }
          }
        }
        SCHED();
        a3[dj] = MFMA16(af[st & 1][dj], st < 8 ? uf[(st >> 1) & 3][st & 1] : uc[st & 3], st ? a3[dj] : ZERO4);
        SCHED();
      }
    }
#pragma unroll
    for (int j = 0; j < 3; ++j) *(volatile LAS f32x4*)(xw3 + j * 1024) = a3[2 + j];
    BAR();
#pragma unroll
    for (int j = 0; j < 3; ++j) xo3[j] = *(const volatile LAS f32x4*)(xr3 + j * 1024);
    SCHED();
    if (!ok4(uh, 4)) {
      _Pragma("unroll 1") for (unsigned spins = 0; spins < SPIN_CAP; ++spins) {
        LD_H(0); LD_H(1); LD_H(2); LD_H(3);
        if (ok4(uh, 4)) break;
        __builtin_amdgcn_s_sleep(1);
      }
    }
#pragma unroll
    for (int cc = 0; cc < 4; ++cc) {
      const u32x4 b = half_swap(uh[cc]);
#pragma unroll
      for (int j = 0; j < 3; ++j) a3[j] = MFMA16(ah[cc][j], b, a3[j]);
      if (cc >= 1) {
        const int j = cc - 1;
        xo3[j] = __builtin_elementwise_fma(gb[j], xo3[j], xf[j]);
        asm volatile("" : "+v"(xo3[j]));
      }
      SCHED();
    }
#pragma unroll
    for (int j = 0; j < 3; ++j) {
      const f32x4 r = __builtin_elementwise_fma(gb[j], a3[j], xo3[j]);
      *(volatile LAS u32x2*)(otw + j * 32) = (u32x2){pack2(r[0], r[1]), pack2(r[2], r[3])};
    }
    const u32x4 oa = *(const volatile LAS u32x4*)(ota);
    const u32x4 ob = *(const volatile LAS u32x4*)(otb);
    const unsigned obase = (unsigned)((R * 64 + 32 * ((w >> 1) & 1) + (w & 1)) * D_ + 80 * c + 48 * cls) * 2u;
    __builtin_amdgcn_raw_buffer_store_b128(oa, rout, oa_o, obase, 0);
    if (cls == 0) __builtin_amdgcn_raw_buffer_store_b128(ob, rout, ob_o, obase, 0);
  }
  STAMP(30);
  asm volatile(".Lmega_end:");
}

extern "C" void kernel_launch(void* const* d_in, const int* in_sizes, int n_in, void* d_out, int out_size,
                              void* d_ws, size_t ws_size, hipStream_t stream) {
  Params p;
  p.q = (const u16*)d_in[0];
  p.k = (const u16*)d_in[1];
  p.v = (const u16*)d_in[2];
  p.shift_k = (const u16*)d_in[3];
  p.ssm_k = (const u16*)d_in[4];
  p.ssm_D = (const u16*)d_in[5];
  p.w_in = (const u16*)d_in[7];
  p.w_sp = (const u16*)d_in[8];
  p.b_sp = (const u16*)d_in[9];
  p.w_out = (const u16*)d_in[10];
  p.cvec = (const u16*)d_in[11];
  p.w_mod = (const u16*)d_in[12];
  p.out = (u16*)d_out;
  p.ws = (char*)d_ws;
  mega<<<256, 512, 0, stream>>>(p);
}
```

```cpp
#include <hip/hip_runtime.h>
#include <stdint.h>

typedef unsigned short u16;
typedef __attribute__((ext_vector_type(8))) short bf16x8;
typedef __attribute__((ext_vector_type(4))) float f32x4;
typedef __attribute__((ext_vector_type(2))) float f32x2;
typedef __attribute__((ext_vector_type(4))) unsigned int u32x4;
typedef __attribute__((ext_vector_type(2))) unsigned int u32x2;
#define LAS __attribute__((address_space(3)))
#define CAS __attribute__((address_space(4)))
#define GAS __attribute__((address_space(1)))
typedef __amdgpu_buffer_rsrc_t rsrc_t;
typedef __attribute__((ext_vector_type(4))) short s16x4;

constexpr int S_ = 2048, D_ = 640, F_ = 1728, FH = 864, CB = 108;

#define X1PAD 256
constexpr unsigned WS_EPOCH = 0;
constexpr unsigned WS_FX = 4096;
constexpr unsigned WS_FZ = WS_FX + 24576;
constexpr unsigned WS_FW2 = WS_FZ + 32768;
constexpr unsigned WS_FW1 = WS_FW2 + 8192;
constexpr unsigned WS_CS = WS_FW1 + 65536;
constexpr unsigned WS_PART = WS_CS + 131072;
constexpr unsigned WS_MOD = WS_PART + 262144;
constexpr unsigned WS_ST1 = WS_MOD + 32768;
constexpr unsigned WS_X1B = WS_ST1 + 16384;
constexpr unsigned WX1P = 40960 + X1PAD;
constexpr unsigned WBLK = 143360;
constexpr unsigned WS_QB = WS_X1B + 2686976;
constexpr unsigned WS_WINF = WS_QB + 2686976;
constexpr unsigned WS_WOUTF = WS_WINF + 16 * WBLK;
constexpr unsigned WS_Z2F = WS_WOUTF + 8 * WBLK;
constexpr unsigned WS_UF = WS_Z2F + 8 * 64 * 7168;
constexpr unsigned WS_KVT = WS_UF + 32 * 4 * 8 * 3584;
constexpr unsigned WS_FK = WS_KVT + 32 * 81920;
constexpr unsigned WS_STAMP = WS_FK + 20480;
constexpr unsigned WS_END = WS_STAMP + 256 * 40 * 8;

constexpr int U_BYTES = (256 + S_) * 2;
constexpr int G_COPY = 4160;
constexpr int CH_BYTES = U_BYTES + 4 * G_COPY;
constexpr int L_YP = 4 * CH_BYTES;
constexpr int L_A0 = 98304;
constexpr int L_RED = 0;
constexpr int L_TAB = 2048;
constexpr int L_J3 = 8192;
constexpr int L_QT = 49152;
constexpr int L_J5 = L_QT;
constexpr int L_XI = 0;
constexpr int L_BUF = 81920;
constexpr int L_AB = 0;
constexpr int L_MISC = 147456;
constexpr int L_Z1S = L_MISC;
constexpr int L_S1 = L_MISC + 14336;
constexpr int L_T2 = L_S1 + 512;
constexpr int L_ST2 = L_T2 + 512;
constexpr int L_GB = L_ST2 + 512;
constexpr int L_CS1 = L_Z1S + 4096;
constexpr int L_WS = 81920;
constexpr int LDS_TOTAL = 163840;

#define NB2B 20
#define NB2C 20
#define PK 10
#define W7S 0
constexpr int POLL_SLEEP = 1;
constexpr int AUX_SLEEP = 36, LATE_SLEEP = 96;
constexpr int CDIST = 4;
constexpr int NRAW = 12;
constexpr int SV_AT = 108, Q_AT = 130;
#define NB2A 2
constexpr int G2W = 7;
constexpr int Z1SPLIT = 12;
constexpr int SC1 = 16;
constexpr unsigned SPIN_CAP = 1u << 16;
constexpr unsigned OOB = 0x80000000u;

__device__ __forceinline__ float bflo(unsigned x) { return __uint_as_float(x << 16); }
__device__ __forceinline__ float bfhi(unsigned x) { return __uint_as_float(x & 0xffff0000u); }
__device__ __forceinline__ f32x2 bf2(unsigned x) { return (f32x2){bflo(x), bfhi(x)}; }
__device__ __forceinline__ unsigned f2bf(float x) {
  __bf16 b = (__bf16)x;
  return (unsigned)__builtin_bit_cast(unsigned short, b);
}
__device__ __forceinline__ float rbf(float x) { return bflo(f2bf(x)); }
typedef __attribute__((ext_vector_type(2))) __bf16 bf2_t;
__device__ __forceinline__ unsigned pack2(float lo, float hi) {
  f32x2 v = {lo, hi};
  return __builtin_bit_cast(unsigned, __builtin_convertvector(v, bf2_t));
}
template <int CTRL>
__device__ __forceinline__ float dpp_f(float v) {
  return __int_as_float(__builtin_amdgcn_update_dpp(0, __float_as_int(v), CTRL, 0xf, 0xf, true));
}
__device__ __forceinline__ float sum_rows(float v) {
  auto a = __builtin_amdgcn_permlane16_swap(__float_as_uint(v), __float_as_uint(v), false, false);
  v = __uint_as_float(a[0]) + __uint_as_float(a[1]);
  auto b = __builtin_amdgcn_permlane32_swap(__float_as_uint(v), __float_as_uint(v), false, false);
  return __uint_as_float(b[0]) + __uint_as_float(b[1]);
}
__device__ __forceinline__ f32x2 gelu2(f32x2 x) {
  const float c0 = -2.0f * 0.7978845608028654f * 1.4426950408889634f;
  const float c1 = c0 * 0.044715f;
  const f32x2 t = x * __builtin_elementwise_fma(x * x, (f32x2){c1, c1}, (f32x2){c0, c0});
  const f32x2 d = (f32x2){__builtin_amdgcn_exp2f(t[0]), __builtin_amdgcn_exp2f(t[1])} + (f32x2){1.0f, 1.0f};
  return x * (f32x2){__builtin_amdgcn_rcpf(d[0]), __builtin_amdgcn_rcpf(d[1])};
}
#define LO2(v) __builtin_shufflevector(v, v, 0, 1)
#define HI2(v) __builtin_shufflevector(v, v, 2, 3)
__device__ __forceinline__ float tr_sum16(const float* v, int l) {
  float x[8], y[4];
  asm volatile(
      "s_nop 1\n\t"
      "v_add_f32_dpp %0, %8, %8 row_ror:8 row_mask:0xf bank_mask:0x3\n\t"
      "v_add_f32_dpp %1, %9, %9 row_ror:8 row_mask:0xf bank_mask:0x3\n\t"
      "v_add_f32_dpp %2, %10, %10 row_ror:8 row_mask:0xf bank_mask:0x3\n\t"
      "v_add_f32_dpp %3, %11, %11 row_ror:8 row_mask:0xf bank_mask:0x3\n\t"
      "v_add_f32_dpp %4, %12, %12 row_ror:8 row_mask:0xf bank_mask:0x3\n\t"
      "v_add_f32_dpp %5, %13, %13 row_ror:8 row_mask:0xf bank_mask:0x3\n\t"
      "v_add_f32_dpp %6, %14, %14 row_ror:8 row_mask:0xf bank_mask:0x3\n\t"
      "v_add_f32_dpp %7, %15, %15 row_ror:8 row_mask:0xf bank_mask:0x3\n\t"
      "v_add_f32_dpp %0, %16, %16 row_ror:8 row_mask:0xf bank_mask:0xc\n\t"
      "v_add_f32_dpp %1, %17, %17 row_ror:8 row_mask:0xf bank_mask:0xc\n\t"
      "v_add_f32_dpp %2, %18, %18 row_ror:8 row_mask:0xf bank_mask:0xc\n\t"
      "v_add_f32_dpp %3, %19, %19 row_ror:8 row_mask:0xf bank_mask:0xc\n\t"
      "v_add_f32_dpp %4, %20, %20 row_ror:8 row_mask:0xf bank_mask:0xc\n\t"
      "v_add_f32_dpp %5, %21, %21 row_ror:8 row_mask:0xf bank_mask:0xc\n\t"
      "v_add_f32_dpp %6, %22, %22 row_ror:8 row_mask:0xf bank_mask:0xc\n\t"
      "v_add_f32_dpp %7, %23, %23 row_ror:8 row_mask:0xf bank_mask:0xc"
      : "=&v"(x[0]), "=&v"(x[1]), "=&v"(x[2]), "=&v"(x[3]), "=&v"(x[4]), "=&v"(x[5]), "=&v"(x[6]), "=&v"(x[7])
      : "v"(v[0]), "v"(v[1]), "v"(v[2]), "v"(v[3]), "v"(v[4]), "v"(v[5]), "v"(v[6]), "v"(v[7]), "v"(v[8]), "v"(v[9]), "v"(v[10]),
        "v"(v[11]), "v"(v[12]), "v"(v[13]), "v"(v[14]), "v"(v[15]));
  asm volatile(
      "s_nop 1\n\t"
      "v_add_f32_dpp %0, %4, %4 row_shl:4 row_mask:0xf bank_mask:0x5\n\t"
      "v_add_f32_dpp %1, %5, %5 row_shl:4 row_mask:0xf bank_mask:0x5\n\t"
      "v_add_f32_dpp %2, %6, %6 row_shl:4 row_mask:0xf bank_mask:0x5\n\t"
      "v_add_f32_dpp %3, %7, %7 row_shl:4 row_mask:0xf bank_mask:0x5\n\t"
      "v_add_f32_dpp %0, %8, %8 row_shr:4 row_mask:0xf bank_mask:0xa\n\t"
      "v_add_f32_dpp %1, %9, %9 row_shr:4 row_mask:0xf bank_mask:0xa\n\t"
      "v_add_f32_dpp %2, %10, %10 row_shr:4 row_mask:0xf bank_mask:0xa\n\t"
      "v_add_f32_dpp %3, %11, %11 row_shr:4 row_mask:0xf bank_mask:0xa\n\t"
      "s_nop 1"
      : "=&v"(y[0]), "=&v"(y[1]), "=&v"(y[2]), "=&v"(y[3])
      : "v"(x[0]), "v"(x[1]), "v"(x[2]), "v"(x[3]), "v"(x[4]), "v"(x[5]), "v"(x[6]), "v"(x[7]));
  const bool b1 = l & 2, b0 = l & 1;
  const float z0 = (b1 ? y[2] : y[0]) + dpp_f<0x4E>(b1 ? y[0] : y[2]);
  const float z1 = (b1 ? y[3] : y[1]) + dpp_f<0x4E>(b1 ? y[1] : y[3]);
  return (b0 ? z1 : z0) + dpp_f<0xB1>(b0 ? z0 : z1);
}
__device__ __forceinline__ u32x4 half_swap(u32x4 v) {
  const auto a = __builtin_amdgcn_permlane32_swap(v[0], v[2], false, false);
  const auto b = __builtin_amdgcn_permlane32_swap(v[1], v[3], false, false);
  return (u32x4){a[0], b[0], a[1], b[1]};
}
#define MFMA16(a, b, c) __builtin_amdgcn_mfma_f32_16x16x32_bf16(__builtin_bit_cast(bf16x8, a), __builtin_bit_cast(bf16x8, b), c, 0, 0, 0)
__device__ __forceinline__ u32x2 trd(const char* p) {
  return __builtin_bit_cast(u32x2, __builtin_amdgcn_ds_read_tr16_b64_v4i16((LAS s16x4*)p));
}
__device__ __forceinline__ u32x4 trfrag(const char* p, int second) {
  u32x2 a = trd(p), b = trd(p + second);
  return (u32x4){a[0], a[1], b[0], b[1]};
}
#define BAR() asm volatile("s_waitcnt lgkmcnt(0)\n\ts_barrier" ::: "memory")
#define DRAIN() do { __builtin_amdgcn_s_waitcnt(0x0F70); asm volatile("" ::: "memory"); } while (0)
#define VMW(n) do { __builtin_amdgcn_s_waitcnt(0x0F70 | ((n) & 15) | (((n) >> 4) << 14)); asm volatile("" ::: "memory"); } while (0)
#define SCHED() __builtin_amdgcn_sched_barrier(0)
#define ZERO4 ((f32x4){0.f, 0.f, 0.f, 0.f})

__device__ __forceinline__ u32x4 ld16(rsrc_t r, unsigned off, unsigned so = 0) { return __builtin_amdgcn_raw_buffer_load_b128(r, off, so, SC1); }
__device__ __forceinline__ u32x2 ld8(rsrc_t r, unsigned off, unsigned so = 0) { return __builtin_amdgcn_raw_buffer_load_b64(r, off, so, SC1); }
__device__ __forceinline__ unsigned ld4(rsrc_t r, unsigned off, unsigned so = 0) { return __builtin_amdgcn_raw_buffer_load_b32(r, off, so, SC1); }
__device__ __forceinline__ void st16(rsrc_t r, unsigned off, u32x4 v) { __builtin_amdgcn_raw_buffer_store_b128(v, r, off, 0, SC1); }
__device__ __forceinline__ void st8(rsrc_t r, unsigned off, u32x2 v) { __builtin_amdgcn_raw_buffer_store_b64(v, r, off, 0, SC1); }
__device__ __forceinline__ void st4(rsrc_t r, unsigned off, unsigned v) { __builtin_amdgcn_raw_buffer_store_b32(v, r, off, 0, SC1); }
__device__ __forceinline__ void dma1k(rsrc_t r, char* lds_dst, unsigned l16, unsigned soff) {
  __builtin_amdgcn_raw_ptr_buffer_load_lds(r, (LAS void*)lds_dst, 16, l16, soff, 0, SC1);
}

#define STAMP(i)

template <int SLEEP>
__device__ __forceinline__ void wait_flag(rsrc_t r, unsigned off, unsigned tag) {
  _Pragma("unroll 1") for (unsigned spins = 0; spins < SPIN_CAP; ++spins) {
    if (__all(ld4(r, off) == tag)) break;
    __builtin_amdgcn_s_sleep(SLEEP);
  }
  asm volatile("" ::: "memory");
}

struct ConvSched {
  signed char pp[144], m[144], rd[144];
  int pre;
};
constexpr ConvSched conv_sched(int dist) {
  ConvSched s = {};
  int need[96] = {}, i = 0;
  for (int r = 0; r < 4; ++r)
    for (int pp = 0; pp < 8; ++pp) {
      const int j = 24 * r + (pp ? 8 + 2 * pp : 0);
      need[j] = need[j + 1] = i;
      for (int m = 7 - pp; m >= 0; --m) {
        if (pp == 0) need[24 * r + 9 - m] = i;
        s.pp[i] = pp; s.m[i] = m; s.rd[i] = -1;
        ++i;
      }
    }
  int next = 144;
  for (int j = 95; j >= 0; --j) {
    next = need[j] - dist < next - 1 ? need[j] - dist : next - 1;
    if (next >= 0) s.rd[next] = j;
    else if (s.pre == 0) s.pre = j + 1;
  }
  return s;
}

template <int I> struct IC { static constexpr int v = I; };
template <int I0, int I1, class F>
__device__ __forceinline__ void static_for(F&& f) {
  if constexpr (I0 < I1) { f(IC<I0>{}); static_for<I0 + 1, I1>(f); }
}

struct Params {
  const u16 *q, *k, *v, *shift_k, *ssm_k, *ssm_D, *w_in, *w_sp, *b_sp, *w_out, *cvec, *w_mod;
  u16* out;
  char* ws;
};

__global__ __launch_bounds__(512, 2) void mega(Params) {
  __shared__ __attribute__((aligned(16))) char lds[LDS_TOTAL];
  const CAS Params* kp = (const CAS Params*)__builtin_amdgcn_kernarg_segment_ptr();
  unsigned ka0, ka1;
  asm volatile("s_load_dword %0, %2, 0x0\n\ts_load_dword %1, %2, 0x40" : "=&s"(ka0), "=&s"(ka1) : "s"(kp));
  SCHED();
  const int tid = threadIdx.x, l = tid & 63;
  const int w = __builtin_amdgcn_readfirstlane(tid >> 6);
  const int bid = blockIdx.x, xcd = bid & 7, idx = bid >> 3;
  const int dg = ((idx & 7) + 2 * (idx >> 3)) & 7;
  const bool is_conv = dg >= 3;
  const int slot = 3 * (idx >> 3) + dg;
  const int cidx = 5 * (idx >> 3) + dg - 3;
  const int quad = xcd * 20 + cidx;
  const unsigned l16 = (unsigned)l * 16u;
  const int blk = (quad / 5) * 8 + w, sl = quad % 5;
  unsigned a0g = (unsigned)(l >> 4) * 1280u + (unsigned)(((l & 15) ^ (4 * (l >> 5))) * 16), a0x = a0g ^ 128u, a0h = a0g + (l < 16 ? 1280u : 0u);
  unsigned a0s = (unsigned)blk * 10240u + (unsigned)sl * 256u, a0p = blk ? a0s - 5120u : a0s, a0l = L_A0 + w * 6144;
  asm volatile("" : "+v"(a0g), "+v"(a0x), "+v"(a0h));
  asm volatile("" : "+s"(a0s), "+s"(a0p), "+s"(a0l));
  SCHED();
  asm volatile("" : "+s"(kp));
  Params p = {kp->q, kp->k, kp->v, kp->shift_k, kp->ssm_k, kp->ssm_D, kp->w_in, kp->w_sp, kp->b_sp, kp->w_out, kp->cvec, kp->w_mod, kp->out, kp->ws};
  asm volatile("s_waitcnt lgkmcnt(0)" : "+s"(p.q), "+s"(p.k), "+s"(p.v), "+s"(p.shift_k), "+s"(p.ssm_k), "+s"(p.ssm_D), "+s"(p.w_in), "+s"(p.w_sp),
               "+s"(p.b_sp), "+s"(p.w_out), "+s"(p.cvec), "+s"(p.w_mod), "+s"(p.out), "+s"(p.ws) : "s"(ka0), "s"(ka1));
  char* const ws = p.ws;
  STAMP(0);
  unsigned cpf = 0;
  if (!is_conv) {
    unsigned long long pc;
    unsigned len, len2;
    asm volatile("s_getpc_b64 %0\n.Lmega_here:\n\ts_mov_b32 %1, .Lmega_end-.Lmega_here\n\ts_mov_b32 %2, .Lmega_aux_end-.Lmega_here" : "=s"(pc), "=s"(len), "=s"(len2));
    len = len > len2 ? len : len2;
    unsigned o = (unsigned)((slot * 8 + w) * 1024 + (l & 7) * 128);
    o = o + 16u <= len ? o : 0u;
    cpf = *(const GAS unsigned*)((pc & ~15ull) + o);
  }
  const rsrc_t rs = __builtin_amdgcn_make_buffer_rsrc(ws, 0, WS_END, 0x00020000);
  const unsigned epv = ld4(rs, WS_EPOCH);
  SCHED();
  u32x2 dd2;
  {
    int cq = dg;
    asm volatile("" : "+s"(cq));
    if (cq >= 3) {
      const rsrc_t rk = __builtin_amdgcn_make_buffer_rsrc((void*)p.k, 0, S_ * D_ * 2, 0x00020000);
      const rsrc_t rv = __builtin_amdgcn_make_buffer_rsrc((void*)p.v, 0, S_ * D_ * 2, 0x00020000);
      const rsrc_t rh = __builtin_amdgcn_make_buffer_rsrc((void*)p.shift_k, 0, D_ * 8, 0x00020000);
      char* const la = lds + a0l;
      __builtin_amdgcn_raw_ptr_buffer_load_lds(rh, (LAS void*)(la + 5120), 16, l16, (unsigned)sl * 1024u, 0, 0);
      __builtin_amdgcn_raw_ptr_buffer_load_lds(rk, (LAS void*)(la + 1024), 16, a0x, a0s, 0, 0);
      __builtin_amdgcn_raw_ptr_buffer_load_lds(rk, (LAS void*)(la + 2048), 16, a0g, a0s + 5120u, 0, 0);
      __builtin_amdgcn_raw_ptr_buffer_load_lds(rv, (LAS void*)(la + 3072), 16, a0g, a0s, 0, 0);
      __builtin_amdgcn_raw_ptr_buffer_load_lds(rv, (LAS void*)(la + 4096), 16, a0x, a0s + 5120u, 0, 0);
      __builtin_amdgcn_raw_ptr_buffer_load_lds(rk, (LAS void*)(la), 16, a0h, a0p, 0, 0);
      SCHED();
      const rsrc_t rf = __builtin_amdgcn_make_buffer_rsrc((void*)p.ssm_k, 0, S_ * D_ * 2, 0x00020000);
      dd2 = *(const CAS u32x2*)((const CAS char*)p.ssm_D + quad * 8);
      STAMP(32);
      {
        const u32x4 z = {0u, 0u, 0u, 0u};
        if (tid < 128) *(u32x4*)(lds + (tid >> 5) * CH_BYTES + (tid & 31) * 16) = z;
        else if (tid < 152) {
          const int i = (tid - 128) >> 2;
          *(u32x4*)(lds + (tid & 3) * CH_BYTES + U_BYTES + (i < 4 ? i * G_COPY : (i & 1 ? 4128 : 16))) = z;
        }
        const LAS char* a0r = (const LAS char*)(lds + L_A0) + w * 6144 + (l & 3) * 512;
#define A0R(k) (a0r + ((((l >> 2) ^ (4 * ((l + (k)) & 3))) * 16)))
        unsigned go = WS_KVT + (unsigned)(quad / 5) * 81920u + (unsigned)sl * 16384u + (unsigned)(l >> 2) * 1024u + (unsigned)w * 64u + (unsigned)(l & 3) * 16u;
        const LAS char* r0 = A0R(0), * r1 = A0R(1), * r2 = A0R(2);
        const LAS char* rsh = (const LAS char*)(lds + L_A0) + w * 6144 + 5120 + (l >> 2) * 64;
        asm volatile("" : "+v"(go), "+v"(r0), "+v"(r1), "+v"(r2), "+v"(rsh));
        SCHED();
#pragma unroll
        for (int j = 0; j < 2; ++j)
          __builtin_amdgcn_raw_ptr_buffer_load_lds(rf, (LAS void*)(lds + (w >> 1) * CH_BYTES + U_BYTES + 32 + (2 * (w & 1) + j) * 1024), 16, l16,
                                                   (unsigned)quad * 16384u + (unsigned)(2 * w + j) * 1024u, 0, 0);
        STAMP(34);
        SCHED();
        VMW(7);
        f32x2 wt[4][4];
#pragma unroll
        for (int e = 0; e < 4; ++e) {
          const u32x4 sh = *(const volatile LAS u32x4*)(rsh + 16 * e);
          wt[e][0] = (f32x2){bflo(sh[0]), bflo(sh[2])}; wt[e][1] = (f32x2){bfhi(sh[0]), bfhi(sh[2])};
          wt[e][2] = (f32x2){bflo(sh[1]), bflo(sh[3])}; wt[e][3] = (f32x2){bfhi(sh[1]), bfhi(sh[3])};
        }
#pragma unroll
        for (int e = 0; e < 4; ++e) asm volatile("" : "+v"(wt[e][0]), "+v"(wt[e][1]), "+v"(wt[e][2]), "+v"(wt[e][3]));
        asm volatile("" : "+s"(dd2));
        SCHED();
        VMW(2);
        STAMP(35);
        u32x4 ka[5], va[2];
        ka[0] = *(const volatile LAS u32x4*)(r0 + 256);
        ka[1] = *(const volatile LAS u32x4*)(r1 + 512);
        ka[2] = *(const volatile LAS u32x4*)(r1 + 768);
        ka[3] = *(const volatile LAS u32x4*)(r2 + 1024);
        ka[4] = *(const volatile LAS u32x4*)(r2 + 1280);
        va[0] = *(const volatile LAS u32x4*)(r0 + 3072);
        va[1] = *(const volatile LAS u32x4*)(r0 + 3328);
        SCHED();
        if (blk == 0) {
          if ((l & 3) < 2) ka[0] = z;
          if ((l & 3) == 0) { ka[1] = z; ka[2] = z; }
        }
        u32x4 ko;
#pragma unroll
        for (int e = 0; e < 4; ++e) {
          const f32x2 w0 = wt[e][0], w1 = wt[e][1], w2 = wt[e][2], w3 = wt[e][3];
          const f32x2 km3 = bf2(ka[0][e]), km2 = bf2(ka[1][e]), km1 = bf2(ka[2][e]), k0 = bf2(ka[3][e]), k1 = bf2(ka[4][e]);
          const f32x2 s0 = __builtin_elementwise_fma(k0, w0, __builtin_elementwise_fma(km1, w1, __builtin_elementwise_fma(km2, w2, km3 * w3)));
          const f32x2 s1 = __builtin_elementwise_fma(k1, w0, __builtin_elementwise_fma(k0, w1, __builtin_elementwise_fma(km1, w2, km2 * w3)));
          const f32x2 o0 = s0 * bf2(va[0][e]), o1 = s1 * bf2(va[1][e]);
          ko[2 * (e & 1)] = pack2(o1[0], o0[0]); ko[2 * (e & 1) + 1] = pack2(o1[1], o0[1]);
          if (e & 1) st16(rs, go + (unsigned)(e >> 1) * 512u, ko);
          SCHED();
        }
      }
    }
  }
  const int R = (xcd & 3) * 8 + (idx & 7);
  const int c = (xcd >> 2) * 4 + (idx >> 3);
  const int n16 = l & 15, h4 = l >> 4;
  const rsrc_t rq = __builtin_amdgcn_make_buffer_rsrc((void*)p.q, 0, S_ * D_ * 2, 0x00020000);
  const rsrc_t rsp = __builtin_amdgcn_make_buffer_rsrc((void*)p.w_sp, 0, S_ * S_ * 2, 0x00020000);
  const int wj = w < 7 ? w : 0;
  u32x4 wsp[4][8];
  const int wk = w ^ (dg == 5 || dg == 6 ? 4 : 0);
  const unsigned wso = (unsigned)(R * 64) * 4096u + (unsigned)wk * 512u;
  const unsigned wdo = (unsigned)(h4 * 8192 + ((n16 ^ h4) * 16));
#define WVO(q) (wdo ^ (unsigned)(((4 - (q)) & 3) * 64))
#define WSO(g, q) (wso + (unsigned)((g) >> 2) * 256u + (unsigned)(32 * (((g) >> 1) & 1) + ((g) & 1) + 8 * (q)) * 4096u)
  u32x4 raw[NRAW];
#define LDW(i) raw[i] = __builtin_amdgcn_raw_buffer_load_b128(rsp, WVO((i) & 3), WSO((i) >> 2, (i) & 3), 0)
  const int trow = R * 64 + 32 * ((w >> 1) & 1) + 2 * n16 + (w & 1);
  unsigned bs_raw;
  unsigned tag;
  unsigned fw2 = OOB;
  unsigned e_fw1;
  const unsigned fw1o = l < 20 ? WS_FX + (unsigned)(w * 20 + l) * 128u : WS_FW1 + (unsigned)(((8 + c) * 8 + wj) * 4 + (l & 3)) * 128u;

  if (__builtin_expect(is_conv, 1)) {
    STAMP(1);
    DRAIN();
    tag = __builtin_amdgcn_readfirstlane(epv) * 2654435761u + 0x9E3779B9u;
    BAR();
    st4(rs, tid == 0 ? WS_FK + (unsigned)quad * 128u : OOB, tag);
    const unsigned xo = (unsigned)(tid >> 4) * WX1P + (unsigned)quad * 256u + (unsigned)(tid & 15) * 16u;
    SCHED();
    st16(rs, WS_X1B + xo, (u32x4){~0u, ~0u, ~0u, ~0u});
    st16(rs, WS_X1B + xo + 32u * WX1P, (u32x4){~0u, ~0u, ~0u, ~0u});
    SCHED();
    unsigned atv, ato;
    const unsigned long long atb = (unsigned long long)(w < 5 ? p.w_in : p.w_out);
    {
      const int i = w < 5 ? (cidx * 5 + w) * 20 + (l < 19 ? l : 19) : (cidx * 3 + w - 5) * 19 + (l < 18 ? l : 18);
      const int r = i / 3;
      ato = w < 5 ? (unsigned)((r < 639 ? r : 639) * (F_ * 2) + xcd * CB * 2 + (i - 3 * r) * 108) : (unsigned)(xcd * CB * D_ * 2 + (i < 1079 ? i : 1079) * 128);
      asm volatile("" : "+v"(ato));
    }
    {
      u32x4 d[3];
      u32x2 n[3];
#pragma unroll
      for (int j = 0; j < 3; ++j) {
        char* G = j < 2 ? lds + ((tid >> 8) + 2 * j) * CH_BYTES + U_BYTES + ((tid & 255) + 2) * 16 : lds + (tid & 3) * CH_BYTES + U_BYTES + 16;
        d[j] = *(const u32x4*)G;
        n[j] = *(const u32x2*)(G + 16);
      }
#pragma unroll
      for (int j = 0; j < 3; ++j) {
        char* G = j < 2 ? lds + ((tid >> 8) + 2 * j) * CH_BYTES + U_BYTES + ((tid & 255) + 2) * 16 : lds + (tid & 3) * CH_BYTES + U_BYTES + 16;
        const unsigned a0 = __builtin_amdgcn_alignbit(d[j][1], d[j][0], 16), a1 = __builtin_amdgcn_alignbit(d[j][2], d[j][1], 16),
                       a2 = __builtin_amdgcn_alignbit(d[j][3], d[j][2], 16), a3 = __builtin_amdgcn_alignbit(n[j][0], d[j][3], 16),
                       a4 = __builtin_amdgcn_alignbit(n[j][1], n[j][0], 16);
        if (j < 2 || tid < 4) {
          *(u32x4*)(G + G_COPY) = (u32x4){a0, a1, a2, a3};
          *(u32x4*)(G + 2 * G_COPY) = (u32x4){d[j][1], d[j][2], d[j][3], n[j][0]};
          *(u32x4*)(G + 3 * G_COPY) = (u32x4){a1, a2, a3, a4};
        }
      }
    }
    const int ch = w & 3, hf = w >> 2;
    const LAS char *ap, *bp;
    {
      const char* U = lds + ch * CH_BYTES;
      const char* G = U + U_BYTES;
      const int a = n16, jj = h4 >> 1, e2 = h4 & 1;
      const int sg = (a + 1) & 3;
      bp = (const LAS char*)U + 512 + 32 * (a - jj) + 16 * e2 - 256 * hf - 192;
      ap = (const LAS char*)G + sg * G_COPY + 2 * (16 * jj + a - 8 * e2 + 9 - sg) + 256 * hf;
      asm volatile("" : "+v"(ap), "+v"(bp));
    }
    STAMP(36);
    {
      wait_flag<1>(rs, WS_FK + (unsigned)(5 * (4 * w + (l & 3)) + (quad >> 5)) * 128u, tag);
      STAMP(29);
      const unsigned gp = WS_KVT + (unsigned)(4 * w + (l >> 5)) * 81920u + (unsigned)quad * 512u + (unsigned)(l & 31) * 16u;
      const u32x4 g0 = ld16(rs, gp), g1 = ld16(rs, gp + 163840u);
      SCHED();
      char* Uw = lds + 512 + (128 * w + (l & ~3) + 3 - (l & 3)) * 4;
#pragma unroll
      for (int i = 0; i < 4; ++i) *(unsigned*)(Uw + i * CH_BYTES) = g0[i];
#pragma unroll
      for (int i = 0; i < 4; ++i) *(unsigned*)(Uw + 256 + i * CH_BYTES) = g1[i];
    }
    STAMP(28);
    DRAIN();
    BAR();
    STAMP(2);
    const int te = 2 * tid;
    u32x4 qa[2];
    u32x4 sv[2];
    u32x2 mg;
    const unsigned mgo = WS_MOD + (unsigned)((l < 12 ? (l >> 2) * D_ + (l & 3) : 0) + quad * 4) * 8u;
    auto ld_sv = [&]() {
      sv[0] = ld16(rs, WS_ST1 + (unsigned)tid * 16u);
      sv[1] = ld16(rs, WS_ST1 + (unsigned)tid * 16u + 8192u);
    };
    auto ld_q = [&]() {
      qa[0] = ld16(rs, WS_QB + xo);
      qa[1] = ld16(rs, WS_QB + xo + 32u * WX1P);
    };
    f32x2 hh[2][2][2], base[2][2][2];
    auto prep = [&]() {
      const LAS char* ur = (const LAS char*)(lds + 512) + ((te & ~7) + 6 - (te & 7)) * 2;
      unsigned uu[2][4];
#pragma unroll
      for (int i = 0; i < 8; ++i) uu[i >> 2][i & 3] = *(const volatile LAS unsigned*)(ur + (i >> 2) * 2048 + (i & 3) * CH_BYTES);
      if (!__all(((sv[0][0] ^ tag) | (sv[0][2] ^ tag) | (sv[1][0] ^ tag) | (sv[1][2] ^ tag)) == 0u)) {
        _Pragma("unroll 1") for (unsigned spins = 0; spins < SPIN_CAP; ++spins) {
          ld_sv();
          if (__all(sv[0][0] == tag && sv[0][2] == tag && sv[1][0] == tag && sv[1][2] == tag)) break;
          __builtin_amdgcn_s_sleep(4);
        }
        asm volatile("" ::: "memory");
        ld_q();
        mg = ld8(rs, mgo);
        DRAIN();
      }
      if (!__all(mg[0] == tag)) {
        _Pragma("unroll 1") for (unsigned spins = 0; spins < SPIN_CAP; ++spins) {
          mg = ld8(rs, mgo);
          if (__all(mg[0] == tag)) break;
          __builtin_amdgcn_s_sleep(2);
        }
      }
      f32x2 cc2[2], ss2[2], dv2[2];
      float cch[4], sch[4];
#pragma unroll
      for (int i = 0; i < 4; ++i) {
        const float sa = __uint_as_float(__builtin_amdgcn_readlane(mg[1], i)), ca = __uint_as_float(__builtin_amdgcn_readlane(mg[1], 4 + i)),
                    ga = __uint_as_float(__builtin_amdgcn_readlane(mg[1], 8 + i));
        cch[i] = ga * rbf(1.0f + ca);
        sch[i] = ga * sa;
      }
      cc2[0] = (f32x2){cch[0], cch[1]}; cc2[1] = (f32x2){cch[2], cch[3]};
      ss2[0] = (f32x2){sch[0], sch[1]}; ss2[1] = (f32x2){sch[2], sch[3]};
      dv2[0] = bf2(dd2[0]); dv2[1] = bf2(dd2[1]);
#pragma unroll
      for (int H = 0; H < 2; ++H)
#pragma unroll
        for (int e = 0; e < 2; ++e) {
          const unsigned st = sv[H][1 + 2 * e];
          const float a = __builtin_amdgcn_rcpf(bfhi(st)), b = -bflo(st) * a;
#pragma unroll
          for (int i = 0; i < 2; ++i) {
            const f32x2 q2 = bf2(qa[H][2 * e + i]);
            const f32x2 u2 = e ? (f32x2){bflo(uu[H][2 * i]), bflo(uu[H][2 * i + 1])} : (f32x2){bfhi(uu[H][2 * i]), bfhi(uu[H][2 * i + 1])};
            const f32x2 n1 = __builtin_elementwise_fma(q2, (f32x2){a, a}, (f32x2){b, b});
            hh[H][e][i] = __builtin_elementwise_fma(n1, cc2[i], ss2[i]);
            base[H][e][i] = __builtin_elementwise_fma(hh[H][e][i], u2 * dv2[i], q2);
            asm volatile("" : "+v"(hh[H][e][i]), "+v"(base[H][e][i]));
          }
        }
    };
    auto HF = [&]() { int x = hf; asm volatile("" : "+s"(x)); return x; };
    {
      const int a = n16;
      char* yp = lds + L_YP + (hf * 4 + ch) * 8192 + (h4 * 16 + ((a + 4 * h4) & 15)) * 16;
      f32x4 acc[8];
      u32x4 A[2][8], B[2][8];
      auto rd = [&](int k) {
        const int r = k / 24, j = k % 24;
        if (j >= 2 && j < 10) B[r & 1][9 - j] = *(const volatile LAS u32x4*)(bp + 512 * (9 - j) + 64 * (3 - r));
        else {
          const int pp = j < 2 ? 0 : (j - 8) >> 1;
          const u32x2 v = *(const volatile LAS u32x2*)(ap + 512 * pp + 64 * r + 8 * (j & 1));
          A[r & 1][pp][2 * (j & 1)] = v[0]; A[r & 1][pp][2 * (j & 1) + 1] = v[1];
        }
      };
#pragma unroll
      for (int k = 0; k < IC<conv_sched(CDIST).pre>::v; ++k) rd(k);
      SCHED();
      auto step = [&](auto ic) {
        constexpr int i = decltype(ic)::v, r = i / 36, pp = conv_sched(CDIST).pp[i], m = conv_sched(CDIST).m[i], k = conv_sched(CDIST).rd[i];
        if (i == 0) __builtin_amdgcn_s_setprio(3);
        if (i == 60) __builtin_amdgcn_s_setprio(2);
        if (i == 100) __builtin_amdgcn_s_setprio(1);
        if (i == 126) __builtin_amdgcn_s_setprio(0);
        acc[m + pp] = MFMA16(A[r & 1][pp], B[r & 1][m], i < 8 ? ZERO4 : acc[m + pp]);
        SCHED();
        if (k >= 0) rd(k);
        if (i == 1) st4(rs, tid == 448 ? WS_FX + (unsigned)quad * 128u : OOB, tag);
        if (i == 0) atv = *(const GAS unsigned*)(atb + ato);
        if (i % 6 == 2 && i / 3 < NRAW) {
          if (!HF()) { LDW(i / 3); LDW(i / 3 + 1); }
        }
        if (i > Q_AT && (i - Q_AT) % 2 == 1 && i - Q_AT < NRAW) {
          if (HF()) { LDW(i - Q_AT - 1); LDW(i - Q_AT); }
        }
        if (i == SV_AT) { ld_sv(); bs_raw = ((const GAS u16*)p.b_sp)[trow]; }
        if (r == 3 && pp >= 1 && m == 7 - pp) *(f32x4*)(yp + (pp - 1) * 1024) = acc[pp - 1];
        SCHED();
      };
      static_for<0, Q_AT>(step);
      STAMP(3);
      DRAIN();
      asm volatile("" ::"v"(atv));
      ld_q();
      mg = ld8(rs, mgo);
      SCHED();
      STAMP(4);
      static_for<Q_AT, 144>(step);
      *(f32x4*)(yp + 7 * 1024) = acc[7];
      STAMP(5);
    }
    prep();
    e_fw1 = ld4(rs, fw1o);
    STAMP(6);
    const LAS char* yr = (const LAS char*)(lds + L_YP) + (te >> 8) * 1024 + (((te >> 2) & 3) * 16 + ((((te >> 4) & 15) + 4 * ((te >> 2) & 3)) & 15)) * 16 + (te & 3) * 4;
    asm volatile("" : "+v"(yr));
    BAR();
    STAMP(7);
    f32x2 y[2][8];
#pragma unroll
    for (int i = 0; i < 16; ++i) y[i >> 3][i & 7] = *(const volatile LAS f32x2*)(yr + (i >> 3) * 4096 + (i & 7) * 8192);
#pragma unroll
    for (int H = 0; H < 2; ++H) {
      u32x4 o;
#pragma unroll
      for (int e = 0; e < 2; ++e)
#pragma unroll
        for (int i = 0; i < 2; ++i) {
          const float x0 = __builtin_fmaf(hh[H][e][i][0], y[H][2 * i][e] + y[H][4 + 2 * i][e], base[H][e][i][0]);
          const float x1 = __builtin_fmaf(hh[H][e][i][1], y[H][2 * i + 1][e] + y[H][5 + 2 * i][e], base[H][e][i][1]);
          o[2 * e + i] = pack2(x0, x1);
        }
      st16(rs, WS_X1B + (unsigned)(32 * H) * WX1P + xo, o);
    }
    STAMP(8);
  } else {
    const rsrc_t rwi = __builtin_amdgcn_make_buffer_rsrc((void*)p.w_in, 0, D_ * F_ * 2, 0x00020000);
    const rsrc_t rwo = __builtin_amdgcn_make_buffer_rsrc((void*)p.w_out, 0, FH * D_ * 2, 0x00020000);
    STAMP(32);
    const int job = xcd + 8 * slot;
    const int T = job >= 50 && w < 3 && job - 50 + 46 * w < 128 ? job - 50 + 46 * w : -1;
    const bool unit = slot >= 8 || (slot >= 2 && slot < 6);
    const int ucb = slot >= 8 ? 8 + xcd : xcd, kg = (slot >= 8 ? slot : slot - 2) & 3;
    float* tab = (float*)(lds + L_TAB);
    float* red = (float*)(lds + L_RED);
    auto issue_unit = [&](int i) {
      const int g = i * 64 + l;
      __builtin_amdgcn_raw_ptr_buffer_load_lds(rwi, (LAS void*)(lds + L_J3 + i * 1024), 16, (unsigned)((g / 14) * (F_ * 2) + (g % 14) * 16),
                                               (unsigned)(kg * 160 * F_ * 2 + ucb * CB * 2), 0, 0);
    };
    if (T >= 0) {
      char* qt = lds + L_QT + w * 20480;
      const unsigned so = (unsigned)(16 * T) * 1280u;
#pragma unroll
      for (int n = 0; n < 16; ++n)
        __builtin_amdgcn_raw_ptr_buffer_load_lds(rq, (LAS void*)(qt + n * 1024), 16, l16 ^ (unsigned)(n * 16), so + (unsigned)(n * 1280), 0, 0);
#pragma unroll
      for (int j = 0; j < 4; ++j)
        __builtin_amdgcn_raw_ptr_buffer_load_lds(rq, (LAS void*)(qt + 16384 + j * 1024), 16, (unsigned)(h4 * 1280 + ((n16 ^ (4 * j + h4)) * 16)), so + (unsigned)(4 * j * 1280 + 1024), 0, 0);
      tag = __builtin_amdgcn_readfirstlane(epv) * 2654435761u + 0x9E3779B9u;
      STAMP(34);
      DRAIN();
      STAMP(35);
      const bf2_t one = {(__bf16)1.0f, (__bf16)1.0f};
      float s1[2] = {0.f, 0.f}, s2[2] = {0.f, 0.f};
      const int n0 = 2 * (l & 7);
      u32x4 qv[20];
#pragma unroll
      for (int i = 0; i < 20; ++i)
#pragma unroll
        for (int e = 0; e < 2; ++e) {
          const int n = n0 + e, ck = 4 * (i & 15) + h4;
          const u32x2 v = *(const volatile LAS u32x2*)(qt + (i < 16 ? n * 1024 : 16384 + n * 256) + ((ck ^ n) * 16) + (l & 8));
          qv[i][2 * e] = v[0]; qv[i][2 * e + 1] = v[1];
        }
#pragma unroll
      for (int i = 0; i < 20; ++i) {
        st16(rs, WS_QB + (unsigned)(T >> 1) * WX1P + (unsigned)i * 2048u + (unsigned)((l >> 3) * 256 + (T & 1) * 128 + (l & 7) * 16), qv[i]);
#pragma unroll
        for (int e = 0; e < 4; ++e) {
          const unsigned x = qv[i][e];
          const bf2_t v = __builtin_bit_cast(bf2_t, x);
          s1[e >> 1] = __builtin_amdgcn_fdot2_f32_bf16(v, one, s1[e >> 1], false);
          s2[e >> 1] = __builtin_amdgcn_fdot2_f32_bf16(v, v, s2[e >> 1], false);
        }
        SCHED();
      }
      u32x4 o = {tag, 0u, tag, 0u};
#pragma unroll
      for (int e = 0; e < 2; ++e) {
        const float t1 = sum_rows(s1[e] + dpp_f<0x128>(s1[e])), t2 = sum_rows(s2[e] + dpp_f<0x128>(s2[e]));
        const float mean = t1 * (1.0f / D_);
        const float var = __builtin_fmaxf(t2 * (1.0f / D_) - mean * mean, 0.f);
        o[2 * e + 1] = pack2(mean, __builtin_amdgcn_sqrtf(rbf(rbf(var) + 1e-5f)));
      }
      STAMP(1);
      DRAIN();
      if (l < 8) st16(rs, WS_ST1 + (unsigned)T * 128u + l16, o);
      STAMP(3);
    }
    if (job < 60) {
      const int grp = job < 10 ? 40 + job : (job < 50 ? job - 10 : job);
      if (job >= 50 && T < 0) __builtin_amdgcn_s_sleep(LATE_SLEEP);
      if (job >= 10 && T < 0) __builtin_amdgcn_s_sleep(AUX_SLEEP);
      const unsigned cv = *(const GAS unsigned*)((const GAS u16*)p.cvec + 80 * w + 2 * (l < 40 ? l : 0));
      u32x4 pre[10];
#pragma unroll
      for (int pp = 0; pp < 10; ++pp) pre[pp] = *(const GAS u32x4*)((const GAS u16*)p.w_mod + (size_t)(80 * w + pp * 8 + (l >> 3)) * 3840 + grp * 64 + (l & 7) * 8);
      SCHED();
      STAMP(34);
      tag = __builtin_amdgcn_readfirstlane(epv) * 2654435761u + 0x9E3779B9u;
      if (l < 40) {
        const float c0 = bflo(cv), c1 = bfhi(cv);
        *(f32x2*)(tab + w * 80 + 2 * l) = (f32x2){rbf(c0 * __builtin_amdgcn_rcpf(1.0f + __expf(-c0))), rbf(c1 * __builtin_amdgcn_rcpf(1.0f + __expf(-c1)))};
      }
      asm volatile("s_waitcnt lgkmcnt(0)" ::: "memory");
      f32x2 acc[4];
#pragma unroll
      for (int e = 0; e < 4; ++e) acc[e] = (f32x2){0.f, 0.f};
#pragma unroll
      for (int pp = 0; pp < 10; ++pp) {
        const float sv = tab[w * 80 + pp * 8 + (l >> 3)];
#pragma unroll
        for (int e = 0; e < 4; ++e) acc[e] = __builtin_elementwise_fma((f32x2){sv, sv}, bf2(pre[pp][e]), acc[e]);
      }
      STAMP(35);
      asm volatile("" : "+v"(acc[0]), "+v"(acc[1]), "+v"(acc[2]), "+v"(acc[3]));
#pragma unroll
      for (int e = 0; e < 8; ++e) { float a = acc[e >> 1][e & 1]; a += dpp_f<0x128>(a); acc[e >> 1][e & 1] = sum_rows(a); }
      if (l < 8) {
        *(f32x4*)(red + w * 64 + l * 8) = (f32x4){acc[0][0], acc[0][1], acc[1][0], acc[1][1]};
        *(f32x4*)(red + w * 64 + l * 8 + 4) = (f32x4){acc[2][0], acc[2][1], acc[3][0], acc[3][1]};
      }
      if (w == 7) __builtin_amdgcn_s_setprio(2);
      BAR();
      if (w == 7) {
        float t = 0.f;
#pragma unroll
        for (int r = 0; r < 8; ++r) t += red[r * 64 + l];
        st8(rs, WS_MOD + (unsigned)(grp * 64 + l) * 8u, (u32x2){tag, __float_as_uint(rbf(t))});
        __builtin_amdgcn_s_setprio(0);
      } else if (unit) {
#pragma unroll
        for (int k = 0; k < 5; ++k) issue_unit(w * 5 + k);
      }
    } else if (unit && w >= 3 && w < 7) {
      __builtin_amdgcn_s_sleep(AUX_SLEEP);
#pragma unroll
      for (int k = 0; k < 9; ++k) issue_unit((w - 3) * 9 + k < 35 ? (w - 3) * 9 + k : 34);
    }
    tag = __builtin_amdgcn_readfirstlane(epv) * 2654435761u + 0x9E3779B9u;
    if (unit) {
      auto mod_tab = [&](int which, float add, float* dst) {
        const unsigned o = WS_MOD + (unsigned)(which * D_ + kg * 160) * 8u;
        u32x4 g0, g1;
        _Pragma("unroll 1") for (unsigned spins = 0; spins < SPIN_CAP; ++spins) {
          g0 = ld16(rs, o + l16);
          g1 = ld16(rs, o + 1024u + (l16 & 255u));
          if (__all(g0[0] == tag && g0[2] == tag && g1[0] == tag && g1[2] == tag)) break;
          __builtin_amdgcn_s_sleep(2);
        }
        *(f32x2*)(dst + 2 * l) = (f32x2){rbf(add + __uint_as_float(g0[1])), rbf(add + __uint_as_float(g0[3]))};
        if (l < 16) *(f32x2*)(dst + 128 + 2 * l) = (f32x2){rbf(add + __uint_as_float(g1[1])), rbf(add + __uint_as_float(g1[3]))};
      };
      if (w == 7) {
        STAMP(1);
        if (slot >= 8) __builtin_amdgcn_s_sleep(64);
        mod_tab(4, 1.0f, tab);
        BAR();
        STAMP(2);
        mod_tab(3, 0.0f, tab + 160);
        BAR();
      } else {
        DRAIN();
        STAMP(36);
        BAR();
        STAMP(2);
        const char* bp = lds + L_J3 + (4 * h4 + (n16 >> 2)) * 224 + (n16 & 3) * 8 + w * 32;
        const float* tp = tab + 4 * h4;
        u32x4 f3[5];
        float cs = 0.f, b1 = 0.f;
        const bool pad = w == 6 && n16 >= 12;
        const unsigned padv = (n16 == 12 && ucb >= 8) ? 0x3f803f80u : 0u;
#pragma unroll
        for (int k = 0; k < 5; ++k) f3[k] = trfrag(bp + k * 7168, 16 * 224);
#pragma unroll
        for (int k = 0; k < 5; ++k) {
          const f32x4 ca = *(const f32x4*)(tp + k * 32), cb4 = *(const f32x4*)(tp + k * 32 + 16);
          u32x4 o;
#pragma unroll
          for (int e = 0; e < 4; ++e) {
            const float c0 = e < 2 ? ca[2 * e] : cb4[2 * e - 4], c1 = e < 2 ? ca[2 * e + 1] : cb4[2 * e - 3];
            const float p0 = bflo(f3[k][e]) * c0, p1 = bfhi(f3[k][e]) * c1;
            cs += p0 + p1;
            o[e] = pad ? padv : pack2(p0, p1);
          }
          st16(rs, WS_WINF + (unsigned)(ucb * 7 + w) * 20480u + (unsigned)(kg * 5 + k) * 1024u + l16, o);
        }
        cs = sum_rows(cs);
        DRAIN();
        if (l == 0) st4(rs, WS_FW1 + (unsigned)((ucb * 8 + w) * 4 + kg) * 128u, tag);
        STAMP(5);
        BAR();
#pragma unroll
        for (int k = 0; k < 5; ++k) {
          const f32x4 sa = *(const f32x4*)(tp + 160 + k * 32), sb4 = *(const f32x4*)(tp + 160 + k * 32 + 16);
#pragma unroll
          for (int e = 0; e < 4; ++e) {
            const float s0 = e < 2 ? sa[2 * e] : sb4[2 * e - 4], s1 = e < 2 ? sa[2 * e + 1] : sb4[2 * e - 3];
            b1 = __builtin_fmaf(s0, bflo(f3[k][e]), __builtin_fmaf(s1, bfhi(f3[k][e]), b1));
          }
        }
        b1 = sum_rows(b1);
        if (pad) { cs = padv ? 160.0f : 0.0f; b1 = 0.0f; }
        if (l < 16) st16(rs, WS_CS + (unsigned)((ucb * 4 + kg) * 112 + w * 16 + l) * 16u, (u32x4){tag, __float_as_uint(cs), tag, __float_as_uint(b1)});
      }
    } else {
      if (job >= 60 && T < 0) __builtin_amdgcn_s_sleep(100);
      if (slot < 2 && w >= 2 && w < 6) {
        const int c5 = slot * 4 + w - 2, c2 = xcd;
        char* st = lds + L_J5 + (w - 2) * 18432;
#pragma unroll
        for (int r = 0; r < 18; ++r) {
          const int g = r * 64 + l;
          __builtin_amdgcn_raw_ptr_buffer_load_lds(rwo, (LAS void*)(st + r * 1024), 16, (unsigned)((g / 10) * (D_ * 2) + (g % 10) * 16),
                                                   (unsigned)(xcd * CB * D_ * 2 + c5 * 160), 0, 0);
        }
        DRAIN();
        const char* bp = st + (4 * h4 + (n16 >> 2)) * 160 + (n16 & 3) * 8;
        const unsigned base = WS_WOUTF + (unsigned)c5 * WBLK;
#pragma unroll
        for (int dj = 0; dj < 5; ++dj) {
          st16(rs, base + (unsigned)c2 * 10240u + (unsigned)dj * 1024u + l16, trfrag(bp + dj * 32, 32 * 160));
          st16(rs, base + (unsigned)c2 * 10240u + (unsigned)(5 + dj) * 1024u + l16, trfrag(bp + 16 * 160 + dj * 32, 32 * 160));
          st16(rs, base + 81920u + (unsigned)c2 * 5120u + (unsigned)dj * 1024u + l16, trfrag(bp + 64 * 160 + dj * 32, 16 * 160));
          st8(rs, base + 122880u + (unsigned)((c2 >> 1) * 5 + dj) * 1024u + (unsigned)(c2 & 1) * 512u + (unsigned)l * 8u, trd(bp + 96 * 160 + dj * 32));
        }
        fw2 = WS_FW2 + (unsigned)(c5 * 8 + xcd) * 128u;
      }
    }
    STAMP(4);
#pragma unroll
    for (int i = 0; i < NRAW; ++i) LDW(i);
    bs_raw = ((const GAS u16*)p.b_sp)[trow];
    e_fw1 = ~tag;
    asm volatile(".Lmega_aux_end:");
    asm volatile("" ::"v"(cpf));
    STAMP(8);
  }

  LAS char* xa = (LAS char*)(lds + L_XI) + l * 16;
  LAS char* xb = xa + 65536;
  asm volatile("" : "+v"(xb));
#define LDA(ks, rt) (*(const volatile LAS u32x4*)((ks) < 16 ? xa + ((ks) * 4 + (rt)) * 1024 : xb + (((ks) - 16) * 4 + (rt)) * 1024))
  auto lda1 = [&](int ks, u32x4* A) {
#pragma unroll
    for (int rt = 0; rt < 4; ++rt) A[rt] = LDA(ks, rt);
  };
  const LAS char* tb4 = (const LAS char*)(lds + L_MISC) + h4 * 16;
  const LAS char* tbn = (const LAS char*)(lds + L_MISC) + n16 * 4;
  asm volatile("" : "+v"(tb4), "+v"(tbn));
  u32x4 xr[5][2];
  auto ld_x1 = [&](int m0, int m1) {
#pragma unroll
    for (int m = m0; m < m1; ++m) {
      if (m == 0) __builtin_amdgcn_s_setprio(2);
      if (m == 1) __builtin_amdgcn_s_setprio(1);
      if (m == 3) __builtin_amdgcn_s_setprio(0);
#pragma unroll
      for (int e = 0; e < 2; ++e)
        xr[m][e] = ld16(rs, l16, WS_X1B + (unsigned)(R * 2 + (w & 1)) * WX1P + (unsigned)(4 * m + (w >> 1)) * 2048u + (unsigned)e * 1024u);
      SCHED();
    }
  };
  LAS char* xw = xa + w * 2048;
  auto bad = [](const u32x4& a, const u32x4& b) {
    auto mx = [](unsigned x, unsigned y) { return __builtin_elementwise_max(x, y); };
    return __any(mx(mx(mx(a[0], a[1]), mx(a[2], a[3])), mx(mx(b[0], b[1]), mx(b[2], b[3]))) == ~0u);
  };
  auto put_x1 = [&](int m) {
    if (bad(xr[m][0], xr[m][1])) {
      _Pragma("unroll 1") for (unsigned spins = 0; spins < SPIN_CAP; ++spins) {
#pragma unroll
        for (int e = 0; e < 2; ++e)
          xr[m][e] = ld16(rs, l16, WS_X1B + (unsigned)(R * 2 + (w & 1)) * WX1P + (unsigned)(4 * m + (w >> 1)) * 2048u + (unsigned)e * 1024u);
        if (!bad(xr[m][0], xr[m][1])) break;
        __builtin_amdgcn_s_sleep(2);
      }
    }
    LAS char* d = m < 4 ? xw + m * 16384 : xb + w * 2048;
#pragma unroll
    for (int e = 0; e < 2; ++e) {
      *(volatile LAS u32x2*)(d + 8 * e) = (u32x2){xr[m][e][0], xr[m][e][1]};
      *(volatile LAS u32x2*)(d + 1024 + 8 * e) = (u32x2){xr[m][e][2], xr[m][e][3]};
    }
  };
  auto poison_u = [&]() {
#pragma unroll
    for (int k = 0; k < 2; ++k) {
      const unsigned o = (unsigned)(wj * 2 + k) * 1024u + l16;
      const unsigned rt = o / 3584u, r = o - rt * 3584u;
      st16(rs, WS_UF + ((unsigned)(R * 4) + rt) * 8u * 3584u + (unsigned)c * 3584u + r, (u32x4){~0u, ~0u, ~0u, ~0u});
    }
  };
  const unsigned wob = WS_WOUTF + (unsigned)c * WBLK;
  const LAS char* wrd = (const LAS char*)(lds + L_WS) + w * 8192 + n16 * 256 + ((h4 ^ ((4 - (n16 >> 2)) & 3)) * 64);
#define DMA_W(g, q) __builtin_amdgcn_raw_ptr_buffer_load_lds(rsp, (LAS void*)(lds + L_WS + w * 8192 + (((g) + 1) & 1) * 4096 + (q) * 1024), 16, WVO(q), WSO(g, q), 0, 0)
#define RD_W(g) do { _Pragma("unroll") for (int i_ = 0; i_ < 4; ++i_) \
                      wsp[(g) & 3][((g) >> 2) * 4 + i_] = *(const volatile LAS u32x4*)(wrd + (((g) + 1) & 1) * 4096 + ((i_ ^ (n16 & 3)) * 16)); } while (0)
  auto park = [&]() {
    LAS char* d = (LAS char*)(lds + L_WS) + w * 8192 + l * 16;
#pragma unroll
    for (int g = 0; g < NRAW / 4; ++g) {
#pragma unroll
      for (int q = 0; q < 4; ++q) *(volatile LAS u32x4*)(d + ((g + 1) & 1) * 4096 + q * 1024) = raw[4 * g + q];
      if (g < 2) RD_W(g);
    }
    asm volatile("s_waitcnt lgkmcnt(0)" ::: "memory");
  };
#define DMA_AB(k) dma1k(rs, lds + L_AB + ((k) * 8 + w) * 1024, l16, wob + (unsigned)((k) * 8 + w) * 1024u)
  const int rto = w & 3, cls = w >> 2;
  u32x2 xp[3];
  constexpr int W = G2W;
  const unsigned zb = WS_Z2F + (unsigned)c * 458752u + (unsigned)wk * 8192u;
  const unsigned fzo = WS_FZ + (unsigned)(c * 32 + wk * 4 + (l & 3)) * 128u;
  u32x4 fr[W];
#define LDZ(i) fr[(i) % W] = ld16(rs, l16, zb + (unsigned)((i) >> 3) * 65536u + (unsigned)((i) & 7) * 1024u)
  u32x4 cr[5];
#define LDCR(k) cr[k] = ld16(rs, l16, wob + 81920u + (unsigned)(w * 5 + (k)) * 1024u)
  auto rd_xp = [&]() {
#pragma unroll
    for (int j = 0; j < 3; ++j) {
      const int q4 = 5 * c + (cls ? (3 + (j < 2 ? j : 1)) : j);
      xp[j] = *(const volatile LAS u32x2*)(xa + ((q4 >> 1) * 4 + rto) * 1024 + (q4 & 1) * 8);
    }
  };
  if (!__all(e_fw1 == tag)) {
    wait_flag<4>(rs, fw1o, tag);
    DRAIN();
    st4(rs, l == 0 ? fw2 : OOB, tag);
  }
  asm volatile("" ::: "memory");
  if (w < 7) {
    u32x4 B2[20], B1[20];
    const unsigned b2o = WS_WINF + (unsigned)((8 + c) * 7 + w) * 20480u, b1o = WS_WINF + (unsigned)(c * 7 + w) * 20480u;
#define LDB2(ks) B2[ks] = ld16(rs, l16, b2o + (unsigned)(ks) * 1024u)
#define LDB1(ks) B1[ks] = ld16(rs, l16, b1o + (unsigned)(ks) * 1024u)
#pragma unroll
    for (int ks = 0; ks < NB2A; ++ks) LDB2(ks);
    SCHED();
    STAMP(33);
    BAR();
    STAMP(37);
#pragma unroll
    for (int ks = NB2A; ks < NB2B; ++ks) {
      LDB2(ks);
      if (ks == PK) { SCHED(); park(); SCHED(); }
    }
    STAMP(10);
    ld_x1(0, 3);
    SCHED();
    if (PK == 99) park();
#pragma unroll
    for (int ks = NB2B; ks < NB2C; ++ks) LDB2(ks);
    SCHED();
#pragma unroll
    for (int m = 0; m < 3; ++m) put_x1(m);
    ld_x1(3, 5);
    poison_u();
    SCHED();
    if (w == 6) __builtin_amdgcn_s_setprio(2);
    BAR();
    STAMP(11);
    f32x4 acc[4];
    u32x4 A[2][4];
    lda1(0, A[0]);
#pragma unroll
    for (int ks = 0; ks < 20; ++ks) {
      if (ks == 6) put_x1(3);
      if (ks == 8) put_x1(4);
      if (ks == 11) BAR();
      if (NB2C + ks < 20) LDB2(NB2C + ks);
      if (ks >= 4) LDB1(ks - 4);
      if (ks >= 16) LDB1(ks);
      if (ks == 12) RD_W(2);
      SCHED();
#pragma unroll
      for (int rt = 0; rt < 4; ++rt) {
        if (ks < 19) A[(ks + 1) & 1][rt] = LDA(ks + 1, rt);
        SCHED();
        acc[rt] = MFMA16(A[ks & 1][rt], B2[ks], ks ? acc[rt] : ZERO4);
        SCHED();
      }
    }
    STAMP(12);
    if (w == 6) {
      if (n16 == 12) {
#pragma unroll
        for (int rt = 0; rt < 4; ++rt) *(f32x4*)(lds + L_S1 + (rt * 16 + 4 * h4) * 4) = acc[rt];
      }
      asm volatile("" ::: "memory");
      *(volatile LAS unsigned*)(lds + L_S1 + 256) = tag;
      __builtin_amdgcn_s_setprio(0);
    }
    VMW(20);
    BAR();
    STAMP(13);
    f32x2 zz[8];
    f32x4 cs1a, cs1b;
    {
      f32x4 rs4[4], nm4[4];
#pragma unroll
      for (int rt = 0; rt < 4; ++rt) {
        rs4[rt] = *(const LAS f32x4*)(tb4 + (L_T2 - L_MISC) + rt * 64);
        nm4[rt] = *(const LAS f32x4*)(tb4 + (L_T2 - L_MISC) + 256 + rt * 64);
      }
      const f32x2 cs2 = *(const f32x2*)(lds + L_CS1 + (112 + 16 * w + n16) * 8);
      const f32x2 csv = {cs2[0], cs2[0]}, b1v = {cs2[1], cs2[1]};
      float v1[16], v2[16];
#pragma unroll
      for (int i = 0; i < 8; ++i) {
        const int rt = i >> 1;
        const f32x2 a2 = i & 1 ? HI2(acc[rt]) : LO2(acc[rt]), n2 = i & 1 ? HI2(nm4[rt]) : LO2(nm4[rt]), r2 = i & 1 ? HI2(rs4[rt]) : LO2(rs4[rt]);
        zz[i] = gelu2(__builtin_elementwise_fma(__builtin_elementwise_fma(n2, csv, a2), r2, b1v));
        asm volatile("" : "+v"(zz[i]));
        DMA_W(3 + (i >> 2), i & 3);
        const f32x2 sq = zz[i] * zz[i];
        v1[2 * i] = zz[i][0]; v1[2 * i + 1] = zz[i][1]; v2[2 * i] = sq[0]; v2[2 * i + 1] = sq[1];
      }
      const float t1 = tr_sum16(v1, l);
      const float t2 = tr_sum16(v2, l);
      *(f32x2*)(lds + L_Z1S + (w * 64 + (n16 >> 2) * 16 + 4 * h4 + (n16 & 3)) * 8) = (f32x2){t1, t2};
      lda1(0, A[0]);
      cs1a = *(const f32x4*)(lds + L_CS1 + (16 * w + 4 * h4) * 8);
      cs1b = *(const f32x4*)(lds + L_CS1 + (16 * w + 4 * h4) * 8 + 16);
    }
    BAR();
    STAMP(14);
#pragma unroll
    for (int ks = 0; ks < 20; ++ks) {
      if (ks == Z1SPLIT) {
        STAMP(15);
        BAR();
        STAMP(16);
        f32x4 ra[4], rb[4];
#pragma unroll
        for (int rt = 0; rt < 4; ++rt) {
          ra[rt] = *(const LAS f32x4*)(tb4 + (L_ST2 - L_MISC) + rt * 64);
          rb[rt] = *(const LAS f32x4*)(tb4 + (L_ST2 - L_MISC) + 256 + rt * 64);
        }
#pragma unroll
        for (int i = 0; i < 8; ++i) {
          zz[i] = __builtin_elementwise_fma(zz[i], i & 1 ? HI2(ra[i >> 1]) : LO2(ra[i >> 1]), i & 1 ? HI2(rb[i >> 1]) : LO2(rb[i >> 1]));
          asm volatile("" : "+v"(zz[i]));
        }
        const u32x4 o0 = {pack2(zz[0][0], zz[2][0]), pack2(zz[0][1], zz[2][1]), pack2(zz[1][0], zz[3][0]), pack2(zz[1][1], zz[3][1])};
        const u32x4 o1 = {pack2(zz[4][0], zz[6][0]), pack2(zz[4][1], zz[6][1]), pack2(zz[5][0], zz[7][0]), pack2(zz[5][1], zz[7][1])};
        const unsigned zo = WS_Z2F + (unsigned)((c * 7 + w) * 64 + 4 * (R >> 1) + h4) * 1024u + (unsigned)(R & 1) * 512u + (unsigned)n16 * 16u;
        st16(rs, zo, o0);
        st16(rs, zo + 256u, o1);
        SCHED();
      }
      if (ks == 0) { VMW(4); RD_W(3); }
      if (ks == 2) { VMW(0); RD_W(4); }
      if (ks == 8) { VMW(4 + 2); RD_W(5); }
      if (ks == 11) rd_xp();
      if (ks == 13) { VMW(4 + 5 + 2); RD_W(6); }
      if (ks >= 13 && ks < 19) DMA_AB(ks - 13);
      if (ks >= 2 && ks < 6) { DMA_W(5 + ((ks - 2) >> 1), 2 * (ks & 1)); DMA_W(5 + ((ks - 2) >> 1), 2 * (ks & 1) + 1); }
      if (ks >= 6 && ks < 9) LDCR(ks - 6);
      if (ks == 12) { LDCR(3); LDCR(4); }
      if (ks == 10 || ks == 11) { DMA_W(7, 2 * (ks & 1)); DMA_W(7, 2 * (ks & 1) + 1); }
      SCHED();
#pragma unroll
      for (int rt = 0; rt < 4; ++rt) {
        if (ks < 19) A[(ks + 1) & 1][rt] = LDA(ks + 1, rt);
        SCHED();
        acc[rt] = MFMA16(B1[ks], A[ks & 1][rt], ks ? acc[rt] : ZERO4);
        SCHED();
      }
    }
    STAMP(18);
    VMW(2 + 2 + 6);
    RD_W(7);
    const f32x2 c01 = {cs1a[0], cs1a[2]}, b01 = {cs1a[1], cs1a[3]}, c23 = {cs1b[0], cs1b[2]}, b23 = {cs1b[1], cs1b[3]};
    u32x2 g1[4];
    auto z1g = [&](int rt) {
      const float r1 = *(const LAS float*)(tbn + (L_T2 - L_MISC) + rt * 64), n1 = *(const LAS float*)(tbn + (L_T2 - L_MISC) + 256 + rt * 64);
      const f32x2 r2 = {r1, r1}, n2 = {n1, n1};
      const f32x2 a = gelu2(__builtin_elementwise_fma(__builtin_elementwise_fma(n2, c01, LO2(acc[rt])), r2, b01));
      const f32x2 b = gelu2(__builtin_elementwise_fma(__builtin_elementwise_fma(n2, c23, HI2(acc[rt])), r2, b23));
      g1[rt] = (u32x2){pack2(a[0], a[1]), pack2(b[0], b[1])};
      asm volatile("" : "+v"(g1[rt]));
    };
    SCHED();
    VMW(2 + 6);
    BAR();
    STAMP(17);
#pragma unroll
    for (int rt = 0; rt < 4; ++rt) {
      if (rt < 2) { DMA_AB(6 + 2 * rt); DMA_AB(7 + 2 * rt); }
      z1g(rt);
      *(u32x2*)(lds + L_Z1S + ((w * 4 + rt) * 64 + l) * 8) = g1[rt];
    }
  } else {
    __builtin_amdgcn_s_setprio(3);
    BAR();
    STAMP(9);
    *(volatile LAS unsigned*)(lds + L_S1 + 256) = ~tag;
    u32x4 gv, cv[2][2][4];
    const unsigned gvo = WS_MOD + (unsigned)(5 * D_ + 80 * c) * 8u + (l < 40 ? l16 : 0u);
    auto ld_consts = [&]() {
      gv = ld16(rs, gvo);
#pragma unroll
      for (int i = 0; i < 16; ++i) cv[i >> 3][(i >> 2) & 1][i & 3] = ld16(rs, l < 56 ? l16 : 0u, WS_CS + (unsigned)(((c + 8 * (i >> 3)) * 4 + (i & 3)) * 112 + 56 * ((i >> 2) & 1)) * 16u);
    };
    auto ok_consts = [&]() {
      unsigned x = (gv[0] ^ tag) | (gv[2] ^ tag);
#pragma unroll
      for (int i = 0; i < 16; ++i) x |= (cv[i >> 3][(i >> 2) & 1][i & 3][0] ^ tag) | (cv[i >> 3][(i >> 2) & 1][i & 3][2] ^ tag);
      asm volatile("" : "+v"(x));
      return __all(x == 0u);
    };
    auto touch_w = [&](int k) {
      cpf ^= __builtin_amdgcn_raw_buffer_load_b32(rsp, (unsigned)(h4 * 4096 + (n16 >> 1) * 512 + (n16 & 1) * 128),
                                                  (unsigned)(R * 64 + (c & 3) * 16 + k * 4) * 4096u + 256u, 0);
    };
    park();
    __builtin_amdgcn_s_sleep(W7S);
    const unsigned fz1 = WS_FW1 + (unsigned)(c * 32 + (l < 28 ? l : 0)) * 128u;
    const unsigned e_z1 = ld4(rs, fz1);
    ld_consts();
    STAMP(10);
    ld_x1(0, 5);
    __builtin_amdgcn_s_setprio(3);
    if (!__all(e_z1 == tag)) wait_flag<2>(rs, fz1, tag);
    if (!ok_consts()) {
      _Pragma("unroll 1") for (unsigned spins = 0; spins < SPIN_CAP; ++spins) {
        ld_consts();
        if (ok_consts()) break;
        __builtin_amdgcn_s_sleep(8);
      }
    }
    if (l < 40) *(f32x2*)(lds + L_GB + l * 8) = (f32x2){__uint_as_float(gv[1]), __uint_as_float(gv[3])};
    if (l < 56) {
#pragma unroll
      for (int i = 0; i < 4; ++i) {
        const u32x4* q4 = cv[i >> 1][i & 1];
        *(f32x2*)(lds + L_CS1 + (56 * i + l) * 8) =
            (f32x2){(__uint_as_float(q4[0][1]) + __uint_as_float(q4[1][1])) + (__uint_as_float(q4[2][1]) + __uint_as_float(q4[3][1])),
                    (__uint_as_float(q4[0][3]) + __uint_as_float(q4[1][3])) + (__uint_as_float(q4[2][3]) + __uint_as_float(q4[3][3]))};
      }
    }
    SCHED();
#pragma unroll
    for (int m = 0; m < 3; ++m) put_x1(m);
    BAR();
    f32x4 acc[4];
    u32x4 A[2][4];
    lda1(0, A[0]);
#pragma unroll
    for (int ks = 0; ks < 20; ++ks) {
      if (ks == 6) put_x1(3);
      if (ks == 8) put_x1(4);
      if (ks == 11) BAR();
      if (ks == 4) RD_W(2);
      if (ks >= 6 && ks < 14) DMA_W(3 + ((ks - 6) >> 2), (ks - 6) & 3);
      if (ks >= 1 && ks < 5) touch_w(ks - 1);
      if (ks < 19) lda1(ks + 1, A[(ks + 1) & 1]);
      SCHED();
#pragma unroll
      for (int rt = 0; rt < 4; ++rt) acc[rt] = MFMA16(A[ks & 1][rt], A[ks & 1][rt], ks ? acc[rt] : ZERO4);
      SCHED();
    }
    {
      if ((n16 >> 2) == h4) {
#pragma unroll
        for (int rt = 0; rt < 4; ++rt) {
          const float lo = (l & 1) ? acc[rt][1] : acc[rt][0], hi = (l & 1) ? acc[rt][3] : acc[rt][2];
          *(volatile LAS float*)(tbn + (L_T2 - L_MISC) + rt * 64) = (l & 2) ? hi : lo;
        }
      }
      const float s2 = *(volatile LAS float*)(lds + L_T2 + l * 4) * (1.0f / D_);
      float s1;
      _Pragma("unroll 1") for (unsigned spins = 0; spins < SPIN_CAP * 16; ++spins) {
        const unsigned f = *(volatile LAS unsigned*)(lds + L_S1 + 256);
        s1 = *(volatile LAS float*)(lds + L_S1 + l * 4);
        if (f == tag) break;
      }
      const float nm = s1 * (-1.0f / D_);
      *(float*)(lds + L_T2 + l * 4) = __builtin_amdgcn_rsqf(__builtin_fmaxf(s2 - nm * nm, 0.f) + 1e-5f);
      *(float*)(lds + L_T2 + 256 + l * 4) = nm;
    }
    BAR();
    __builtin_amdgcn_s_setprio(0);
    asm volatile("" ::"v"(cpf));
    DRAIN();
    RD_W(3); RD_W(4);
    rd_xp();
    asm volatile("s_waitcnt lgkmcnt(0)" ::: "memory");
#pragma unroll
    for (int i = 0; i < 8; ++i) DMA_W(5 + (i >> 2), i & 3);
    const unsigned e_fw2 = ld4(rs, WS_FW2 + (unsigned)(c * 8 + (l & 7)) * 128u);
    if (!__all(e_fw2 == tag)) wait_flag<2>(rs, WS_FW2 + (unsigned)(c * 8 + (l & 7)) * 128u, tag);
    __builtin_amdgcn_s_setprio(3);
    BAR();
    {
      f32x2 t = {0.f, 0.f};
#pragma unroll
      for (int k = 0; k < 7; ++k) t += *(const f32x2*)(lds + L_Z1S + (k * 64 + l) * 8);
      st16(rs, WS_PART + (unsigned)(R * 8 + c) * 1024u + l16, (u32x4){tag, __float_as_uint(t[0]), tag, __float_as_uint(t[1])});
    }
    STAMP(14);
    {
      u32x4 v[8];
      const unsigned po = WS_PART + (unsigned)R * 8192u + l16;
      __builtin_amdgcn_s_sleep(16);
      _Pragma("unroll 1") for (unsigned spins = 0; spins < SPIN_CAP; ++spins) {
#pragma unroll
        for (int k = 0; k < 8; ++k) v[k] = ld16(rs, po + (unsigned)k * 1024u);
        unsigned x = 0;
#pragma unroll
        for (int k = 0; k < 8; ++k) x |= (v[k][0] ^ tag) | (v[k][2] ^ tag);
        asm volatile("" : "+v"(x));
        if (__all(x == 0u)) break;
        __builtin_amdgcn_s_sleep(1);
      }
      float t1 = 0.f, t2 = 0.f;
#pragma unroll
      for (int k = 0; k < 8; ++k) { t1 += __uint_as_float(v[k][1]); t2 += __uint_as_float(v[k][3]); }
      const float mean = t1 * (1.0f / FH);
      const float var = __builtin_fmaxf(t2 * (1.0f / FH) - mean * mean, 0.f);
      const float rstd = __builtin_amdgcn_rsqf(var + 1e-5f);
      *(float*)(lds + L_ST2 + l * 4) = rstd;
      *(float*)(lds + L_ST2 + 256 + l * 4) = -mean * rstd;
    }
    STAMP(15);
    BAR();
    RD_W(5); RD_W(6);
    asm volatile("s_waitcnt lgkmcnt(0)" ::: "memory");
#pragma unroll
    for (int q = 0; q < 4; ++q) DMA_W(7, q);
    DRAIN();
    RD_W(7);
    BAR();
    if (l == 0) st4(rs, WS_FZ + (unsigned)(c * 32 + R) * 128u, tag);
    STAMP(17);
    __builtin_amdgcn_s_setprio(0);
#pragma unroll
    for (int k = 0; k < 10; ++k) DMA_AB(k);
#pragma unroll
    for (int k = 0; k < 5; ++k) LDCR(k);
  }
  const rsrc_t rout = __builtin_amdgcn_make_buffer_rsrc((void*)p.out, 0, S_ * D_ * 2, 0x00020000);
  STAMP(19);
  u32x2 z1o[4];
  wait_flag<POLL_SLEEP>(rs, fzo, tag);
#pragma unroll
  for (int i = 0; i < W; ++i) { LDZ(i); SCHED(); }
  STAMP(20);

  const unsigned ub = WS_UF + (unsigned)((R * 4 + rto) * 8 + c) * 3584u;
  const float bs = bflo(bs_raw);
  const unsigned ur = WS_UF + (unsigned)((R * 4 + rto) * 8 + cls) * 3584u;
  u32x4 uf[4][2];
  f32x4 acc[2][4];
  f32x4* const bufw = (f32x4*)(lds + L_BUF) + w * 256 + l;
#define BUFW(slot, rt) bufw[(slot) * 2048 + (rt) * 64]
  {
    const f32x4* const rb = (const f32x4*)(lds + L_BUF) + cls * 2048 + rto * 64 + l;
    f32x4 rdv[4], s;
    u32x4 uo;
#pragma unroll
    for (int j = 0; j < 7; ++j) {
#pragma unroll
      for (int ks = 0; ks < 8; ++ks) {
        const int i = 8 * j + ks;
#pragma unroll
        for (int rt = 0; rt < 4; ++rt) acc[j & 1][rt] = MFMA16(fr[i % W], wsp[rt][ks], ks ? acc[j & 1][rt] : ZERO4);
        if (i == 0) STAMP(21);
        if (i == 24) STAMP(22);
        SCHED();
        if (i + W < 56) LDZ(i + W);
        if (j > 0) {
          if (ks == 0 && (j & 1)) BAR();
          if (j == 1 && ks == 0) {
#pragma unroll
            for (int k = 0; k < 4; ++k) z1o[k] = *(const u32x2*)(lds + L_Z1S + (((k < 3 ? 2 * k + cls : 6) * 4 + rto) * 64 + l) * 8);
          }
          if (i >= (56 - G2W) && i < 53) {
#pragma unroll
            for (int k = 0; k < 8; ++k)
              if ((56 - G2W) + k * (53 - (56 - G2W)) / 8 == i) uf[k >> 1][k & 1] = ld16(rs, l16, ur + (unsigned)(k >> 1) * 7168u + (unsigned)(k & 1) * 1024u);
            SCHED();
          }
          const bool fast = j == 6;
          if (!fast && ks < 4) BUFW((j - 1) & 1, ks) = acc[(j - 1) & 1][ks];
          if (fast && ks < 2) { BUFW(1, 2 * ks) = acc[1][2 * ks]; BUFW(1, 2 * ks + 1) = acc[1][2 * ks + 1]; }
          if (!(j & 1)) {
            const int k0 = fast ? 2 : 5;
            if (ks == k0) {
              BAR();
#pragma unroll
              for (int k = 0; k < 4; ++k) rdv[k] = rb[k * 256];
            }
            if (ks == k0 + 1) {
              s = (rdv[0] + rdv[1]) + (rdv[2] + rdv[3]);
#pragma unroll
              for (int k = 0; k < 4; ++k) rdv[k] = rb[(4 + k) * 256];
            }
            if (ks == k0 + 2) {
              s += (rdv[0] + rdv[1]) + (rdv[2] + rdv[3]);
              const u32x2 z = z1o[(j >> 1) - 1];
              const int e = j == 4 ? 2 : 0;
              uo[e] = pack2(bflo(z[0]) * (s[0] + bs), bfhi(z[0]) * (s[1] + bs));
              uo[e + 1] = pack2(bflo(z[1]) * (s[2] + bs), bfhi(z[1]) * (s[3] + bs));
              if (j == 4) st16(rs, ub + (unsigned)cls * 1024u + l16, uo);
              if (j == 6) st16(rs, l < 32 ? ub + 2048u + (unsigned)cls * 512u + l16 : OOB, half_swap((u32x4){uo[0], uo[1], uo[0], uo[1]}));
            }
            if (j == 6 && ks == 4) BAR();
            if (j == 6 && ks >= 4 && ks < 7) {
#pragma unroll
              for (int k = 0; k < 5; ++k)
                if (k / 2 == ks - 4) *(u32x4*)(lds + (w < 6 ? L_BUF + 32768 + w * 5120 : L_Z1S + (w - 6) * 5120) + k * 1024 + l16) = cr[k];
            }
          }
        }
        SCHED();
      }
    }
  }
  STAMP(23);

  {
    u32x4 uc[4], uh[4];
    const unsigned uho = (unsigned)(l >> 5) * 3584u + (unsigned)(l & 31) * 16u;
#define LD_C(cc) uc[cc] = ld16(rs, l16, ur + (unsigned)(cc) * 7168u + 2048u)
#define LD_H(cc) uh[cc] = ld16(rs, uho, WS_UF + (unsigned)((R * 4 + rto) * 8) * 3584u + (unsigned)(cc) * 7168u + 3072u)
    auto mx = [](unsigned a, unsigned b) { return __builtin_elementwise_max(a, b); };
    auto ok4 = [&](const u32x4* v, int n) {
      unsigned m = 0;
#pragma unroll
      for (int i = 0; i < 8; ++i)
        if (i < n) m = mx(m, mx(mx(v[i][0], v[i][1]), mx(v[i][2], v[i][3])));
      return __all(m != ~0u);
    };
#pragma unroll
    for (int rt = 0; rt < 4; ++rt) BUFW(0, rt) = acc[0][rt];
    BAR();
    f32x4 a3[5];
    u32x4 af[2][5];
    const LAS char* wb[2] = {(const LAS char*)(lds + L_AB) + cls * (10240 + 3072) + l16, (const LAS char*)(lds + L_AB) + cls * (10240 - 2048) + l16};
    const LAS char* wb2[2] = {wb[0] + 40960, wb[1] + 40960};
    asm volatile("" : "+v"(wb[0]), "+v"(wb2[0]), "+v"(wb[1]), "+v"(wb2[1]));
    u32x4 ho = {tag, tag, tag, tag};
    if (w < 4) {
      __builtin_amdgcn_s_setprio(2);
      const f32x4* const rb = (const f32x4*)(lds + L_BUF) + rto * 64 + l;
      f32x4 r8[8];
#pragma unroll
      for (int k = 0; k < 8; ++k) r8[k] = rb[k * 256];
      const f32x4 s = ((r8[0] + r8[1]) + (r8[2] + r8[3])) + ((r8[4] + r8[5]) + (r8[6] + r8[7]));
      const u32x2 z = z1o[3];
      const unsigned o0 = pack2(bflo(z[0]) * (s[0] + bs), bfhi(z[0]) * (s[1] + bs)), o1 = pack2(bflo(z[1]) * (s[2] + bs), bfhi(z[1]) * (s[3] + bs));
      ho = half_swap((u32x4){o0, o1, o0, o1});
    }
    st16(rs, tid < 256 ? (l < 32 ? ub + 3072u + l16 : OOB) : (bid == 0 && tid == 256 ? WS_EPOCH : OOB), ho);
    SCHED();
    __builtin_amdgcn_s_setprio(0);
#pragma unroll
    for (int dj = 0; dj < 5; ++dj) af[0][dj] = *(const LAS u32x4*)(wb[dj >= 2] + dj * 1024);
    const bool ab_ok = ok4(&uf[0][0], 8);
    STAMP(24);
    if (!ab_ok) {
      _Pragma("unroll 1") for (unsigned spins = 0; spins < SPIN_CAP; ++spins) {
#pragma unroll
        for (int i = 0; i < 8; ++i) uf[i >> 1][i & 1] = ld16(rs, l16, ur + (unsigned)(i >> 1) * 7168u + (unsigned)(i & 1) * 1024u);
        if (ok4(&uf[0][0], 8)) break;
        __builtin_amdgcn_s_sleep(1);
      }
    }
    STAMP(25);
    const LAS char* cb0[2] = {(const LAS char*)(lds + L_BUF + 32768) + cls * (5120 + 3072) + l16, (const LAS char*)(lds + L_BUF + 32768) + cls * (5120 - 2048) + l16};
    u32x4 ah[4][3];
    const LAS char* hb = (const LAS char*)(lds + L_BUF) + cls * 12288 + l * 8;
    f32x4 gb[3], xo3[3], xf[3];
    LAS char *otw, *xw3;
    const LAS char *ota, *otb, *xr3;
    unsigned oa_o, ob_o;
#pragma unroll
    for (int st = 0; st < 12; ++st) {
      if (st < 2) { LD_C(2 * st); LD_C(2 * st + 1); }
      else if (st < 7 && w < 4) dma1k(rs, lds + L_BUF + ((st - 2) * 4 + w) * 1024, l16, wob + 122880u + (unsigned)(w * 5 + st - 2) * 1024u);
      if (st == 2) {
        LAS char* ot = (LAS char*)(lds + L_AB + 32768) + w * 2048;
        otw = ot + n16 * 112 + h4 * 8;
        ota = ot + (l >> 2) * 112 + (l & 3) * 16;
        otb = ot + ((l >> 1) & 15) * 112 + 64 + (l & 1) * 16;
        asm volatile("" : "+v"(otw), "+v"(ota), "+v"(otb));
      }
      if (st == 3) {
        oa_o = (unsigned)(l >> 2) * 2560u + (unsigned)(l & 3) * 16u;
        ob_o = l < 32 ? (unsigned)(l >> 1) * 2560u + 64u + (unsigned)(l & 1) * 16u : OOB;
        xw3 = (LAS char*)(lds + L_AB) + w * 3072 + l16;
        xr3 = (const LAS char*)(lds + L_AB) + (w ^ 4) * 3072 + cls * 1024 + l16;
        asm volatile("" : "+v"(oa_o), "+v"(ob_o), "+v"(xw3), "+v"(xr3));
      }
      if (st >= 4 && st < 7) {
        const int j = st - 4;
        gb[j] = *(const f32x4*)(lds + L_GB + (48 * cls + 16 * (cls && j == 2 ? 1 : j) + 4 * h4) * 4);
        xf[j] = (f32x4){bflo(xp[j][0]), bfhi(xp[j][0]), bflo(xp[j][1]), bfhi(xp[j][1])};
        asm volatile("" : "+v"(xf[j]));
      }
      if (st == 8) {
        STAMP(26);
        if (!ok4(uc, 4)) {
          _Pragma("unroll 1") for (unsigned spins = 0; spins < SPIN_CAP; ++spins) {
            LD_C(0); LD_C(1); LD_C(2); LD_C(3);
            if (ok4(uc, 4)) break;
            __builtin_amdgcn_s_sleep(1);
          }
        }
        LD_H(0); LD_H(1); LD_H(2); LD_H(3);
#pragma unroll
        for (int cc = 0; cc < 4; ++cc) uc[cc] = half_swap(uc[cc]);
        VMW(4);
        __builtin_amdgcn_s_barrier();
        asm volatile("" ::: "memory");
        STAMP(27);
      }
      SCHED();
#pragma unroll
      for (int dj = 0; dj < 5; ++dj) {
        const int sn = st + 1;
        if (sn < 8) af[sn & 1][dj] = *(const LAS u32x4*)((sn < 4 ? wb : wb2)[dj >= 2] + ((sn >> 1) & 1) * 20480 + ((sn & 1) * 5 + dj) * 1024);
        else if (sn < 12) af[sn & 1][dj] = *(const LAS u32x4*)(cb0[dj >= 2] + (sn < 11 ? (sn - 8) * 10240 : L_Z1S - L_BUF - 32768) + dj * 1024);
        if (st >= 8) {
#pragma unroll
          for (int k = 0; k < 2; ++k) {
            const int h = st < 11 ? (k ? 99 : (st - 8) * 5 + dj) : 15 + 2 * dj + k;
            if (h < 24) {
              const u32x2 v = *(const volatile LAS u32x2*)(hb + (((h >> 1) % 3) * 4 + (h >> 1) / 3) * 1024 + (h & 1) * 512);
              ah[(h >> 1) / 3][(h >> 1) % 3][2 * (h & 1)] = v[0]; ah[(h >> 1) / 3][(h >> 1) % 3][2 * (h & 1) + 1] = v[1];
            }
          }
        }
        SCHED();
        a3[dj] = MFMA16(af[st & 1][dj], st < 8 ? uf[(st >> 1) & 3][st & 1] : uc[st & 3], st ? a3[dj] : ZERO4);
        SCHED();
      }
    }
#pragma unroll
    for (int j = 0; j < 3; ++j) *(volatile LAS f32x4*)(xw3 + j * 1024) = a3[2 + j];
    BAR();
#pragma unroll
    for (int j = 0; j < 3; ++j) xo3[j] = *(const volatile LAS f32x4*)(xr3 + j * 1024);
    SCHED();
    if (!ok4(uh, 4)) {
      _Pragma("unroll 1") for (unsigned spins = 0; spins < SPIN_CAP; ++spins) {
        LD_H(0); LD_H(1); LD_H(2); LD_H(3);
        if (ok4(uh, 4)) break;
        __builtin_amdgcn_s_sleep(1);
      }
    }
#pragma unroll
    for (int cc = 0; cc < 4; ++cc) {
      const u32x4 b = half_swap(uh[cc]);
#pragma unroll
      for (int j = 0; j < 3; ++j) a3[j] = MFMA16(ah[cc][j], b, a3[j]);
      if (cc >= 1) {
        const int j = cc - 1;
        xo3[j] = __builtin_elementwise_fma(gb[j], xo3[j], xf[j]);
        asm volatile("" : "+v"(xo3[j]));
      }
      SCHED();
    }
#pragma unroll
    for (int j = 0; j < 3; ++j) {
      const f32x4 r = __builtin_elementwise_fma(gb[j], a3[j], xo3[j]);
      *(volatile LAS u32x2*)(otw + j * 32) = (u32x2){pack2(r[0], r[1]), pack2(r[2], r[3])};
    }
    const u32x4 oa = *(const volatile LAS u32x4*)(ota);
    const u32x4 ob = *(const volatile LAS u32x4*)(otb);
    const unsigned obase = (unsigned)((R * 64 + 32 * ((w >> 1) & 1) + (w & 1)) * D_ + 80 * c + 48 * cls) * 2u;
    __builtin_amdgcn_raw_buffer_store_b128(oa, rout, oa_o, obase, 0);
    if (cls == 0) __builtin_amdgcn_raw_buffer_store_b128(ob, rout, ob_o, obase, 0);
  }
  STAMP(30);
  asm volatile(".Lmega_end:");
}

extern "C" void kernel_launch(void* const* d_in, const int* in_sizes, int n_in, void* d_out, int out_size,
                              void* d_ws, size_t ws_size, hipStream_t stream) {
  Params p;
  p.q = (const u16*)d_in[0];
  p.k = (const u16*)d_in[1];
  p.v = (const u16*)d_in[2];
  p.shift_k = (const u16*)d_in[3];
  p.ssm_k = (const u16*)d_in[4];
  p.ssm_D = (const u16*)d_in[5];
  p.w_in = (const u16*)d_in[7];
  p.w_sp = (const u16*)d_in[8];
  p.b_sp = (const u16*)d_in[9];
  p.w_out = (const u16*)d_in[10];
  p.cvec = (const u16*)d_in[11];
  p.w_mod = (const u16*)d_in[12];
  p.out = (u16*)d_out;
  p.ws = (char*)d_ws;
  mega<<<256, 512, 0, stream>>>(p);
}
```

```cpp
#include <hip/hip_runtime.h>
#include <stdint.h>

typedef unsigned short u16;
typedef __attribute__((ext_vector_type(8))) short bf16x8;
typedef __attribute__((ext_vector_type(4))) float f32x4;
typedef __attribute__((ext_vector_type(2))) float f32x2;
typedef __attribute__((ext_vector_type(4))) unsigned int u32x4;
typedef __attribute__((ext_vector_type(2))) unsigned int u32x2;
#define LAS __attribute__((address_space(3)))
#define CAS __attribute__((address_space(4)))
#define GAS __attribute__((address_space(1)))
typedef __amdgpu_buffer_rsrc_t rsrc_t;
typedef __attribute__((ext_vector_type(4))) short s16x4;

constexpr int S_ = 2048, D_ = 640, F_ = 1728, FH = 864, CB = 108;

#define X1PAD 256
constexpr unsigned WS_EPOCH = 0;
constexpr unsigned WS_FX = 4096;
constexpr unsigned WS_FZ = WS_FX + 24576;
constexpr unsigned WS_FW2 = WS_FZ + 32768;
constexpr unsigned WS_FW1 = WS_FW2 + 8192;
constexpr unsigned WS_CS = WS_FW1 + 65536;
constexpr unsigned WS_PART = WS_CS + 131072;
constexpr unsigned WS_MOD = WS_PART + 262144;
constexpr unsigned WS_ST1 = WS_MOD + 32768;
constexpr unsigned WS_X1B = WS_ST1 + 16384;
constexpr unsigned WX1P = 40960 + X1PAD;
constexpr unsigned WBLK = 143360;
constexpr unsigned WS_QB = WS_X1B + 2686976;
constexpr unsigned WS_WINF = WS_QB + 2686976;
constexpr unsigned WS_WOUTF = WS_WINF + 16 * WBLK;
constexpr unsigned WS_Z2F = WS_WOUTF + 8 * WBLK;
constexpr unsigned WS_UF = WS_Z2F + 8 * 64 * 7168;
constexpr unsigned WS_KVT = WS_UF + 32 * 4 * 8 * 3584;
constexpr unsigned WS_FK = WS_KVT + 32 * 81920;
constexpr unsigned WS_STAMP = WS_FK + 20480;
constexpr unsigned WS_END = WS_STAMP + 256 * 40 * 8;

constexpr int U_BYTES = (256 + S_) * 2;
constexpr int G_COPY = 4160;
constexpr int CH_BYTES = U_BYTES + 4 * G_COPY;
constexpr int L_YP = 4 * CH_BYTES;
constexpr int L_A0 = 98304;
constexpr int L_RED = 0;
constexpr int L_TAB = 2048;
constexpr int L_J3 = 8192;
constexpr int L_QT = 49152;
constexpr int L_J5 = L_QT;
constexpr int L_XI = 0;
constexpr int L_BUF = 81920;
constexpr int L_AB = 0;
constexpr int L_MISC = 147456;
constexpr int L_Z1S = L_MISC;
constexpr int L_S1 = L_MISC + 14336;
constexpr int L_T2 = L_S1 + 512;
constexpr int L_ST2 = L_T2 + 512;
constexpr int L_GB = L_ST2 + 512;
constexpr int L_CS1 = L_Z1S + 4096;
constexpr int L_WS = 81920;
constexpr int LDS_TOTAL = 163840;

constexpr int POLL_SLEEP = 1;
constexpr int AUX_SLEEP = 36, LATE_SLEEP = 96;
constexpr int CDIST = 4;
constexpr int NRAW = 12;
constexpr int SV_AT = 108, Q_AT = 130;
constexpr int NB2A = 2;
constexpr int G2W = 7;
constexpr int Z1SPLIT = 12;
constexpr int SC1 = 16;
constexpr unsigned SPIN_CAP = 1u << 16;
constexpr unsigned OOB = 0x80000000u;

__device__ __forceinline__ float bflo(unsigned x) { return __uint_as_float(x << 16); }
__device__ __forceinline__ float bfhi(unsigned x) { return __uint_as_float(x & 0xffff0000u); }
__device__ __forceinline__ f32x2 bf2(unsigned x) { return (f32x2){bflo(x), bfhi(x)}; }
__device__ __forceinline__ unsigned f2bf(float x) {
  __bf16 b = (__bf16)x;
  return (unsigned)__builtin_bit_cast(unsigned short, b);
}
__device__ __forceinline__ float rbf(float x) { return bflo(f2bf(x)); }
typedef __attribute__((ext_vector_type(2))) __bf16 bf2_t;
__device__ __forceinline__ unsigned pack2(float lo, float hi) {
  f32x2 v = {lo, hi};
  return __builtin_bit_cast(unsigned, __builtin_convertvector(v, bf2_t));
}
template <int CTRL>
__device__ __forceinline__ float dpp_f(float v) {
  return __int_as_float(__builtin_amdgcn_update_dpp(0, __float_as_int(v), CTRL, 0xf, 0xf, true));
}
__device__ __forceinline__ float sum_rows(float v) {
  auto a = __builtin_amdgcn_permlane16_swap(__float_as_uint(v), __float_as_uint(v), false, false);
  v = __uint_as_float(a[0]) + __uint_as_float(a[1]);
  auto b = __builtin_amdgcn_permlane32_swap(__float_as_uint(v), __float_as_uint(v), false, false);
  return __uint_as_float(b[0]) + __uint_as_float(b[1]);
}
__device__ __forceinline__ f32x2 gelu2(f32x2 x) {
  const float c0 = -2.0f * 0.7978845608028654f * 1.4426950408889634f;
  const float c1 = c0 * 0.044715f;
  const f32x2 t = x * __builtin_elementwise_fma(x * x, (f32x2){c1, c1}, (f32x2){c0, c0});
  const f32x2 d = (f32x2){__builtin_amdgcn_exp2f(t[0]), __builtin_amdgcn_exp2f(t[1])} + (f32x2){1.0f, 1.0f};
  return x * (f32x2){__builtin_amdgcn_rcpf(d[0]), __builtin_amdgcn_rcpf(d[1])};
}
#define LO2(v) __builtin_shufflevector(v, v, 0, 1)
#define HI2(v) __builtin_shufflevector(v, v, 2, 3)
__device__ __forceinline__ float tr_sum16(const float* v, int l) {
  float x[8], y[4];
  asm volatile(
      "s_nop 1\n\t"
      "v_add_f32_dpp %0, %8, %8 row_ror:8 row_mask:0xf bank_mask:0x3\n\t"
      "v_add_f32_dpp %1, %9, %9 row_ror:8 row_mask:0xf bank_mask:0x3\n\t"
      "v_add_f32_dpp %2, %10, %10 row_ror:8 row_mask:0xf bank_mask:0x3\n\t"
      "v_add_f32_dpp %3, %11, %11 row_ror:8 row_mask:0xf bank_mask:0x3\n\t"
      "v_add_f32_dpp %4, %12, %12 row_ror:8 row_mask:0xf bank_mask:0x3\n\t"
      "v_add_f32_dpp %5, %13, %13 row_ror:8 row_mask:0xf bank_mask:0x3\n\t"
      "v_add_f32_dpp %6, %14, %14 row_ror:8 row_mask:0xf bank_mask:0x3\n\t"
      "v_add_f32_dpp %7, %15, %15 row_ror:8 row_mask:0xf bank_mask:0x3\n\t"
      "v_add_f32_dpp %0, %16, %16 row_ror:8 row_mask:0xf bank_mask:0xc\n\t"
      "v_add_f32_dpp %1, %17, %17 row_ror:8 row_mask:0xf bank_mask:0xc\n\t"
      "v_add_f32_dpp %2, %18, %18 row_ror:8 row_mask:0xf bank_mask:0xc\n\t"
      "v_add_f32_dpp %3, %19, %19 row_ror:8 row_mask:0xf bank_mask:0xc\n\t"
      "v_add_f32_dpp %4, %20, %20 row_ror:8 row_mask:0xf bank_mask:0xc\n\t"
      "v_add_f32_dpp %5, %21, %21 row_ror:8 row_mask:0xf bank_mask:0xc\n\t"
      "v_add_f32_dpp %6, %22, %22 row_ror:8 row_mask:0xf bank_mask:0xc\n\t"
      "v_add_f32_dpp %7, %23, %23 row_ror:8 row_mask:0xf bank_mask:0xc"
      : "=&v"(x[0]), "=&v"(x[1]), "=&v"(x[2]), "=&v"(x[3]), "=&v"(x[4]), "=&v"(x[5]), "=&v"(x[6]), "=&v"(x[7])
      : "v"(v[0]), "v"(v[1]), "v"(v[2]), "v"(v[3]), "v"(v[4]), "v"(v[5]), "v"(v[6]), "v"(v[7]), "v"(v[8]), "v"(v[9]), "v"(v[10]),
        "v"(v[11]), "v"(v[12]), "v"(v[13]), "v"(v[14]), "v"(v[15]));
  asm volatile(
      "s_nop 1\n\t"
      "v_add_f32_dpp %0, %4, %4 row_shl:4 row_mask:0xf bank_mask:0x5\n\t"
      "v_add_f32_dpp %1, %5, %5 row_shl:4 row_mask:0xf bank_mask:0x5\n\t"
      "v_add_f32_dpp %2, %6, %6 row_shl:4 row_mask:0xf bank_mask:0x5\n\t"
      "v_add_f32_dpp %3, %7, %7 row_shl:4 row_mask:0xf bank_mask:0x5\n\t"
      "v_add_f32_dpp %0, %8, %8 row_shr:4 row_mask:0xf bank_mask:0xa\n\t"
      "v_add_f32_dpp %1, %9, %9 row_shr:4 row_mask:0xf bank_mask:0xa\n\t"
      "v_add_f32_dpp %2, %10, %10 row_shr:4 row_mask:0xf bank_mask:0xa\n\t"
      "v_add_f32_dpp %3, %11, %11 row_shr:4 row_mask:0xf bank_mask:0xa\n\t"
      "s_nop 1"
      : "=&v"(y[0]), "=&v"(y[1]), "=&v"(y[2]), "=&v"(y[3])
      : "v"(x[0]), "v"(x[1]), "v"(x[2]), "v"(x[3]), "v"(x[4]), "v"(x[5]), "v"(x[6]), "v"(x[7]));
  const bool b1 = l & 2, b0 = l & 1;
  const float z0 = (b1 ? y[2] : y[0]) + dpp_f<0x4E>(b1 ? y[0] : y[2]);
  const float z1 = (b1 ? y[3] : y[1]) + dpp_f<0x4E>(b1 ? y[1] : y[3]);
  return (b0 ? z1 : z0) + dpp_f<0xB1>(b0 ? z0 : z1);
}
__device__ __forceinline__ u32x4 half_swap(u32x4 v) {
  const auto a = __builtin_amdgcn_permlane32_swap(v[0], v[2], false, false);
  const auto b = __builtin_amdgcn_permlane32_swap(v[1], v[3], false, false);
  return (u32x4){a[0], b[0], a[1], b[1]};
}
#define MFMA16(a, b, c) __builtin_amdgcn_mfma_f32_16x16x32_bf16(__builtin_bit_cast(bf16x8, a), __builtin_bit_cast(bf16x8, b), c, 0, 0, 0)
__device__ __forceinline__ u32x2 trd(const char* p) {
  return __builtin_bit_cast(u32x2, __builtin_amdgcn_ds_read_tr16_b64_v4i16((LAS s16x4*)p));
}
__device__ __forceinline__ u32x4 trfrag(const char* p, int second) {
  u32x2 a = trd(p), b = trd(p + second);
  return (u32x4){a[0], a[1], b[0], b[1]};
}
#define BAR() asm volatile("s_waitcnt lgkmcnt(0)\n\ts_barrier" ::: "memory")
#define DRAIN() do { __builtin_amdgcn_s_waitcnt(0x0F70); asm volatile("" ::: "memory"); } while (0)
#define VMW(n) do { __builtin_amdgcn_s_waitcnt(0x0F70 | ((n) & 15) | (((n) >> 4) << 14)); asm volatile("" ::: "memory"); } while (0)
#define SCHED() __builtin_amdgcn_sched_barrier(0)
#define ZERO4 ((f32x4){0.f, 0.f, 0.f, 0.f})

__device__ __forceinline__ u32x4 ld16(rsrc_t r, unsigned off, unsigned so = 0) { return __builtin_amdgcn_raw_buffer_load_b128(r, off, so, SC1); }
__device__ __forceinline__ u32x2 ld8(rsrc_t r, unsigned off, unsigned so = 0) { return __builtin_amdgcn_raw_buffer_load_b64(r, off, so, SC1); }
__device__ __forceinline__ unsigned ld4(rsrc_t r, unsigned off, unsigned so = 0) { return __builtin_amdgcn_raw_buffer_load_b32(r, off, so, SC1); }
__device__ __forceinline__ void st16(rsrc_t r, unsigned off, u32x4 v) { __builtin_amdgcn_raw_buffer_store_b128(v, r, off, 0, SC1); }
__device__ __forceinline__ void st8(rsrc_t r, unsigned off, u32x2 v) { __builtin_amdgcn_raw_buffer_store_b64(v, r, off, 0, SC1); }
__device__ __forceinline__ void st4(rsrc_t r, unsigned off, unsigned v) { __builtin_amdgcn_raw_buffer_store_b32(v, r, off, 0, SC1); }
__device__ __forceinline__ void dma1k(rsrc_t r, char* lds_dst, unsigned l16, unsigned soff) {
  __builtin_amdgcn_raw_ptr_buffer_load_lds(r, (LAS void*)lds_dst, 16, l16, soff, 0, SC1);
}

#define STAMP(i)

template <int SLEEP>
__device__ __forceinline__ void wait_flag(rsrc_t r, unsigned off, unsigned tag) {
  _Pragma("unroll 1") for (unsigned spins = 0; spins < SPIN_CAP; ++spins) {
    if (__all(ld4(r, off) == tag)) break;
    __builtin_amdgcn_s_sleep(SLEEP);
  }
  asm volatile("" ::: "memory");
}

struct ConvSched {
  signed char pp[144], m[144], rd[144];
  int pre;
};
constexpr ConvSched conv_sched(int dist) {
  ConvSched s = {};
  int need[96] = {}, i = 0;
  for (int r = 0; r < 4; ++r)
    for (int pp = 0; pp < 8; ++pp) {
      const int j = 24 * r + (pp ? 8 + 2 * pp : 0);
      need[j] = need[j + 1] = i;
      for (int m = 7 - pp; m >= 0; --m) {
        if (pp == 0) need[24 * r + 9 - m] = i;
        s.pp[i] = pp; s.m[i] = m; s.rd[i] = -1;
        ++i;
      }
    }
  int next = 144;
  for (int j = 95; j >= 0; --j) {
    next = need[j] - dist < next - 1 ? need[j] - dist : next - 1;
    if (next >= 0) s.rd[next] = j;
    else if (s.pre == 0) s.pre = j + 1;
  }
  return s;
}

template <int I> struct IC { static constexpr int v = I; };
template <int I0, int I1, class F>
__device__ __forceinline__ void static_for(F&& f) {
  if constexpr (I0 < I1) { f(IC<I0>{}); static_for<I0 + 1, I1>(f); }
}

struct Params {
  const u16 *q, *k, *v, *shift_k, *ssm_k, *ssm_D, *w_in, *w_sp, *b_sp, *w_out, *cvec, *w_mod;
  u16* out;
  char* ws;
};

__global__ __launch_bounds__(512, 2) void mega(Params) {
  __shared__ __attribute__((aligned(16))) char lds[LDS_TOTAL];
  const CAS Params* kp = (const CAS Params*)__builtin_amdgcn_kernarg_segment_ptr();
  unsigned ka0, ka1;
  asm volatile("s_load_dword %0, %2, 0x0\n\ts_load_dword %1, %2, 0x40" : "=&s"(ka0), "=&s"(ka1) : "s"(kp));
  SCHED();
  const int tid = threadIdx.x, l = tid & 63;
  const int w = __builtin_amdgcn_readfirstlane(tid >> 6);
  const int bid = blockIdx.x, xcd = bid & 7, idx = bid >> 3;
  const int dg = ((idx & 7) + 2 * (idx >> 3)) & 7;
  const bool is_conv = dg >= 3;
  const int slot = 3 * (idx >> 3) + dg;
  const int cidx = 5 * (idx >> 3) + dg - 3;
  const int quad = xcd * 20 + cidx;
  const unsigned l16 = (unsigned)l * 16u;
  const int blk = (quad / 5) * 8 + w, sl = quad % 5;
  unsigned a0g = (unsigned)(l >> 4) * 1280u + (unsigned)(((l & 15) ^ (4 * (l >> 5))) * 16), a0x = a0g ^ 128u, a0h = a0g + (l < 16 ? 1280u : 0u);
  unsigned a0s = (unsigned)blk * 10240u + (unsigned)sl * 256u, a0p = blk ? a0s - 5120u : a0s, a0l = L_A0 + w * 6144;
  asm volatile("" : "+v"(a0g), "+v"(a0x), "+v"(a0h));
  asm volatile("" : "+s"(a0s), "+s"(a0p), "+s"(a0l));
  SCHED();
  asm volatile("" : "+s"(kp));
  Params p = {kp->q, kp->k, kp->v, kp->shift_k, kp->ssm_k, kp->ssm_D, kp->w_in, kp->w_sp, kp->b_sp, kp->w_out, kp->cvec, kp->w_mod, kp->out, kp->ws};
  asm volatile("s_waitcnt lgkmcnt(0)" : "+s"(p.q), "+s"(p.k), "+s"(p.v), "+s"(p.shift_k), "+s"(p.ssm_k), "+s"(p.ssm_D), "+s"(p.w_in), "+s"(p.w_sp),
               "+s"(p.b_sp), "+s"(p.w_out), "+s"(p.cvec), "+s"(p.w_mod), "+s"(p.out), "+s"(p.ws) : "s"(ka0), "s"(ka1));
  char* const ws = p.ws;
  STAMP(0);
  unsigned cpf = 0;
  if (!is_conv) {
    unsigned long long pc;
    unsigned len, len2;
    asm volatile("s_getpc_b64 %0\n.Lmega_here:\n\ts_mov_b32 %1, .Lmega_end-.Lmega_here\n\ts_mov_b32 %2, .Lmega_aux_end-.Lmega_here" : "=s"(pc), "=s"(len), "=s"(len2));
    len = len > len2 ? len : len2;
    unsigned o = (unsigned)((slot * 8 + w) * 1024 + (l & 7) * 128);
    o = o + 16u <= len ? o : 0u;
    cpf = *(const GAS unsigned*)((pc & ~15ull) + o);
  }
  const rsrc_t rs = __builtin_amdgcn_make_buffer_rsrc(ws, 0, WS_END, 0x00020000);
  const unsigned epv = ld4(rs, WS_EPOCH);
  SCHED();
  u32x2 dd2;
  {
    int cq = dg;
    asm volatile("" : "+s"(cq));
    if (cq >= 3) {
      const rsrc_t rk = __builtin_amdgcn_make_buffer_rsrc((void*)p.k, 0, S_ * D_ * 2, 0x00020000);
      const rsrc_t rv = __builtin_amdgcn_make_buffer_rsrc((void*)p.v, 0, S_ * D_ * 2, 0x00020000);
      const rsrc_t rh = __builtin_amdgcn_make_buffer_rsrc((void*)p.shift_k, 0, D_ * 8, 0x00020000);
      char* const la = lds + a0l;
      __builtin_amdgcn_raw_ptr_buffer_load_lds(rh, (LAS void*)(la + 5120), 16, l16, (unsigned)sl * 1024u, 0, 0);
      __builtin_amdgcn_raw_ptr_buffer_load_lds(rk, (LAS void*)(la + 1024), 16, a0x, a0s, 0, 0);
      __builtin_amdgcn_raw_ptr_buffer_load_lds(rk, (LAS void*)(la + 2048), 16, a0g, a0s + 5120u, 0, 0);
      __builtin_amdgcn_raw_ptr_buffer_load_lds(rv, (LAS void*)(la + 3072), 16, a0g, a0s, 0, 0);
      __builtin_amdgcn_raw_ptr_buffer_load_lds(rv, (LAS void*)(la + 4096), 16, a0x, a0s + 5120u, 0, 0);
      __builtin_amdgcn_raw_ptr_buffer_load_lds(rk, (LAS void*)(la), 16, a0h, a0p, 0, 0);
      SCHED();
      const rsrc_t rf = __builtin_amdgcn_make_buffer_rsrc((void*)p.ssm_k, 0, S_ * D_ * 2, 0x00020000);
      dd2 = *(const CAS u32x2*)((const CAS char*)p.ssm_D + quad * 8);
      STAMP(32);
      {
        const u32x4 z = {0u, 0u, 0u, 0u};
        if (tid < 128) *(u32x4*)(lds + (tid >> 5) * CH_BYTES + (tid & 31) * 16) = z;
        else if (tid < 152) {
          const int i = (tid - 128) >> 2;
          *(u32x4*)(lds + (tid & 3) * CH_BYTES + U_BYTES + (i < 4 ? i * G_COPY : (i & 1 ? 4128 : 16))) = z;
        }
        const LAS char* a0r = (const LAS char*)(lds + L_A0) + w * 6144 + (l & 3) * 512;
#define A0R(k) (a0r + ((((l >> 2) ^ (4 * ((l + (k)) & 3))) * 16)))
        unsigned go = WS_KVT + (unsigned)(quad / 5) * 81920u + (unsigned)sl * 16384u + (unsigned)(l >> 2) * 1024u + (unsigned)w * 64u + (unsigned)(l & 3) * 16u;
        const LAS char* r0 = A0R(0), * r1 = A0R(1), * r2 = A0R(2);
        const LAS char* rsh = (const LAS char*)(lds + L_A0) + w * 6144 + 5120 + (l >> 2) * 64;
        asm volatile("" : "+v"(go), "+v"(r0), "+v"(r1), "+v"(r2), "+v"(rsh));
        SCHED();
#pragma unroll
        for (int j = 0; j < 2; ++j)
          __builtin_amdgcn_raw_ptr_buffer_load_lds(rf, (LAS void*)(lds + (w >> 1) * CH_BYTES + U_BYTES + 32 + (2 * (w & 1) + j) * 1024), 16, l16,
                                                   (unsigned)quad * 16384u + (unsigned)(2 * w + j) * 1024u, 0, 0);
        STAMP(34);
        SCHED();
        VMW(7);
        f32x2 wt[4][4];
#pragma unroll
        for (int e = 0; e < 4; ++e) {
          const u32x4 sh = *(const volatile LAS u32x4*)(rsh + 16 * e);
          wt[e][0] = (f32x2){bflo(sh[0]), bflo(sh[2])}; wt[e][1] = (f32x2){bfhi(sh[0]), bfhi(sh[2])};
          wt[e][2] = (f32x2){bflo(sh[1]), bflo(sh[3])}; wt[e][3] = (f32x2){bfhi(sh[1]), bfhi(sh[3])};
        }
#pragma unroll
        for (int e = 0; e < 4; ++e) asm volatile("" : "+v"(wt[e][0]), "+v"(wt[e][1]), "+v"(wt[e][2]), "+v"(wt[e][3]));
        asm volatile("" : "+s"(dd2));
        SCHED();
        VMW(2);
        STAMP(35);
        u32x4 ka[5], va[2];
        ka[0] = *(const volatile LAS u32x4*)(r0 + 256);
        ka[1] = *(const volatile LAS u32x4*)(r1 + 512);
        ka[2] = *(const volatile LAS u32x4*)(r1 + 768);
        ka[3] = *(const volatile LAS u32x4*)(r2 + 1024);
        ka[4] = *(const volatile LAS u32x4*)(r2 + 1280);
        va[0] = *(const volatile LAS u32x4*)(r0 + 3072);
        va[1] = *(const volatile LAS u32x4*)(r0 + 3328);
        SCHED();
        if (blk == 0) {
          if ((l & 3) < 2) ka[0] = z;
          if ((l & 3) == 0) { ka[1] = z; ka[2] = z; }
        }
        u32x4 ko;
#pragma unroll
        for (int e = 0; e < 4; ++e) {
          const f32x2 w0 = wt[e][0], w1 = wt[e][1], w2 = wt[e][2], w3 = wt[e][3];
          const f32x2 km3 = bf2(ka[0][e]), km2 = bf2(ka[1][e]), km1 = bf2(ka[2][e]), k0 = bf2(ka[3][e]), k1 = bf2(ka[4][e]);
          const f32x2 s0 = __builtin_elementwise_fma(k0, w0, __builtin_elementwise_fma(km1, w1, __builtin_elementwise_fma(km2, w2, km3 * w3)));
          const f32x2 s1 = __builtin_elementwise_fma(k1, w0, __builtin_elementwise_fma(k0, w1, __builtin_elementwise_fma(km1, w2, km2 * w3)));
          const f32x2 o0 = s0 * bf2(va[0][e]), o1 = s1 * bf2(va[1][e]);
          ko[2 * (e & 1)] = pack2(o1[0], o0[0]); ko[2 * (e & 1) + 1] = pack2(o1[1], o0[1]);
          if (e & 1) st16(rs, go + (unsigned)(e >> 1) * 512u, ko);
          SCHED();
        }
      }
    }
  }
  const int R = (xcd & 3) * 8 + (idx & 7);
  const int c = (xcd >> 2) * 4 + (idx >> 3);
  const int n16 = l & 15, h4 = l >> 4;
  const rsrc_t rq = __builtin_amdgcn_make_buffer_rsrc((void*)p.q, 0, S_ * D_ * 2, 0x00020000);
  const rsrc_t rsp = __builtin_amdgcn_make_buffer_rsrc((void*)p.w_sp, 0, S_ * S_ * 2, 0x00020000);
  const int wj = w < 7 ? w : 0;
  u32x4 wsp[4][8];
  const int wk = w ^ (dg == 5 || dg == 6 ? 4 : 0);
  const unsigned wso = (unsigned)(R * 64) * 4096u + (unsigned)wk * 512u;
  const unsigned wdo = (unsigned)(h4 * 8192 + ((n16 ^ h4) * 16));
#define WVO(q) (wdo ^ (unsigned)(((4 - (q)) & 3) * 64))
#define WSO(g, q) (wso + (unsigned)((g) >> 2) * 256u + (unsigned)(32 * (((g) >> 1) & 1) + ((g) & 1) + 8 * (q)) * 4096u)
  u32x4 raw[NRAW];
#define LDW(i) raw[i] = __builtin_amdgcn_raw_buffer_load_b128(rsp, WVO((i) & 3), WSO((i) >> 2, (i) & 3), 0)
  const int trow = R * 64 + 32 * ((w >> 1) & 1) + 2 * n16 + (w & 1);
  unsigned bs_raw;
  unsigned tag;
  unsigned fw2 = OOB;
  unsigned e_fw1;
  const unsigned fw1o = l < 20 ? WS_FX + (unsigned)(w * 20 + l) * 128u : WS_FW1 + (unsigned)(((8 + c) * 8 + wj) * 4 + (l & 3)) * 128u;

  if (__builtin_expect(is_conv, 1)) {
    STAMP(1);
    DRAIN();
    tag = __builtin_amdgcn_readfirstlane(epv) * 2654435761u + 0x9E3779B9u;
    BAR();
    st4(rs, tid == 0 ? WS_FK + (unsigned)quad * 128u : OOB, tag);
    const unsigned xo = (unsigned)(tid >> 4) * WX1P + (unsigned)quad * 256u + (unsigned)(tid & 15) * 16u;
    SCHED();
    st16(rs, WS_X1B + xo, (u32x4){~0u, ~0u, ~0u, ~0u});
    st16(rs, WS_X1B + xo + 32u * WX1P, (u32x4){~0u, ~0u, ~0u, ~0u});
    SCHED();
    unsigned atv, ato;
    const unsigned long long atb = (unsigned long long)(w < 5 ? p.w_in : p.w_out);
    {
      const int i = w < 5 ? (cidx * 5 + w) * 20 + (l < 19 ? l : 19) : (cidx * 3 + w - 5) * 19 + (l < 18 ? l : 18);
      const int r = i / 3;
      ato = w < 5 ? (unsigned)((r < 639 ? r : 639) * (F_ * 2) + xcd * CB * 2 + (i - 3 * r) * 108) : (unsigned)(xcd * CB * D_ * 2 + (i < 1079 ? i : 1079) * 128);
      asm volatile("" : "+v"(ato));
    }
    {
      u32x4 d[3];
      u32x2 n[3];
#pragma unroll
      for (int j = 0; j < 3; ++j) {
        char* G = j < 2 ? lds + ((tid >> 8) + 2 * j) * CH_BYTES + U_BYTES + ((tid & 255) + 2) * 16 : lds + (tid & 3) * CH_BYTES + U_BYTES + 16;
        d[j] = *(const u32x4*)G;
        n[j] = *(const u32x2*)(G + 16);
      }
#pragma unroll
      for (int j = 0; j < 3; ++j) {
        char* G = j < 2 ? lds + ((tid >> 8) + 2 * j) * CH_BYTES + U_BYTES + ((tid & 255) + 2) * 16 : lds + (tid & 3) * CH_BYTES + U_BYTES + 16;
        const unsigned a0 = __builtin_amdgcn_alignbit(d[j][1], d[j][0], 16), a1 = __builtin_amdgcn_alignbit(d[j][2], d[j][1], 16),
                       a2 = __builtin_amdgcn_alignbit(d[j][3], d[j][2], 16), a3 = __builtin_amdgcn_alignbit(n[j][0], d[j][3], 16),
                       a4 = __builtin_amdgcn_alignbit(n[j][1], n[j][0], 16);
        if (j < 2 || tid < 4) {
          *(u32x4*)(G + G_COPY) = (u32x4){a0, a1, a2, a3};
          *(u32x4*)(G + 2 * G_COPY) = (u32x4){d[j][1], d[j][2], d[j][3], n[j][0]};
          *(u32x4*)(G + 3 * G_COPY) = (u32x4){a1, a2, a3, a4};
        }
      }
    }
    const int ch = w & 3, hf = w >> 2;
    const LAS char *ap, *bp;
    {
      const char* U = lds + ch * CH_BYTES;
      const char* G = U + U_BYTES;
      const int a = n16, jj = h4 >> 1, e2 = h4 & 1;
      const int sg = (a + 1) & 3;
      bp = (const LAS char*)U + 512 + 32 * (a - jj) + 16 * e2 - 256 * hf - 192;
      ap = (const LAS char*)G + sg * G_COPY + 2 * (16 * jj + a - 8 * e2 + 9 - sg) + 256 * hf;
      asm volatile("" : "+v"(ap), "+v"(bp));
    }
    STAMP(36);
    {
      wait_flag<1>(rs, WS_FK + (unsigned)(5 * (4 * w + (l & 3)) + (quad >> 5)) * 128u, tag);
      STAMP(29);
      const unsigned gp = WS_KVT + (unsigned)(4 * w + (l >> 5)) * 81920u + (unsigned)quad * 512u + (unsigned)(l & 31) * 16u;
      const u32x4 g0 = ld16(rs, gp), g1 = ld16(rs, gp + 163840u);
      SCHED();
      char* Uw = lds + 512 + (128 * w + (l & ~3) + 3 - (l & 3)) * 4;
#pragma unroll
      for (int i = 0; i < 4; ++i) *(unsigned*)(Uw + i * CH_BYTES) = g0[i];
#pragma unroll
      for (int i = 0; i < 4; ++i) *(unsigned*)(Uw + 256 + i * CH_BYTES) = g1[i];
    }
    STAMP(28);
    DRAIN();
    BAR();
    STAMP(2);
    const int te = 2 * tid;
    u32x4 qa[2];
    u32x4 sv[2];
    u32x2 mg;
    const unsigned mgo = WS_MOD + (unsigned)((l < 12 ? (l >> 2) * D_ + (l & 3) : 0) + quad * 4) * 8u;
    auto ld_sv = [&]() {
      sv[0] = ld16(rs, WS_ST1 + (unsigned)tid * 16u);
      sv[1] = ld16(rs, WS_ST1 + (unsigned)tid * 16u + 8192u);
    };
    auto ld_q = [&]() {
      qa[0] = ld16(rs, WS_QB + xo);
      qa[1] = ld16(rs, WS_QB + xo + 32u * WX1P);
    };
    f32x2 hh[2][2][2], base[2][2][2];
    auto prep = [&]() {
      const LAS char* ur = (const LAS char*)(lds + 512) + ((te & ~7) + 6 - (te & 7)) * 2;
      unsigned uu[2][4];
#pragma unroll
      for (int i = 0; i < 8; ++i) uu[i >> 2][i & 3] = *(const volatile LAS unsigned*)(ur + (i >> 2) * 2048 + (i & 3) * CH_BYTES);
      if (!__all(((sv[0][0] ^ tag) | (sv[0][2] ^ tag) | (sv[1][0] ^ tag) | (sv[1][2] ^ tag)) == 0u)) {
        _Pragma("unroll 1") for (unsigned spins = 0; spins < SPIN_CAP; ++spins) {
          ld_sv();
          if (__all(sv[0][0] == tag && sv[0][2] == tag && sv[1][0] == tag && sv[1][2] == tag)) break;
          __builtin_amdgcn_s_sleep(4);
        }
        asm volatile("" ::: "memory");
        ld_q();
        mg = ld8(rs, mgo);
        DRAIN();
      }
      if (!__all(mg[0] == tag)) {
        _Pragma("unroll 1") for (unsigned spins = 0; spins < SPIN_CAP; ++spins) {
          mg = ld8(rs, mgo);
          if (__all(mg[0] == tag)) break;
          __builtin_amdgcn_s_sleep(2);
        }
      }
      f32x2 cc2[2], ss2[2], dv2[2];
      float cch[4], sch[4];
#pragma unroll
      for (int i = 0; i < 4; ++i) {
        const float sa = __uint_as_float(__builtin_amdgcn_readlane(mg[1], i)), ca = __uint_as_float(__builtin_amdgcn_readlane(mg[1], 4 + i)),
                    ga = __uint_as_float(__builtin_amdgcn_readlane(mg[1], 8 + i));
        cch[i] = ga * rbf(1.0f + ca);
        sch[i] = ga * sa;
      }
      cc2[0] = (f32x2){cch[0], cch[1]}; cc2[1] = (f32x2){cch[2], cch[3]};
      ss2[0] = (f32x2){sch[0], sch[1]}; ss2[1] = (f32x2){sch[2], sch[3]};
      dv2[0] = bf2(dd2[0]); dv2[1] = bf2(dd2[1]);
#pragma unroll
      for (int H = 0; H < 2; ++H)
#pragma unroll
        for (int e = 0; e < 2; ++e) {
          const unsigned st = sv[H][1 + 2 * e];
          const float a = __builtin_amdgcn_rcpf(bfhi(st)), b = -bflo(st) * a;
#pragma unroll
          for (int i = 0; i < 2; ++i) {
            const f32x2 q2 = bf2(qa[H][2 * e + i]);
            const f32x2 u2 = e ? (f32x2){bflo(uu[H][2 * i]), bflo(uu[H][2 * i + 1])} : (f32x2){bfhi(uu[H][2 * i]), bfhi(uu[H][2 * i + 1])};
            const f32x2 n1 = __builtin_elementwise_fma(q2, (f32x2){a, a}, (f32x2){b, b});
            hh[H][e][i] = __builtin_elementwise_fma(n1, cc2[i], ss2[i]);
            base[H][e][i] = __builtin_elementwise_fma(hh[H][e][i], u2 * dv2[i], q2);
            asm volatile("" : "+v"(hh[H][e][i]), "+v"(base[H][e][i]));
          }
        }
    };
    auto HF = [&]() { int x = hf; asm volatile("" : "+s"(x)); return x; };
    {
      const int a = n16;
      char* yp = lds + L_YP + (hf * 4 + ch) * 8192 + (h4 * 16 + ((a + 4 * h4) & 15)) * 16;
      f32x4 acc[8];
      u32x4 A[2][8], B[2][8];
      auto rd = [&](int k) {
        const int r = k / 24, j = k % 24;
        if (j >= 2 && j < 10) B[r & 1][9 - j] = *(const volatile LAS u32x4*)(bp + 512 * (9 - j) + 64 * (3 - r));
        else {
          const int pp = j < 2 ? 0 : (j - 8) >> 1;
          const u32x2 v = *(const volatile LAS u32x2*)(ap + 512 * pp + 64 * r + 8 * (j & 1));
          A[r & 1][pp][2 * (j & 1)] = v[0]; A[r & 1][pp][2 * (j & 1) + 1] = v[1];
        }
      };
#pragma unroll
      for (int k = 0; k < IC<conv_sched(CDIST).pre>::v; ++k) rd(k);
      SCHED();
      auto step = [&](auto ic) {
        constexpr int i = decltype(ic)::v, r = i / 36, pp = conv_sched(CDIST).pp[i], m = conv_sched(CDIST).m[i], k = conv_sched(CDIST).rd[i];
        if (i == 0) __builtin_amdgcn_s_setprio(3);
        if (i == 60) __builtin_amdgcn_s_setprio(2);
        if (i == 100) __builtin_amdgcn_s_setprio(1);
        if (i == 126) __builtin_amdgcn_s_setprio(0);
        acc[m + pp] = MFMA16(A[r & 1][pp], B[r & 1][m], i < 8 ? ZERO4 : acc[m + pp]);
        SCHED();
        if (k >= 0) rd(k);
        if (i == 1) st4(rs, tid == 448 ? WS_FX + (unsigned)quad * 128u : OOB, tag);
        if (i == 0) atv = *(const GAS unsigned*)(atb + ato);
        if (i % 6 == 2 && i / 3 < NRAW) {
          if (!HF()) { LDW(i / 3); LDW(i / 3 + 1); }
        }
        if (i > Q_AT && (i - Q_AT) % 2 == 1 && i - Q_AT < NRAW) {
          if (HF()) { LDW(i - Q_AT - 1); LDW(i - Q_AT); }
        }
        if (i == SV_AT) { ld_sv(); bs_raw = ((const GAS u16*)p.b_sp)[trow]; }
        if (r == 3 && pp >= 1 && m == 7 - pp) *(f32x4*)(yp + (pp - 1) * 1024) = acc[pp - 1];
        SCHED();
      };
      static_for<0, Q_AT>(step);
      STAMP(3);
      DRAIN();
      asm volatile("" ::"v"(atv));
      ld_q();
      mg = ld8(rs, mgo);
      SCHED();
      STAMP(4);
      static_for<Q_AT, 144>(step);
      *(f32x4*)(yp + 7 * 1024) = acc[7];
      STAMP(5);
    }
    prep();
    e_fw1 = ld4(rs, fw1o);
    STAMP(6);
    const LAS char* yr = (const LAS char*)(lds + L_YP) + (te >> 8) * 1024 + (((te >> 2) & 3) * 16 + ((((te >> 4) & 15) + 4 * ((te >> 2) & 3)) & 15)) * 16 + (te & 3) * 4;
    asm volatile("" : "+v"(yr));
    BAR();
    STAMP(7);
    f32x2 y[2][8];
#pragma unroll
    for (int i = 0; i < 16; ++i) y[i >> 3][i & 7] = *(const volatile LAS f32x2*)(yr + (i >> 3) * 4096 + (i & 7) * 8192);
#pragma unroll
    for (int H = 0; H < 2; ++H) {
      u32x4 o;
#pragma unroll
      for (int e = 0; e < 2; ++e)
#pragma unroll
        for (int i = 0; i < 2; ++i) {
          const float x0 = __builtin_fmaf(hh[H][e][i][0], y[H][2 * i][e] + y[H][4 + 2 * i][e], base[H][e][i][0]);
          const float x1 = __builtin_fmaf(hh[H][e][i][1], y[H][2 * i + 1][e] + y[H][5 + 2 * i][e], base[H][e][i][1]);
          o[2 * e + i] = pack2(x0, x1);
        }
      st16(rs, WS_X1B + (unsigned)(32 * H) * WX1P + xo, o);
    }
    STAMP(8);
  } else {
    const rsrc_t rwi = __builtin_amdgcn_make_buffer_rsrc((void*)p.w_in, 0, D_ * F_ * 2, 0x00020000);
    const rsrc_t rwo = __builtin_amdgcn_make_buffer_rsrc((void*)p.w_out, 0, FH * D_ * 2, 0x00020000);
    STAMP(32);
    const int job = xcd + 8 * slot;
    const int T = job >= 50 && w < 3 && job - 50 + 46 * w < 128 ? job - 50 + 46 * w : -1;
    const bool unit = slot >= 8 || (slot >= 2 && slot < 6);
    const int ucb = slot >= 8 ? 8 + xcd : xcd, kg = (slot >= 8 ? slot : slot - 2) & 3;
    float* tab = (float*)(lds + L_TAB);
    float* red = (float*)(lds + L_RED);
    auto issue_unit = [&](int i) {
      const int g = i * 64 + l;
      __builtin_amdgcn_raw_ptr_buffer_load_lds(rwi, (LAS void*)(lds + L_J3 + i * 1024), 16, (unsigned)((g / 14) * (F_ * 2) + (g % 14) * 16),
                                               (unsigned)(kg * 160 * F_ * 2 + ucb * CB * 2), 0, 0);
    };
    if (T >= 0) {
      char* qt = lds + L_QT + w * 20480;
      const unsigned so = (unsigned)(16 * T) * 1280u;
#pragma unroll
      for (int n = 0; n < 16; ++n)
        __builtin_amdgcn_raw_ptr_buffer_load_lds(rq, (LAS void*)(qt + n * 1024), 16, l16 ^ (unsigned)(n * 16), so + (unsigned)(n * 1280), 0, 0);
#pragma unroll
      for (int j = 0; j < 4; ++j)
        __builtin_amdgcn_raw_ptr_buffer_load_lds(rq, (LAS void*)(qt + 16384 + j * 1024), 16, (unsigned)(h4 * 1280 + ((n16 ^ (4 * j + h4)) * 16)), so + (unsigned)(4 * j * 1280 + 1024), 0, 0);
      tag = __builtin_amdgcn_readfirstlane(epv) * 2654435761u + 0x9E3779B9u;
      STAMP(34);
      DRAIN();
      STAMP(35);
      const bf2_t one = {(__bf16)1.0f, (__bf16)1.0f};
      float s1[2] = {0.f, 0.f}, s2[2] = {0.f, 0.f};
      const int n0 = 2 * (l & 7);
      u32x4 qv[20];
#pragma unroll
      for (int i = 0; i < 20; ++i)
#pragma unroll
        for (int e = 0; e < 2; ++e) {
          const int n = n0 + e, ck = 4 * (i & 15) + h4;
          const u32x2 v = *(const volatile LAS u32x2*)(qt + (i < 16 ? n * 1024 : 16384 + n * 256) + ((ck ^ n) * 16) + (l & 8));
          qv[i][2 * e] = v[0]; qv[i][2 * e + 1] = v[1];
        }
#pragma unroll
      for (int i = 0; i < 20; ++i) {
        st16(rs, WS_QB + (unsigned)(T >> 1) * WX1P + (unsigned)i * 2048u + (unsigned)((l >> 3) * 256 + (T & 1) * 128 + (l & 7) * 16), qv[i]);
#pragma unroll
        for (int e = 0; e < 4; ++e) {
          const unsigned x = qv[i][e];
          const bf2_t v = __builtin_bit_cast(bf2_t, x);
          s1[e >> 1] = __builtin_amdgcn_fdot2_f32_bf16(v, one, s1[e >> 1], false);
          s2[e >> 1] = __builtin_amdgcn_fdot2_f32_bf16(v, v, s2[e >> 1], false);
        }
        SCHED();
      }
      u32x4 o = {tag, 0u, tag, 0u};
#pragma unroll
      for (int e = 0; e < 2; ++e) {
        const float t1 = sum_rows(s1[e] + dpp_f<0x128>(s1[e])), t2 = sum_rows(s2[e] + dpp_f<0x128>(s2[e]));
        const float mean = t1 * (1.0f / D_);
        const float var = __builtin_fmaxf(t2 * (1.0f / D_) - mean * mean, 0.f);
        o[2 * e + 1] = pack2(mean, __builtin_amdgcn_sqrtf(rbf(rbf(var) + 1e-5f)));
      }
      STAMP(1);
      DRAIN();
      if (l < 8) st16(rs, WS_ST1 + (unsigned)T * 128u + l16, o);
      STAMP(3);
    }
    if (job < 60) {
      const int grp = job < 10 ? 40 + job : (job < 50 ? job - 10 : job);
      if (job >= 50 && T < 0) __builtin_amdgcn_s_sleep(LATE_SLEEP);
      if (job >= 10 && T < 0) __builtin_amdgcn_s_sleep(AUX_SLEEP);
      const unsigned cv = *(const GAS unsigned*)((const GAS u16*)p.cvec + 80 * w + 2 * (l < 40 ? l : 0));
      u32x4 pre[10];
#pragma unroll
      for (int pp = 0; pp < 10; ++pp) pre[pp] = *(const GAS u32x4*)((const GAS u16*)p.w_mod + (size_t)(80 * w + pp * 8 + (l >> 3)) * 3840 + grp * 64 + (l & 7) * 8);
      SCHED();
      STAMP(34);
      tag = __builtin_amdgcn_readfirstlane(epv) * 2654435761u + 0x9E3779B9u;
      if (l < 40) {
        const float c0 = bflo(cv), c1 = bfhi(cv);
        *(f32x2*)(tab + w * 80 + 2 * l) = (f32x2){rbf(c0 * __builtin_amdgcn_rcpf(1.0f + __expf(-c0))), rbf(c1 * __builtin_amdgcn_rcpf(1.0f + __expf(-c1)))};
      }
      asm volatile("s_waitcnt lgkmcnt(0)" ::: "memory");
      f32x2 acc[4];
#pragma unroll
      for (int e = 0; e < 4; ++e) acc[e] = (f32x2){0.f, 0.f};
#pragma unroll
      for (int pp = 0; pp < 10; ++pp) {
        const float sv = tab[w * 80 + pp * 8 + (l >> 3)];
#pragma unroll
        for (int e = 0; e < 4; ++e) acc[e] = __builtin_elementwise_fma((f32x2){sv, sv}, bf2(pre[pp][e]), acc[e]);
      }
      STAMP(35);
      asm volatile("" : "+v"(acc[0]), "+v"(acc[1]), "+v"(acc[2]), "+v"(acc[3]));
#pragma unroll
      for (int e = 0; e < 8; ++e) { float a = acc[e >> 1][e & 1]; a += dpp_f<0x128>(a); acc[e >> 1][e & 1] = sum_rows(a); }
      if (l < 8) {
        *(f32x4*)(red + w * 64 + l * 8) = (f32x4){acc[0][0], acc[0][1], acc[1][0], acc[1][1]};
        *(f32x4*)(red + w * 64 + l * 8 + 4) = (f32x4){acc[2][0], acc[2][1], acc[3][0], acc[3][1]};
      }
      if (w == 7) __builtin_amdgcn_s_setprio(2);
      BAR();
      if (w == 7) {
        float t = 0.f;
#pragma unroll
        for (int r = 0; r < 8; ++r) t += red[r * 64 + l];
        st8(rs, WS_MOD + (unsigned)(grp * 64 + l) * 8u, (u32x2){tag, __float_as_uint(rbf(t))});
        __builtin_amdgcn_s_setprio(0);
      } else if (unit) {
#pragma unroll
        for (int k = 0; k < 5; ++k) issue_unit(w * 5 + k);
      }
    } else if (unit && w >= 3 && w < 7) {
      __builtin_amdgcn_s_sleep(AUX_SLEEP);
#pragma unroll
      for (int k = 0; k < 9; ++k) issue_unit((w - 3) * 9 + k < 35 ? (w - 3) * 9 + k : 34);
    }
    tag = __builtin_amdgcn_readfirstlane(epv) * 2654435761u + 0x9E3779B9u;
    if (unit) {
      auto mod_tab = [&](int which, float add, float* dst) {
        const unsigned o = WS_MOD + (unsigned)(which * D_ + kg * 160) * 8u;
        u32x4 g0, g1;
        _Pragma("unroll 1") for (unsigned spins = 0; spins < SPIN_CAP; ++spins) {
          g0 = ld16(rs, o + l16);
          g1 = ld16(rs, o + 1024u + (l16 & 255u));
          if (__all(g0[0] == tag && g0[2] == tag && g1[0] == tag && g1[2] == tag)) break;
          __builtin_amdgcn_s_sleep(2);
        }
        *(f32x2*)(dst + 2 * l) = (f32x2){rbf(add + __uint_as_float(g0[1])), rbf(add + __uint_as_float(g0[3]))};
        if (l < 16) *(f32x2*)(dst + 128 + 2 * l) = (f32x2){rbf(add + __uint_as_float(g1[1])), rbf(add + __uint_as_float(g1[3]))};
      };
      if (w == 7) {
        STAMP(1);
        if (slot >= 8) __builtin_amdgcn_s_sleep(64);
        mod_tab(4, 1.0f, tab);
        BAR();
        STAMP(2);
        mod_tab(3, 0.0f, tab + 160);
        BAR();
      } else {
        DRAIN();
        STAMP(36);
        BAR();
        STAMP(2);
        const char* bp = lds + L_J3 + (4 * h4 + (n16 >> 2)) * 224 + (n16 & 3) * 8 + w * 32;
        const float* tp = tab + 4 * h4;
        u32x4 f3[5];
        float cs = 0.f, b1 = 0.f;
        const bool pad = w == 6 && n16 >= 12;
        const unsigned padv = (n16 == 12 && ucb >= 8) ? 0x3f803f80u : 0u;
#pragma unroll
        for (int k = 0; k < 5; ++k) f3[k] = trfrag(bp + k * 7168, 16 * 224);
#pragma unroll
        for (int k = 0; k < 5; ++k) {
          const f32x4 ca = *(const f32x4*)(tp + k * 32), cb4 = *(const f32x4*)(tp + k * 32 + 16);
          u32x4 o;
#pragma unroll
          for (int e = 0; e < 4; ++e) {
            const float c0 = e < 2 ? ca[2 * e] : cb4[2 * e - 4], c1 = e < 2 ? ca[2 * e + 1] : cb4[2 * e - 3];
            const float p0 = bflo(f3[k][e]) * c0, p1 = bfhi(f3[k][e]) * c1;
            cs += p0 + p1;
            o[e] = pad ? padv : pack2(p0, p1);
          }
          st16(rs, WS_WINF + (unsigned)(ucb * 7 + w) * 20480u + (unsigned)(kg * 5 + k) * 1024u + l16, o);
        }
        cs = sum_rows(cs);
        DRAIN();
        if (l == 0) st4(rs, WS_FW1 + (unsigned)((ucb * 8 + w) * 4 + kg) * 128u, tag);
        STAMP(5);
        BAR();
#pragma unroll
        for (int k = 0; k < 5; ++k) {
          const f32x4 sa = *(const f32x4*)(tp + 160 + k * 32), sb4 = *(const f32x4*)(tp + 160 + k * 32 + 16);
#pragma unroll
          for (int e = 0; e < 4; ++e) {
            const float s0 = e < 2 ? sa[2 * e] : sb4[2 * e - 4], s1 = e < 2 ? sa[2 * e + 1] : sb4[2 * e - 3];
            b1 = __builtin_fmaf(s0, bflo(f3[k][e]), __builtin_fmaf(s1, bfhi(f3[k][e]), b1));
          }
        }
        b1 = sum_rows(b1);
        if (pad) { cs = padv ? 160.0f : 0.0f; b1 = 0.0f; }
        if (l < 16) st16(rs, WS_CS + (unsigned)((ucb * 4 + kg) * 112 + w * 16 + l) * 16u, (u32x4){tag, __float_as_uint(cs), tag, __float_as_uint(b1)});
      }
    } else {
      if (job >= 60 && T < 0) __builtin_amdgcn_s_sleep(100);
      if (slot < 2 && w >= 2 && w < 6) {
        const int c5 = slot * 4 + w - 2, c2 = xcd;
        char* st = lds + L_J5 + (w - 2) * 18432;
#pragma unroll
        for (int r = 0; r < 18; ++r) {
          const int g = r * 64 + l;
          __builtin_amdgcn_raw_ptr_buffer_load_lds(rwo, (LAS void*)(st + r * 1024), 16, (unsigned)((g / 10) * (D_ * 2) + (g % 10) * 16),
                                                   (unsigned)(xcd * CB * D_ * 2 + c5 * 160), 0, 0);
        }
        DRAIN();
        const char* bp = st + (4 * h4 + (n16 >> 2)) * 160 + (n16 & 3) * 8;
        const unsigned base = WS_WOUTF + (unsigned)c5 * WBLK;
#pragma unroll
        for (int dj = 0; dj < 5; ++dj) {
          st16(rs, base + (unsigned)c2 * 10240u + (unsigned)dj * 1024u + l16, trfrag(bp + dj * 32, 32 * 160));
          st16(rs, base + (unsigned)c2 * 10240u + (unsigned)(5 + dj) * 1024u + l16, trfrag(bp + 16 * 160 + dj * 32, 32 * 160));
          st16(rs, base + 81920u + (unsigned)c2 * 5120u + (unsigned)dj * 1024u + l16, trfrag(bp + 64 * 160 + dj * 32, 16 * 160));
          st8(rs, base + 122880u + (unsigned)((c2 >> 1) * 5 + dj) * 1024u + (unsigned)(c2 & 1) * 512u + (unsigned)l * 8u, trd(bp + 96 * 160 + dj * 32));
        }
        fw2 = WS_FW2 + (unsigned)(c5 * 8 + xcd) * 128u;
      }
    }
    STAMP(4);
#pragma unroll
    for (int i = 0; i < NRAW; ++i) LDW(i);
    bs_raw = ((const GAS u16*)p.b_sp)[trow];
    e_fw1 = ~tag;
    asm volatile(".Lmega_aux_end:");
    asm volatile("" ::"v"(cpf));
    STAMP(8);
  }

  LAS char* xa = (LAS char*)(lds + L_XI) + l * 16;
  LAS char* xb = xa + 65536;
  asm volatile("" : "+v"(xb));
#define LDA(ks, rt) (*(const volatile LAS u32x4*)((ks) < 16 ? xa + ((ks) * 4 + (rt)) * 1024 : xb + (((ks) - 16) * 4 + (rt)) * 1024))
  auto lda1 = [&](int ks, u32x4* A) {
#pragma unroll
    for (int rt = 0; rt < 4; ++rt) A[rt] = LDA(ks, rt);
  };
  const LAS char* tb4 = (const LAS char*)(lds + L_MISC) + h4 * 16;
  const LAS char* tbn = (const LAS char*)(lds + L_MISC) + n16 * 4;
  asm volatile("" : "+v"(tb4), "+v"(tbn));
  u32x4 xr[5][2];
  auto ld_x1 = [&](int m0, int m1) {
#pragma unroll
    for (int m = m0; m < m1; ++m) {
      if (m == 0) __builtin_amdgcn_s_setprio(2);
      if (m == 1) __builtin_amdgcn_s_setprio(1);
      if (m == 3) __builtin_amdgcn_s_setprio(0);
#pragma unroll
      for (int e = 0; e < 2; ++e)
        xr[m][e] = ld16(rs, l16, WS_X1B + (unsigned)(R * 2 + (w & 1)) * WX1P + (unsigned)(4 * m + (w >> 1)) * 2048u + (unsigned)e * 1024u);
      SCHED();
    }
  };
  LAS char* xw = xa + w * 2048;
  auto bad = [](const u32x4& a, const u32x4& b) {
    auto mx = [](unsigned x, unsigned y) { return __builtin_elementwise_max(x, y); };
    return __any(mx(mx(mx(a[0], a[1]), mx(a[2], a[3])), mx(mx(b[0], b[1]), mx(b[2], b[3]))) == ~0u);
  };
  auto put_x1 = [&](int m) {
    if (bad(xr[m][0], xr[m][1])) {
      _Pragma("unroll 1") for (unsigned spins = 0; spins < SPIN_CAP; ++spins) {
#pragma unroll
        for (int e = 0; e < 2; ++e)
          xr[m][e] = ld16(rs, l16, WS_X1B + (unsigned)(R * 2 + (w & 1)) * WX1P + (unsigned)(4 * m + (w >> 1)) * 2048u + (unsigned)e * 1024u);
        if (!bad(xr[m][0], xr[m][1])) break;
        __builtin_amdgcn_s_sleep(2);
      }
    }
    LAS char* d = m < 4 ? xw + m * 16384 : xb + w * 2048;
#pragma unroll
    for (int e = 0; e < 2; ++e) {
      *(volatile LAS u32x2*)(d + 8 * e) = (u32x2){xr[m][e][0], xr[m][e][1]};
      *(volatile LAS u32x2*)(d + 1024 + 8 * e) = (u32x2){xr[m][e][2], xr[m][e][3]};
    }
  };
  auto poison_u = [&]() {
#pragma unroll
    for (int k = 0; k < 2; ++k) {
      const unsigned o = (unsigned)(wj * 2 + k) * 1024u + l16;
      const unsigned rt = o / 3584u, r = o - rt * 3584u;
      st16(rs, WS_UF + ((unsigned)(R * 4) + rt) * 8u * 3584u + (unsigned)c * 3584u + r, (u32x4){~0u, ~0u, ~0u, ~0u});
    }
  };
  const unsigned wob = WS_WOUTF + (unsigned)c * WBLK;
  const LAS char* wrd = (const LAS char*)(lds + L_WS) + w * 8192 + n16 * 256 + ((h4 ^ ((4 - (n16 >> 2)) & 3)) * 64);
#define DMA_W(g, q) __builtin_amdgcn_raw_ptr_buffer_load_lds(rsp, (LAS void*)(lds + L_WS + w * 8192 + (((g) + 1) & 1) * 4096 + (q) * 1024), 16, WVO(q), WSO(g, q), 0, 0)
#define RD_W(g) do { _Pragma("unroll") for (int i_ = 0; i_ < 4; ++i_) \
                      wsp[(g) & 3][((g) >> 2) * 4 + i_] = *(const volatile LAS u32x4*)(wrd + (((g) + 1) & 1) * 4096 + ((i_ ^ (n16 & 3)) * 16)); } while (0)
  auto park = [&]() {
    LAS char* d = (LAS char*)(lds + L_WS) + w * 8192 + l * 16;
#pragma unroll
    for (int g = 0; g < NRAW / 4; ++g) {
#pragma unroll
      for (int q = 0; q < 4; ++q) *(volatile LAS u32x4*)(d + ((g + 1) & 1) * 4096 + q * 1024) = raw[4 * g + q];
      if (g < 2) RD_W(g);
    }
    asm volatile("s_waitcnt lgkmcnt(0)" ::: "memory");
  };
#define DMA_AB(k) dma1k(rs, lds + L_AB + ((k) * 8 + w) * 1024, l16, wob + (unsigned)((k) * 8 + w) * 1024u)
  const int rto = w & 3, cls = w >> 2;
  u32x2 xp[3];
  constexpr int W = G2W;
  const unsigned zb = WS_Z2F + (unsigned)c * 458752u + (unsigned)wk * 8192u;
  const unsigned fzo = WS_FZ + (unsigned)(c * 32 + wk * 4 + (l & 3)) * 128u;
  u32x4 fr[W];
#define LDZ(i) fr[(i) % W] = ld16(rs, l16, zb + (unsigned)((i) >> 3) * 65536u + (unsigned)((i) & 7) * 1024u)
  u32x4 cr[5];
#define LDCR(k) cr[k] = ld16(rs, l16, wob + 81920u + (unsigned)(w * 5 + (k)) * 1024u)
  auto rd_xp = [&]() {
#pragma unroll
    for (int j = 0; j < 3; ++j) {
      const int q4 = 5 * c + (cls ? (3 + (j < 2 ? j : 1)) : j);
      xp[j] = *(const volatile LAS u32x2*)(xa + ((q4 >> 1) * 4 + rto) * 1024 + (q4 & 1) * 8);
    }
  };
  if (!__all(e_fw1 == tag)) {
    wait_flag<4>(rs, fw1o, tag);
    DRAIN();
    st4(rs, l == 0 ? fw2 : OOB, tag);
  }
  asm volatile("" ::: "memory");
  if (w < 7) {
    u32x4 B2[20], B1[20];
    const unsigned b2o = WS_WINF + (unsigned)((8 + c) * 7 + w) * 20480u, b1o = WS_WINF + (unsigned)(c * 7 + w) * 20480u;
#define LDB2(ks) B2[ks] = ld16(rs, l16, b2o + (unsigned)(ks) * 1024u)
#define LDB1(ks) B1[ks] = ld16(rs, l16, b1o + (unsigned)(ks) * 1024u)
#pragma unroll
    for (int ks = 0; ks < NB2A; ++ks) LDB2(ks);
    SCHED();
    STAMP(33);
    BAR();
    STAMP(37);
#pragma unroll
    for (int ks = NB2A; ks < 20; ++ks) {
      LDB2(ks);
      if (ks == 10) { SCHED(); park(); SCHED(); }
    }
    STAMP(10);
    ld_x1(0, 3);
    SCHED();
#pragma unroll
    for (int m = 0; m < 3; ++m) put_x1(m);
    ld_x1(3, 5);
    poison_u();
    SCHED();
    if (w == 6) __builtin_amdgcn_s_setprio(2);
    BAR();
    STAMP(11);
    f32x4 acc[4];
    u32x4 A[2][4];
    lda1(0, A[0]);
#pragma unroll
    for (int ks = 0; ks < 20; ++ks) {
      if (ks == 6) put_x1(3);
      if (ks == 8) put_x1(4);
      if (ks == 11) BAR();
      if (ks >= 4) LDB1(ks - 4);
      if (ks >= 16) LDB1(ks);
      if (ks == 12) RD_W(2);
      SCHED();
#pragma unroll
      for (int rt = 0; rt < 4; ++rt) {
        if (ks < 19) A[(ks + 1) & 1][rt] = LDA(ks + 1, rt);
        SCHED();
        acc[rt] = MFMA16(A[ks & 1][rt], B2[ks], ks ? acc[rt] : ZERO4);
        SCHED();
      }
    }
    STAMP(12);
    if (w == 6) {
      if (n16 == 12) {
#pragma unroll
        for (int rt = 0; rt < 4; ++rt) *(f32x4*)(lds + L_S1 + (rt * 16 + 4 * h4) * 4) = acc[rt];
      }
      asm volatile("" ::: "memory");
      *(volatile LAS unsigned*)(lds + L_S1 + 256) = tag;
      __builtin_amdgcn_s_setprio(0);
    }
    VMW(20);
    BAR();
    STAMP(13);
    f32x2 zz[8];
    f32x4 cs1a, cs1b;
    {
      f32x4 rs4[4], nm4[4];
#pragma unroll
      for (int rt = 0; rt < 4; ++rt) {
        rs4[rt] = *(const LAS f32x4*)(tb4 + (L_T2 - L_MISC) + rt * 64);
        nm4[rt] = *(const LAS f32x4*)(tb4 + (L_T2 - L_MISC) + 256 + rt * 64);
      }
      const f32x2 cs2 = *(const f32x2*)(lds + L_CS1 + (112 + 16 * w + n16) * 8);
      const f32x2 csv = {cs2[0], cs2[0]}, b1v = {cs2[1], cs2[1]};
      float v1[16], v2[16];
#pragma unroll
      for (int i = 0; i < 8; ++i) {
        const int rt = i >> 1;
        const f32x2 a2 = i & 1 ? HI2(acc[rt]) : LO2(acc[rt]), n2 = i & 1 ? HI2(nm4[rt]) : LO2(nm4[rt]), r2 = i & 1 ? HI2(rs4[rt]) : LO2(rs4[rt]);
        zz[i] = gelu2(__builtin_elementwise_fma(__builtin_elementwise_fma(n2, csv, a2), r2, b1v));
        asm volatile("" : "+v"(zz[i]));
        DMA_W(3 + (i >> 2), i & 3);
        const f32x2 sq = zz[i] * zz[i];
        v1[2 * i] = zz[i][0]; v1[2 * i + 1] = zz[i][1]; v2[2 * i] = sq[0]; v2[2 * i + 1] = sq[1];
      }
      const float t1 = tr_sum16(v1, l);
      const float t2 = tr_sum16(v2, l);
      *(f32x2*)(lds + L_Z1S + (w * 64 + (n16 >> 2) * 16 + 4 * h4 + (n16 & 3)) * 8) = (f32x2){t1, t2};
      lda1(0, A[0]);
      cs1a = *(const f32x4*)(lds + L_CS1 + (16 * w + 4 * h4) * 8);
      cs1b = *(const f32x4*)(lds + L_CS1 + (16 * w + 4 * h4) * 8 + 16);
    }
    BAR();
    STAMP(14);
#pragma unroll
    for (int ks = 0; ks < 20; ++ks) {
      if (ks == Z1SPLIT) {
        STAMP(15);
        BAR();
        STAMP(16);
        f32x4 ra[4], rb[4];
#pragma unroll
        for (int rt = 0; rt < 4; ++rt) {
          ra[rt] = *(const LAS f32x4*)(tb4 + (L_ST2 - L_MISC) + rt * 64);
          rb[rt] = *(const LAS f32x4*)(tb4 + (L_ST2 - L_MISC) + 256 + rt * 64);
        }
#pragma unroll
        for (int i = 0; i < 8; ++i) {
          zz[i] = __builtin_elementwise_fma(zz[i], i & 1 ? HI2(ra[i >> 1]) : LO2(ra[i >> 1]), i & 1 ? HI2(rb[i >> 1]) : LO2(rb[i >> 1]));
          asm volatile("" : "+v"(zz[i]));
        }
        const u32x4 o0 = {pack2(zz[0][0], zz[2][0]), pack2(zz[0][1], zz[2][1]), pack2(zz[1][0], zz[3][0]), pack2(zz[1][1], zz[3][1])};
        const u32x4 o1 = {pack2(zz[4][0], zz[6][0]), pack2(zz[4][1], zz[6][1]), pack2(zz[5][0], zz[7][0]), pack2(zz[5][1], zz[7][1])};
        const unsigned zo = WS_Z2F + (unsigned)((c * 7 + w) * 64 + 4 * (R >> 1) + h4) * 1024u + (unsigned)(R & 1) * 512u + (unsigned)n16 * 16u;
        st16(rs, zo, o0);
        st16(rs, zo + 256u, o1);
        SCHED();
      }
      if (ks == 0) { VMW(4); RD_W(3); }
      if (ks == 2) { VMW(0); RD_W(4); }
      if (ks == 8) { VMW(4 + 2); RD_W(5); }
      if (ks == 11) rd_xp();
      if (ks == 13) { VMW(4 + 5 + 2); RD_W(6); }
      if (ks >= 13 && ks < 19) DMA_AB(ks - 13);
      if (ks >= 2 && ks < 6) { DMA_W(5 + ((ks - 2) >> 1), 2 * (ks & 1)); DMA_W(5 + ((ks - 2) >> 1), 2 * (ks & 1) + 1); }
      if (ks >= 6 && ks < 9) LDCR(ks - 6);
      if (ks == 12) { LDCR(3); LDCR(4); }
      if (ks == 10 || ks == 11) { DMA_W(7, 2 * (ks & 1)); DMA_W(7, 2 * (ks & 1) + 1); }
      SCHED();
#pragma unroll
      for (int rt = 0; rt < 4; ++rt) {
        if (ks < 19) A[(ks + 1) & 1][rt] = LDA(ks + 1, rt);
        SCHED();
        acc[rt] = MFMA16(B1[ks], A[ks & 1][rt], ks ? acc[rt] : ZERO4);
        SCHED();
      }
    }
    STAMP(18);
    VMW(2 + 2 + 6);
    RD_W(7);
    const f32x2 c01 = {cs1a[0], cs1a[2]}, b01 = {cs1a[1], cs1a[3]}, c23 = {cs1b[0], cs1b[2]}, b23 = {cs1b[1], cs1b[3]};
    u32x2 g1[4];
    auto z1g = [&](int rt) {
      const float r1 = *(const LAS float*)(tbn + (L_T2 - L_MISC) + rt * 64), n1 = *(const LAS float*)(tbn + (L_T2 - L_MISC) + 256 + rt * 64);
      const f32x2 r2 = {r1, r1}, n2 = {n1, n1};
      const f32x2 a = gelu2(__builtin_elementwise_fma(__builtin_elementwise_fma(n2, c01, LO2(acc[rt])), r2, b01));
      const f32x2 b = gelu2(__builtin_elementwise_fma(__builtin_elementwise_fma(n2, c23, HI2(acc[rt])), r2, b23));
      g1[rt] = (u32x2){pack2(a[0], a[1]), pack2(b[0], b[1])};
      asm volatile("" : "+v"(g1[rt]));
    };
    SCHED();
    VMW(2 + 6);
    BAR();
    STAMP(17);
#pragma unroll
    for (int rt = 0; rt < 4; ++rt) {
      if (rt < 2) { DMA_AB(6 + 2 * rt); DMA_AB(7 + 2 * rt); }
      z1g(rt);
      *(u32x2*)(lds + L_Z1S + ((w * 4 + rt) * 64 + l) * 8) = g1[rt];
    }
  } else {
    __builtin_amdgcn_s_setprio(3);
    BAR();
    STAMP(9);
    *(volatile LAS unsigned*)(lds + L_S1 + 256) = ~tag;
    u32x4 gv, cv[2][2][4];
    const unsigned gvo = WS_MOD + (unsigned)(5 * D_ + 80 * c) * 8u + (l < 40 ? l16 : 0u);
    auto ld_consts = [&]() {
      gv = ld16(rs, gvo);
#pragma unroll
      for (int i = 0; i < 16; ++i) cv[i >> 3][(i >> 2) & 1][i & 3] = ld16(rs, l < 56 ? l16 : 0u, WS_CS + (unsigned)(((c + 8 * (i >> 3)) * 4 + (i & 3)) * 112 + 56 * ((i >> 2) & 1)) * 16u);
    };
    auto ok_consts = [&]() {
      unsigned x = (gv[0] ^ tag) | (gv[2] ^ tag);
#pragma unroll
      for (int i = 0; i < 16; ++i) x |= (cv[i >> 3][(i >> 2) & 1][i & 3][0] ^ tag) | (cv[i >> 3][(i >> 2) & 1][i & 3][2] ^ tag);
      asm volatile("" : "+v"(x));
      return __all(x == 0u);
    };
    auto touch_w = [&](int k) {
      cpf ^= __builtin_amdgcn_raw_buffer_load_b32(rsp, (unsigned)(h4 * 4096 + (n16 >> 1) * 512 + (n16 & 1) * 128),
                                                  (unsigned)(R * 64 + (c & 3) * 16 + k * 4) * 4096u + 256u, 0);
    };
    park();
    const unsigned fz1 = WS_FW1 + (unsigned)(c * 32 + (l < 28 ? l : 0)) * 128u;
    const unsigned e_z1 = ld4(rs, fz1);
    ld_consts();
    STAMP(10);
    ld_x1(0, 5);
    __builtin_amdgcn_s_setprio(3);
    if (!__all(e_z1 == tag)) wait_flag<2>(rs, fz1, tag);
    if (!ok_consts()) {
      _Pragma("unroll 1") for (unsigned spins = 0; spins < SPIN_CAP; ++spins) {
        ld_consts();
        if (ok_consts()) break;
        __builtin_amdgcn_s_sleep(8);
      }
    }
    if (l < 40) *(f32x2*)(lds + L_GB + l * 8) = (f32x2){__uint_as_float(gv[1]), __uint_as_float(gv[3])};
    if (l < 56) {
#pragma unroll
      for (int i = 0; i < 4; ++i) {
        const u32x4* q4 = cv[i >> 1][i & 1];
        *(f32x2*)(lds + L_CS1 + (56 * i + l) * 8) =
            (f32x2){(__uint_as_float(q4[0][1]) + __uint_as_float(q4[1][1])) + (__uint_as_float(q4[2][1]) + __uint_as_float(q4[3][1])),
                    (__uint_as_float(q4[0][3]) + __uint_as_float(q4[1][3])) + (__uint_as_float(q4[2][3]) + __uint_as_float(q4[3][3]))};
      }
    }
    SCHED();
#pragma unroll
    for (int m = 0; m < 3; ++m) put_x1(m);
    BAR();
    f32x4 acc[4];
    u32x4 A[2][4];
    lda1(0, A[0]);
#pragma unroll
    for (int ks = 0; ks < 20; ++ks) {
      if (ks == 6) put_x1(3);
      if (ks == 8) put_x1(4);
      if (ks == 11) BAR();
      if (ks == 4) RD_W(2);
      if (ks >= 6 && ks < 14) DMA_W(3 + ((ks - 6) >> 2), (ks - 6) & 3);
      if (ks >= 1 && ks < 5) touch_w(ks - 1);
      if (ks < 19) lda1(ks + 1, A[(ks + 1) & 1]);
      SCHED();
#pragma unroll
      for (int rt = 0; rt < 4; ++rt) acc[rt] = MFMA16(A[ks & 1][rt], A[ks & 1][rt], ks ? acc[rt] : ZERO4);
      SCHED();
    }
    {
      if ((n16 >> 2) == h4) {
#pragma unroll
        for (int rt = 0; rt < 4; ++rt) {
          const float lo = (l & 1) ? acc[rt][1] : acc[rt][0], hi = (l & 1) ? acc[rt][3] : acc[rt][2];
          *(volatile LAS float*)(tbn + (L_T2 - L_MISC) + rt * 64) = (l & 2) ? hi : lo;
        }
      }
      const float s2 = *(volatile LAS float*)(lds + L_T2 + l * 4) * (1.0f / D_);
      float s1;
      _Pragma("unroll 1") for (unsigned spins = 0; spins < SPIN_CAP * 16; ++spins) {
        const unsigned f = *(volatile LAS unsigned*)(lds + L_S1 + 256);
        s1 = *(volatile LAS float*)(lds + L_S1 + l * 4);
        if (f == tag) break;
      }
      const float nm = s1 * (-1.0f / D_);
      *(float*)(lds + L_T2 + l * 4) = __builtin_amdgcn_rsqf(__builtin_fmaxf(s2 - nm * nm, 0.f) + 1e-5f);
      *(float*)(lds + L_T2 + 256 + l * 4) = nm;
    }
    BAR();
    __builtin_amdgcn_s_setprio(0);
    asm volatile("" ::"v"(cpf));
    DRAIN();
    RD_W(3); RD_W(4);
    rd_xp();
    asm volatile("s_waitcnt lgkmcnt(0)" ::: "memory");
#pragma unroll
    for (int i = 0; i < 8; ++i) DMA_W(5 + (i >> 2), i & 3);
    const unsigned e_fw2 = ld4(rs, WS_FW2 + (unsigned)(c * 8 + (l & 7)) * 128u);
    if (!__all(e_fw2 == tag)) wait_flag<2>(rs, WS_FW2 + (unsigned)(c * 8 + (l & 7)) * 128u, tag);
    __builtin_amdgcn_s_setprio(3);
    BAR();
    {
      f32x2 t = {0.f, 0.f};
#pragma unroll
      for (int k = 0; k < 7; ++k) t += *(const f32x2*)(lds + L_Z1S + (k * 64 + l) * 8);
      st16(rs, WS_PART + (unsigned)(R * 8 + c) * 1024u + l16, (u32x4){tag, __float_as_uint(t[0]), tag, __float_as_uint(t[1])});
    }
    STAMP(14);
    {
      u32x4 v[8];
      const unsigned po = WS_PART + (unsigned)R * 8192u + l16;
      __builtin_amdgcn_s_sleep(16);
      _Pragma("unroll 1") for (unsigned spins = 0; spins < SPIN_CAP; ++spins) {
#pragma unroll
        for (int k = 0; k < 8; ++k) v[k] = ld16(rs, po + (unsigned)k * 1024u);
        unsigned x = 0;
#pragma unroll
        for (int k = 0; k < 8; ++k) x |= (v[k][0] ^ tag) | (v[k][2] ^ tag);
        asm volatile("" : "+v"(x));
        if (__all(x == 0u)) break;
        __builtin_amdgcn_s_sleep(1);
      }
      float t1 = 0.f, t2 = 0.f;
#pragma unroll
      for (int k = 0; k < 8; ++k) { t1 += __uint_as_float(v[k][1]); t2 += __uint_as_float(v[k][3]); }
      const float mean = t1 * (1.0f / FH);
      const float var = __builtin_fmaxf(t2 * (1.0f / FH) - mean * mean, 0.f);
      const float rstd = __builtin_amdgcn_rsqf(var + 1e-5f);
      *(float*)(lds + L_ST2 + l * 4) = rstd;
      *(float*)(lds + L_ST2 + 256 + l * 4) = -mean * rstd;
    }
    STAMP(15);
    BAR();
    RD_W(5); RD_W(6);
    asm volatile("s_waitcnt lgkmcnt(0)" ::: "memory");
#pragma unroll
    for (int q = 0; q < 4; ++q) DMA_W(7, q);
    DRAIN();
    RD_W(7);
    BAR();
    if (l == 0) st4(rs, WS_FZ + (unsigned)(c * 32 + R) * 128u, tag);
    STAMP(17);
    __builtin_amdgcn_s_setprio(0);
#pragma unroll
    for (int k = 0; k < 10; ++k) DMA_AB(k);
#pragma unroll
    for (int k = 0; k < 5; ++k) LDCR(k);
  }
  const rsrc_t rout = __builtin_amdgcn_make_buffer_rsrc((void*)p.out, 0, S_ * D_ * 2, 0x00020000);
  STAMP(19);
  u32x2 z1o[4];
  wait_flag<POLL_SLEEP>(rs, fzo, tag);
#pragma unroll
  for (int i = 0; i < W; ++i) { LDZ(i); SCHED(); }
  STAMP(20);

  const unsigned ub = WS_UF + (unsigned)((R * 4 + rto) * 8 + c) * 3584u;
  const float bs = bflo(bs_raw);
  const unsigned ur = WS_UF + (unsigned)((R * 4 + rto) * 8 + cls) * 3584u;
  u32x4 uf[4][2];
  f32x4 acc[2][4];
  f32x4* const bufw = (f32x4*)(lds + L_BUF) + w * 256 + l;
#define BUFW(slot, rt) bufw[(slot) * 2048 + (rt) * 64]
  {
    const f32x4* const rb = (const f32x4*)(lds + L_BUF) + cls * 2048 + rto * 64 + l;
    f32x4 rdv[4], s;
    u32x4 uo;
#pragma unroll
    for (int j = 0; j < 7; ++j) {
#pragma unroll
      for (int ks = 0; ks < 8; ++ks) {
        const int i = 8 * j + ks;
#pragma unroll
        for (int rt = 0; rt < 4; ++rt) acc[j & 1][rt] = MFMA16(fr[i % W], wsp[rt][ks], ks ? acc[j & 1][rt] : ZERO4);
        if (i == 0) STAMP(21);
        if (i == 24) STAMP(22);
        SCHED();
        if (i + W < 56) LDZ(i + W);
        if (j > 0) {
          if (ks == 0 && (j & 1)) BAR();
          if (j == 1 && ks == 0) {
#pragma unroll
            for (int k = 0; k < 4; ++k) z1o[k] = *(const u32x2*)(lds + L_Z1S + (((k < 3 ? 2 * k + cls : 6) * 4 + rto) * 64 + l) * 8);
          }
          if (i >= (56 - G2W) && i < 53) {
#pragma unroll
            for (int k = 0; k < 8; ++k)
              if ((56 - G2W) + k * (53 - (56 - G2W)) / 8 == i) uf[k >> 1][k & 1] = ld16(rs, l16, ur + (unsigned)(k >> 1) * 7168u + (unsigned)(k & 1) * 1024u);
            SCHED();
          }
          const bool fast = j == 6;
          if (!fast && ks < 4) BUFW((j - 1) & 1, ks) = acc[(j - 1) & 1][ks];
          if (fast && ks < 2) { BUFW(1, 2 * ks) = acc[1][2 * ks]; BUFW(1, 2 * ks + 1) = acc[1][2 * ks + 1]; }
          if (!(j & 1)) {
            const int k0 = fast ? 2 : 5;
            if (ks == k0) {
              BAR();
#pragma unroll
              for (int k = 0; k < 4; ++k) rdv[k] = rb[k * 256];
            }
            if (ks == k0 + 1) {
              s = (rdv[0] + rdv[1]) + (rdv[2] + rdv[3]);
#pragma unroll
              for (int k = 0; k < 4; ++k) rdv[k] = rb[(4 + k) * 256];
            }
            if (ks == k0 + 2) {
              s += (rdv[0] + rdv[1]) + (rdv[2] + rdv[3]);
              const u32x2 z = z1o[(j >> 1) - 1];
              const int e = j == 4 ? 2 : 0;
              uo[e] = pack2(bflo(z[0]) * (s[0] + bs), bfhi(z[0]) * (s[1] + bs));
              uo[e + 1] = pack2(bflo(z[1]) * (s[2] + bs), bfhi(z[1]) * (s[3] + bs));
              if (j == 4) st16(rs, ub + (unsigned)cls * 1024u + l16, uo);
              if (j == 6) st16(rs, l < 32 ? ub + 2048u + (unsigned)cls * 512u + l16 : OOB, half_swap((u32x4){uo[0], uo[1], uo[0], uo[1]}));
            }
            if (j == 6 && ks == 4) BAR();
            if (j == 6 && ks >= 4 && ks < 7) {
#pragma unroll
              for (int k = 0; k < 5; ++k)
                if (k / 2 == ks - 4) *(u32x4*)(lds + (w < 6 ? L_BUF + 32768 + w * 5120 : L_Z1S + (w - 6) * 5120) + k * 1024 + l16) = cr[k];
            }
          }
        }
        SCHED();
      }
    }
  }
  STAMP(23);

  {
    u32x4 uc[4], uh[4];
    const unsigned uho = (unsigned)(l >> 5) * 3584u + (unsigned)(l & 31) * 16u;
#define LD_C(cc) uc[cc] = ld16(rs, l16, ur + (unsigned)(cc) * 7168u + 2048u)
#define LD_H(cc) uh[cc] = ld16(rs, uho, WS_UF + (unsigned)((R * 4 + rto) * 8) * 3584u + (unsigned)(cc) * 7168u + 3072u)
    auto mx = [](unsigned a, unsigned b) { return __builtin_elementwise_max(a, b); };
    auto ok4 = [&](const u32x4* v, int n) {
      unsigned m = 0;
#pragma unroll
      for (int i = 0; i < 8; ++i)
        if (i < n) m = mx(m, mx(mx(v[i][0], v[i][1]), mx(v[i][2], v[i][3])));
      return __all(m != ~0u);
    };
#pragma unroll
    for (int rt = 0; rt < 4; ++rt) BUFW(0, rt) = acc[0][rt];
    BAR();
    f32x4 a3[5];
    u32x4 af[2][5];
    const LAS char* wb[2] = {(const LAS char*)(lds + L_AB) + cls * (10240 + 3072) + l16, (const LAS char*)(lds + L_AB) + cls * (10240 - 2048) + l16};
    const LAS char* wb2[2] = {wb[0] + 40960, wb[1] + 40960};
    asm volatile("" : "+v"(wb[0]), "+v"(wb2[0]), "+v"(wb[1]), "+v"(wb2[1]));
    u32x4 ho = {tag, tag, tag, tag};
    if (w < 4) {
      __builtin_amdgcn_s_setprio(2);
      const f32x4* const rb = (const f32x4*)(lds + L_BUF) + rto * 64 + l;
      f32x4 r8[8];
#pragma unroll
      for (int k = 0; k < 8; ++k) r8[k] = rb[k * 256];
      const f32x4 s = ((r8[0] + r8[1]) + (r8[2] + r8[3])) + ((r8[4] + r8[5]) + (r8[6] + r8[7]));
      const u32x2 z = z1o[3];
      const unsigned o0 = pack2(bflo(z[0]) * (s[0] + bs), bfhi(z[0]) * (s[1] + bs)), o1 = pack2(bflo(z[1]) * (s[2] + bs), bfhi(z[1]) * (s[3] + bs));
      ho = half_swap((u32x4){o0, o1, o0, o1});
    }
    st16(rs, tid < 256 ? (l < 32 ? ub + 3072u + l16 : OOB) : (bid == 0 && tid == 256 ? WS_EPOCH : OOB), ho);
    SCHED();
    __builtin_amdgcn_s_setprio(0);
#pragma unroll
    for (int dj = 0; dj < 5; ++dj) af[0][dj] = *(const LAS u32x4*)(wb[dj >= 2] + dj * 1024);
    const bool ab_ok = ok4(&uf[0][0], 8);
    STAMP(24);
    if (!ab_ok) {
      _Pragma("unroll 1") for (unsigned spins = 0; spins < SPIN_CAP; ++spins) {
#pragma unroll
        for (int i = 0; i < 8; ++i) uf[i >> 1][i & 1] = ld16(rs, l16, ur + (unsigned)(i >> 1) * 7168u + (unsigned)(i & 1) * 1024u);
        if (ok4(&uf[0][0], 8)) break;
        __builtin_amdgcn_s_sleep(1);
      }
    }
    STAMP(25);
    const LAS char* cb0[2] = {(const LAS char*)(lds + L_BUF + 32768) + cls * (5120 + 3072) + l16, (const LAS char*)(lds + L_BUF + 32768) + cls * (5120 - 2048) + l16};
    u32x4 ah[4][3];
    const LAS char* hb = (const LAS char*)(lds + L_BUF) + cls * 12288 + l * 8;
    f32x4 gb[3], xo3[3], xf[3];
    LAS char *otw, *xw3;
    const LAS char *ota, *otb, *xr3;
    unsigned oa_o, ob_o;
#pragma unroll
    for (int st = 0; st < 12; ++st) {
      if (st < 2) { LD_C(2 * st); LD_C(2 * st + 1); }
      else if (st < 7 && w < 4) dma1k(rs, lds + L_BUF + ((st - 2) * 4 + w) * 1024, l16, wob + 122880u + (unsigned)(w * 5 + st - 2) * 1024u);
      if (st == 2) {
        LAS char* ot = (LAS char*)(lds + L_AB + 32768) + w * 2048;
        otw = ot + n16 * 112 + h4 * 8;
        ota = ot + (l >> 2) * 112 + (l & 3) * 16;
        otb = ot + ((l >> 1) & 15) * 112 + 64 + (l & 1) * 16;
        asm volatile("" : "+v"(otw), "+v"(ota), "+v"(otb));
      }
      if (st == 3) {
        oa_o = (unsigned)(l >> 2) * 2560u + (unsigned)(l & 3) * 16u;
        ob_o = l < 32 ? (unsigned)(l >> 1) * 2560u + 64u + (unsigned)(l & 1) * 16u : OOB;
        xw3 = (LAS char*)(lds + L_AB) + w * 3072 + l16;
        xr3 = (const LAS char*)(lds + L_AB) + (w ^ 4) * 3072 + cls * 1024 + l16;
        asm volatile("" : "+v"(oa_o), "+v"(ob_o), "+v"(xw3), "+v"(xr3));
      }
      if (st >= 4 && st < 7) {
        const int j = st - 4;
        gb[j] = *(const f32x4*)(lds + L_GB + (48 * cls + 16 * (cls && j == 2 ? 1 : j) + 4 * h4) * 4);
        xf[j] = (f32x4){bflo(xp[j][0]), bfhi(xp[j][0]), bflo(xp[j][1]), bfhi(xp[j][1])};
        asm volatile("" : "+v"(xf[j]));
      }
      if (st == 8) {
        STAMP(26);
        if (!ok4(uc, 4)) {
          _Pragma("unroll 1") for (unsigned spins = 0; spins < SPIN_CAP; ++spins) {
            LD_C(0); LD_C(1); LD_C(2); LD_C(3);
            if (ok4(uc, 4)) break;
            __builtin_amdgcn_s_sleep(1);
          }
        }
        LD_H(0); LD_H(1); LD_H(2); LD_H(3);
#pragma unroll
        for (int cc = 0; cc < 4; ++cc) uc[cc] = half_swap(uc[cc]);
        VMW(4);
        __builtin_amdgcn_s_barrier();
        asm volatile("" ::: "memory");
        STAMP(27);
      }
      SCHED();
#pragma unroll
      for (int dj = 0; dj < 5; ++dj) {
        const int sn = st + 1;
        if (sn < 8) af[sn & 1][dj] = *(const LAS u32x4*)((sn < 4 ? wb : wb2)[dj >= 2] + ((sn >> 1) & 1) * 20480 + ((sn & 1) * 5 + dj) * 1024);
        else if (sn < 12) af[sn & 1][dj] = *(const LAS u32x4*)(cb0[dj >= 2] + (sn < 11 ? (sn - 8) * 10240 : L_Z1S - L_BUF - 32768) + dj * 1024);
        if (st >= 8) {
#pragma unroll
          for (int k = 0; k < 2; ++k) {
            const int h = st < 11 ? (k ? 99 : (st - 8) * 5 + dj) : 15 + 2 * dj + k;
            if (h < 24) {
              const u32x2 v = *(const volatile LAS u32x2*)(hb + (((h >> 1) % 3) * 4 + (h >> 1) / 3) * 1024 + (h & 1) * 512);
              ah[(h >> 1) / 3][(h >> 1) % 3][2 * (h & 1)] = v[0]; ah[(h >> 1) / 3][(h >> 1) % 3][2 * (h & 1) + 1] = v[1];
            }
          }
        }
        SCHED();
        a3[dj] = MFMA16(af[st & 1][dj], st < 8 ? uf[(st >> 1) & 3][st & 1] : uc[st & 3], st ? a3[dj] : ZERO4);
        SCHED();
      }
    }
#pragma unroll
    for (int j = 0; j < 3; ++j) *(volatile LAS f32x4*)(xw3 + j * 1024) = a3[2 + j];
    BAR();
#pragma unroll
    for (int j = 0; j < 3; ++j) xo3[j] = *(const volatile LAS f32x4*)(xr3 + j * 1024);
    SCHED();
    if (!ok4(uh, 4)) {
      _Pragma("unroll 1") for (unsigned spins = 0; spins < SPIN_CAP; ++spins) {
        LD_H(0); LD_H(1); LD_H(2); LD_H(3);
        if (ok4(uh, 4)) break;
        __builtin_amdgcn_s_sleep(1);
      }
    }
#pragma unroll
    for (int cc = 0; cc < 4; ++cc) {
      const u32x4 b = half_swap(uh[cc]);
#pragma unroll
      for (int j = 0; j < 3; ++j) a3[j] = MFMA16(ah[cc][j], b, a3[j]);
      if (cc >= 1) {
        const int j = cc - 1;
        xo3[j] = __builtin_elementwise_fma(gb[j], xo3[j], xf[j]);
        asm volatile("" : "+v"(xo3[j]));
      }
      SCHED();
    }
#pragma unroll
    for (int j = 0; j < 3; ++j) {
      const f32x4 r = __builtin_elementwise_fma(gb[j], a3[j], xo3[j]);
      *(volatile LAS u32x2*)(otw + j * 32) = (u32x2){pack2(r[0], r[1]), pack2(r[2], r[3])};
    }
    const u32x4 oa = *(const volatile LAS u32x4*)(ota);
    const u32x4 ob = *(const volatile LAS u32x4*)(otb);
    const unsigned obase = (unsigned)((R * 64 + 32 * ((w >> 1) & 1) + (w & 1)) * D_ + 80 * c + 48 * cls) * 2u;
    __builtin_amdgcn_raw_buffer_store_b128(oa, rout, oa_o, obase, 0);
    if (cls == 0) __builtin_amdgcn_raw_buffer_store_b128(ob, rout, ob_o, obase, 0);
  }
  STAMP(30);
  asm volatile(".Lmega_end:");
}

extern "C" void kernel_launch(void* const* d_in, const int* in_sizes, int n_in, void* d_out, int out_size,
                              void* d_ws, size_t ws_size, hipStream_t stream) {
  Params p;
  p.q = (const u16*)d_in[0];
  p.k = (const u16*)d_in[1];
  p.v = (const u16*)d_in[2];
  p.shift_k = (const u16*)d_in[3];
  p.ssm_k = (const u16*)d_in[4];
  p.ssm_D = (const u16*)d_in[5];
  p.w_in = (const u16*)d_in[7];
  p.w_sp = (const u16*)d_in[8];
  p.b_sp = (const u16*)d_in[9];
  p.w_out = (const u16*)d_in[10];
  p.cvec = (const u16*)d_in[11];
  p.w_mod = (const u16*)d_in[12];
  p.out = (u16*)d_out;
  p.ws = (char*)d_ws;
  mega<<<256, 512, 0, stream>>>(p);
}
```
